```python
import torch

def _softmax(x, axis=-1):
    return torch.softmax(x, dim=axis)
def _gelu(x):
    return torch.nn.functional.gelu(x, approximate="tanh")
def _silu(x):
    return torch.nn.functional.silu(x)
def _sigmoid(x):
    return torch.sigmoid(x)
def _layernorm(x, axis=-1, eps=1e-5):
    m = x.mean(dim=axis, keepdim=True)
    v = x.var(dim=axis, keepdim=True, unbiased=False)
    return (x - m) / torch.sqrt(v + eps)
def _scatter_add(tgt, idx, src, axis=0):
    return tgt.clone().index_add_(axis, idx, src)
def _segment_sum(src, seg, n):
    out = torch.zeros((n,) + src.shape[1:], dtype=src.dtype)
    return out.index_add_(0, seg, src)
def _pad(x, pw):
    flat = tuple(v for pair in reversed(pw) for v in pair)
    return torch.nn.functional.pad(x, flat)

kernel_name = 'standalone__gqa_softmax__near_ridge'

def setup_inputs():

    _w = (lambda a: (lambda t: t.to(torch.bfloat16) if t.is_floating_point() else t)(torch.as_tensor(a)))
    B, Hq, Hkv, Sq, Sk, D = 16, 32, 32, 384, 384, 192
    return {
        "q": _w((lambda *s: torch.randn(*s, dtype=torch.bfloat16) * 0.8)(B, Hq, Sq, D)),
        "k": _w((lambda *s: torch.randn(*s, dtype=torch.bfloat16) * 0.8)(B, Hkv, Sk, D)),
        "v": _w((lambda *s: torch.randn(*s, dtype=torch.bfloat16) * 0.05)(B, Hkv, Sk, D)),
    }

def reference(q, k, v):
    B, Hq, Sq, D = q.shape
    Hkv = k.shape[1]
    rep = Hq // Hkv
    k = (lambda x, n, axis: torch.repeat_interleave(x, n, dim=axis))(k, rep, axis=1)
    v = (lambda x, n, axis: torch.repeat_interleave(x, n, dim=axis))(v, rep, axis=1)
    scale = D ** -0.5
    scores = torch.einsum("bhqd,bhkd->bhqk", q, k) * scale
    p = _softmax(scores, axis=-1)
    return torch.einsum("bhqk,bhkd->bhqd", p, v)
```

```cpp
#define PNT -1
#define NT_Q 0
#define NT_KV 0
#define NT_O 0
#include <hip/hip_runtime.h>
#include <hip/hip_bf16.h>

typedef __bf16   bf16x8 __attribute__((ext_vector_type(8)));
typedef __bf16   bf16x2 __attribute__((ext_vector_type(2)));
typedef short    s16x4  __attribute__((ext_vector_type(4)));
typedef short    s16x8  __attribute__((ext_vector_type(8)));
typedef float    f32x16 __attribute__((ext_vector_type(16)));
typedef float    f32x2  __attribute__((ext_vector_type(2)));
typedef unsigned u32x4  __attribute__((ext_vector_type(4)));
typedef __attribute__((address_space(3))) const char* lds_cptr;
typedef __attribute__((address_space(3))) char* lds_ptr;

#define LBW 1

constexpr int SEQ = 384, HD = 192, NTILE = 12, TILEB = 12288, HEADB = SEQ * HD * 2;
constexpr int KRING = 0, VRING = 3 * TILEB, LDS_BYTES = 6 * TILEB;
constexpr float C2 = 0.07216878364870322f * 1.4426950408889634f;
constexpr float SUM_THR = 1024.0f;

#define DI __device__ __forceinline__
#define SBAR() __builtin_amdgcn_sched_barrier(0)
#define PIN(x) asm volatile("" : "+v"(x))
#define MFMA(a, b, c) __builtin_amdgcn_mfma_f32_32x32x16_bf16(a, b, c, 0, 0, 0)
#define KV_NT ""
#define Q_NT ""

DI unsigned cvtpk(float lo, float hi) { f32x2 v = {lo, hi}; bf16x2 b = __builtin_convertvector(v, bf16x2); return __builtin_bit_cast(unsigned, b); }
DI bf16x8 kread(lds_cptr p) { return *(const __attribute__((address_space(3))) bf16x8*)p; }
DI s16x4 vtr(lds_cptr p) { return __builtin_amdgcn_ds_read_tr16_b64_v4i16((__attribute__((address_space(3))) s16x4*)p); }
DI bf16x8 cat(s16x4 a, s16x4 b) { return __builtin_bit_cast(bf16x8, __builtin_shufflevector(a, b, 0, 1, 2, 3, 4, 5, 6, 7)); }
DI void dma16(unsigned m0v, unsigned voff, __amdgpu_buffer_rsrc_t srd, unsigned soff) {
    asm volatile("s_mov_b32 m0, %0\n\ts_nop 0\n\tbuffer_load_dwordx4 %1, %2, %3 offen" KV_NT " lds" :: "s"(m0v), "v"(voff), "s"(srd), "s"(soff) : "memory");
}
#define MX3(a, b, c) __builtin_fmaxf(__builtin_fmaxf((a), (b)), (c))
DI float rowmax16(const f32x16& p) {
    float a = MX3(p[0], p[1], p[2]), b = MX3(p[3], p[4], p[5]);
    a = MX3(a, p[6], p[7]); b = MX3(b, p[8], p[9]); a = MX3(a, p[10], p[11]); b = MX3(b, p[12], p[13]); a = MX3(a, p[14], p[15]);
    float m = __builtin_fmaxf(a, b);
    auto rr = __builtin_amdgcn_permlane32_swap(__float_as_uint(m), __float_as_uint(m), false, false);
    return __builtin_fmaxf(__uint_as_float(rr[0]), __uint_as_float(rr[1]));
}

__global__ __launch_bounds__(256, LBW) void attn_fwd(const unsigned short* __restrict__ Q, const unsigned short* __restrict__ K,
                                                    const unsigned short* __restrict__ V, unsigned short* __restrict__ O) {
    extern __shared__ __attribute__((aligned(16))) char lds[];
    const unsigned tid = threadIdx.x, lane = tid & 63u, r32 = lane & 31u, hi = lane >> 5;
    const unsigned wid = __builtin_amdgcn_readfirstlane(tid >> 6);
    const unsigned bid = blockIdx.x, xcd = bid & 7u, idx = bid >> 3;
    const unsigned head = xcd * 64u + idx / 3u, qblk = idx % 3u;
    const unsigned lds0 = (unsigned)(uintptr_t)lds;

    __amdgpu_buffer_rsrc_t srdK = __builtin_amdgcn_make_buffer_rsrc((void*)((const char*)K + (size_t)head * HEADB), (short)0, HEADB, 0x00020000);
    __amdgpu_buffer_rsrc_t srdV = __builtin_amdgcn_make_buffer_rsrc((void*)((const char*)V + (size_t)head * HEADB), (short)0, HEADB, 0x00020000);
    const unsigned krow = lane >> 3, kf = ((krow >> 1) & 3u) | ((wid & 1u) << 2);
    const unsigned ksrc = krow * 384u + 16u * ((lane & 7u) ^ kf);
    const unsigned vsrc = ((lane >> 2) & 7u) * 384u + hi * 64u + (lane & 3u) * 16u;
    const unsigned m0k = lds0 + KRING + wid * 3072u, m0v = lds0 + VRING + wid * 3072u, sof = wid * 3072u;
    auto dmaK = [&](int t, unsigned slot) {
#pragma unroll
        for (int c = 0; c < 3; ++c) dma16(m0k + slot + 1024u * c, ksrc, srdK, sof + (unsigned)t * TILEB + 128u * c);
    };
    auto dmaV = [&](int t, unsigned slot) {
#pragma unroll
        for (int c = 0; c < 3; ++c) dma16(m0v + slot + 1024u * c, vsrc, srdV, sof + (unsigned)t * TILEB + 128u * c);
    };
    const unsigned rf = ((r32 >> 1) & 3u) | (((r32 >> 3) & 1u) << 2);
    lds_cptr kb[4];
#pragma unroll
    for (int a = 0; a < 4; ++a) kb[a] = (lds_cptr)lds + KRING + (r32 >> 3) * 3072u + (r32 & 7u) * 128u + 16u * ((2u * a + hi) ^ rf);
    lds_cptr vb = (lds_cptr)lds + VRING + (4u * hi + ((lane & 15u) >> 2)) * 64u + ((lane >> 4) & 1u) * 32u + (lane & 3u) * 8u;

    dmaK(0, 0);
    bf16x8 qf[12];
    {
        const char* qp = (const char*)Q + (size_t)head * HEADB + (size_t)(qblk * 128u + wid * 32u + r32) * 384u + hi * 16u;
#pragma unroll
        for (int s = 0; s < 12; ++s) asm volatile("global_load_dwordx4 %0, %1, off offset:%2" Q_NT : "=v"(qf[s]) : "v"(qp), "i"(32 * s) : "memory");
    }
    dmaK(1, TILEB); dmaV(0, 0); dmaK(2, 2 * TILEB);
    asm volatile("s_waitcnt vmcnt(9)\n\ts_barrier" : "+v"(qf[0]), "+v"(qf[1]), "+v"(qf[2]), "+v"(qf[3]), "+v"(qf[4]), "+v"(qf[5]), "+v"(qf[6]), "+v"(qf[7]), "+v"(qf[8]), "+v"(qf[9]), "+v"(qf[10]), "+v"(qf[11]) :: "memory");

    f32x16 o[6];
#pragma unroll
    for (int d = 0; d < 6; ++d) o[d] = f32x16{};
    f32x16 SA, SB;
    float nm, lsum = 0.f;
    u32x4 P0, P1;
    bf16x8 kf0, kf1, kf2, kf3;
    s16x4 vl0, vh0, vl1, vh1, vl2, vh2;
#define EX(v) __builtin_amdgcn_exp2f(__builtin_fmaf((v), C2, nm))
#define KRD(slot, s) kread(kb[(s) & 3] + (slot) + ((s) >> 2) * 1024)
    {
        SA = f32x16{};
#pragma unroll
        for (int s = 0; s < 12; ++s) SA = MFMA(KRD(0, s), qf[s], SA);
    }
    asm volatile("s_waitcnt vmcnt(6) lgkmcnt(0)\n\ts_barrier" ::: "memory");
    dmaK(3, 0); dmaV(1, TILEB);
    nm = -rowmax16(SA) * C2;
    float ts0;
    {
        float p[8];
#pragma unroll
        for (int i = 0; i < 8; ++i) p[i] = EX(SA[i]);
        ts0 = ((p[0] + p[1]) + (p[2] + p[3])) + ((p[4] + p[5]) + (p[6] + p[7]));
#pragma unroll
        for (int i = 0; i < 4; ++i) P0[i] = cvtpk(p[2 * i], p[2 * i + 1]);
    }
    kf0 = KRD(TILEB, 0); kf1 = KRD(TILEB, 1); kf2 = KRD(TILEB, 2);
    kb[0] += TILEB; kb[1] += TILEB; kb[2] += TILEB; kb[3] += TILEB;
    unsigned ks1 = TILEB, ks2 = 2 * TILEB, vs0 = 0, vs2 = 2 * TILEB;

#define SMP(S, e, PW, TS) do { float pa_ = EX(S[e]), pb_ = EX(S[(e) + 1]); TS += pa_; TS += pb_; PW = cvtpk(pa_, pb_); } while (0)
#define VOFF(d, s) (((s) * 6144) + ((d) >> 1) * 1024 + ((d) & 1) * 512)
#define VRD(L, H, d, s) do { L = vtr(vp + VOFF(d, s)); H = vtr(vp + VOFF(d, s) + 3072); } while (0)
    auto step = [&](f32x16& Sc, f32x16& Sn, int t) __attribute__((always_inline)) {
        float ts1 = 0.f;
#define KR(s) kread(kb[(s) & 3] + ((s) >> 2) * 1024)
#define vp vb
        SBAR();
        Sn = MFMA(kf0, qf[0], f32x16{});  kf3 = KR(3);  SBAR();
        Sn = MFMA(kf1, qf[1], Sn);      kf0 = KR(4);  SMP(Sc, 8, P1[0], ts1);  PIN(P1); PIN(ts1); SBAR();
        Sn = MFMA(kf2, qf[2], Sn);      kf1 = KR(5);  SBAR();
        Sn = MFMA(kf3, qf[3], Sn);      kf2 = KR(6);  SMP(Sc, 10, P1[1], ts1); PIN(P1); PIN(ts1); SBAR();
        Sn = MFMA(kf0, qf[4], Sn);      kf3 = KR(7);  SBAR();
        Sn = MFMA(kf1, qf[5], Sn);      kf0 = KR(8);  SMP(Sc, 12, P1[2], ts1); PIN(P1); PIN(ts1); SBAR();
        Sn = MFMA(kf2, qf[6], Sn);      kf1 = KR(9);  SBAR();
        Sn = MFMA(kf3, qf[7], Sn);      kf2 = KR(10); SMP(Sc, 14, P1[3], ts1); PIN(P1); PIN(ts1); SBAR();
        Sn = MFMA(kf0, qf[8], Sn);      kf3 = KR(11); SBAR();
        Sn = MFMA(kf1, qf[9], Sn);      SBAR();
        float ts = ts0 + ts1;
        const bool bad = __any(!(ts <= SUM_THR));
        PIN(kf2); PIN(kf3);
        asm volatile("s_waitcnt vmcnt(6) lgkmcnt(0)\n\ts_barrier" ::: "memory");
        VRD(vl0, vh0, 0, 0); VRD(vl1, vh1, 1, 0);
        dmaK(t + 4, ks1); dmaV(t + 2, vs2);
        SBAR();
        Sn = MFMA(kf2, qf[10], Sn);     SBAR();
        Sn = MFMA(kf3, qf[11], Sn);     VRD(vl2, vh2, 2, 0); SBAR();
        if (__builtin_expect(bad, 0)) {
            const float nmn = __builtin_fminf(nm, -rowmax16(Sc) * C2);
            const float al = __builtin_amdgcn_exp2f(nmn - nm);
            nm = nmn; lsum *= al;
#pragma unroll
            for (int d = 0; d < 6; ++d) {
#pragma unroll
                for (int i = 0; i < 16; ++i) { float x = o[d][i]; asm volatile("v_mul_f32 %0, %1, %0" : "+v"(x) : "v"(al)); o[d][i] = x; } }
            float pp[16]; ts = 0.f;
#pragma unroll
            for (int i = 0; i < 16; ++i) { pp[i] = EX(Sc[i]); ts += pp[i]; }
#pragma unroll
            for (int i = 0; i < 4; ++i) { P0[i] = cvtpk(pp[2 * i], pp[2 * i + 1]); P1[i] = cvtpk(pp[8 + 2 * i], pp[8 + 2 * i + 1]); }
        }
        lsum += ts;
        const bf16x8 pf0 = __builtin_bit_cast(bf16x8, P0), pf1 = __builtin_bit_cast(bf16x8, P1);
        { const int dk = (int)ks2 - (int)ks1; kb[0] += dk; kb[1] += dk; kb[2] += dk; kb[3] += dk; PIN(kb[0]); PIN(kb[1]); PIN(kb[2]); PIN(kb[3]); }
        u32x4 N0; float tn = 0.f;
        SBAR();
        o[0] = MFMA(cat(vl0, vh0), pf0, o[0]); VRD(vl0, vh0, 3, 0); SBAR();
        o[1] = MFMA(cat(vl1, vh1), pf0, o[1]); VRD(vl1, vh1, 4, 0); SBAR();
        o[2] = MFMA(cat(vl2, vh2), pf0, o[2]); VRD(vl2, vh2, 5, 0); SBAR();
        o[3] = MFMA(cat(vl0, vh0), pf0, o[3]); VRD(vl0, vh0, 0, 1); SMP(Sn, 0, N0[0], tn); PIN(N0); PIN(tn); SBAR();
        o[4] = MFMA(cat(vl1, vh1), pf0, o[4]); VRD(vl1, vh1, 1, 1); SBAR();
        o[5] = MFMA(cat(vl2, vh2), pf0, o[5]); VRD(vl2, vh2, 2, 1); SMP(Sn, 2, N0[1], tn); PIN(N0); PIN(tn); SBAR();
        o[0] = MFMA(cat(vl0, vh0), pf1, o[0]); VRD(vl0, vh0, 3, 1); SBAR();
        o[1] = MFMA(cat(vl1, vh1), pf1, o[1]); VRD(vl1, vh1, 4, 1); SMP(Sn, 4, N0[2], tn); PIN(N0); PIN(tn); SBAR();
        o[2] = MFMA(cat(vl2, vh2), pf1, o[2]); VRD(vl2, vh2, 5, 1); SBAR();
        o[3] = MFMA(cat(vl0, vh0), pf1, o[3]); kf0 = kread(kb[0]); SMP(Sn, 6, N0[3], tn); PIN(N0); PIN(tn); SBAR();
        o[4] = MFMA(cat(vl1, vh1), pf1, o[4]); kf1 = kread(kb[1]); SBAR();
        o[5] = MFMA(cat(vl2, vh2), pf1, o[5]); kf2 = kread(kb[2]); SBAR();
        P0 = N0; ts0 = tn;
        ks1 = ks2; ks2 = (ks2 == 2 * TILEB) ? 0u : ks2 + TILEB;
        { const unsigned nv = (vs0 == 2 * TILEB) ? 0u : vs0 + TILEB; vb += (int)nv - (int)vs0; PIN(vb); vs2 = vs0; vs0 = nv; }
    };
    for (int t = 0; t < NTILE; t += 2) { step(SA, SB, t); step(SB, SA, t + 1); }

    asm volatile("s_waitcnt vmcnt(0)\n\ts_barrier" ::: "memory");
    { auto rr = __builtin_amdgcn_permlane32_swap(__float_as_uint(lsum), __float_as_uint(lsum), false, false); lsum = __uint_as_float(rr[0]) + __uint_as_float(rr[1]); }
    const float il = __builtin_amdgcn_rcpf(lsum);
    lds_ptr stg = (lds_ptr)lds + wid * TILEB;
#pragma unroll
    for (int d = 0; d < 6; ++d) {
#pragma unroll
        for (int gp = 0; gp < 2; ++gp) {
            unsigned a0 = cvtpk(o[d][8 * gp + 0] * il, o[d][8 * gp + 1] * il), a1 = cvtpk(o[d][8 * gp + 2] * il, o[d][8 * gp + 3] * il);
            unsigned b0 = cvtpk(o[d][8 * gp + 4] * il, o[d][8 * gp + 5] * il), b1 = cvtpk(o[d][8 * gp + 6] * il, o[d][8 * gp + 7] * il);
            auto r0 = __builtin_amdgcn_permlane32_swap(a0, b0, false, false); auto r1 = __builtin_amdgcn_permlane32_swap(a1, b1, false, false);
            u32x4 w = {r0[0], r1[0], r0[1], r1[1]};
            const unsigned c = 4u * d + 2u * gp + hi;
            *(__attribute__((address_space(3))) u32x4*)(stg + r32 * 384u + 16u * ((c & ~7u) | ((c & 7u) ^ (r32 & 7u)))) = w;
        }
    }
    asm volatile("s_waitcnt lgkmcnt(0)" ::: "memory");
    char* op = (char*)O + (size_t)head * HEADB + (size_t)(qblk * 128u + wid * 32u) * 384u;
#pragma unroll
    for (int i = 0; i < 12; ++i) {
        const unsigned n = 64u * i + lane, row = n / 24u;
        u32x4 w = *(const __attribute__((address_space(3))) u32x4*)(stg + n * 16u);
        u32x4* dst = (u32x4*)(op + 16u * (n ^ (row & 7u)));
        *dst = w;
    }
}

typedef unsigned pu32x4 __attribute__((ext_vector_type(4)));
__global__ __launch_bounds__(256) void probe(const pu32x4* __restrict__ q, const pu32x4* __restrict__ k, const pu32x4* __restrict__ v, pu32x4* __restrict__ o, int n16) {
    for (int i0 = blockIdx.x * 256 * 2 + threadIdx.x; i0 < n16; i0 += gridDim.x * 256 * 2) {
        pu32x4 a[2], b[2], c[2];
#pragma unroll
        for (int u = 0; u < 2; ++u) { int i = i0 + u * 256;
        if (PNT) { a[u] = __builtin_nontemporal_load(q + i); b[u] = __builtin_nontemporal_load(k + i); c[u] = __builtin_nontemporal_load(v + i); }
        else { a[u] = q[i]; b[u] = k[i]; c[u] = v[i]; } }
#pragma unroll
        for (int u = 0; u < 2; ++u) { int i = i0 + u * 256;
        pu32x4 r = a[u] ^ b[u] ^ c[u];
        if (PNT == 1) __builtin_nontemporal_store(r, o + i); else o[i] = r; }
    }
}
extern "C" void kernel_launch(void* const* d_in, const int* in_sizes, int n_in, void* d_out, int out_size, void* d_ws, size_t ws_size, hipStream_t stream) {
    (void)in_sizes; (void)n_in; (void)out_size; (void)d_ws; (void)ws_size;
    hipFuncSetAttribute((const void*)attn_fwd, hipFuncAttributeMaxDynamicSharedMemorySize, LDS_BYTES);
    if (PNT >= 0) probe<<<2048, 256, 0, stream>>>((const pu32x4*)d_in[0], (const pu32x4*)d_in[1], (const pu32x4*)d_in[2], (pu32x4*)d_ws, out_size / 8);
    hipLaunchKernelGGL(attn_fwd, dim3(1536), dim3(256), LDS_BYTES, stream, (const unsigned short*)d_in[0], (const unsigned short*)d_in[1], (const unsigned short*)d_in[2], (unsigned short*)d_out);
}
```

```cpp
#include <hip/hip_runtime.h>
#include <hip/hip_bf16.h>

constexpr int LDS_BYTES = 163840;
constexpr unsigned HEADB = 147456;

__global__ __launch_bounds__(256, 1) void attn_fwd(const void* __restrict__ Q, const void* __restrict__ K, const void* __restrict__ V, void* __restrict__ O, void* __restrict__ WS) {
    extern __shared__ __attribute__((aligned(16))) char lds[];
    const unsigned bid = blockIdx.x;
    const unsigned h0 = bid * HEADB, h1 = (bid + 256u) * HEADB;
    const unsigned ldsb = (unsigned)(uintptr_t)lds;
    asm volatile(
"s_mov_b64 s[40:41], %0\n"
"s_and_b32 s41, s41, 0xffff\n"
"s_mov_b32 s42, 75497472\n"
"s_mov_b32 s43, 0x20000\n"
"s_mov_b64 s[44:45], %1\n"
"s_and_b32 s45, s45, 0xffff\n"
"s_mov_b32 s46, 75497472\n"
"s_mov_b32 s47, 0x20000\n"
"s_mov_b64 s[48:49], %2\n"
"s_and_b32 s49, s49, 0xffff\n"
"s_mov_b32 s50, 75497472\n"
"s_mov_b32 s51, 0x20000\n"
"s_mov_b64 s[52:53], %3\n"
"s_and_b32 s53, s53, 0xffff\n"
"s_mov_b32 s54, 75497472\n"
"s_mov_b32 s55, 0x20000\n"
"v_lshrrev_b32 v51, 6, %7\n"
"v_and_b32 v48, 63, %7\n"
"v_readfirstlane_b32 s66, v51\n"
"v_and_b32 v49, 15, v48\n"
"v_lshrrev_b32 v50, 4, v48\n"
"s_nop 3\n"
"s_mov_b32 s72, %6\n"
"s_mul_i32 s67, s66, 3072\n"
"s_add_u32 s56, s72, s67\n"
"s_add_u32 s57, %4, s67\n"
"s_add_u32 s58, %5, s67\n"
"s_mul_i32 s61, s66, 36864\n"
"s_add_u32 s59, %4, s61\n"
"s_add_u32 s60, %5, s61\n"
"s_mul_i32 s68, s66, 12288\n"
"s_add_u32 s68, s68, 98304\n"
"s_add_u32 s69, s68, s72\n"
"s_lshl_b32 s73, s66, 12\n"
"s_add_u32 s73, s73, 147456\n"
"s_add_u32 s73, s73, s72\n"
"s_mul_i32 s75, s66, 5120\n"
"s_mul_i32 s79, s66, 1024\n"
"s_mov_b32 s62, 0x3dd53b95\n"
"s_mov_b32 s63, 0x46800000\n"
"s_mov_b32 s64, 0\n"
"s_mov_b32 s65, 0\n"
"s_mov_b32 s71, 0\n"
"v_lshrrev_b32 v51, 3, v48\n"
"v_lshrrev_b32 v52, 1, v51\n"
"v_and_b32 v52, 3, v52\n"
"v_and_b32 v53, 7, v48\n"
"v_xor_b32 v53, v53, v52\n"
"v_lshlrev_b32 v53, 4, v53\n"
"v_mul_u32_u24 v51, 0x180, v51\n"
"v_add_u32 v7, v51, v53\n"
"s_and_b32 s70, s66, 1\n"
"s_lshl_b32 s70, s70, 6\n"
"s_xor_b32 s74, s70, 64\n"
"s_lshr_b32 s61, %9, 5\n"
"s_and_b32 s61, s61, 1\n"
".Lstagl:\n"
"s_cmp_eq_u32 s61, 0\n"
"s_cbranch_scc1 .Lnostag\n"
"s_sleep 110\n"
"s_sub_u32 s61, s61, 1\n"
"s_branch .Lstagl\n"
".Lnostag:\n"
"s_add_u32 m0, s56, 49152\n"
"s_add_u32 s61, s59, 0\n"
"buffer_load_dwordx4 v7, s[40:43], s61 offen nt lds\n"
"s_add_u32 m0, s56, 50176\n"
"s_add_u32 s61, s59, 128\n"
"buffer_load_dwordx4 v7, s[40:43], s61 offen nt lds\n"
"s_add_u32 m0, s56, 51200\n"
"s_add_u32 s61, s59, 256\n"
"buffer_load_dwordx4 v7, s[40:43], s61 offen nt lds\n"
"v_xor_b32 v7, 64, v7\n"
"s_add_u32 m0, s56, 0\n"
"s_add_u32 s61, s59, 3072\n"
"buffer_load_dwordx4 v7, s[40:43], s61 offen nt lds\n"
"s_add_u32 m0, s56, 1024\n"
"s_add_u32 s61, s59, 3200\n"
"buffer_load_dwordx4 v7, s[40:43], s61 offen nt lds\n"
"s_add_u32 m0, s56, 2048\n"
"s_add_u32 s61, s59, 3328\n"
"buffer_load_dwordx4 v7, s[40:43], s61 offen nt lds\n"
"v_xor_b32 v7, 64, v7\n"
"s_add_u32 m0, s56, 61440\n"
"s_add_u32 s61, s59, 6144\n"
"buffer_load_dwordx4 v7, s[40:43], s61 offen nt lds\n"
"s_add_u32 m0, s56, 62464\n"
"s_add_u32 s61, s59, 6272\n"
"buffer_load_dwordx4 v7, s[40:43], s61 offen nt lds\n"
"s_add_u32 m0, s56, 63488\n"
"s_add_u32 s61, s59, 6400\n"
"buffer_load_dwordx4 v7, s[40:43], s61 offen nt lds\n"
"v_xor_b32 v7, 64, v7\n"
"s_add_u32 m0, s56, 12288\n"
"s_add_u32 s61, s59, 9216\n"
"buffer_load_dwordx4 v7, s[40:43], s61 offen nt lds\n"
"s_add_u32 m0, s56, 13312\n"
"s_add_u32 s61, s59, 9344\n"
"buffer_load_dwordx4 v7, s[40:43], s61 offen nt lds\n"
"s_add_u32 m0, s56, 14336\n"
"s_add_u32 s61, s59, 9472\n"
"buffer_load_dwordx4 v7, s[40:43], s61 offen nt lds\n"
"v_xor_b32 v7, 64, v7\n"
"s_add_u32 m0, s56, 73728\n"
"s_add_u32 s61, s59, 12288\n"
"buffer_load_dwordx4 v7, s[40:43], s61 offen nt lds\n"
"s_add_u32 m0, s56, 74752\n"
"s_add_u32 s61, s59, 12416\n"
"buffer_load_dwordx4 v7, s[40:43], s61 offen nt lds\n"
"s_add_u32 m0, s56, 75776\n"
"s_add_u32 s61, s59, 12544\n"
"buffer_load_dwordx4 v7, s[40:43], s61 offen nt lds\n"
"v_xor_b32 v7, 64, v7\n"
"s_add_u32 m0, s56, 24576\n"
"s_add_u32 s61, s59, 15360\n"
"buffer_load_dwordx4 v7, s[40:43], s61 offen nt lds\n"
"s_add_u32 m0, s56, 25600\n"
"s_add_u32 s61, s59, 15488\n"
"buffer_load_dwordx4 v7, s[40:43], s61 offen nt lds\n"
"s_add_u32 m0, s56, 26624\n"
"s_add_u32 s61, s59, 15616\n"
"buffer_load_dwordx4 v7, s[40:43], s61 offen nt lds\n"
"v_xor_b32 v7, 64, v7\n"
"s_add_u32 m0, s56, 86016\n"
"s_add_u32 s61, s59, 18432\n"
"buffer_load_dwordx4 v7, s[40:43], s61 offen nt lds\n"
"s_add_u32 m0, s56, 87040\n"
"s_add_u32 s61, s59, 18560\n"
"buffer_load_dwordx4 v7, s[40:43], s61 offen nt lds\n"
"s_add_u32 m0, s56, 88064\n"
"s_add_u32 s61, s59, 18688\n"
"buffer_load_dwordx4 v7, s[40:43], s61 offen nt lds\n"
"v_xor_b32 v7, 64, v7\n"
"s_add_u32 m0, s56, 36864\n"
"s_add_u32 s61, s59, 21504\n"
"buffer_load_dwordx4 v7, s[40:43], s61 offen nt lds\n"
"s_add_u32 m0, s56, 37888\n"
"s_add_u32 s61, s59, 21632\n"
"buffer_load_dwordx4 v7, s[40:43], s61 offen nt lds\n"
"s_add_u32 m0, s56, 38912\n"
"s_add_u32 s61, s59, 21760\n"
"buffer_load_dwordx4 v7, s[40:43], s61 offen nt lds\n"
"v_xor_b32 v7, 64, v7\n"
"s_add_u32 m0, s69, 0\n"
"s_add_u32 s61, s59, 24576\n"
"buffer_load_dwordx4 v7, s[40:43], s61 offen nt lds\n"
"s_add_u32 m0, s69, 1024\n"
"s_add_u32 s61, s59, 24704\n"
"buffer_load_dwordx4 v7, s[40:43], s61 offen nt lds\n"
"s_add_u32 m0, s69, 2048\n"
"s_add_u32 s61, s59, 24832\n"
"buffer_load_dwordx4 v7, s[40:43], s61 offen nt lds\n"
"v_xor_b32 v7, 64, v7\n"
"s_add_u32 m0, s69, 3072\n"
"s_add_u32 s61, s59, 27648\n"
"buffer_load_dwordx4 v7, s[40:43], s61 offen nt lds\n"
"s_add_u32 m0, s69, 4096\n"
"s_add_u32 s61, s59, 27776\n"
"buffer_load_dwordx4 v7, s[40:43], s61 offen nt lds\n"
"s_add_u32 m0, s69, 5120\n"
"s_add_u32 s61, s59, 27904\n"
"buffer_load_dwordx4 v7, s[40:43], s61 offen nt lds\n"
"v_xor_b32 v7, 64, v7\n"
"s_add_u32 m0, s69, 6144\n"
"s_add_u32 s61, s59, 30720\n"
"buffer_load_dwordx4 v7, s[40:43], s61 offen nt lds\n"
"s_add_u32 m0, s69, 7168\n"
"s_add_u32 s61, s59, 30848\n"
"buffer_load_dwordx4 v7, s[40:43], s61 offen nt lds\n"
"s_add_u32 m0, s69, 8192\n"
"s_add_u32 s61, s59, 30976\n"
"buffer_load_dwordx4 v7, s[40:43], s61 offen nt lds\n"
"v_xor_b32 v7, 64, v7\n"
"s_add_u32 m0, s69, 9216\n"
"s_add_u32 s61, s59, 33792\n"
"buffer_load_dwordx4 v7, s[40:43], s61 offen nt lds\n"
"s_add_u32 m0, s69, 10240\n"
"s_add_u32 s61, s59, 33920\n"
"buffer_load_dwordx4 v7, s[40:43], s61 offen nt lds\n"
"s_add_u32 m0, s69, 11264\n"
"s_add_u32 s61, s59, 34048\n"
"buffer_load_dwordx4 v7, s[40:43], s61 offen nt lds\n"
"s_getpc_b64 s[88:89]\n"
".Lpc2:\n"
"s_add_u32 s88, s88, .Ltop-.Lpc2\n"
"s_addc_u32 s89, s89, 0\n"
"s_and_b32 s89, s89, 0xffff\n"
"s_mov_b32 s90, .Lbodyend-.Ltop\n"
"s_mov_b32 s91, 0x20000\n"
"v_and_b32 v51, 15, v48\n"
"v_lshlrev_b32 v51, 7, v51\n"
"s_lshr_b32 s61, %9, 3\n"
"s_and_b32 s61, s61, 7\n"
"s_lshl_b32 s61, s61, 2\n"
"s_add_u32 s61, s61, s66\n"
"s_lshl_b32 s61, s61, 11\n"
"buffer_load_dword v120, v51, s[88:91], s61 offen\n"
"v_lshrrev_b32 v51, 2, v48\n"
"v_and_b32 v51, 7, v51\n"
"v_lshrrev_b32 v53, 2, v51\n"
"v_lshlrev_b32 v53, 1, v53\n"
"v_and_b32 v52, 3, v48\n"
"v_xor_b32 v52, v52, v53\n"
"v_lshlrev_b32 v52, 4, v52\n"
"v_mul_u32_u24 v51, 0x180, v51\n"
"v_add_u32 v51, v51, v52\n"
"v_lshrrev_b32 v52, 5, v48\n"
"v_lshlrev_b32 v52, 6, v52\n"
"v_add_u32 v8, v51, v52\n"
"v_lshrrev_b32 v51, 3, v49\n"
"v_mul_u32_u24 v52, 0xc00, v51\n"
"v_and_b32 v53, 7, v49\n"
"v_lshlrev_b32 v53, 7, v53\n"
"v_add_u32 v52, v52, v53\n"
"v_add_u32 v52, s72, v52\n"
"v_lshrrev_b32 v51, 1, v49\n"
"v_add_u32 v53, 0, v50\n"
"v_xor_b32 v53, v53, v51\n"
"v_lshlrev_b32 v53, 4, v53\n"
"v_add_u32 v1, v52, v53\n"
"v_add_u32 v3, s68, v1\n"
"v_add_u32 v53, 4, v50\n"
"v_xor_b32 v53, v53, v51\n"
"v_lshlrev_b32 v53, 4, v53\n"
"v_add_u32 v2, v52, v53\n"
"v_add_u32 v4, s68, v2\n"
"v_lshrrev_b32 v51, 1, v50\n"
"v_mul_u32_u24 v51, 0xc00, v51\n"
"v_and_b32 v52, 1, v50\n"
"v_lshlrev_b32 v53, 2, v52\n"
"v_lshrrev_b32 v54, 2, v49\n"
"v_add_u32 v53, v53, v54\n"
"v_lshlrev_b32 v53, 6, v53\n"
"v_add_u32 v51, v51, v53\n"
"v_and_b32 v53, 3, v48\n"
"v_lshlrev_b32 v53, 3, v53\n"
"v_add_u32 v51, v51, v53\n"
"v_add_u32 v51, s72, v51\n"
"v_add_u32 v51, 0xc000, v51\n"
"v_lshlrev_b32 v52, 5, v52\n"
"v_add_u32 v5, v51, v52\n"
"v_xor_b32 v52, 32, v52\n"
"v_add_u32 v6, v51, v52\n"
"s_waitcnt vmcnt(25)\n"
"v_mbcnt_lo_u32_b32 v24, -1, 0\n"
"v_mbcnt_hi_u32_b32 v24, -1, v24\n"
"v_bfe_u32 v24, v24, 3, 1\n"
"v_mul_u32_u24 v25, 0xc00, v24\n"
"v_xor_b32 v24, 1, v24\n"
"v_mul_u32_u24 v24, 0xc000, v24\n"
"v_sub_u32 v24, v24, v25\n"
"v_add_u32 v24, s67, v24\n"
"v_add_u32 v22, v1, v24\n"
"v_add_u32 v23, v2, v24\n"
"ds_read_b128 a[0:3], v22 offset:0\n"
"ds_read_b128 a[4:7], v23 offset:0\n"
"ds_read_b128 a[8:11], v22 offset:1024\n"
"ds_read_b128 a[12:15], v23 offset:1024\n"
"ds_read_b128 a[16:19], v22 offset:2048\n"
"ds_read_b128 a[20:23], v23 offset:2048\n"
"ds_read_b128 a[24:27], v22 offset:12288\n"
"ds_read_b128 a[28:31], v23 offset:12288\n"
"ds_read_b128 a[32:35], v22 offset:13312\n"
"ds_read_b128 a[36:39], v23 offset:13312\n"
"ds_read_b128 a[40:43], v22 offset:14336\n"
"ds_read_b128 a[44:47], v23 offset:14336\n"
"s_waitcnt lgkmcnt(0)\n"
"s_barrier\n"
"v_xor_b32 v7, s74, v7\n"
"s_add_u32 m0, s56, 49152\n"
"s_add_u32 s61, s57, 0\n"
"buffer_load_dwordx4 v7, s[44:47], s61 offen nt lds\n"
"s_add_u32 m0, s56, 50176\n"
"s_add_u32 s61, s57, 128\n"
"buffer_load_dwordx4 v7, s[44:47], s61 offen nt lds\n"
"s_add_u32 m0, s56, 51200\n"
"s_add_u32 s61, s57, 256\n"
"buffer_load_dwordx4 v7, s[44:47], s61 offen nt lds\n"
"s_add_u32 m0, s56, 0\n"
"s_add_u32 s61, s57, 12288\n"
"buffer_load_dwordx4 v7, s[44:47], s61 offen nt lds\n"
"s_add_u32 m0, s56, 1024\n"
"s_add_u32 s61, s57, 12416\n"
"buffer_load_dwordx4 v7, s[44:47], s61 offen nt lds\n"
"s_add_u32 m0, s56, 2048\n"
"s_add_u32 s61, s57, 12544\n"
"buffer_load_dwordx4 v7, s[44:47], s61 offen nt lds\n"
"s_add_u32 m0, s56, 61440\n"
"s_add_u32 s61, s57, 0\n"
"buffer_load_dwordx4 v8, s[48:51], s61 offen nt lds\n"
"s_add_u32 m0, s56, 62464\n"
"s_add_u32 s61, s57, 128\n"
"buffer_load_dwordx4 v8, s[48:51], s61 offen nt lds\n"
"s_add_u32 m0, s56, 63488\n"
"s_add_u32 s61, s57, 256\n"
"buffer_load_dwordx4 v8, s[48:51], s61 offen nt lds\n"
"s_add_u32 m0, s56, 12288\n"
"s_add_u32 s61, s57, 24576\n"
"buffer_load_dwordx4 v7, s[44:47], s61 offen nt lds\n"
"s_add_u32 m0, s56, 13312\n"
"s_add_u32 s61, s57, 24704\n"
"buffer_load_dwordx4 v7, s[44:47], s61 offen nt lds\n"
"s_add_u32 m0, s56, 14336\n"
"s_add_u32 s61, s57, 24832\n"
"buffer_load_dwordx4 v7, s[44:47], s61 offen nt lds\n"
"s_waitcnt vmcnt(25)\n"
"v_mbcnt_lo_u32_b32 v24, -1, 0\n"
"v_mbcnt_hi_u32_b32 v24, -1, v24\n"
"v_bfe_u32 v24, v24, 3, 1\n"
"v_mul_u32_u24 v25, 0xc00, v24\n"
"v_xor_b32 v24, 1, v24\n"
"v_mul_u32_u24 v24, 0xc000, v24\n"
"v_sub_u32 v24, v24, v25\n"
"v_add_u32 v24, s67, v24\n"
"v_add_u32 v22, v1, v24\n"
"v_add_u32 v23, v2, v24\n"
"ds_read_b128 a[48:51], v22 offset:24576\n"
"ds_read_b128 a[52:55], v23 offset:24576\n"
"ds_read_b128 a[56:59], v22 offset:25600\n"
"ds_read_b128 a[60:63], v23 offset:25600\n"
"ds_read_b128 a[64:67], v22 offset:26624\n"
"ds_read_b128 a[68:71], v23 offset:26624\n"
"ds_read_b128 a[72:75], v22 offset:36864\n"
"ds_read_b128 a[76:79], v23 offset:36864\n"
"ds_read_b128 a[80:83], v22 offset:37888\n"
"ds_read_b128 a[84:87], v23 offset:37888\n"
"ds_read_b128 a[88:91], v22 offset:38912\n"
"ds_read_b128 a[92:95], v23 offset:38912\n"
".Ltop:\n"
"s_cmp_eq_u32 s65, 1\n"
"s_cbranch_scc1 .Lvw_1\n"
"s_waitcnt vmcnt(9)\n"
".Lvw_1:\n"
"s_waitcnt lgkmcnt(0)\n"
"s_barrier\n"
"ds_read_b128 v[32:35], v1 offset:49152\n"
"ds_read_b128 v[40:43], v3 offset:0\n"
"ds_read_b128 v[44:47], v3 offset:6144\n"
"ds_read_b128 v[36:39], v1 offset:55296\n"
"s_waitcnt lgkmcnt(3)\n"
"v_mfma_f32_16x16x32_bf16 v[48:51], v[32:35], a[0:3], 0\n"
"v_mfma_f32_16x16x32_bf16 v[52:55], v[32:35], a[24:27], 0\n"
"v_mfma_f32_16x16x32_bf16 v[56:59], v[32:35], a[48:51], 0\n"
"v_mfma_f32_16x16x32_bf16 v[60:63], v[32:35], a[72:75], 0\n"
"s_waitcnt lgkmcnt(2)\n"
"v_mfma_f32_16x16x32_bf16 v[64:67], v[32:35], v[40:43], 0\n"
"s_waitcnt lgkmcnt(1)\n"
"v_mfma_f32_16x16x32_bf16 v[68:71], v[32:35], v[44:47], 0\n"
"s_waitcnt lgkmcnt(0)\n"
"ds_read_b128 v[32:35], v2 offset:49152\n"
"v_mfma_f32_16x16x32_bf16 v[88:91], v[36:39], v[40:43], 0\n"
"ds_read_b128 v[40:43], v4 offset:0\n"
"v_mfma_f32_16x16x32_bf16 v[92:95], v[36:39], v[44:47], 0\n"
"ds_read_b128 v[44:47], v4 offset:6144\n"
"v_mfma_f32_16x16x32_bf16 v[72:75], v[36:39], a[0:3], 0\n"
"v_mfma_f32_16x16x32_bf16 v[76:79], v[36:39], a[24:27], 0\n"
"v_mfma_f32_16x16x32_bf16 v[80:83], v[36:39], a[48:51], 0\n"
"v_mfma_f32_16x16x32_bf16 v[84:87], v[36:39], a[72:75], 0\n"
"s_waitcnt lgkmcnt(2)\n"
"ds_read_b128 v[36:39], v2 offset:55296\n"
"v_mfma_f32_16x16x32_bf16 v[48:51], v[32:35], a[4:7], v[48:51]\n"
"v_mfma_f32_16x16x32_bf16 v[52:55], v[32:35], a[28:31], v[52:55]\n"
"v_mfma_f32_16x16x32_bf16 v[56:59], v[32:35], a[52:55], v[56:59]\n"
"v_mfma_f32_16x16x32_bf16 v[60:63], v[32:35], a[76:79], v[60:63]\n"
"s_waitcnt lgkmcnt(2)\n"
"v_mfma_f32_16x16x32_bf16 v[64:67], v[32:35], v[40:43], v[64:67]\n"
"s_waitcnt lgkmcnt(1)\n"
"v_mfma_f32_16x16x32_bf16 v[68:71], v[32:35], v[44:47], v[68:71]\n"
"s_waitcnt lgkmcnt(0)\n"
"ds_read_b128 v[32:35], v1 offset:50176\n"
"v_mfma_f32_16x16x32_bf16 v[88:91], v[36:39], v[40:43], v[88:91]\n"
"ds_read_b128 v[40:43], v3 offset:1024\n"
"v_mfma_f32_16x16x32_bf16 v[92:95], v[36:39], v[44:47], v[92:95]\n"
"ds_read_b128 v[44:47], v3 offset:7168\n"
"v_mfma_f32_16x16x32_bf16 v[72:75], v[36:39], a[4:7], v[72:75]\n"
"v_mfma_f32_16x16x32_bf16 v[76:79], v[36:39], a[28:31], v[76:79]\n"
"v_mfma_f32_16x16x32_bf16 v[80:83], v[36:39], a[52:55], v[80:83]\n"
"v_mfma_f32_16x16x32_bf16 v[84:87], v[36:39], a[76:79], v[84:87]\n"
"s_waitcnt lgkmcnt(2)\n"
"ds_read_b128 v[36:39], v1 offset:56320\n"
"v_mfma_f32_16x16x32_bf16 v[48:51], v[32:35], a[8:11], v[48:51]\n"
"v_mfma_f32_16x16x32_bf16 v[52:55], v[32:35], a[32:35], v[52:55]\n"
"v_mfma_f32_16x16x32_bf16 v[56:59], v[32:35], a[56:59], v[56:59]\n"
"v_mfma_f32_16x16x32_bf16 v[60:63], v[32:35], a[80:83], v[60:63]\n"
"s_waitcnt lgkmcnt(2)\n"
"v_mfma_f32_16x16x32_bf16 v[64:67], v[32:35], v[40:43], v[64:67]\n"
"s_waitcnt lgkmcnt(1)\n"
"v_mfma_f32_16x16x32_bf16 v[68:71], v[32:35], v[44:47], v[68:71]\n"
"s_waitcnt lgkmcnt(0)\n"
"ds_read_b128 v[32:35], v2 offset:50176\n"
"v_mfma_f32_16x16x32_bf16 v[88:91], v[36:39], v[40:43], v[88:91]\n"
"ds_read_b128 v[40:43], v4 offset:1024\n"
"v_mfma_f32_16x16x32_bf16 v[92:95], v[36:39], v[44:47], v[92:95]\n"
"ds_read_b128 v[44:47], v4 offset:7168\n"
"v_mfma_f32_16x16x32_bf16 v[72:75], v[36:39], a[8:11], v[72:75]\n"
"s_add_u32 m0, s56, 73728\n"
"v_mfma_f32_16x16x32_bf16 v[76:79], v[36:39], a[32:35], v[76:79]\n"
"s_add_u32 s61, s57, 12288\n"
"v_mfma_f32_16x16x32_bf16 v[80:83], v[36:39], a[56:59], v[80:83]\n"
"buffer_load_dwordx4 v8, s[48:51], s61 offen nt lds\n"
"v_mfma_f32_16x16x32_bf16 v[84:87], v[36:39], a[80:83], v[84:87]\n"
"s_waitcnt lgkmcnt(2)\n"
"ds_read_b128 v[36:39], v2 offset:56320\n"
"v_mfma_f32_16x16x32_bf16 v[48:51], v[32:35], a[12:15], v[48:51]\n"
"v_mfma_f32_16x16x32_bf16 v[52:55], v[32:35], a[36:39], v[52:55]\n"
"v_mfma_f32_16x16x32_bf16 v[56:59], v[32:35], a[60:63], v[56:59]\n"
"s_add_u32 m0, s56, 74752\n"
"v_mfma_f32_16x16x32_bf16 v[60:63], v[32:35], a[84:87], v[60:63]\n"
"s_waitcnt lgkmcnt(2)\n"
"s_add_u32 s61, s57, 12416\n"
"v_mfma_f32_16x16x32_bf16 v[64:67], v[32:35], v[40:43], v[64:67]\n"
"s_waitcnt lgkmcnt(1)\n"
"buffer_load_dwordx4 v8, s[48:51], s61 offen nt lds\n"
"v_mfma_f32_16x16x32_bf16 v[68:71], v[32:35], v[44:47], v[68:71]\n"
"s_waitcnt lgkmcnt(0)\n"
"ds_read_b128 v[32:35], v1 offset:51200\n"
"v_mfma_f32_16x16x32_bf16 v[88:91], v[36:39], v[40:43], v[88:91]\n"
"ds_read_b128 v[40:43], v3 offset:2048\n"
"v_mfma_f32_16x16x32_bf16 v[92:95], v[36:39], v[44:47], v[92:95]\n"
"ds_read_b128 v[44:47], v3 offset:8192\n"
"v_mfma_f32_16x16x32_bf16 v[72:75], v[36:39], a[12:15], v[72:75]\n"
"s_add_u32 m0, s56, 75776\n"
"v_mfma_f32_16x16x32_bf16 v[76:79], v[36:39], a[36:39], v[76:79]\n"
"s_add_u32 s61, s57, 12544\n"
"v_mfma_f32_16x16x32_bf16 v[80:83], v[36:39], a[60:63], v[80:83]\n"
"buffer_load_dwordx4 v8, s[48:51], s61 offen nt lds\n"
"v_mfma_f32_16x16x32_bf16 v[84:87], v[36:39], a[84:87], v[84:87]\n"
"s_waitcnt lgkmcnt(2)\n"
"ds_read_b128 v[36:39], v1 offset:57344\n"
"v_mfma_f32_16x16x32_bf16 v[48:51], v[32:35], a[16:19], v[48:51]\n"
"v_mfma_f32_16x16x32_bf16 v[52:55], v[32:35], a[40:43], v[52:55]\n"
"v_mfma_f32_16x16x32_bf16 v[56:59], v[32:35], a[64:67], v[56:59]\n"
"s_add_u32 m0, s56, 24576\n"
"v_mfma_f32_16x16x32_bf16 v[60:63], v[32:35], a[88:91], v[60:63]\n"
"s_waitcnt lgkmcnt(2)\n"
"s_add_u32 s61, s57, 36864\n"
"v_mfma_f32_16x16x32_bf16 v[64:67], v[32:35], v[40:43], v[64:67]\n"
"s_waitcnt lgkmcnt(1)\n"
"buffer_load_dwordx4 v7, s[44:47], s61 offen nt lds\n"
"v_mfma_f32_16x16x32_bf16 v[68:71], v[32:35], v[44:47], v[68:71]\n"
"s_waitcnt lgkmcnt(0)\n"
"ds_read_b128 v[32:35], v2 offset:51200\n"
"v_mfma_f32_16x16x32_bf16 v[88:91], v[36:39], v[40:43], v[88:91]\n"
"ds_read_b128 v[40:43], v4 offset:2048\n"
"v_mfma_f32_16x16x32_bf16 v[92:95], v[36:39], v[44:47], v[92:95]\n"
"ds_read_b128 v[44:47], v4 offset:8192\n"
"v_mfma_f32_16x16x32_bf16 v[72:75], v[36:39], a[16:19], v[72:75]\n"
"s_add_u32 m0, s56, 25600\n"
"v_mfma_f32_16x16x32_bf16 v[76:79], v[36:39], a[40:43], v[76:79]\n"
"s_add_u32 s61, s57, 36992\n"
"v_mfma_f32_16x16x32_bf16 v[80:83], v[36:39], a[64:67], v[80:83]\n"
"buffer_load_dwordx4 v7, s[44:47], s61 offen nt lds\n"
"v_mfma_f32_16x16x32_bf16 v[84:87], v[36:39], a[88:91], v[84:87]\n"
"s_waitcnt lgkmcnt(2)\n"
"ds_read_b128 v[36:39], v2 offset:57344\n"
"v_mfma_f32_16x16x32_bf16 v[48:51], v[32:35], a[20:23], v[48:51]\n"
"v_mfma_f32_16x16x32_bf16 v[52:55], v[32:35], a[44:47], v[52:55]\n"
"v_mfma_f32_16x16x32_bf16 v[56:59], v[32:35], a[68:71], v[56:59]\n"
"s_add_u32 m0, s56, 26624\n"
"v_mfma_f32_16x16x32_bf16 v[60:63], v[32:35], a[92:95], v[60:63]\n"
"s_waitcnt lgkmcnt(2)\n"
"s_add_u32 s61, s57, 37120\n"
"v_mfma_f32_16x16x32_bf16 v[64:67], v[32:35], v[40:43], v[64:67]\n"
"s_waitcnt lgkmcnt(1)\n"
"buffer_load_dwordx4 v7, s[44:47], s61 offen nt lds\n"
"v_mfma_f32_16x16x32_bf16 v[68:71], v[32:35], v[44:47], v[68:71]\n"
"s_waitcnt lgkmcnt(0)\n"
"v_mfma_f32_16x16x32_bf16 v[88:91], v[36:39], v[40:43], v[88:91]\n"
"v_mfma_f32_16x16x32_bf16 v[92:95], v[36:39], v[44:47], v[92:95]\n"
"v_mfma_f32_16x16x32_bf16 v[72:75], v[36:39], a[20:23], v[72:75]\n"
"v_mfma_f32_16x16x32_bf16 v[76:79], v[36:39], a[44:47], v[76:79]\n"
"v_mfma_f32_16x16x32_bf16 v[80:83], v[36:39], a[68:71], v[80:83]\n"
"v_mfma_f32_16x16x32_bf16 v[84:87], v[36:39], a[92:95], v[84:87]\n"
"s_cmp_eq_u32 s65, 1\n"
"s_cbranch_scc1 .Lvw_2\n"
"s_waitcnt vmcnt(12)\n"
".Lvw_2:\n"
"s_waitcnt lgkmcnt(0)\n"
"s_barrier\n"
"ds_read_b128 v[32:35], v1 offset:0\n"
"ds_read_b128 v[40:43], v3 offset:0\n"
"ds_read_b128 v[44:47], v3 offset:6144\n"
"ds_read_b128 v[36:39], v1 offset:6144\n"
"v_max3_f32 v22, v48, v49, v50\n"
"v_max3_f32 v24, v52, v53, v54\n"
"v_max3_f32 v26, v56, v57, v58\n"
"v_max3_f32 v23, v51, v72, v73\n"
"v_max3_f32 v25, v55, v76, v77\n"
"v_max3_f32 v27, v59, v80, v81\n"
"v_max3_f32 v22, v22, v74, v75\n"
"v_max3_f32 v24, v24, v78, v79\n"
"v_max3_f32 v26, v26, v82, v83\n"
"v_max_f32 v22, v22, v23\n"
"v_max_f32 v24, v24, v25\n"
"v_max_f32 v26, v26, v27\n"
"v_mov_b32 v23, v22\n"
"v_mov_b32 v25, v24\n"
"v_mov_b32 v27, v26\n"
"v_permlane32_swap_b32 v22, v23\n"
"v_permlane32_swap_b32 v24, v25\n"
"v_permlane32_swap_b32 v26, v27\n"
"v_max_f32 v22, v22, v23\n"
"v_max_f32 v24, v24, v25\n"
"v_max_f32 v26, v26, v27\n"
"v_mov_b32 v23, v22\n"
"v_mov_b32 v25, v24\n"
"v_mov_b32 v27, v26\n"
"v_permlane16_swap_b32 v22, v23\n"
"v_permlane16_swap_b32 v24, v25\n"
"v_permlane16_swap_b32 v26, v27\n"
"v_max_f32 v22, v22, v23\n"
"v_max_f32 v24, v24, v25\n"
"v_max_f32 v26, v26, v27\n"
"v_mul_f32 v22, s62, v22\n"
"v_mul_f32 v24, s62, v24\n"
"v_mul_f32 v26, s62, v26\n"
"v_sub_f32 v9, 0, v22\n"
"v_sub_f32 v10, 0, v24\n"
"v_sub_f32 v11, 0, v26\n"
"v_mov_b32 v15, 0\n"
"v_mov_b32 v16, 0\n"
"v_mov_b32 v17, 0\n"
"v_max3_f32 v22, v60, v61, v62\n"
"v_max3_f32 v24, v64, v65, v66\n"
"v_max3_f32 v26, v68, v69, v70\n"
"v_max3_f32 v23, v63, v84, v85\n"
"v_max3_f32 v25, v67, v88, v89\n"
"v_max3_f32 v27, v71, v92, v93\n"
"v_max3_f32 v22, v22, v86, v87\n"
"v_max3_f32 v24, v24, v90, v91\n"
"v_max3_f32 v26, v26, v94, v95\n"
"v_max_f32 v22, v22, v23\n"
"v_max_f32 v24, v24, v25\n"
"v_max_f32 v26, v26, v27\n"
"v_mov_b32 v23, v22\n"
"v_mov_b32 v25, v24\n"
"v_mov_b32 v27, v26\n"
"v_permlane32_swap_b32 v22, v23\n"
"v_permlane32_swap_b32 v24, v25\n"
"v_permlane32_swap_b32 v26, v27\n"
"v_max_f32 v22, v22, v23\n"
"v_max_f32 v24, v24, v25\n"
"v_max_f32 v26, v26, v27\n"
"v_mov_b32 v23, v22\n"
"v_mov_b32 v25, v24\n"
"v_mov_b32 v27, v26\n"
"v_permlane16_swap_b32 v22, v23\n"
"v_permlane16_swap_b32 v24, v25\n"
"v_permlane16_swap_b32 v26, v27\n"
"v_max_f32 v22, v22, v23\n"
"v_max_f32 v24, v24, v25\n"
"v_max_f32 v26, v26, v27\n"
"v_mul_f32 v22, s62, v22\n"
"v_mul_f32 v24, s62, v24\n"
"v_mul_f32 v26, s62, v26\n"
"v_sub_f32 v12, 0, v22\n"
"v_sub_f32 v13, 0, v24\n"
"v_sub_f32 v14, 0, v26\n"
"v_mov_b32 v18, 0\n"
"v_mov_b32 v19, 0\n"
"v_mov_b32 v20, 0\n"
"v_fma_f32 v22, v48, s62, v9\n"
"v_fma_f32 v23, v49, s62, v9\n"
"v_exp_f32 v22, v22\n"
"v_fma_f32 v24, v50, s62, v9\n"
"v_exp_f32 v23, v23\n"
"v_fma_f32 v25, v51, s62, v9\n"
"v_exp_f32 v24, v24\n"
"v_add_f32 v21, v22, v23\n"
"v_fma_f32 v26, v72, s62, v9\n"
"v_exp_f32 v25, v25\n"
"v_cvt_pk_bf16_f32 v96, v22, v23\n"
"v_fma_f32 v27, v73, s62, v9\n"
"v_exp_f32 v26, v26\n"
"v_add_f32 v21, v21, v24\n"
"v_fma_f32 v22, v74, s62, v9\n"
"v_exp_f32 v27, v27\n"
"v_add_f32 v21, v21, v25\n"
"v_cvt_pk_bf16_f32 v97, v24, v25\n"
"v_fma_f32 v23, v75, s62, v9\n"
"v_exp_f32 v22, v22\n"
"v_add_f32 v21, v21, v26\n"
"v_exp_f32 v23, v23\n"
"v_cvt_pk_bf16_f32 v98, v26, v27\n"
"v_add_f32 v21, v21, v27\n"
"v_add_f32 v21, v21, v22\n"
"v_cvt_pk_bf16_f32 v99, v22, v23\n"
"v_add_f32 v21, v21, v23\n"
"v_add_f32 v15, v15, v21\n"
"v_fma_f32 v22, v52, s62, v10\n"
"v_fma_f32 v23, v53, s62, v10\n"
"v_exp_f32 v22, v22\n"
"v_fma_f32 v24, v54, s62, v10\n"
"v_exp_f32 v23, v23\n"
"v_fma_f32 v25, v55, s62, v10\n"
"v_exp_f32 v24, v24\n"
"v_add_f32 v21, v22, v23\n"
"v_fma_f32 v26, v76, s62, v10\n"
"v_exp_f32 v25, v25\n"
"v_cvt_pk_bf16_f32 v100, v22, v23\n"
"v_fma_f32 v27, v77, s62, v10\n"
"v_exp_f32 v26, v26\n"
"v_add_f32 v21, v21, v24\n"
"v_fma_f32 v22, v78, s62, v10\n"
"v_exp_f32 v27, v27\n"
"v_add_f32 v21, v21, v25\n"
"v_cvt_pk_bf16_f32 v101, v24, v25\n"
"v_fma_f32 v23, v79, s62, v10\n"
"v_exp_f32 v22, v22\n"
"v_add_f32 v21, v21, v26\n"
"v_exp_f32 v23, v23\n"
"v_cvt_pk_bf16_f32 v102, v26, v27\n"
"v_add_f32 v21, v21, v27\n"
"v_add_f32 v21, v21, v22\n"
"v_cvt_pk_bf16_f32 v103, v22, v23\n"
"v_add_f32 v21, v21, v23\n"
"v_add_f32 v16, v16, v21\n"
"v_fma_f32 v22, v56, s62, v11\n"
"v_fma_f32 v23, v57, s62, v11\n"
"v_exp_f32 v22, v22\n"
"v_fma_f32 v24, v58, s62, v11\n"
"v_exp_f32 v23, v23\n"
"v_fma_f32 v25, v59, s62, v11\n"
"v_exp_f32 v24, v24\n"
"v_add_f32 v21, v22, v23\n"
"v_fma_f32 v26, v80, s62, v11\n"
"v_exp_f32 v25, v25\n"
"v_cvt_pk_bf16_f32 v104, v22, v23\n"
"v_fma_f32 v27, v81, s62, v11\n"
"v_exp_f32 v26, v26\n"
"v_add_f32 v21, v21, v24\n"
"v_fma_f32 v22, v82, s62, v11\n"
"v_exp_f32 v27, v27\n"
"v_add_f32 v21, v21, v25\n"
"v_cvt_pk_bf16_f32 v105, v24, v25\n"
"v_fma_f32 v23, v83, s62, v11\n"
"v_exp_f32 v22, v22\n"
"v_add_f32 v21, v21, v26\n"
"v_exp_f32 v23, v23\n"
"v_cvt_pk_bf16_f32 v106, v26, v27\n"
"v_add_f32 v21, v21, v27\n"
"v_add_f32 v21, v21, v22\n"
"v_cvt_pk_bf16_f32 v107, v22, v23\n"
"v_add_f32 v21, v21, v23\n"
"v_add_f32 v17, v17, v21\n"
"v_fma_f32 v22, v60, s62, v12\n"
"v_fma_f32 v23, v61, s62, v12\n"
"v_exp_f32 v22, v22\n"
"v_fma_f32 v24, v62, s62, v12\n"
"v_exp_f32 v23, v23\n"
"v_fma_f32 v25, v63, s62, v12\n"
"v_exp_f32 v24, v24\n"
"v_add_f32 v21, v22, v23\n"
"v_fma_f32 v26, v84, s62, v12\n"
"v_exp_f32 v25, v25\n"
"v_cvt_pk_bf16_f32 v108, v22, v23\n"
"v_fma_f32 v27, v85, s62, v12\n"
"v_exp_f32 v26, v26\n"
"v_add_f32 v21, v21, v24\n"
"v_fma_f32 v22, v86, s62, v12\n"
"v_exp_f32 v27, v27\n"
"v_add_f32 v21, v21, v25\n"
"v_cvt_pk_bf16_f32 v109, v24, v25\n"
"v_fma_f32 v23, v87, s62, v12\n"
"v_exp_f32 v22, v22\n"
"v_add_f32 v21, v21, v26\n"
"v_exp_f32 v23, v23\n"
"v_cvt_pk_bf16_f32 v110, v26, v27\n"
"v_add_f32 v21, v21, v27\n"
"v_add_f32 v21, v21, v22\n"
"v_cvt_pk_bf16_f32 v111, v22, v23\n"
"v_add_f32 v21, v21, v23\n"
"v_add_f32 v18, v18, v21\n"
"s_waitcnt lgkmcnt(3)\n"
"v_mfma_f32_16x16x32_bf16 v[112:115], v[32:35], a[0:3], 0\n"
"v_mfma_f32_16x16x32_bf16 v[116:119], v[32:35], a[24:27], 0\n"
"s_add_u32 m0, s56, 86016\n"
"v_fma_f32 v22, v64, s62, v13\n"
"v_fma_f32 v23, v65, s62, v13\n"
"v_mfma_f32_16x16x32_bf16 v[120:123], v[32:35], a[48:51], 0\n"
"s_add_u32 s61, s57, 24576\n"
"v_exp_f32 v22, v22\n"
"v_mfma_f32_16x16x32_bf16 v[124:127], v[32:35], a[72:75], 0\n"
"s_waitcnt lgkmcnt(2)\n"
"buffer_load_dwordx4 v8, s[48:51], s61 offen nt lds\n"
"v_fma_f32 v24, v66, s62, v13\n"
"v_mfma_f32_16x16x32_bf16 v[28:31], v[32:35], v[40:43], 0\n"
"s_waitcnt lgkmcnt(1)\n"
"v_exp_f32 v23, v23\n"
"v_fma_f32 v25, v67, s62, v13\n"
"v_mfma_f32_16x16x32_bf16 v[48:51], v[32:35], v[44:47], 0\n"
"s_waitcnt lgkmcnt(0)\n"
"ds_read_b128 v[32:35], v2 offset:0\n"
"v_mfma_f32_16x16x32_bf16 v[80:83], v[36:39], v[40:43], 0\n"
"ds_read_b128 v[40:43], v4 offset:0\n"
"v_mfma_f32_16x16x32_bf16 v[60:63], v[36:39], v[44:47], 0\n"
"ds_read_b128 v[44:47], v4 offset:6144\n"
"v_mfma_f32_16x16x32_bf16 v[72:75], v[36:39], a[0:3], 0\n"
"v_exp_f32 v24, v24\n"
"v_mfma_f32_16x16x32_bf16 v[52:55], v[36:39], a[24:27], 0\n"
"v_add_f32 v21, v22, v23\n"
"v_fma_f32 v26, v88, s62, v13\n"
"v_mfma_f32_16x16x32_bf16 v[76:79], v[36:39], a[48:51], 0\n"
"v_exp_f32 v25, v25\n"
"v_mfma_f32_16x16x32_bf16 v[56:59], v[36:39], a[72:75], 0\n"
"s_waitcnt lgkmcnt(2)\n"
"ds_read_b128 v[36:39], v2 offset:6144\n"
"v_mfma_f32_16x16x32_bf16 v[112:115], v[32:35], a[4:7], v[112:115]\n"
"v_cvt_pk_bf16_f32 v84, v22, v23\n"
"v_fma_f32 v27, v89, s62, v13\n"
"v_mfma_f32_16x16x32_bf16 v[116:119], v[32:35], a[28:31], v[116:119]\n"
"s_add_u32 m0, s56, 87040\n"
"v_exp_f32 v26, v26\n"
"v_mfma_f32_16x16x32_bf16 v[120:123], v[32:35], a[52:55], v[120:123]\n"
"s_add_u32 s61, s57, 24704\n"
"v_add_f32 v21, v21, v24\n"
"v_fma_f32 v22, v90, s62, v13\n"
"v_mfma_f32_16x16x32_bf16 v[124:127], v[32:35], a[76:79], v[124:127]\n"
"s_waitcnt lgkmcnt(2)\n"
"buffer_load_dwordx4 v8, s[48:51], s61 offen nt lds\n"
"v_exp_f32 v27, v27\n"
"v_mfma_f32_16x16x32_bf16 v[28:31], v[32:35], v[40:43], v[28:31]\n"
"s_waitcnt lgkmcnt(1)\n"
"v_add_f32 v21, v21, v25\n"
"v_cvt_pk_bf16_f32 v85, v24, v25\n"
"v_mfma_f32_16x16x32_bf16 v[48:51], v[32:35], v[44:47], v[48:51]\n"
"s_waitcnt lgkmcnt(0)\n"
"ds_read_b128 v[32:35], v1 offset:1024\n"
"v_mfma_f32_16x16x32_bf16 v[80:83], v[36:39], v[40:43], v[80:83]\n"
"ds_read_b128 v[40:43], v3 offset:1024\n"
"v_mfma_f32_16x16x32_bf16 v[60:63], v[36:39], v[44:47], v[60:63]\n"
"ds_read_b128 v[44:47], v3 offset:7168\n"
"s_add_u32 m0, s56, 88064\n"
"v_mfma_f32_16x16x32_bf16 v[72:75], v[36:39], a[4:7], v[72:75]\n"
"s_add_u32 s61, s57, 24832\n"
"v_fma_f32 v23, v91, s62, v13\n"
"v_mfma_f32_16x16x32_bf16 v[52:55], v[36:39], a[28:31], v[52:55]\n"
"buffer_load_dwordx4 v8, s[48:51], s61 offen nt lds\n"
"v_exp_f32 v22, v22\n"
"v_add_f32 v21, v21, v26\n"
"v_mfma_f32_16x16x32_bf16 v[76:79], v[36:39], a[52:55], v[76:79]\n"
"v_exp_f32 v23, v23\n"
"v_mfma_f32_16x16x32_bf16 v[56:59], v[36:39], a[76:79], v[56:59]\n"
"s_waitcnt lgkmcnt(2)\n"
"ds_read_b128 v[36:39], v1 offset:7168\n"
"v_mfma_f32_16x16x32_bf16 v[112:115], v[32:35], a[8:11], v[112:115]\n"
"v_cvt_pk_bf16_f32 v86, v26, v27\n"
"v_add_f32 v21, v21, v27\n"
"v_mfma_f32_16x16x32_bf16 v[116:119], v[32:35], a[32:35], v[116:119]\n"
"v_add_f32 v21, v21, v22\n"
"v_cvt_pk_bf16_f32 v87, v22, v23\n"
"v_mfma_f32_16x16x32_bf16 v[120:123], v[32:35], a[56:59], v[120:123]\n"
"v_add_f32 v21, v21, v23\n"
"v_add_f32 v19, v19, v21\n"
"v_mfma_f32_16x16x32_bf16 v[124:127], v[32:35], a[80:83], v[124:127]\n"
"s_waitcnt lgkmcnt(2)\n"
"v_fma_f32 v22, v68, s62, v14\n"
"v_fma_f32 v23, v69, s62, v14\n"
"v_mfma_f32_16x16x32_bf16 v[28:31], v[32:35], v[40:43], v[28:31]\n"
"s_waitcnt lgkmcnt(1)\n"
"v_exp_f32 v22, v22\n"
"v_mfma_f32_16x16x32_bf16 v[48:51], v[32:35], v[44:47], v[48:51]\n"
"s_waitcnt lgkmcnt(0)\n"
"ds_read_b128 v[32:35], v2 offset:1024\n"
"v_mfma_f32_16x16x32_bf16 v[80:83], v[36:39], v[40:43], v[80:83]\n"
"ds_read_b128 v[40:43], v4 offset:1024\n"
"v_mfma_f32_16x16x32_bf16 v[60:63], v[36:39], v[44:47], v[60:63]\n"
"ds_read_b128 v[44:47], v4 offset:7168\n"
"s_add_u32 m0, s56, 36864\n"
"v_mfma_f32_16x16x32_bf16 v[72:75], v[36:39], a[8:11], v[72:75]\n"
"s_add_u32 s61, s57, 49152\n"
"v_fma_f32 v24, v70, s62, v14\n"
"v_mfma_f32_16x16x32_bf16 v[52:55], v[36:39], a[32:35], v[52:55]\n"
"buffer_load_dwordx4 v7, s[44:47], s61 offen nt lds\n"
"v_exp_f32 v23, v23\n"
"v_fma_f32 v25, v71, s62, v14\n"
"v_mfma_f32_16x16x32_bf16 v[76:79], v[36:39], a[56:59], v[76:79]\n"
"v_exp_f32 v24, v24\n"
"v_mfma_f32_16x16x32_bf16 v[56:59], v[36:39], a[80:83], v[56:59]\n"
"s_waitcnt lgkmcnt(2)\n"
"ds_read_b128 v[36:39], v2 offset:7168\n"
"v_mfma_f32_16x16x32_bf16 v[112:115], v[32:35], a[12:15], v[112:115]\n"
"v_add_f32 v21, v22, v23\n"
"v_fma_f32 v26, v92, s62, v14\n"
"v_mfma_f32_16x16x32_bf16 v[116:119], v[32:35], a[36:39], v[116:119]\n"
"v_exp_f32 v25, v25\n"
"v_mfma_f32_16x16x32_bf16 v[120:123], v[32:35], a[60:63], v[120:123]\n"
"v_cvt_pk_bf16_f32 v64, v22, v23\n"
"v_fma_f32 v27, v93, s62, v14\n"
"v_mfma_f32_16x16x32_bf16 v[124:127], v[32:35], a[84:87], v[124:127]\n"
"s_waitcnt lgkmcnt(2)\n"
"v_exp_f32 v26, v26\n"
"v_mfma_f32_16x16x32_bf16 v[28:31], v[32:35], v[40:43], v[28:31]\n"
"s_waitcnt lgkmcnt(1)\n"
"v_add_f32 v21, v21, v24\n"
"v_fma_f32 v22, v94, s62, v14\n"
"v_mfma_f32_16x16x32_bf16 v[48:51], v[32:35], v[44:47], v[48:51]\n"
"s_waitcnt lgkmcnt(0)\n"
"ds_read_b128 v[32:35], v1 offset:2048\n"
"v_mfma_f32_16x16x32_bf16 v[80:83], v[36:39], v[40:43], v[80:83]\n"
"ds_read_b128 v[40:43], v3 offset:2048\n"
"v_mfma_f32_16x16x32_bf16 v[60:63], v[36:39], v[44:47], v[60:63]\n"
"ds_read_b128 v[44:47], v3 offset:8192\n"
"s_add_u32 m0, s56, 37888\n"
"v_mfma_f32_16x16x32_bf16 v[72:75], v[36:39], a[12:15], v[72:75]\n"
"s_add_u32 s61, s57, 49280\n"
"v_exp_f32 v27, v27\n"
"v_mfma_f32_16x16x32_bf16 v[52:55], v[36:39], a[36:39], v[52:55]\n"
"buffer_load_dwordx4 v7, s[44:47], s61 offen nt lds\n"
"v_add_f32 v21, v21, v25\n"
"v_cvt_pk_bf16_f32 v65, v24, v25\n"
"v_mfma_f32_16x16x32_bf16 v[76:79], v[36:39], a[60:63], v[76:79]\n"
"v_fma_f32 v23, v95, s62, v14\n"
"v_mfma_f32_16x16x32_bf16 v[56:59], v[36:39], a[84:87], v[56:59]\n"
"s_waitcnt lgkmcnt(2)\n"
"ds_read_b128 v[36:39], v1 offset:8192\n"
"v_mfma_f32_16x16x32_bf16 v[112:115], v[32:35], a[16:19], v[112:115]\n"
"v_exp_f32 v22, v22\n"
"v_add_f32 v21, v21, v26\n"
"v_mfma_f32_16x16x32_bf16 v[116:119], v[32:35], a[40:43], v[116:119]\n"
"s_add_u32 m0, s56, 38912\n"
"v_exp_f32 v23, v23\n"
"v_mfma_f32_16x16x32_bf16 v[120:123], v[32:35], a[64:67], v[120:123]\n"
"s_add_u32 s61, s57, 49408\n"
"v_cvt_pk_bf16_f32 v66, v26, v27\n"
"v_add_f32 v21, v21, v27\n"
"v_mfma_f32_16x16x32_bf16 v[124:127], v[32:35], a[88:91], v[124:127]\n"
"s_waitcnt lgkmcnt(2)\n"
"buffer_load_dwordx4 v7, s[44:47], s61 offen nt lds\n"
"v_add_f32 v21, v21, v22\n"
"v_cvt_pk_bf16_f32 v67, v22, v23\n"
"v_mfma_f32_16x16x32_bf16 v[28:31], v[32:35], v[40:43], v[28:31]\n"
"s_waitcnt lgkmcnt(1)\n"
"v_add_f32 v21, v21, v23\n"
"v_add_f32 v20, v20, v21\n"
"v_mfma_f32_16x16x32_bf16 v[48:51], v[32:35], v[44:47], v[48:51]\n"
"s_waitcnt lgkmcnt(0)\n"
"ds_read_b128 v[32:35], v2 offset:2048\n"
"v_mfma_f32_16x16x32_bf16 v[80:83], v[36:39], v[40:43], v[80:83]\n"
"ds_read_b128 v[40:43], v4 offset:2048\n"
"v_mfma_f32_16x16x32_bf16 v[60:63], v[36:39], v[44:47], v[60:63]\n"
"ds_read_b128 v[44:47], v4 offset:8192\n"
"v_mfma_f32_16x16x32_bf16 v[72:75], v[36:39], a[16:19], v[72:75]\n"
"v_mfma_f32_16x16x32_bf16 v[52:55], v[36:39], a[40:43], v[52:55]\n"
"v_mfma_f32_16x16x32_bf16 v[76:79], v[36:39], a[64:67], v[76:79]\n"
"v_mfma_f32_16x16x32_bf16 v[56:59], v[36:39], a[88:91], v[56:59]\n"
"s_waitcnt lgkmcnt(2)\n"
"ds_read_b128 v[36:39], v2 offset:8192\n"
"v_mfma_f32_16x16x32_bf16 v[112:115], v[32:35], a[20:23], v[112:115]\n"
"v_mfma_f32_16x16x32_bf16 v[116:119], v[32:35], a[44:47], v[116:119]\n"
"v_mfma_f32_16x16x32_bf16 v[120:123], v[32:35], a[68:71], v[120:123]\n"
"v_mfma_f32_16x16x32_bf16 v[124:127], v[32:35], a[92:95], v[124:127]\n"
"s_waitcnt lgkmcnt(2)\n"
"v_mfma_f32_16x16x32_bf16 v[28:31], v[32:35], v[40:43], v[28:31]\n"
"s_cmp_eq_u32 s65, 1\n"
"s_cbranch_scc1 .Lvw_3\n"
"s_waitcnt vmcnt(12)\n"
".Lvw_3:\n"
"s_waitcnt lgkmcnt(0)\n"
"s_barrier\n"
"v_mfma_f32_16x16x32_bf16 v[48:51], v[32:35], v[44:47], v[48:51]\n"
"v_mfma_f32_16x16x32_bf16 v[80:83], v[36:39], v[40:43], v[80:83]\n"
"ds_read_b64_tr_b16 v[32:33], v5 offset:12288\n"
"ds_read_b64_tr_b16 v[34:35], v5 offset:18432\n"
"v_mfma_f32_16x16x32_bf16 v[60:63], v[36:39], v[44:47], v[60:63]\n"
"v_mfma_f32_16x16x32_bf16 v[72:75], v[36:39], a[20:23], v[72:75]\n"
"ds_read_b64_tr_b16 v[40:41], v6 offset:12288\n"
"ds_read_b64_tr_b16 v[42:43], v6 offset:18432\n"
"v_mfma_f32_16x16x32_bf16 v[52:55], v[36:39], a[44:47], v[52:55]\n"
"v_mfma_f32_16x16x32_bf16 v[76:79], v[36:39], a[68:71], v[76:79]\n"
"ds_read_b64_tr_b16 v[44:45], v5 offset:12800\n"
"ds_read_b64_tr_b16 v[46:47], v5 offset:18944\n"
"v_mfma_f32_16x16x32_bf16 v[56:59], v[36:39], a[92:95], v[56:59]\n"
"s_waitcnt lgkmcnt(4)\n"
"v_mfma_f32_16x16x32_bf16 a[96:99], v[32:35], v[96:99], 0\n"
"ds_read_b64_tr_b16 v[36:37], v6 offset:12800\n"
"ds_read_b64_tr_b16 v[38:39], v6 offset:18944\n"
"v_mfma_f32_16x16x32_bf16 a[100:103], v[32:35], v[100:103], 0\n"
"v_mfma_f32_16x16x32_bf16 a[104:107], v[32:35], v[104:107], 0\n"
"v_fma_f32 v22, v112, s62, v9\n"
"v_fma_f32 v23, v113, s62, v9\n"
"v_mfma_f32_16x16x32_bf16 a[108:111], v[32:35], v[108:111], 0\n"
"v_exp_f32 v22, v22\n"
"v_fma_f32 v24, v114, s62, v9\n"
"v_mfma_f32_16x16x32_bf16 a[112:115], v[32:35], v[84:87], 0\n"
"v_exp_f32 v23, v23\n"
"v_mfma_f32_16x16x32_bf16 a[116:119], v[32:35], v[64:67], 0\n"
"s_waitcnt lgkmcnt(4)\n"
"ds_read_b64_tr_b16 v[32:33], v5 offset:13312\n"
"ds_read_b64_tr_b16 v[34:35], v5 offset:19456\n"
"v_mfma_f32_16x16x32_bf16 a[120:123], v[40:43], v[96:99], 0\n"
"v_fma_f32 v25, v115, s62, v9\n"
"v_exp_f32 v24, v24\n"
"v_mfma_f32_16x16x32_bf16 a[124:127], v[40:43], v[100:103], 0\n"
"v_add_f32 v21, v22, v23\n"
"v_fma_f32 v26, v72, s62, v9\n"
"v_mfma_f32_16x16x32_bf16 a[128:131], v[40:43], v[104:107], 0\n"
"v_exp_f32 v25, v25\n"
"v_cvt_pk_bf16_f32 v88, v22, v23\n"
"v_mfma_f32_16x16x32_bf16 a[132:135], v[40:43], v[108:111], 0\n"
"v_fma_f32 v27, v73, s62, v9\n"
"v_mfma_f32_16x16x32_bf16 a[136:139], v[40:43], v[84:87], 0\n"
"v_exp_f32 v26, v26\n"
"v_add_f32 v21, v21, v24\n"
"v_fma_f32 v22, v74, s62, v9\n"
"v_mfma_f32_16x16x32_bf16 a[140:143], v[40:43], v[64:67], 0\n"
"s_waitcnt lgkmcnt(4)\n"
"ds_read_b64_tr_b16 v[40:41], v6 offset:13312\n"
"ds_read_b64_tr_b16 v[42:43], v6 offset:19456\n"
"v_mfma_f32_16x16x32_bf16 a[144:147], v[44:47], v[96:99], 0\n"
"v_exp_f32 v27, v27\n"
"v_mfma_f32_16x16x32_bf16 a[148:151], v[44:47], v[100:103], 0\n"
"v_add_f32 v21, v21, v25\n"
"v_cvt_pk_bf16_f32 v89, v24, v25\n"
"v_mfma_f32_16x16x32_bf16 a[152:155], v[44:47], v[104:107], 0\n"
"v_fma_f32 v23, v75, s62, v9\n"
"v_exp_f32 v22, v22\n"
"v_mfma_f32_16x16x32_bf16 a[156:159], v[44:47], v[108:111], 0\n"
"v_add_f32 v21, v21, v26\n"
"v_mfma_f32_16x16x32_bf16 a[160:163], v[44:47], v[84:87], 0\n"
"v_exp_f32 v23, v23\n"
"v_cvt_pk_bf16_f32 v90, v26, v27\n"
"v_add_f32 v21, v21, v27\n"
"v_mfma_f32_16x16x32_bf16 a[164:167], v[44:47], v[64:67], 0\n"
"s_waitcnt lgkmcnt(4)\n"
"ds_read_b64_tr_b16 v[44:45], v5 offset:13824\n"
"ds_read_b64_tr_b16 v[46:47], v5 offset:19968\n"
"v_mfma_f32_16x16x32_bf16 a[168:171], v[36:39], v[96:99], 0\n"
"v_add_f32 v21, v21, v22\n"
"v_cvt_pk_bf16_f32 v91, v22, v23\n"
"v_mfma_f32_16x16x32_bf16 a[172:175], v[36:39], v[100:103], 0\n"
"v_add_f32 v21, v21, v23\n"
"v_fma_f32 v22, v116, s62, v10\n"
"v_fma_f32 v23, v117, s62, v10\n"
"v_mfma_f32_16x16x32_bf16 a[176:179], v[36:39], v[104:107], 0\n"
"v_exp_f32 v22, v22\n"
"v_mfma_f32_16x16x32_bf16 a[180:183], v[36:39], v[108:111], 0\n"
"v_fma_f32 v24, v118, s62, v10\n"
"v_exp_f32 v23, v23\n"
"v_mfma_f32_16x16x32_bf16 a[184:187], v[36:39], v[84:87], 0\n"
"v_fma_f32 v25, v119, s62, v10\n"
"v_mfma_f32_16x16x32_bf16 a[188:191], v[36:39], v[64:67], 0\n"
"s_waitcnt lgkmcnt(4)\n"
"ds_read_b64_tr_b16 v[36:37], v6 offset:13824\n"
"ds_read_b64_tr_b16 v[38:39], v6 offset:19968\n"
"v_mfma_f32_16x16x32_bf16 a[192:195], v[32:35], v[96:99], 0\n"
"v_exp_f32 v24, v24\n"
"v_add_f32 v3, v22, v23\n"
"v_mfma_f32_16x16x32_bf16 a[196:199], v[32:35], v[100:103], 0\n"
"v_fma_f32 v26, v52, s62, v10\n"
"v_exp_f32 v25, v25\n"
"v_mfma_f32_16x16x32_bf16 a[200:203], v[32:35], v[104:107], 0\n"
"v_cvt_pk_bf16_f32 v68, v22, v23\n"
"v_fma_f32 v27, v53, s62, v10\n"
"v_mfma_f32_16x16x32_bf16 a[204:207], v[32:35], v[108:111], 0\n"
"v_exp_f32 v26, v26\n"
"v_add_f32 v3, v3, v24\n"
"v_mfma_f32_16x16x32_bf16 a[208:211], v[32:35], v[84:87], 0\n"
"v_fma_f32 v22, v54, s62, v10\n"
"v_mfma_f32_16x16x32_bf16 a[212:215], v[32:35], v[64:67], 0\n"
"s_waitcnt lgkmcnt(4)\n"
"ds_read_b64_tr_b16 v[32:33], v5 offset:14336\n"
"ds_read_b64_tr_b16 v[34:35], v5 offset:20480\n"
"v_mfma_f32_16x16x32_bf16 a[216:219], v[40:43], v[96:99], 0\n"
"v_exp_f32 v27, v27\n"
"v_add_f32 v3, v3, v25\n"
"v_cvt_pk_bf16_f32 v69, v24, v25\n"
"v_mfma_f32_16x16x32_bf16 a[220:223], v[40:43], v[100:103], 0\n"
"v_fma_f32 v23, v55, s62, v10\n"
"v_mfma_f32_16x16x32_bf16 a[224:227], v[40:43], v[104:107], 0\n"
"v_exp_f32 v22, v22\n"
"v_add_f32 v3, v3, v26\n"
"v_mfma_f32_16x16x32_bf16 a[228:231], v[40:43], v[108:111], 0\n"
"v_exp_f32 v23, v23\n"
"v_cvt_pk_bf16_f32 v70, v26, v27\n"
"v_mfma_f32_16x16x32_bf16 a[232:235], v[40:43], v[84:87], 0\n"
"v_add_f32 v3, v3, v27\n"
"v_add_f32 v3, v3, v22\n"
"v_mfma_f32_16x16x32_bf16 a[236:239], v[40:43], v[64:67], 0\n"
"s_waitcnt lgkmcnt(4)\n"
"ds_read_b64_tr_b16 v[40:41], v6 offset:14336\n"
"ds_read_b64_tr_b16 v[42:43], v6 offset:20480\n"
"v_mfma_f32_16x16x32_bf16 a[240:243], v[44:47], v[96:99], 0\n"
"v_cvt_pk_bf16_f32 v71, v22, v23\n"
"v_add_f32 v3, v3, v23\n"
"v_max_f32 v27, v21, v3\n"
"v_mfma_f32_16x16x32_bf16 a[244:247], v[44:47], v[100:103], 0\n"
"v_cmp_nge_f32 vcc, s63, v27\n"
"v_fma_f32 v22, v120, s62, v11\n"
"v_mfma_f32_16x16x32_bf16 a[248:251], v[44:47], v[104:107], 0\n"
"v_fma_f32 v23, v121, s62, v11\n"
"v_mfma_f32_16x16x32_bf16 a[252:255], v[44:47], v[108:111], 0\n"
"s_cbranch_vccnz .Lrare_1_a\n"
".Lret_1_a:\n"
"v_add_f32 v15, v15, v21\n"
"v_add_f32 v16, v16, v3\n"
"v_add_u32 v3, s68, v1\n"
"v_mfma_f32_16x16x32_bf16 v[128:131], v[44:47], v[84:87], 0\n"
"v_exp_f32 v22, v22\n"
"v_fma_f32 v24, v122, s62, v11\n"
"v_mfma_f32_16x16x32_bf16 v[132:135], v[44:47], v[64:67], 0\n"
"s_waitcnt lgkmcnt(4)\n"
"ds_read_b64_tr_b16 v[44:45], v5 offset:14848\n"
"ds_read_b64_tr_b16 v[46:47], v5 offset:20992\n"
"v_mfma_f32_16x16x32_bf16 v[136:139], v[36:39], v[96:99], 0\n"
"v_exp_f32 v23, v23\n"
"v_fma_f32 v25, v123, s62, v11\n"
"v_mfma_f32_16x16x32_bf16 v[140:143], v[36:39], v[100:103], 0\n"
"v_exp_f32 v24, v24\n"
"v_mfma_f32_16x16x32_bf16 v[144:147], v[36:39], v[104:107], 0\n"
"v_add_f32 v72, v22, v23\n"
"v_fma_f32 v26, v76, s62, v11\n"
"v_mfma_f32_16x16x32_bf16 v[148:151], v[36:39], v[108:111], 0\n"
"v_exp_f32 v25, v25\n"
"v_cvt_pk_bf16_f32 v92, v22, v23\n"
"v_mfma_f32_16x16x32_bf16 v[152:155], v[36:39], v[84:87], 0\n"
"v_fma_f32 v27, v77, s62, v11\n"
"v_exp_f32 v26, v26\n"
"v_mfma_f32_16x16x32_bf16 v[156:159], v[36:39], v[64:67], 0\n"
"s_waitcnt lgkmcnt(4)\n"
"ds_read_b64_tr_b16 v[36:37], v6 offset:14848\n"
"ds_read_b64_tr_b16 v[38:39], v6 offset:20992\n"
"v_mfma_f32_16x16x32_bf16 v[160:163], v[32:35], v[96:99], 0\n"
"v_add_f32 v72, v72, v24\n"
"v_fma_f32 v22, v78, s62, v11\n"
"v_mfma_f32_16x16x32_bf16 v[164:167], v[32:35], v[100:103], 0\n"
"v_exp_f32 v27, v27\n"
"v_add_f32 v72, v72, v25\n"
"v_mfma_f32_16x16x32_bf16 v[168:171], v[32:35], v[104:107], 0\n"
"v_cvt_pk_bf16_f32 v93, v24, v25\n"
"v_fma_f32 v23, v79, s62, v11\n"
"v_mfma_f32_16x16x32_bf16 v[172:175], v[32:35], v[108:111], 0\n"
"v_exp_f32 v22, v22\n"
"v_add_f32 v72, v72, v26\n"
"v_mfma_f32_16x16x32_bf16 v[176:179], v[32:35], v[84:87], 0\n"
"v_exp_f32 v23, v23\n"
"v_mfma_f32_16x16x32_bf16 v[180:183], v[32:35], v[64:67], 0\n"
"s_waitcnt lgkmcnt(4)\n"
"v_cvt_pk_bf16_f32 v94, v26, v27\n"
"v_add_f32 v72, v72, v27\n"
"v_mfma_f32_16x16x32_bf16 v[184:187], v[40:43], v[96:99], 0\n"
"v_add_f32 v72, v72, v22\n"
"v_cvt_pk_bf16_f32 v95, v22, v23\n"
"v_add_f32 v72, v72, v23\n"
"v_mfma_f32_16x16x32_bf16 v[188:191], v[40:43], v[100:103], 0\n"
"ds_read_b128 v[32:35], v1 offset:12288\n"
"v_fma_f32 v22, v124, s62, v12\n"
"v_fma_f32 v23, v125, s62, v12\n"
"v_mfma_f32_16x16x32_bf16 v[192:195], v[40:43], v[104:107], 0\n"
"v_exp_f32 v22, v22\n"
"v_fma_f32 v24, v126, s62, v12\n"
"v_mfma_f32_16x16x32_bf16 v[196:199], v[40:43], v[108:111], 0\n"
"v_exp_f32 v23, v23\n"
"v_mfma_f32_16x16x32_bf16 v[200:203], v[40:43], v[84:87], 0\n"
"v_fma_f32 v25, v127, s62, v12\n"
"v_exp_f32 v24, v24\n"
"v_mfma_f32_16x16x32_bf16 v[204:207], v[40:43], v[64:67], 0\n"
"s_waitcnt lgkmcnt(3)\n"
"v_add_f32 v73, v22, v23\n"
"v_fma_f32 v26, v56, s62, v12\n"
"v_mfma_f32_16x16x32_bf16 v[208:211], v[44:47], v[96:99], 0\n"
"v_exp_f32 v25, v25\n"
"v_cvt_pk_bf16_f32 v112, v22, v23\n"
"v_mfma_f32_16x16x32_bf16 v[212:215], v[44:47], v[100:103], 0\n"
"ds_read_b128 v[40:43], v3 offset:0\n"
"v_fma_f32 v27, v57, s62, v12\n"
"v_mfma_f32_16x16x32_bf16 v[216:219], v[44:47], v[104:107], 0\n"
"v_exp_f32 v26, v26\n"
"v_add_f32 v73, v73, v24\n"
"v_mfma_f32_16x16x32_bf16 v[220:223], v[44:47], v[108:111], 0\n"
"v_fma_f32 v22, v58, s62, v12\n"
"v_exp_f32 v27, v27\n"
"v_mfma_f32_16x16x32_bf16 v[224:227], v[44:47], v[84:87], 0\n"
"v_add_f32 v73, v73, v25\n"
"v_cvt_pk_bf16_f32 v113, v24, v25\n"
"v_mfma_f32_16x16x32_bf16 v[228:231], v[44:47], v[64:67], 0\n"
"s_waitcnt lgkmcnt(2)\n"
"v_fma_f32 v23, v59, s62, v12\n"
"v_exp_f32 v22, v22\n"
"v_mfma_f32_16x16x32_bf16 v[232:235], v[36:39], v[96:99], 0\n"
"ds_read_b128 v[44:47], v3 offset:6144\n"
"v_add_f32 v73, v73, v26\n"
"v_mfma_f32_16x16x32_bf16 v[236:239], v[36:39], v[100:103], 0\n"
"v_exp_f32 v23, v23\n"
"v_cvt_pk_bf16_f32 v114, v26, v27\n"
"v_add_f32 v73, v73, v27\n"
"v_mfma_f32_16x16x32_bf16 v[240:243], v[36:39], v[104:107], 0\n"
"v_add_f32 v73, v73, v22\n"
"v_cvt_pk_bf16_f32 v115, v22, v23\n"
"v_mfma_f32_16x16x32_bf16 v[244:247], v[36:39], v[108:111], 0\n"
"v_add_f32 v73, v73, v23\n"
"v_max_f32 v27, v72, v73\n"
"v_cmp_nge_f32 vcc, s63, v27\n"
"v_mfma_f32_16x16x32_bf16 v[248:251], v[36:39], v[84:87], 0\n"
"v_fma_f32 v22, v28, s62, v13\n"
"v_fma_f32 v23, v29, s62, v13\n"
"v_mfma_f32_16x16x32_bf16 v[252:255], v[36:39], v[64:67], 0\n"
"s_waitcnt lgkmcnt(2)\n"
"s_cbranch_vccnz .Lrare_1_b\n"
".Lret_1_b:\n"
"ds_read_b128 v[36:39], v1 offset:18432\n"
"v_add_f32 v17, v17, v72\n"
"v_add_f32 v18, v18, v73\n"
"v_mfma_f32_16x16x32_bf16 v[116:119], v[32:35], a[0:3], 0\n"
"v_mfma_f32_16x16x32_bf16 v[52:55], v[32:35], a[24:27], 0\n"
"s_add_u32 m0, s56, 49152\n"
"v_exp_f32 v22, v22\n"
"v_mfma_f32_16x16x32_bf16 v[72:75], v[32:35], a[48:51], 0\n"
"s_add_u32 s61, s57, 36864\n"
"v_fma_f32 v24, v30, s62, v13\n"
"v_mfma_f32_16x16x32_bf16 v[120:123], v[32:35], a[72:75], 0\n"
"s_waitcnt lgkmcnt(2)\n"
"buffer_load_dwordx4 v8, s[48:51], s61 offen nt lds\n"
"v_exp_f32 v23, v23\n"
"v_fma_f32 v25, v31, s62, v13\n"
"v_mfma_f32_16x16x32_bf16 v[76:79], v[32:35], v[40:43], 0\n"
"s_waitcnt lgkmcnt(1)\n"
"v_exp_f32 v24, v24\n"
"v_mfma_f32_16x16x32_bf16 v[124:127], v[32:35], v[44:47], 0\n"
"s_waitcnt lgkmcnt(0)\n"
"ds_read_b128 v[32:35], v2 offset:12288\n"
"v_mfma_f32_16x16x32_bf16 v[108:111], v[36:39], v[40:43], 0\n"
"ds_read_b128 v[40:43], v4 offset:0\n"
"v_mfma_f32_16x16x32_bf16 v[84:87], v[36:39], v[44:47], 0\n"
"ds_read_b128 v[44:47], v4 offset:6144\n"
"v_mfma_f32_16x16x32_bf16 v[56:59], v[36:39], a[0:3], 0\n"
"v_add_f32 v21, v22, v23\n"
"v_fma_f32 v26, v80, s62, v13\n"
"v_mfma_f32_16x16x32_bf16 v[96:99], v[36:39], a[24:27], 0\n"
"v_exp_f32 v25, v25\n"
"v_mfma_f32_16x16x32_bf16 v[100:103], v[36:39], a[48:51], 0\n"
"v_cvt_pk_bf16_f32 v64, v22, v23\n"
"v_fma_f32 v27, v81, s62, v13\n"
"v_mfma_f32_16x16x32_bf16 v[104:107], v[36:39], a[72:75], 0\n"
"s_waitcnt lgkmcnt(2)\n"
"ds_read_b128 v[36:39], v2 offset:18432\n"
"v_mfma_f32_16x16x32_bf16 v[116:119], v[32:35], a[4:7], v[116:119]\n"
"v_exp_f32 v26, v26\n"
"v_mfma_f32_16x16x32_bf16 v[52:55], v[32:35], a[28:31], v[52:55]\n"
"s_add_u32 m0, s56, 50176\n"
"v_add_f32 v21, v21, v24\n"
"v_fma_f32 v22, v82, s62, v13\n"
"v_mfma_f32_16x16x32_bf16 v[72:75], v[32:35], a[52:55], v[72:75]\n"
"s_add_u32 s61, s57, 36992\n"
"v_exp_f32 v27, v27\n"
"v_mfma_f32_16x16x32_bf16 v[120:123], v[32:35], a[76:79], v[120:123]\n"
"s_waitcnt lgkmcnt(2)\n"
"buffer_load_dwordx4 v8, s[48:51], s61 offen nt lds\n"
"v_add_f32 v21, v21, v25\n"
"v_cvt_pk_bf16_f32 v65, v24, v25\n"
"v_mfma_f32_16x16x32_bf16 v[76:79], v[32:35], v[40:43], v[76:79]\n"
"s_waitcnt lgkmcnt(1)\n"
"v_fma_f32 v23, v83, s62, v13\n"
"v_mfma_f32_16x16x32_bf16 v[124:127], v[32:35], v[44:47], v[124:127]\n"
"s_waitcnt lgkmcnt(0)\n"
"ds_read_b128 v[32:35], v1 offset:13312\n"
"v_mfma_f32_16x16x32_bf16 v[108:111], v[36:39], v[40:43], v[108:111]\n"
"ds_read_b128 v[40:43], v3 offset:1024\n"
"v_mfma_f32_16x16x32_bf16 v[84:87], v[36:39], v[44:47], v[84:87]\n"
"ds_read_b128 v[44:47], v3 offset:7168\n"
"s_add_u32 m0, s56, 51200\n"
"v_mfma_f32_16x16x32_bf16 v[56:59], v[36:39], a[4:7], v[56:59]\n"
"s_add_u32 s61, s57, 37120\n"
"v_exp_f32 v22, v22\n"
"v_add_f32 v21, v21, v26\n"
"v_mfma_f32_16x16x32_bf16 v[96:99], v[36:39], a[28:31], v[96:99]\n"
"buffer_load_dwordx4 v8, s[48:51], s61 offen nt lds\n"
"v_exp_f32 v23, v23\n"
"v_mfma_f32_16x16x32_bf16 v[100:103], v[36:39], a[52:55], v[100:103]\n"
"v_cvt_pk_bf16_f32 v66, v26, v27\n"
"v_add_f32 v21, v21, v27\n"
"v_mfma_f32_16x16x32_bf16 v[104:107], v[36:39], a[76:79], v[104:107]\n"
"s_waitcnt lgkmcnt(2)\n"
"ds_read_b128 v[36:39], v1 offset:19456\n"
"v_mfma_f32_16x16x32_bf16 v[116:119], v[32:35], a[8:11], v[116:119]\n"
"v_add_f32 v21, v21, v22\n"
"v_cvt_pk_bf16_f32 v67, v22, v23\n"
"v_mfma_f32_16x16x32_bf16 v[52:55], v[32:35], a[32:35], v[52:55]\n"
"v_add_f32 v21, v21, v23\n"
"v_cmp_nge_f32 vcc, s63, v21\n"
"v_mfma_f32_16x16x32_bf16 v[72:75], v[32:35], a[56:59], v[72:75]\n"
"v_fma_f32 v22, v48, s62, v14\n"
"v_fma_f32 v23, v49, s62, v14\n"
"v_mfma_f32_16x16x32_bf16 v[120:123], v[32:35], a[80:83], v[120:123]\n"
"s_waitcnt lgkmcnt(2)\n"
"s_cbranch_vccnz .Lrare_1_d4\n"
".Lret_1_d4:\n"
"v_add_f32 v19, v19, v21\n"
"v_mfma_f32_16x16x32_bf16 v[76:79], v[32:35], v[40:43], v[76:79]\n"
"s_waitcnt lgkmcnt(1)\n"
"v_exp_f32 v22, v22\n"
"v_mfma_f32_16x16x32_bf16 v[124:127], v[32:35], v[44:47], v[124:127]\n"
"s_waitcnt lgkmcnt(0)\n"
"ds_read_b128 v[32:35], v2 offset:13312\n"
"v_mfma_f32_16x16x32_bf16 v[108:111], v[36:39], v[40:43], v[108:111]\n"
"ds_read_b128 v[40:43], v4 offset:1024\n"
"v_mfma_f32_16x16x32_bf16 v[84:87], v[36:39], v[44:47], v[84:87]\n"
"ds_read_b128 v[44:47], v4 offset:7168\n"
"s_add_u32 m0, s56, 0\n"
"v_mfma_f32_16x16x32_bf16 v[56:59], v[36:39], a[8:11], v[56:59]\n"
"s_add_u32 s61, s57, 61440\n"
"v_fma_f32 v24, v50, s62, v14\n"
"v_mfma_f32_16x16x32_bf16 v[96:99], v[36:39], a[32:35], v[96:99]\n"
"buffer_load_dwordx4 v7, s[44:47], s61 offen nt lds\n"
"v_exp_f32 v23, v23\n"
"v_fma_f32 v25, v51, s62, v14\n"
"v_mfma_f32_16x16x32_bf16 v[100:103], v[36:39], a[56:59], v[100:103]\n"
"v_exp_f32 v24, v24\n"
"v_mfma_f32_16x16x32_bf16 v[104:107], v[36:39], a[80:83], v[104:107]\n"
"s_waitcnt lgkmcnt(2)\n"
"ds_read_b128 v[36:39], v2 offset:19456\n"
"v_mfma_f32_16x16x32_bf16 v[116:119], v[32:35], a[12:15], v[116:119]\n"
"v_add_f32 v21, v22, v23\n"
"v_fma_f32 v26, v60, s62, v14\n"
"v_mfma_f32_16x16x32_bf16 v[52:55], v[32:35], a[36:39], v[52:55]\n"
"v_exp_f32 v25, v25\n"
"v_mfma_f32_16x16x32_bf16 v[72:75], v[32:35], a[60:63], v[72:75]\n"
"v_cvt_pk_bf16_f32 v28, v22, v23\n"
"v_fma_f32 v27, v61, s62, v14\n"
"v_mfma_f32_16x16x32_bf16 v[120:123], v[32:35], a[84:87], v[120:123]\n"
"s_waitcnt lgkmcnt(2)\n"
"v_exp_f32 v26, v26\n"
"v_mfma_f32_16x16x32_bf16 v[76:79], v[32:35], v[40:43], v[76:79]\n"
"s_waitcnt lgkmcnt(1)\n"
"v_add_f32 v21, v21, v24\n"
"v_fma_f32 v22, v62, s62, v14\n"
"v_mfma_f32_16x16x32_bf16 v[124:127], v[32:35], v[44:47], v[124:127]\n"
"s_waitcnt lgkmcnt(0)\n"
"ds_read_b128 v[32:35], v1 offset:14336\n"
"v_mfma_f32_16x16x32_bf16 v[108:111], v[36:39], v[40:43], v[108:111]\n"
"ds_read_b128 v[40:43], v3 offset:2048\n"
"v_mfma_f32_16x16x32_bf16 v[84:87], v[36:39], v[44:47], v[84:87]\n"
"ds_read_b128 v[44:47], v3 offset:8192\n"
"s_add_u32 m0, s56, 1024\n"
"v_mfma_f32_16x16x32_bf16 v[56:59], v[36:39], a[12:15], v[56:59]\n"
"s_add_u32 s61, s57, 61568\n"
"v_exp_f32 v27, v27\n"
"v_mfma_f32_16x16x32_bf16 v[96:99], v[36:39], a[36:39], v[96:99]\n"
"buffer_load_dwordx4 v7, s[44:47], s61 offen nt lds\n"
"v_add_f32 v21, v21, v25\n"
"v_cvt_pk_bf16_f32 v29, v24, v25\n"
"v_mfma_f32_16x16x32_bf16 v[100:103], v[36:39], a[60:63], v[100:103]\n"
"v_fma_f32 v23, v63, s62, v14\n"
"v_mfma_f32_16x16x32_bf16 v[104:107], v[36:39], a[84:87], v[104:107]\n"
"s_waitcnt lgkmcnt(2)\n"
"ds_read_b128 v[36:39], v1 offset:20480\n"
"v_mfma_f32_16x16x32_bf16 v[116:119], v[32:35], a[16:19], v[116:119]\n"
"v_exp_f32 v22, v22\n"
"v_add_f32 v21, v21, v26\n"
"v_mfma_f32_16x16x32_bf16 v[52:55], v[32:35], a[40:43], v[52:55]\n"
"s_add_u32 m0, s56, 2048\n"
"v_exp_f32 v23, v23\n"
"v_mfma_f32_16x16x32_bf16 v[72:75], v[32:35], a[64:67], v[72:75]\n"
"s_add_u32 s61, s57, 61696\n"
"v_cvt_pk_bf16_f32 v30, v26, v27\n"
"v_add_f32 v21, v21, v27\n"
"v_mfma_f32_16x16x32_bf16 v[120:123], v[32:35], a[88:91], v[120:123]\n"
"s_waitcnt lgkmcnt(2)\n"
"buffer_load_dwordx4 v7, s[44:47], s61 offen nt lds\n"
"v_add_f32 v21, v21, v22\n"
"v_cvt_pk_bf16_f32 v31, v22, v23\n"
"v_mfma_f32_16x16x32_bf16 v[76:79], v[32:35], v[40:43], v[76:79]\n"
"s_waitcnt lgkmcnt(1)\n"
"v_add_f32 v21, v21, v23\n"
"v_cmp_nge_f32 vcc, s63, v21\n"
"v_mfma_f32_16x16x32_bf16 v[124:127], v[32:35], v[44:47], v[124:127]\n"
"s_waitcnt lgkmcnt(0)\n"
"ds_read_b128 v[32:35], v2 offset:14336\n"
"v_mfma_f32_16x16x32_bf16 v[108:111], v[36:39], v[40:43], v[108:111]\n"
"ds_read_b128 v[40:43], v4 offset:2048\n"
"v_mfma_f32_16x16x32_bf16 v[84:87], v[36:39], v[44:47], v[84:87]\n"
"ds_read_b128 v[44:47], v4 offset:8192\n"
"s_cbranch_vccnz .Lrare_1_d5\n"
".Lret_1_d5:\n"
"v_add_f32 v20, v20, v21\n"
"v_mfma_f32_16x16x32_bf16 v[56:59], v[36:39], a[16:19], v[56:59]\n"
"v_mbcnt_lo_u32_b32 v22, -1, 0\n"
"v_mbcnt_hi_u32_b32 v22, -1, v22\n"
"v_mfma_f32_16x16x32_bf16 v[96:99], v[36:39], a[40:43], v[96:99]\n"
"v_and_b32 v22, 31, v22\n"
"v_mfma_f32_16x16x32_bf16 v[100:103], v[36:39], a[64:67], v[100:103]\n"
"v_lshlrev_b32 v22, 7, v22\n"
"v_mfma_f32_16x16x32_bf16 v[104:107], v[36:39], a[88:91], v[104:107]\n"
"s_waitcnt lgkmcnt(2)\n"
"ds_read_b128 v[36:39], v2 offset:20480\n"
"s_add_u32 s61, s60, 12288\n"
"v_mfma_f32_16x16x32_bf16 v[116:119], v[32:35], a[20:23], v[116:119]\n"
"s_mov_b32 m0, s73\n"
"v_mfma_f32_16x16x32_bf16 v[52:55], v[32:35], a[44:47], v[52:55]\n"
"buffer_load_dword v22, s[40:43], s61 offen lds\n"
"v_mfma_f32_16x16x32_bf16 v[72:75], v[32:35], a[68:71], v[72:75]\n"
"v_mfma_f32_16x16x32_bf16 v[120:123], v[32:35], a[92:95], v[120:123]\n"
"s_waitcnt lgkmcnt(2)\n"
"v_mfma_f32_16x16x32_bf16 v[76:79], v[32:35], v[40:43], v[76:79]\n"
"s_waitcnt vmcnt(13)\n"
"s_waitcnt lgkmcnt(0)\n"
"s_barrier\n"
"v_mfma_f32_16x16x32_bf16 v[124:127], v[32:35], v[44:47], v[124:127]\n"
"v_mfma_f32_16x16x32_bf16 v[108:111], v[36:39], v[40:43], v[108:111]\n"
"ds_read_b64_tr_b16 v[32:33], v5 offset:24576\n"
"ds_read_b64_tr_b16 v[34:35], v5 offset:30720\n"
"v_mfma_f32_16x16x32_bf16 v[84:87], v[36:39], v[44:47], v[84:87]\n"
"v_mfma_f32_16x16x32_bf16 v[56:59], v[36:39], a[20:23], v[56:59]\n"
"ds_read_b64_tr_b16 v[40:41], v6 offset:24576\n"
"ds_read_b64_tr_b16 v[42:43], v6 offset:30720\n"
"v_mfma_f32_16x16x32_bf16 v[96:99], v[36:39], a[44:47], v[96:99]\n"
"v_mfma_f32_16x16x32_bf16 v[100:103], v[36:39], a[68:71], v[100:103]\n"
"ds_read_b64_tr_b16 v[44:45], v5 offset:25088\n"
"ds_read_b64_tr_b16 v[46:47], v5 offset:31232\n"
"v_mfma_f32_16x16x32_bf16 v[104:107], v[36:39], a[92:95], v[104:107]\n"
"s_waitcnt lgkmcnt(4)\n"
"v_mfma_f32_16x16x32_bf16 a[96:99], v[32:35], v[88:91], a[96:99]\n"
"ds_read_b64_tr_b16 v[36:37], v6 offset:25088\n"
"ds_read_b64_tr_b16 v[38:39], v6 offset:31232\n"
"v_mfma_f32_16x16x32_bf16 a[100:103], v[32:35], v[68:71], a[100:103]\n"
"v_mfma_f32_16x16x32_bf16 a[104:107], v[32:35], v[92:95], a[104:107]\n"
"v_fma_f32 v22, v116, s62, v9\n"
"v_fma_f32 v23, v117, s62, v9\n"
"v_mfma_f32_16x16x32_bf16 a[108:111], v[32:35], v[112:115], a[108:111]\n"
"v_exp_f32 v22, v22\n"
"v_fma_f32 v24, v118, s62, v9\n"
"v_mfma_f32_16x16x32_bf16 a[112:115], v[32:35], v[64:67], a[112:115]\n"
"v_exp_f32 v23, v23\n"
"v_mfma_f32_16x16x32_bf16 a[116:119], v[32:35], v[28:31], a[116:119]\n"
"s_waitcnt lgkmcnt(4)\n"
"ds_read_b64_tr_b16 v[32:33], v5 offset:25600\n"
"ds_read_b64_tr_b16 v[34:35], v5 offset:31744\n"
"v_mfma_f32_16x16x32_bf16 a[120:123], v[40:43], v[88:91], a[120:123]\n"
"v_fma_f32 v25, v119, s62, v9\n"
"v_exp_f32 v24, v24\n"
"v_mfma_f32_16x16x32_bf16 a[124:127], v[40:43], v[68:71], a[124:127]\n"
"v_add_f32 v21, v22, v23\n"
"v_fma_f32 v26, v56, s62, v9\n"
"v_mfma_f32_16x16x32_bf16 a[128:131], v[40:43], v[92:95], a[128:131]\n"
"v_exp_f32 v25, v25\n"
"v_cvt_pk_bf16_f32 v80, v22, v23\n"
"v_mfma_f32_16x16x32_bf16 a[132:135], v[40:43], v[112:115], a[132:135]\n"
"v_fma_f32 v27, v57, s62, v9\n"
"v_mfma_f32_16x16x32_bf16 a[136:139], v[40:43], v[64:67], a[136:139]\n"
"v_exp_f32 v26, v26\n"
"v_add_f32 v21, v21, v24\n"
"v_fma_f32 v22, v58, s62, v9\n"
"v_mfma_f32_16x16x32_bf16 a[140:143], v[40:43], v[28:31], a[140:143]\n"
"s_waitcnt lgkmcnt(4)\n"
"ds_read_b64_tr_b16 v[40:41], v6 offset:25600\n"
"ds_read_b64_tr_b16 v[42:43], v6 offset:31744\n"
"v_mfma_f32_16x16x32_bf16 a[144:147], v[44:47], v[88:91], a[144:147]\n"
"v_exp_f32 v27, v27\n"
"v_mfma_f32_16x16x32_bf16 a[148:151], v[44:47], v[68:71], a[148:151]\n"
"v_add_f32 v21, v21, v25\n"
"v_cvt_pk_bf16_f32 v81, v24, v25\n"
"v_mfma_f32_16x16x32_bf16 a[152:155], v[44:47], v[92:95], a[152:155]\n"
"v_fma_f32 v23, v59, s62, v9\n"
"v_exp_f32 v22, v22\n"
"v_mfma_f32_16x16x32_bf16 a[156:159], v[44:47], v[112:115], a[156:159]\n"
"v_add_f32 v21, v21, v26\n"
"v_mfma_f32_16x16x32_bf16 a[160:163], v[44:47], v[64:67], a[160:163]\n"
"v_exp_f32 v23, v23\n"
"v_cvt_pk_bf16_f32 v82, v26, v27\n"
"v_add_f32 v21, v21, v27\n"
"v_mfma_f32_16x16x32_bf16 a[164:167], v[44:47], v[28:31], a[164:167]\n"
"s_waitcnt lgkmcnt(4)\n"
"ds_read_b64_tr_b16 v[44:45], v5 offset:26112\n"
"ds_read_b64_tr_b16 v[46:47], v5 offset:32256\n"
"v_mfma_f32_16x16x32_bf16 a[168:171], v[36:39], v[88:91], a[168:171]\n"
"v_add_f32 v21, v21, v22\n"
"v_cvt_pk_bf16_f32 v83, v22, v23\n"
"v_mfma_f32_16x16x32_bf16 a[172:175], v[36:39], v[68:71], a[172:175]\n"
"v_add_f32 v21, v21, v23\n"
"v_fma_f32 v22, v52, s62, v10\n"
"v_fma_f32 v23, v53, s62, v10\n"
"v_mfma_f32_16x16x32_bf16 a[176:179], v[36:39], v[92:95], a[176:179]\n"
"v_exp_f32 v22, v22\n"
"v_mfma_f32_16x16x32_bf16 a[180:183], v[36:39], v[112:115], a[180:183]\n"
"v_fma_f32 v24, v54, s62, v10\n"
"v_exp_f32 v23, v23\n"
"v_mfma_f32_16x16x32_bf16 a[184:187], v[36:39], v[64:67], a[184:187]\n"
"v_fma_f32 v25, v55, s62, v10\n"
"v_mfma_f32_16x16x32_bf16 a[188:191], v[36:39], v[28:31], a[188:191]\n"
"s_waitcnt lgkmcnt(4)\n"
"ds_read_b64_tr_b16 v[36:37], v6 offset:26112\n"
"ds_read_b64_tr_b16 v[38:39], v6 offset:32256\n"
"v_mfma_f32_16x16x32_bf16 a[192:195], v[32:35], v[88:91], a[192:195]\n"
"v_exp_f32 v24, v24\n"
"v_add_f32 v3, v22, v23\n"
"v_mfma_f32_16x16x32_bf16 a[196:199], v[32:35], v[68:71], a[196:199]\n"
"v_fma_f32 v26, v96, s62, v10\n"
"v_exp_f32 v25, v25\n"
"v_mfma_f32_16x16x32_bf16 a[200:203], v[32:35], v[92:95], a[200:203]\n"
"v_cvt_pk_bf16_f32 v48, v22, v23\n"
"v_fma_f32 v27, v97, s62, v10\n"
"v_mfma_f32_16x16x32_bf16 a[204:207], v[32:35], v[112:115], a[204:207]\n"
"v_exp_f32 v26, v26\n"
"v_add_f32 v3, v3, v24\n"
"v_mfma_f32_16x16x32_bf16 a[208:211], v[32:35], v[64:67], a[208:211]\n"
"v_fma_f32 v22, v98, s62, v10\n"
"v_mfma_f32_16x16x32_bf16 a[212:215], v[32:35], v[28:31], a[212:215]\n"
"s_waitcnt lgkmcnt(4)\n"
"ds_read_b64_tr_b16 v[32:33], v5 offset:26624\n"
"ds_read_b64_tr_b16 v[34:35], v5 offset:32768\n"
"v_mfma_f32_16x16x32_bf16 a[216:219], v[40:43], v[88:91], a[216:219]\n"
"v_exp_f32 v27, v27\n"
"v_add_f32 v3, v3, v25\n"
"v_cvt_pk_bf16_f32 v49, v24, v25\n"
"v_mfma_f32_16x16x32_bf16 a[220:223], v[40:43], v[68:71], a[220:223]\n"
"v_fma_f32 v23, v99, s62, v10\n"
"v_mfma_f32_16x16x32_bf16 a[224:227], v[40:43], v[92:95], a[224:227]\n"
"v_exp_f32 v22, v22\n"
"v_add_f32 v3, v3, v26\n"
"v_mfma_f32_16x16x32_bf16 a[228:231], v[40:43], v[112:115], a[228:231]\n"
"v_exp_f32 v23, v23\n"
"v_cvt_pk_bf16_f32 v50, v26, v27\n"
"v_mfma_f32_16x16x32_bf16 a[232:235], v[40:43], v[64:67], a[232:235]\n"
"v_add_f32 v3, v3, v27\n"
"v_add_f32 v3, v3, v22\n"
"v_mfma_f32_16x16x32_bf16 a[236:239], v[40:43], v[28:31], a[236:239]\n"
"s_waitcnt lgkmcnt(4)\n"
"ds_read_b64_tr_b16 v[40:41], v6 offset:26624\n"
"ds_read_b64_tr_b16 v[42:43], v6 offset:32768\n"
"v_mfma_f32_16x16x32_bf16 a[240:243], v[44:47], v[88:91], a[240:243]\n"
"v_cvt_pk_bf16_f32 v51, v22, v23\n"
"v_add_f32 v3, v3, v23\n"
"v_max_f32 v27, v21, v3\n"
"v_mfma_f32_16x16x32_bf16 a[244:247], v[44:47], v[68:71], a[244:247]\n"
"v_cmp_nge_f32 vcc, s63, v27\n"
"v_fma_f32 v22, v72, s62, v11\n"
"v_mfma_f32_16x16x32_bf16 a[248:251], v[44:47], v[92:95], a[248:251]\n"
"v_fma_f32 v23, v73, s62, v11\n"
"v_mfma_f32_16x16x32_bf16 a[252:255], v[44:47], v[112:115], a[252:255]\n"
"s_cbranch_vccnz .Lrare_2_a\n"
".Lret_2_a:\n"
"v_add_f32 v15, v15, v21\n"
"v_add_f32 v16, v16, v3\n"
"v_add_u32 v3, s68, v1\n"
"v_mfma_f32_16x16x32_bf16 v[128:131], v[44:47], v[64:67], v[128:131]\n"
"v_exp_f32 v22, v22\n"
"v_fma_f32 v24, v74, s62, v11\n"
"v_mfma_f32_16x16x32_bf16 v[132:135], v[44:47], v[28:31], v[132:135]\n"
"s_waitcnt lgkmcnt(4)\n"
"ds_read_b64_tr_b16 v[44:45], v5 offset:27136\n"
"ds_read_b64_tr_b16 v[46:47], v5 offset:33280\n"
"v_mfma_f32_16x16x32_bf16 v[136:139], v[36:39], v[88:91], v[136:139]\n"
"v_exp_f32 v23, v23\n"
"v_fma_f32 v25, v75, s62, v11\n"
"v_mfma_f32_16x16x32_bf16 v[140:143], v[36:39], v[68:71], v[140:143]\n"
"v_exp_f32 v24, v24\n"
"v_mfma_f32_16x16x32_bf16 v[144:147], v[36:39], v[92:95], v[144:147]\n"
"v_add_f32 v56, v22, v23\n"
"v_fma_f32 v26, v100, s62, v11\n"
"v_mfma_f32_16x16x32_bf16 v[148:151], v[36:39], v[112:115], v[148:151]\n"
"v_exp_f32 v25, v25\n"
"v_cvt_pk_bf16_f32 v60, v22, v23\n"
"v_mfma_f32_16x16x32_bf16 v[152:155], v[36:39], v[64:67], v[152:155]\n"
"v_fma_f32 v27, v101, s62, v11\n"
"v_exp_f32 v26, v26\n"
"v_mfma_f32_16x16x32_bf16 v[156:159], v[36:39], v[28:31], v[156:159]\n"
"s_waitcnt lgkmcnt(4)\n"
"ds_read_b64_tr_b16 v[36:37], v6 offset:27136\n"
"ds_read_b64_tr_b16 v[38:39], v6 offset:33280\n"
"v_mfma_f32_16x16x32_bf16 v[160:163], v[32:35], v[88:91], v[160:163]\n"
"v_add_f32 v56, v56, v24\n"
"v_fma_f32 v22, v102, s62, v11\n"
"v_mfma_f32_16x16x32_bf16 v[164:167], v[32:35], v[68:71], v[164:167]\n"
"v_exp_f32 v27, v27\n"
"v_add_f32 v56, v56, v25\n"
"v_mfma_f32_16x16x32_bf16 v[168:171], v[32:35], v[92:95], v[168:171]\n"
"v_cvt_pk_bf16_f32 v61, v24, v25\n"
"v_fma_f32 v23, v103, s62, v11\n"
"v_mfma_f32_16x16x32_bf16 v[172:175], v[32:35], v[112:115], v[172:175]\n"
"v_exp_f32 v22, v22\n"
"v_add_f32 v56, v56, v26\n"
"v_mfma_f32_16x16x32_bf16 v[176:179], v[32:35], v[64:67], v[176:179]\n"
"v_exp_f32 v23, v23\n"
"v_mfma_f32_16x16x32_bf16 v[180:183], v[32:35], v[28:31], v[180:183]\n"
"s_waitcnt lgkmcnt(4)\n"
"v_cvt_pk_bf16_f32 v62, v26, v27\n"
"v_add_f32 v56, v56, v27\n"
"v_mfma_f32_16x16x32_bf16 v[184:187], v[40:43], v[88:91], v[184:187]\n"
"v_add_f32 v56, v56, v22\n"
"v_cvt_pk_bf16_f32 v63, v22, v23\n"
"v_add_f32 v56, v56, v23\n"
"v_mfma_f32_16x16x32_bf16 v[188:191], v[40:43], v[68:71], v[188:191]\n"
"ds_read_b128 v[32:35], v1 offset:24576\n"
"v_fma_f32 v22, v120, s62, v12\n"
"v_fma_f32 v23, v121, s62, v12\n"
"v_mfma_f32_16x16x32_bf16 v[192:195], v[40:43], v[92:95], v[192:195]\n"
"v_exp_f32 v22, v22\n"
"v_fma_f32 v24, v122, s62, v12\n"
"v_mfma_f32_16x16x32_bf16 v[196:199], v[40:43], v[112:115], v[196:199]\n"
"v_exp_f32 v23, v23\n"
"v_mfma_f32_16x16x32_bf16 v[200:203], v[40:43], v[64:67], v[200:203]\n"
"v_fma_f32 v25, v123, s62, v12\n"
"v_exp_f32 v24, v24\n"
"v_mfma_f32_16x16x32_bf16 v[204:207], v[40:43], v[28:31], v[204:207]\n"
"s_waitcnt lgkmcnt(3)\n"
"v_add_f32 v57, v22, v23\n"
"v_fma_f32 v26, v104, s62, v12\n"
"v_mfma_f32_16x16x32_bf16 v[208:211], v[44:47], v[88:91], v[208:211]\n"
"v_exp_f32 v25, v25\n"
"v_cvt_pk_bf16_f32 v116, v22, v23\n"
"v_mfma_f32_16x16x32_bf16 v[212:215], v[44:47], v[68:71], v[212:215]\n"
"ds_read_b128 v[40:43], v3 offset:0\n"
"v_fma_f32 v27, v105, s62, v12\n"
"v_mfma_f32_16x16x32_bf16 v[216:219], v[44:47], v[92:95], v[216:219]\n"
"v_exp_f32 v26, v26\n"
"v_add_f32 v57, v57, v24\n"
"v_mfma_f32_16x16x32_bf16 v[220:223], v[44:47], v[112:115], v[220:223]\n"
"v_fma_f32 v22, v106, s62, v12\n"
"v_exp_f32 v27, v27\n"
"v_mfma_f32_16x16x32_bf16 v[224:227], v[44:47], v[64:67], v[224:227]\n"
"v_add_f32 v57, v57, v25\n"
"v_cvt_pk_bf16_f32 v117, v24, v25\n"
"v_mfma_f32_16x16x32_bf16 v[228:231], v[44:47], v[28:31], v[228:231]\n"
"s_waitcnt lgkmcnt(2)\n"
"v_fma_f32 v23, v107, s62, v12\n"
"v_exp_f32 v22, v22\n"
"v_mfma_f32_16x16x32_bf16 v[232:235], v[36:39], v[88:91], v[232:235]\n"
"ds_read_b128 v[44:47], v3 offset:6144\n"
"v_add_f32 v57, v57, v26\n"
"v_mfma_f32_16x16x32_bf16 v[236:239], v[36:39], v[68:71], v[236:239]\n"
"v_exp_f32 v23, v23\n"
"v_cvt_pk_bf16_f32 v118, v26, v27\n"
"v_add_f32 v57, v57, v27\n"
"v_mfma_f32_16x16x32_bf16 v[240:243], v[36:39], v[92:95], v[240:243]\n"
"v_add_f32 v57, v57, v22\n"
"v_cvt_pk_bf16_f32 v119, v22, v23\n"
"v_mfma_f32_16x16x32_bf16 v[244:247], v[36:39], v[112:115], v[244:247]\n"
"v_add_f32 v57, v57, v23\n"
"v_max_f32 v27, v56, v57\n"
"v_cmp_nge_f32 vcc, s63, v27\n"
"v_mfma_f32_16x16x32_bf16 v[248:251], v[36:39], v[64:67], v[248:251]\n"
"v_fma_f32 v22, v76, s62, v13\n"
"v_fma_f32 v23, v77, s62, v13\n"
"v_mfma_f32_16x16x32_bf16 v[252:255], v[36:39], v[28:31], v[252:255]\n"
"s_waitcnt lgkmcnt(2)\n"
"s_cbranch_vccnz .Lrare_2_b\n"
".Lret_2_b:\n"
"ds_read_b128 v[36:39], v1 offset:30720\n"
"v_add_f32 v17, v17, v56\n"
"v_add_f32 v18, v18, v57\n"
"v_mfma_f32_16x16x32_bf16 v[52:55], v[32:35], a[0:3], 0\n"
"v_mfma_f32_16x16x32_bf16 v[96:99], v[32:35], a[24:27], 0\n"
"s_add_u32 m0, s56, 61440\n"
"v_exp_f32 v22, v22\n"
"v_mfma_f32_16x16x32_bf16 v[56:59], v[32:35], a[48:51], 0\n"
"s_add_u32 s61, s57, 49152\n"
"v_fma_f32 v24, v78, s62, v13\n"
"v_mfma_f32_16x16x32_bf16 v[72:75], v[32:35], a[72:75], 0\n"
"s_waitcnt lgkmcnt(2)\n"
"buffer_load_dwordx4 v8, s[48:51], s61 offen nt lds\n"
"v_exp_f32 v23, v23\n"
"v_fma_f32 v25, v79, s62, v13\n"
"v_mfma_f32_16x16x32_bf16 v[100:103], v[32:35], v[40:43], 0\n"
"s_waitcnt lgkmcnt(1)\n"
"v_exp_f32 v24, v24\n"
"v_mfma_f32_16x16x32_bf16 v[120:123], v[32:35], v[44:47], 0\n"
"s_waitcnt lgkmcnt(0)\n"
"ds_read_b128 v[32:35], v2 offset:24576\n"
"v_mfma_f32_16x16x32_bf16 v[112:115], v[36:39], v[40:43], 0\n"
"ds_read_b128 v[40:43], v4 offset:0\n"
"v_mfma_f32_16x16x32_bf16 v[64:67], v[36:39], v[44:47], 0\n"
"ds_read_b128 v[44:47], v4 offset:6144\n"
"v_mfma_f32_16x16x32_bf16 v[104:107], v[36:39], a[0:3], 0\n"
"v_add_f32 v21, v22, v23\n"
"v_fma_f32 v26, v108, s62, v13\n"
"v_mfma_f32_16x16x32_bf16 v[88:91], v[36:39], a[24:27], 0\n"
"v_exp_f32 v25, v25\n"
"v_mfma_f32_16x16x32_bf16 v[68:71], v[36:39], a[48:51], 0\n"
"v_cvt_pk_bf16_f32 v28, v22, v23\n"
"v_fma_f32 v27, v109, s62, v13\n"
"v_mfma_f32_16x16x32_bf16 v[92:95], v[36:39], a[72:75], 0\n"
"s_waitcnt lgkmcnt(2)\n"
"ds_read_b128 v[36:39], v2 offset:30720\n"
"v_mfma_f32_16x16x32_bf16 v[52:55], v[32:35], a[4:7], v[52:55]\n"
"v_exp_f32 v26, v26\n"
"v_mfma_f32_16x16x32_bf16 v[96:99], v[32:35], a[28:31], v[96:99]\n"
"s_add_u32 m0, s56, 62464\n"
"v_add_f32 v21, v21, v24\n"
"v_fma_f32 v22, v110, s62, v13\n"
"v_mfma_f32_16x16x32_bf16 v[56:59], v[32:35], a[52:55], v[56:59]\n"
"s_add_u32 s61, s57, 49280\n"
"v_exp_f32 v27, v27\n"
"v_mfma_f32_16x16x32_bf16 v[72:75], v[32:35], a[76:79], v[72:75]\n"
"s_waitcnt lgkmcnt(2)\n"
"buffer_load_dwordx4 v8, s[48:51], s61 offen nt lds\n"
"v_add_f32 v21, v21, v25\n"
"v_cvt_pk_bf16_f32 v29, v24, v25\n"
"v_mfma_f32_16x16x32_bf16 v[100:103], v[32:35], v[40:43], v[100:103]\n"
"s_waitcnt lgkmcnt(1)\n"
"v_fma_f32 v23, v111, s62, v13\n"
"v_mfma_f32_16x16x32_bf16 v[120:123], v[32:35], v[44:47], v[120:123]\n"
"s_waitcnt lgkmcnt(0)\n"
"ds_read_b128 v[32:35], v1 offset:25600\n"
"v_mfma_f32_16x16x32_bf16 v[112:115], v[36:39], v[40:43], v[112:115]\n"
"ds_read_b128 v[40:43], v3 offset:1024\n"
"v_mfma_f32_16x16x32_bf16 v[64:67], v[36:39], v[44:47], v[64:67]\n"
"ds_read_b128 v[44:47], v3 offset:7168\n"
"s_add_u32 m0, s56, 63488\n"
"v_mfma_f32_16x16x32_bf16 v[104:107], v[36:39], a[4:7], v[104:107]\n"
"s_add_u32 s61, s57, 49408\n"
"v_exp_f32 v22, v22\n"
"v_add_f32 v21, v21, v26\n"
"v_mfma_f32_16x16x32_bf16 v[88:91], v[36:39], a[28:31], v[88:91]\n"
"buffer_load_dwordx4 v8, s[48:51], s61 offen nt lds\n"
"v_exp_f32 v23, v23\n"
"v_mfma_f32_16x16x32_bf16 v[68:71], v[36:39], a[52:55], v[68:71]\n"
"v_cvt_pk_bf16_f32 v30, v26, v27\n"
"v_add_f32 v21, v21, v27\n"
"v_mfma_f32_16x16x32_bf16 v[92:95], v[36:39], a[76:79], v[92:95]\n"
"s_waitcnt lgkmcnt(2)\n"
"ds_read_b128 v[36:39], v1 offset:31744\n"
"v_mfma_f32_16x16x32_bf16 v[52:55], v[32:35], a[8:11], v[52:55]\n"
"v_add_f32 v21, v21, v22\n"
"v_cvt_pk_bf16_f32 v31, v22, v23\n"
"v_mfma_f32_16x16x32_bf16 v[96:99], v[32:35], a[32:35], v[96:99]\n"
"v_add_f32 v21, v21, v23\n"
"v_cmp_nge_f32 vcc, s63, v21\n"
"v_mfma_f32_16x16x32_bf16 v[56:59], v[32:35], a[56:59], v[56:59]\n"
"v_fma_f32 v22, v124, s62, v14\n"
"v_fma_f32 v23, v125, s62, v14\n"
"v_mfma_f32_16x16x32_bf16 v[72:75], v[32:35], a[80:83], v[72:75]\n"
"s_waitcnt lgkmcnt(2)\n"
"s_cbranch_vccnz .Lrare_2_d4\n"
".Lret_2_d4:\n"
"v_add_f32 v19, v19, v21\n"
"v_mfma_f32_16x16x32_bf16 v[100:103], v[32:35], v[40:43], v[100:103]\n"
"s_waitcnt lgkmcnt(1)\n"
"v_exp_f32 v22, v22\n"
"v_mfma_f32_16x16x32_bf16 v[120:123], v[32:35], v[44:47], v[120:123]\n"
"s_waitcnt lgkmcnt(0)\n"
"ds_read_b128 v[32:35], v2 offset:25600\n"
"v_mfma_f32_16x16x32_bf16 v[112:115], v[36:39], v[40:43], v[112:115]\n"
"ds_read_b128 v[40:43], v4 offset:1024\n"
"v_mfma_f32_16x16x32_bf16 v[64:67], v[36:39], v[44:47], v[64:67]\n"
"ds_read_b128 v[44:47], v4 offset:7168\n"
"s_add_u32 m0, s56, 12288\n"
"v_mfma_f32_16x16x32_bf16 v[104:107], v[36:39], a[8:11], v[104:107]\n"
"s_add_u32 s61, s57, 73728\n"
"v_fma_f32 v24, v126, s62, v14\n"
"v_mfma_f32_16x16x32_bf16 v[88:91], v[36:39], a[32:35], v[88:91]\n"
"buffer_load_dwordx4 v7, s[44:47], s61 offen nt lds\n"
"v_exp_f32 v23, v23\n"
"v_fma_f32 v25, v127, s62, v14\n"
"v_mfma_f32_16x16x32_bf16 v[68:71], v[36:39], a[56:59], v[68:71]\n"
"v_exp_f32 v24, v24\n"
"v_mfma_f32_16x16x32_bf16 v[92:95], v[36:39], a[80:83], v[92:95]\n"
"s_waitcnt lgkmcnt(2)\n"
"ds_read_b128 v[36:39], v2 offset:31744\n"
"v_mfma_f32_16x16x32_bf16 v[52:55], v[32:35], a[12:15], v[52:55]\n"
"v_add_f32 v21, v22, v23\n"
"v_fma_f32 v26, v84, s62, v14\n"
"v_mfma_f32_16x16x32_bf16 v[96:99], v[32:35], a[36:39], v[96:99]\n"
"v_exp_f32 v25, v25\n"
"v_mfma_f32_16x16x32_bf16 v[56:59], v[32:35], a[60:63], v[56:59]\n"
"v_cvt_pk_bf16_f32 v76, v22, v23\n"
"v_fma_f32 v27, v85, s62, v14\n"
"v_mfma_f32_16x16x32_bf16 v[72:75], v[32:35], a[84:87], v[72:75]\n"
"s_waitcnt lgkmcnt(2)\n"
"v_exp_f32 v26, v26\n"
"v_mfma_f32_16x16x32_bf16 v[100:103], v[32:35], v[40:43], v[100:103]\n"
"s_waitcnt lgkmcnt(1)\n"
"v_add_f32 v21, v21, v24\n"
"v_fma_f32 v22, v86, s62, v14\n"
"v_mfma_f32_16x16x32_bf16 v[120:123], v[32:35], v[44:47], v[120:123]\n"
"s_waitcnt lgkmcnt(0)\n"
"ds_read_b128 v[32:35], v1 offset:26624\n"
"v_mfma_f32_16x16x32_bf16 v[112:115], v[36:39], v[40:43], v[112:115]\n"
"ds_read_b128 v[40:43], v3 offset:2048\n"
"v_mfma_f32_16x16x32_bf16 v[64:67], v[36:39], v[44:47], v[64:67]\n"
"ds_read_b128 v[44:47], v3 offset:8192\n"
"s_add_u32 m0, s56, 13312\n"
"v_mfma_f32_16x16x32_bf16 v[104:107], v[36:39], a[12:15], v[104:107]\n"
"s_add_u32 s61, s57, 73856\n"
"v_exp_f32 v27, v27\n"
"v_mfma_f32_16x16x32_bf16 v[88:91], v[36:39], a[36:39], v[88:91]\n"
"buffer_load_dwordx4 v7, s[44:47], s61 offen nt lds\n"
"v_add_f32 v21, v21, v25\n"
"v_cvt_pk_bf16_f32 v77, v24, v25\n"
"v_mfma_f32_16x16x32_bf16 v[68:71], v[36:39], a[60:63], v[68:71]\n"
"v_fma_f32 v23, v87, s62, v14\n"
"v_mfma_f32_16x16x32_bf16 v[92:95], v[36:39], a[84:87], v[92:95]\n"
"s_waitcnt lgkmcnt(2)\n"
"ds_read_b128 v[36:39], v1 offset:32768\n"
"v_mfma_f32_16x16x32_bf16 v[52:55], v[32:35], a[16:19], v[52:55]\n"
"v_exp_f32 v22, v22\n"
"v_add_f32 v21, v21, v26\n"
"v_mfma_f32_16x16x32_bf16 v[96:99], v[32:35], a[40:43], v[96:99]\n"
"s_add_u32 m0, s56, 14336\n"
"v_exp_f32 v23, v23\n"
"v_mfma_f32_16x16x32_bf16 v[56:59], v[32:35], a[64:67], v[56:59]\n"
"s_add_u32 s61, s57, 73984\n"
"v_cvt_pk_bf16_f32 v78, v26, v27\n"
"v_add_f32 v21, v21, v27\n"
"v_mfma_f32_16x16x32_bf16 v[72:75], v[32:35], a[88:91], v[72:75]\n"
"s_waitcnt lgkmcnt(2)\n"
"buffer_load_dwordx4 v7, s[44:47], s61 offen nt lds\n"
"v_add_f32 v21, v21, v22\n"
"v_cvt_pk_bf16_f32 v79, v22, v23\n"
"v_mfma_f32_16x16x32_bf16 v[100:103], v[32:35], v[40:43], v[100:103]\n"
"s_waitcnt lgkmcnt(1)\n"
"v_add_f32 v21, v21, v23\n"
"v_cmp_nge_f32 vcc, s63, v21\n"
"v_mfma_f32_16x16x32_bf16 v[120:123], v[32:35], v[44:47], v[120:123]\n"
"s_waitcnt lgkmcnt(0)\n"
"ds_read_b128 v[32:35], v2 offset:26624\n"
"v_mfma_f32_16x16x32_bf16 v[112:115], v[36:39], v[40:43], v[112:115]\n"
"ds_read_b128 v[40:43], v4 offset:2048\n"
"v_mfma_f32_16x16x32_bf16 v[64:67], v[36:39], v[44:47], v[64:67]\n"
"ds_read_b128 v[44:47], v4 offset:8192\n"
"s_cbranch_vccnz .Lrare_2_d5\n"
".Lret_2_d5:\n"
"v_add_f32 v20, v20, v21\n"
"v_mfma_f32_16x16x32_bf16 v[104:107], v[36:39], a[16:19], v[104:107]\n"
"v_mbcnt_lo_u32_b32 v22, -1, 0\n"
"v_mbcnt_hi_u32_b32 v22, -1, v22\n"
"v_mfma_f32_16x16x32_bf16 v[88:91], v[36:39], a[40:43], v[88:91]\n"
"v_and_b32 v22, 31, v22\n"
"v_mfma_f32_16x16x32_bf16 v[68:71], v[36:39], a[64:67], v[68:71]\n"
"v_lshlrev_b32 v22, 7, v22\n"
"v_mfma_f32_16x16x32_bf16 v[92:95], v[36:39], a[88:91], v[92:95]\n"
"s_waitcnt lgkmcnt(2)\n"
"ds_read_b128 v[36:39], v2 offset:32768\n"
"s_add_u32 s61, s60, 16384\n"
"v_mfma_f32_16x16x32_bf16 v[52:55], v[32:35], a[20:23], v[52:55]\n"
"s_mov_b32 m0, s73\n"
"v_mfma_f32_16x16x32_bf16 v[96:99], v[32:35], a[44:47], v[96:99]\n"
"buffer_load_dword v22, s[40:43], s61 offen lds\n"
"v_mfma_f32_16x16x32_bf16 v[56:59], v[32:35], a[68:71], v[56:59]\n"
"v_mfma_f32_16x16x32_bf16 v[72:75], v[32:35], a[92:95], v[72:75]\n"
"s_waitcnt lgkmcnt(2)\n"
"v_mfma_f32_16x16x32_bf16 v[100:103], v[32:35], v[40:43], v[100:103]\n"
"s_waitcnt vmcnt(14)\n"
"s_waitcnt lgkmcnt(0)\n"
"s_barrier\n"
"v_mfma_f32_16x16x32_bf16 v[120:123], v[32:35], v[44:47], v[120:123]\n"
"v_mfma_f32_16x16x32_bf16 v[112:115], v[36:39], v[40:43], v[112:115]\n"
"ds_read_b64_tr_b16 v[32:33], v5 offset:36864\n"
"ds_read_b64_tr_b16 v[34:35], v5 offset:43008\n"
"v_mfma_f32_16x16x32_bf16 v[64:67], v[36:39], v[44:47], v[64:67]\n"
"v_mfma_f32_16x16x32_bf16 v[104:107], v[36:39], a[20:23], v[104:107]\n"
"ds_read_b64_tr_b16 v[40:41], v6 offset:36864\n"
"ds_read_b64_tr_b16 v[42:43], v6 offset:43008\n"
"v_mfma_f32_16x16x32_bf16 v[88:91], v[36:39], a[44:47], v[88:91]\n"
"v_mfma_f32_16x16x32_bf16 v[68:71], v[36:39], a[68:71], v[68:71]\n"
"ds_read_b64_tr_b16 v[44:45], v5 offset:37376\n"
"ds_read_b64_tr_b16 v[46:47], v5 offset:43520\n"
"v_mfma_f32_16x16x32_bf16 v[92:95], v[36:39], a[92:95], v[92:95]\n"
"s_waitcnt lgkmcnt(4)\n"
"v_mfma_f32_16x16x32_bf16 a[96:99], v[32:35], v[80:83], a[96:99]\n"
"ds_read_b64_tr_b16 v[36:37], v6 offset:37376\n"
"ds_read_b64_tr_b16 v[38:39], v6 offset:43520\n"
"v_mfma_f32_16x16x32_bf16 a[100:103], v[32:35], v[48:51], a[100:103]\n"
"v_mfma_f32_16x16x32_bf16 a[104:107], v[32:35], v[60:63], a[104:107]\n"
"v_fma_f32 v22, v52, s62, v9\n"
"v_fma_f32 v23, v53, s62, v9\n"
"v_mfma_f32_16x16x32_bf16 a[108:111], v[32:35], v[116:119], a[108:111]\n"
"v_exp_f32 v22, v22\n"
"v_fma_f32 v24, v54, s62, v9\n"
"v_mfma_f32_16x16x32_bf16 a[112:115], v[32:35], v[28:31], a[112:115]\n"
"v_exp_f32 v23, v23\n"
"v_mfma_f32_16x16x32_bf16 a[116:119], v[32:35], v[76:79], a[116:119]\n"
"s_waitcnt lgkmcnt(4)\n"
"ds_read_b64_tr_b16 v[32:33], v5 offset:37888\n"
"ds_read_b64_tr_b16 v[34:35], v5 offset:44032\n"
"v_mfma_f32_16x16x32_bf16 a[120:123], v[40:43], v[80:83], a[120:123]\n"
"v_fma_f32 v25, v55, s62, v9\n"
"v_exp_f32 v24, v24\n"
"v_mfma_f32_16x16x32_bf16 a[124:127], v[40:43], v[48:51], a[124:127]\n"
"v_add_f32 v21, v22, v23\n"
"v_fma_f32 v26, v104, s62, v9\n"
"v_mfma_f32_16x16x32_bf16 a[128:131], v[40:43], v[60:63], a[128:131]\n"
"v_exp_f32 v25, v25\n"
"v_cvt_pk_bf16_f32 v108, v22, v23\n"
"v_mfma_f32_16x16x32_bf16 a[132:135], v[40:43], v[116:119], a[132:135]\n"
"v_fma_f32 v27, v105, s62, v9\n"
"v_mfma_f32_16x16x32_bf16 a[136:139], v[40:43], v[28:31], a[136:139]\n"
"v_exp_f32 v26, v26\n"
"v_add_f32 v21, v21, v24\n"
"v_fma_f32 v22, v106, s62, v9\n"
"v_mfma_f32_16x16x32_bf16 a[140:143], v[40:43], v[76:79], a[140:143]\n"
"s_waitcnt lgkmcnt(4)\n"
"ds_read_b64_tr_b16 v[40:41], v6 offset:37888\n"
"ds_read_b64_tr_b16 v[42:43], v6 offset:44032\n"
"v_mfma_f32_16x16x32_bf16 a[144:147], v[44:47], v[80:83], a[144:147]\n"
"v_exp_f32 v27, v27\n"
"v_mfma_f32_16x16x32_bf16 a[148:151], v[44:47], v[48:51], a[148:151]\n"
"v_add_f32 v21, v21, v25\n"
"v_cvt_pk_bf16_f32 v109, v24, v25\n"
"v_mfma_f32_16x16x32_bf16 a[152:155], v[44:47], v[60:63], a[152:155]\n"
"v_fma_f32 v23, v107, s62, v9\n"
"v_exp_f32 v22, v22\n"
"v_mfma_f32_16x16x32_bf16 a[156:159], v[44:47], v[116:119], a[156:159]\n"
"v_add_f32 v21, v21, v26\n"
"v_mfma_f32_16x16x32_bf16 a[160:163], v[44:47], v[28:31], a[160:163]\n"
"v_exp_f32 v23, v23\n"
"v_cvt_pk_bf16_f32 v110, v26, v27\n"
"v_add_f32 v21, v21, v27\n"
"v_mfma_f32_16x16x32_bf16 a[164:167], v[44:47], v[76:79], a[164:167]\n"
"s_waitcnt lgkmcnt(4)\n"
"ds_read_b64_tr_b16 v[44:45], v5 offset:38400\n"
"ds_read_b64_tr_b16 v[46:47], v5 offset:44544\n"
"v_mfma_f32_16x16x32_bf16 a[168:171], v[36:39], v[80:83], a[168:171]\n"
"v_add_f32 v21, v21, v22\n"
"v_cvt_pk_bf16_f32 v111, v22, v23\n"
"v_mfma_f32_16x16x32_bf16 a[172:175], v[36:39], v[48:51], a[172:175]\n"
"v_add_f32 v21, v21, v23\n"
"v_fma_f32 v22, v96, s62, v10\n"
"v_fma_f32 v23, v97, s62, v10\n"
"v_mfma_f32_16x16x32_bf16 a[176:179], v[36:39], v[60:63], a[176:179]\n"
"v_exp_f32 v22, v22\n"
"v_mfma_f32_16x16x32_bf16 a[180:183], v[36:39], v[116:119], a[180:183]\n"
"v_fma_f32 v24, v98, s62, v10\n"
"v_exp_f32 v23, v23\n"
"v_mfma_f32_16x16x32_bf16 a[184:187], v[36:39], v[28:31], a[184:187]\n"
"v_fma_f32 v25, v99, s62, v10\n"
"v_mfma_f32_16x16x32_bf16 a[188:191], v[36:39], v[76:79], a[188:191]\n"
"s_waitcnt lgkmcnt(4)\n"
"ds_read_b64_tr_b16 v[36:37], v6 offset:38400\n"
"ds_read_b64_tr_b16 v[38:39], v6 offset:44544\n"
"v_mfma_f32_16x16x32_bf16 a[192:195], v[32:35], v[80:83], a[192:195]\n"
"v_exp_f32 v24, v24\n"
"v_add_f32 v3, v22, v23\n"
"v_mfma_f32_16x16x32_bf16 a[196:199], v[32:35], v[48:51], a[196:199]\n"
"v_fma_f32 v26, v88, s62, v10\n"
"v_exp_f32 v25, v25\n"
"v_mfma_f32_16x16x32_bf16 a[200:203], v[32:35], v[60:63], a[200:203]\n"
"v_cvt_pk_bf16_f32 v124, v22, v23\n"
"v_fma_f32 v27, v89, s62, v10\n"
"v_mfma_f32_16x16x32_bf16 a[204:207], v[32:35], v[116:119], a[204:207]\n"
"v_exp_f32 v26, v26\n"
"v_add_f32 v3, v3, v24\n"
"v_mfma_f32_16x16x32_bf16 a[208:211], v[32:35], v[28:31], a[208:211]\n"
"v_fma_f32 v22, v90, s62, v10\n"
"v_mfma_f32_16x16x32_bf16 a[212:215], v[32:35], v[76:79], a[212:215]\n"
"s_waitcnt lgkmcnt(4)\n"
"ds_read_b64_tr_b16 v[32:33], v5 offset:38912\n"
"ds_read_b64_tr_b16 v[34:35], v5 offset:45056\n"
"v_mfma_f32_16x16x32_bf16 a[216:219], v[40:43], v[80:83], a[216:219]\n"
"v_exp_f32 v27, v27\n"
"v_add_f32 v3, v3, v25\n"
"v_cvt_pk_bf16_f32 v125, v24, v25\n"
"v_mfma_f32_16x16x32_bf16 a[220:223], v[40:43], v[48:51], a[220:223]\n"
"v_fma_f32 v23, v91, s62, v10\n"
"v_mfma_f32_16x16x32_bf16 a[224:227], v[40:43], v[60:63], a[224:227]\n"
"v_exp_f32 v22, v22\n"
"v_add_f32 v3, v3, v26\n"
"v_mfma_f32_16x16x32_bf16 a[228:231], v[40:43], v[116:119], a[228:231]\n"
"v_exp_f32 v23, v23\n"
"v_cvt_pk_bf16_f32 v126, v26, v27\n"
"v_mfma_f32_16x16x32_bf16 a[232:235], v[40:43], v[28:31], a[232:235]\n"
"v_add_f32 v3, v3, v27\n"
"v_add_f32 v3, v3, v22\n"
"v_mfma_f32_16x16x32_bf16 a[236:239], v[40:43], v[76:79], a[236:239]\n"
"s_waitcnt lgkmcnt(4)\n"
"ds_read_b64_tr_b16 v[40:41], v6 offset:38912\n"
"ds_read_b64_tr_b16 v[42:43], v6 offset:45056\n"
"v_mfma_f32_16x16x32_bf16 a[240:243], v[44:47], v[80:83], a[240:243]\n"
"v_cvt_pk_bf16_f32 v127, v22, v23\n"
"v_add_f32 v3, v3, v23\n"
"v_max_f32 v27, v21, v3\n"
"v_mfma_f32_16x16x32_bf16 a[244:247], v[44:47], v[48:51], a[244:247]\n"
"v_cmp_nge_f32 vcc, s63, v27\n"
"v_fma_f32 v22, v56, s62, v11\n"
"v_mfma_f32_16x16x32_bf16 a[248:251], v[44:47], v[60:63], a[248:251]\n"
"v_fma_f32 v23, v57, s62, v11\n"
"v_mfma_f32_16x16x32_bf16 a[252:255], v[44:47], v[116:119], a[252:255]\n"
"s_cbranch_vccnz .Lrare_3_a\n"
".Lret_3_a:\n"
"v_add_f32 v15, v15, v21\n"
"v_add_f32 v16, v16, v3\n"
"v_add_u32 v3, s68, v1\n"
"v_mfma_f32_16x16x32_bf16 v[128:131], v[44:47], v[28:31], v[128:131]\n"
"v_exp_f32 v22, v22\n"
"v_fma_f32 v24, v58, s62, v11\n"
"v_mfma_f32_16x16x32_bf16 v[132:135], v[44:47], v[76:79], v[132:135]\n"
"s_waitcnt lgkmcnt(4)\n"
"ds_read_b64_tr_b16 v[44:45], v5 offset:39424\n"
"ds_read_b64_tr_b16 v[46:47], v5 offset:45568\n"
"v_mfma_f32_16x16x32_bf16 v[136:139], v[36:39], v[80:83], v[136:139]\n"
"v_exp_f32 v23, v23\n"
"v_fma_f32 v25, v59, s62, v11\n"
"v_mfma_f32_16x16x32_bf16 v[140:143], v[36:39], v[48:51], v[140:143]\n"
"v_exp_f32 v24, v24\n"
"v_mfma_f32_16x16x32_bf16 v[144:147], v[36:39], v[60:63], v[144:147]\n"
"v_add_f32 v104, v22, v23\n"
"v_fma_f32 v26, v68, s62, v11\n"
"v_mfma_f32_16x16x32_bf16 v[148:151], v[36:39], v[116:119], v[148:151]\n"
"v_exp_f32 v25, v25\n"
"v_cvt_pk_bf16_f32 v84, v22, v23\n"
"v_mfma_f32_16x16x32_bf16 v[152:155], v[36:39], v[28:31], v[152:155]\n"
"v_fma_f32 v27, v69, s62, v11\n"
"v_exp_f32 v26, v26\n"
"v_mfma_f32_16x16x32_bf16 v[156:159], v[36:39], v[76:79], v[156:159]\n"
"s_waitcnt lgkmcnt(4)\n"
"ds_read_b64_tr_b16 v[36:37], v6 offset:39424\n"
"ds_read_b64_tr_b16 v[38:39], v6 offset:45568\n"
"v_mfma_f32_16x16x32_bf16 v[160:163], v[32:35], v[80:83], v[160:163]\n"
"v_add_f32 v104, v104, v24\n"
"v_fma_f32 v22, v70, s62, v11\n"
"v_mfma_f32_16x16x32_bf16 v[164:167], v[32:35], v[48:51], v[164:167]\n"
"v_exp_f32 v27, v27\n"
"v_add_f32 v104, v104, v25\n"
"v_mfma_f32_16x16x32_bf16 v[168:171], v[32:35], v[60:63], v[168:171]\n"
"v_cvt_pk_bf16_f32 v85, v24, v25\n"
"v_fma_f32 v23, v71, s62, v11\n"
"v_mfma_f32_16x16x32_bf16 v[172:175], v[32:35], v[116:119], v[172:175]\n"
"v_exp_f32 v22, v22\n"
"v_add_f32 v104, v104, v26\n"
"v_mfma_f32_16x16x32_bf16 v[176:179], v[32:35], v[28:31], v[176:179]\n"
"v_exp_f32 v23, v23\n"
"v_mfma_f32_16x16x32_bf16 v[180:183], v[32:35], v[76:79], v[180:183]\n"
"s_waitcnt lgkmcnt(4)\n"
"v_cvt_pk_bf16_f32 v86, v26, v27\n"
"v_add_f32 v104, v104, v27\n"
"v_mfma_f32_16x16x32_bf16 v[184:187], v[40:43], v[80:83], v[184:187]\n"
"v_add_f32 v104, v104, v22\n"
"v_cvt_pk_bf16_f32 v87, v22, v23\n"
"v_add_f32 v104, v104, v23\n"
"v_mfma_f32_16x16x32_bf16 v[188:191], v[40:43], v[48:51], v[188:191]\n"
"ds_read_b128 v[32:35], v1 offset:36864\n"
"v_fma_f32 v22, v72, s62, v12\n"
"v_fma_f32 v23, v73, s62, v12\n"
"v_mfma_f32_16x16x32_bf16 v[192:195], v[40:43], v[60:63], v[192:195]\n"
"v_exp_f32 v22, v22\n"
"v_fma_f32 v24, v74, s62, v12\n"
"v_mfma_f32_16x16x32_bf16 v[196:199], v[40:43], v[116:119], v[196:199]\n"
"v_exp_f32 v23, v23\n"
"v_mfma_f32_16x16x32_bf16 v[200:203], v[40:43], v[28:31], v[200:203]\n"
"v_fma_f32 v25, v75, s62, v12\n"
"v_exp_f32 v24, v24\n"
"v_mfma_f32_16x16x32_bf16 v[204:207], v[40:43], v[76:79], v[204:207]\n"
"s_waitcnt lgkmcnt(3)\n"
"v_add_f32 v105, v22, v23\n"
"v_fma_f32 v26, v92, s62, v12\n"
"v_mfma_f32_16x16x32_bf16 v[208:211], v[44:47], v[80:83], v[208:211]\n"
"v_exp_f32 v25, v25\n"
"v_cvt_pk_bf16_f32 v52, v22, v23\n"
"v_mfma_f32_16x16x32_bf16 v[212:215], v[44:47], v[48:51], v[212:215]\n"
"ds_read_b128 v[40:43], v3 offset:0\n"
"v_fma_f32 v27, v93, s62, v12\n"
"v_mfma_f32_16x16x32_bf16 v[216:219], v[44:47], v[60:63], v[216:219]\n"
"v_exp_f32 v26, v26\n"
"v_add_f32 v105, v105, v24\n"
"v_mfma_f32_16x16x32_bf16 v[220:223], v[44:47], v[116:119], v[220:223]\n"
"v_fma_f32 v22, v94, s62, v12\n"
"v_exp_f32 v27, v27\n"
"v_mfma_f32_16x16x32_bf16 v[224:227], v[44:47], v[28:31], v[224:227]\n"
"v_add_f32 v105, v105, v25\n"
"v_cvt_pk_bf16_f32 v53, v24, v25\n"
"v_mfma_f32_16x16x32_bf16 v[228:231], v[44:47], v[76:79], v[228:231]\n"
"s_waitcnt lgkmcnt(2)\n"
"v_fma_f32 v23, v95, s62, v12\n"
"v_exp_f32 v22, v22\n"
"v_mfma_f32_16x16x32_bf16 v[232:235], v[36:39], v[80:83], v[232:235]\n"
"ds_read_b128 v[44:47], v3 offset:6144\n"
"v_add_f32 v105, v105, v26\n"
"v_mfma_f32_16x16x32_bf16 v[236:239], v[36:39], v[48:51], v[236:239]\n"
"v_exp_f32 v23, v23\n"
"v_cvt_pk_bf16_f32 v54, v26, v27\n"
"v_add_f32 v105, v105, v27\n"
"v_mfma_f32_16x16x32_bf16 v[240:243], v[36:39], v[60:63], v[240:243]\n"
"v_add_f32 v105, v105, v22\n"
"v_cvt_pk_bf16_f32 v55, v22, v23\n"
"v_mfma_f32_16x16x32_bf16 v[244:247], v[36:39], v[116:119], v[244:247]\n"
"v_add_f32 v105, v105, v23\n"
"v_max_f32 v27, v104, v105\n"
"v_cmp_nge_f32 vcc, s63, v27\n"
"v_mfma_f32_16x16x32_bf16 v[248:251], v[36:39], v[28:31], v[248:251]\n"
"v_fma_f32 v22, v100, s62, v13\n"
"v_fma_f32 v23, v101, s62, v13\n"
"v_mfma_f32_16x16x32_bf16 v[252:255], v[36:39], v[76:79], v[252:255]\n"
"s_waitcnt lgkmcnt(2)\n"
"s_cbranch_vccnz .Lrare_3_b\n"
".Lret_3_b:\n"
"ds_read_b128 v[36:39], v1 offset:43008\n"
"v_add_f32 v17, v17, v104\n"
"v_add_f32 v18, v18, v105\n"
"v_mfma_f32_16x16x32_bf16 v[96:99], v[32:35], a[0:3], 0\n"
"v_mfma_f32_16x16x32_bf16 v[88:91], v[32:35], a[24:27], 0\n"
"s_add_u32 m0, s56, 73728\n"
"v_exp_f32 v22, v22\n"
"v_mfma_f32_16x16x32_bf16 v[104:107], v[32:35], a[48:51], 0\n"
"s_add_u32 s61, s57, 61440\n"
"v_fma_f32 v24, v102, s62, v13\n"
"v_mfma_f32_16x16x32_bf16 v[56:59], v[32:35], a[72:75], 0\n"
"s_waitcnt lgkmcnt(2)\n"
"buffer_load_dwordx4 v8, s[48:51], s61 offen nt lds\n"
"v_exp_f32 v23, v23\n"
"v_fma_f32 v25, v103, s62, v13\n"
"v_mfma_f32_16x16x32_bf16 v[68:71], v[32:35], v[40:43], 0\n"
"s_waitcnt lgkmcnt(1)\n"
"v_exp_f32 v24, v24\n"
"v_mfma_f32_16x16x32_bf16 v[72:75], v[32:35], v[44:47], 0\n"
"s_waitcnt lgkmcnt(0)\n"
"ds_read_b128 v[32:35], v2 offset:36864\n"
"v_mfma_f32_16x16x32_bf16 v[116:119], v[36:39], v[40:43], 0\n"
"ds_read_b128 v[40:43], v4 offset:0\n"
"v_mfma_f32_16x16x32_bf16 v[28:31], v[36:39], v[44:47], 0\n"
"ds_read_b128 v[44:47], v4 offset:6144\n"
"v_mfma_f32_16x16x32_bf16 v[92:95], v[36:39], a[0:3], 0\n"
"v_add_f32 v21, v22, v23\n"
"v_fma_f32 v26, v112, s62, v13\n"
"v_mfma_f32_16x16x32_bf16 v[80:83], v[36:39], a[24:27], 0\n"
"v_exp_f32 v25, v25\n"
"v_mfma_f32_16x16x32_bf16 v[48:51], v[36:39], a[48:51], 0\n"
"v_cvt_pk_bf16_f32 v76, v22, v23\n"
"v_fma_f32 v27, v113, s62, v13\n"
"v_mfma_f32_16x16x32_bf16 v[60:63], v[36:39], a[72:75], 0\n"
"s_waitcnt lgkmcnt(2)\n"
"ds_read_b128 v[36:39], v2 offset:43008\n"
"v_mfma_f32_16x16x32_bf16 v[96:99], v[32:35], a[4:7], v[96:99]\n"
"v_exp_f32 v26, v26\n"
"v_mfma_f32_16x16x32_bf16 v[88:91], v[32:35], a[28:31], v[88:91]\n"
"s_add_u32 m0, s56, 74752\n"
"v_add_f32 v21, v21, v24\n"
"v_fma_f32 v22, v114, s62, v13\n"
"v_mfma_f32_16x16x32_bf16 v[104:107], v[32:35], a[52:55], v[104:107]\n"
"s_add_u32 s61, s57, 61568\n"
"v_exp_f32 v27, v27\n"
"v_mfma_f32_16x16x32_bf16 v[56:59], v[32:35], a[76:79], v[56:59]\n"
"s_waitcnt lgkmcnt(2)\n"
"buffer_load_dwordx4 v8, s[48:51], s61 offen nt lds\n"
"v_add_f32 v21, v21, v25\n"
"v_cvt_pk_bf16_f32 v77, v24, v25\n"
"v_mfma_f32_16x16x32_bf16 v[68:71], v[32:35], v[40:43], v[68:71]\n"
"s_waitcnt lgkmcnt(1)\n"
"v_fma_f32 v23, v115, s62, v13\n"
"v_mfma_f32_16x16x32_bf16 v[72:75], v[32:35], v[44:47], v[72:75]\n"
"s_waitcnt lgkmcnt(0)\n"
"ds_read_b128 v[32:35], v1 offset:37888\n"
"v_mfma_f32_16x16x32_bf16 v[116:119], v[36:39], v[40:43], v[116:119]\n"
"ds_read_b128 v[40:43], v3 offset:1024\n"
"v_mfma_f32_16x16x32_bf16 v[28:31], v[36:39], v[44:47], v[28:31]\n"
"ds_read_b128 v[44:47], v3 offset:7168\n"
"s_add_u32 m0, s56, 75776\n"
"v_mfma_f32_16x16x32_bf16 v[92:95], v[36:39], a[4:7], v[92:95]\n"
"s_add_u32 s61, s57, 61696\n"
"v_exp_f32 v22, v22\n"
"v_add_f32 v21, v21, v26\n"
"v_mfma_f32_16x16x32_bf16 v[80:83], v[36:39], a[28:31], v[80:83]\n"
"buffer_load_dwordx4 v8, s[48:51], s61 offen nt lds\n"
"v_exp_f32 v23, v23\n"
"v_mfma_f32_16x16x32_bf16 v[48:51], v[36:39], a[52:55], v[48:51]\n"
"v_cvt_pk_bf16_f32 v78, v26, v27\n"
"v_add_f32 v21, v21, v27\n"
"v_mfma_f32_16x16x32_bf16 v[60:63], v[36:39], a[76:79], v[60:63]\n"
"s_waitcnt lgkmcnt(2)\n"
"ds_read_b128 v[36:39], v1 offset:44032\n"
"v_mfma_f32_16x16x32_bf16 v[96:99], v[32:35], a[8:11], v[96:99]\n"
"v_add_f32 v21, v21, v22\n"
"v_cvt_pk_bf16_f32 v79, v22, v23\n"
"v_mfma_f32_16x16x32_bf16 v[88:91], v[32:35], a[32:35], v[88:91]\n"
"v_add_f32 v21, v21, v23\n"
"v_cmp_nge_f32 vcc, s63, v21\n"
"v_mfma_f32_16x16x32_bf16 v[104:107], v[32:35], a[56:59], v[104:107]\n"
"v_fma_f32 v22, v120, s62, v14\n"
"v_fma_f32 v23, v121, s62, v14\n"
"v_mfma_f32_16x16x32_bf16 v[56:59], v[32:35], a[80:83], v[56:59]\n"
"s_waitcnt lgkmcnt(2)\n"
"s_cbranch_vccnz .Lrare_3_d4\n"
".Lret_3_d4:\n"
"v_add_f32 v19, v19, v21\n"
"v_mfma_f32_16x16x32_bf16 v[68:71], v[32:35], v[40:43], v[68:71]\n"
"s_waitcnt lgkmcnt(1)\n"
"v_exp_f32 v22, v22\n"
"v_mfma_f32_16x16x32_bf16 v[72:75], v[32:35], v[44:47], v[72:75]\n"
"s_waitcnt lgkmcnt(0)\n"
"ds_read_b128 v[32:35], v2 offset:37888\n"
"v_mfma_f32_16x16x32_bf16 v[116:119], v[36:39], v[40:43], v[116:119]\n"
"ds_read_b128 v[40:43], v4 offset:1024\n"
"v_mfma_f32_16x16x32_bf16 v[28:31], v[36:39], v[44:47], v[28:31]\n"
"ds_read_b128 v[44:47], v4 offset:7168\n"
"s_add_u32 m0, s56, 24576\n"
"v_mfma_f32_16x16x32_bf16 v[92:95], v[36:39], a[8:11], v[92:95]\n"
"s_add_u32 s61, s57, 86016\n"
"v_fma_f32 v24, v122, s62, v14\n"
"v_mfma_f32_16x16x32_bf16 v[80:83], v[36:39], a[32:35], v[80:83]\n"
"buffer_load_dwordx4 v7, s[44:47], s61 offen nt lds\n"
"v_exp_f32 v23, v23\n"
"v_fma_f32 v25, v123, s62, v14\n"
"v_mfma_f32_16x16x32_bf16 v[48:51], v[36:39], a[56:59], v[48:51]\n"
"v_exp_f32 v24, v24\n"
"v_mfma_f32_16x16x32_bf16 v[60:63], v[36:39], a[80:83], v[60:63]\n"
"s_waitcnt lgkmcnt(2)\n"
"ds_read_b128 v[36:39], v2 offset:44032\n"
"v_mfma_f32_16x16x32_bf16 v[96:99], v[32:35], a[12:15], v[96:99]\n"
"v_add_f32 v21, v22, v23\n"
"v_fma_f32 v26, v64, s62, v14\n"
"v_mfma_f32_16x16x32_bf16 v[88:91], v[32:35], a[36:39], v[88:91]\n"
"v_exp_f32 v25, v25\n"
"v_mfma_f32_16x16x32_bf16 v[104:107], v[32:35], a[60:63], v[104:107]\n"
"v_cvt_pk_bf16_f32 v100, v22, v23\n"
"v_fma_f32 v27, v65, s62, v14\n"
"v_mfma_f32_16x16x32_bf16 v[56:59], v[32:35], a[84:87], v[56:59]\n"
"s_waitcnt lgkmcnt(2)\n"
"v_exp_f32 v26, v26\n"
"v_mfma_f32_16x16x32_bf16 v[68:71], v[32:35], v[40:43], v[68:71]\n"
"s_waitcnt lgkmcnt(1)\n"
"v_add_f32 v21, v21, v24\n"
"v_fma_f32 v22, v66, s62, v14\n"
"v_mfma_f32_16x16x32_bf16 v[72:75], v[32:35], v[44:47], v[72:75]\n"
"s_waitcnt lgkmcnt(0)\n"
"ds_read_b128 v[32:35], v1 offset:38912\n"
"v_mfma_f32_16x16x32_bf16 v[116:119], v[36:39], v[40:43], v[116:119]\n"
"ds_read_b128 v[40:43], v3 offset:2048\n"
"v_mfma_f32_16x16x32_bf16 v[28:31], v[36:39], v[44:47], v[28:31]\n"
"ds_read_b128 v[44:47], v3 offset:8192\n"
"s_add_u32 m0, s56, 25600\n"
"v_mfma_f32_16x16x32_bf16 v[92:95], v[36:39], a[12:15], v[92:95]\n"
"s_add_u32 s61, s57, 86144\n"
"v_exp_f32 v27, v27\n"
"v_mfma_f32_16x16x32_bf16 v[80:83], v[36:39], a[36:39], v[80:83]\n"
"buffer_load_dwordx4 v7, s[44:47], s61 offen nt lds\n"
"v_add_f32 v21, v21, v25\n"
"v_cvt_pk_bf16_f32 v101, v24, v25\n"
"v_mfma_f32_16x16x32_bf16 v[48:51], v[36:39], a[60:63], v[48:51]\n"
"v_fma_f32 v23, v67, s62, v14\n"
"v_mfma_f32_16x16x32_bf16 v[60:63], v[36:39], a[84:87], v[60:63]\n"
"s_waitcnt lgkmcnt(2)\n"
"ds_read_b128 v[36:39], v1 offset:45056\n"
"v_mfma_f32_16x16x32_bf16 v[96:99], v[32:35], a[16:19], v[96:99]\n"
"v_exp_f32 v22, v22\n"
"v_add_f32 v21, v21, v26\n"
"v_mfma_f32_16x16x32_bf16 v[88:91], v[32:35], a[40:43], v[88:91]\n"
"s_add_u32 m0, s56, 26624\n"
"v_exp_f32 v23, v23\n"
"v_mfma_f32_16x16x32_bf16 v[104:107], v[32:35], a[64:67], v[104:107]\n"
"s_add_u32 s61, s57, 86272\n"
"v_cvt_pk_bf16_f32 v102, v26, v27\n"
"v_add_f32 v21, v21, v27\n"
"v_mfma_f32_16x16x32_bf16 v[56:59], v[32:35], a[88:91], v[56:59]\n"
"s_waitcnt lgkmcnt(2)\n"
"buffer_load_dwordx4 v7, s[44:47], s61 offen nt lds\n"
"v_add_f32 v21, v21, v22\n"
"v_cvt_pk_bf16_f32 v103, v22, v23\n"
"v_mfma_f32_16x16x32_bf16 v[68:71], v[32:35], v[40:43], v[68:71]\n"
"s_waitcnt lgkmcnt(1)\n"
"v_add_f32 v21, v21, v23\n"
"v_cmp_nge_f32 vcc, s63, v21\n"
"v_mfma_f32_16x16x32_bf16 v[72:75], v[32:35], v[44:47], v[72:75]\n"
"s_waitcnt lgkmcnt(0)\n"
"ds_read_b128 v[32:35], v2 offset:38912\n"
"v_mfma_f32_16x16x32_bf16 v[116:119], v[36:39], v[40:43], v[116:119]\n"
"ds_read_b128 v[40:43], v4 offset:2048\n"
"v_mfma_f32_16x16x32_bf16 v[28:31], v[36:39], v[44:47], v[28:31]\n"
"ds_read_b128 v[44:47], v4 offset:8192\n"
"s_cbranch_vccnz .Lrare_3_d5\n"
".Lret_3_d5:\n"
"v_add_f32 v20, v20, v21\n"
"v_mfma_f32_16x16x32_bf16 v[92:95], v[36:39], a[16:19], v[92:95]\n"
"v_mbcnt_lo_u32_b32 v22, -1, 0\n"
"v_mbcnt_hi_u32_b32 v22, -1, v22\n"
"v_mfma_f32_16x16x32_bf16 v[80:83], v[36:39], a[40:43], v[80:83]\n"
"v_and_b32 v22, 31, v22\n"
"v_mfma_f32_16x16x32_bf16 v[48:51], v[36:39], a[64:67], v[48:51]\n"
"v_lshlrev_b32 v22, 7, v22\n"
"v_mfma_f32_16x16x32_bf16 v[60:63], v[36:39], a[88:91], v[60:63]\n"
"s_waitcnt lgkmcnt(2)\n"
"ds_read_b128 v[36:39], v2 offset:45056\n"
"s_add_u32 s61, s60, 20480\n"
"v_mfma_f32_16x16x32_bf16 v[96:99], v[32:35], a[20:23], v[96:99]\n"
"s_mov_b32 m0, s73\n"
"v_mfma_f32_16x16x32_bf16 v[88:91], v[32:35], a[44:47], v[88:91]\n"
"buffer_load_dword v22, s[40:43], s61 offen lds\n"
"v_mfma_f32_16x16x32_bf16 v[104:107], v[32:35], a[68:71], v[104:107]\n"
"v_mfma_f32_16x16x32_bf16 v[56:59], v[32:35], a[92:95], v[56:59]\n"
"s_waitcnt lgkmcnt(2)\n"
"v_mfma_f32_16x16x32_bf16 v[68:71], v[32:35], v[40:43], v[68:71]\n"
"s_waitcnt vmcnt(15)\n"
"s_waitcnt lgkmcnt(0)\n"
"s_barrier\n"
"v_mfma_f32_16x16x32_bf16 v[72:75], v[32:35], v[44:47], v[72:75]\n"
"v_mfma_f32_16x16x32_bf16 v[116:119], v[36:39], v[40:43], v[116:119]\n"
"ds_read_b64_tr_b16 v[32:33], v5 offset:0\n"
"ds_read_b64_tr_b16 v[34:35], v5 offset:6144\n"
"v_mfma_f32_16x16x32_bf16 v[28:31], v[36:39], v[44:47], v[28:31]\n"
"v_mfma_f32_16x16x32_bf16 v[92:95], v[36:39], a[20:23], v[92:95]\n"
"ds_read_b64_tr_b16 v[40:41], v6 offset:0\n"
"ds_read_b64_tr_b16 v[42:43], v6 offset:6144\n"
"v_mfma_f32_16x16x32_bf16 v[80:83], v[36:39], a[44:47], v[80:83]\n"
"v_mfma_f32_16x16x32_bf16 v[48:51], v[36:39], a[68:71], v[48:51]\n"
"ds_read_b64_tr_b16 v[44:45], v5 offset:512\n"
"ds_read_b64_tr_b16 v[46:47], v5 offset:6656\n"
"v_mfma_f32_16x16x32_bf16 v[60:63], v[36:39], a[92:95], v[60:63]\n"
"s_waitcnt lgkmcnt(4)\n"
"v_mfma_f32_16x16x32_bf16 a[96:99], v[32:35], v[108:111], a[96:99]\n"
"ds_read_b64_tr_b16 v[36:37], v6 offset:512\n"
"ds_read_b64_tr_b16 v[38:39], v6 offset:6656\n"
"v_mfma_f32_16x16x32_bf16 a[100:103], v[32:35], v[124:127], a[100:103]\n"
"v_mfma_f32_16x16x32_bf16 a[104:107], v[32:35], v[84:87], a[104:107]\n"
"v_fma_f32 v22, v96, s62, v9\n"
"v_fma_f32 v23, v97, s62, v9\n"
"v_mfma_f32_16x16x32_bf16 a[108:111], v[32:35], v[52:55], a[108:111]\n"
"v_exp_f32 v22, v22\n"
"v_fma_f32 v24, v98, s62, v9\n"
"v_mfma_f32_16x16x32_bf16 a[112:115], v[32:35], v[76:79], a[112:115]\n"
"v_exp_f32 v23, v23\n"
"v_mfma_f32_16x16x32_bf16 a[116:119], v[32:35], v[100:103], a[116:119]\n"
"s_waitcnt lgkmcnt(4)\n"
"ds_read_b64_tr_b16 v[32:33], v5 offset:1024\n"
"ds_read_b64_tr_b16 v[34:35], v5 offset:7168\n"
"v_mfma_f32_16x16x32_bf16 a[120:123], v[40:43], v[108:111], a[120:123]\n"
"v_fma_f32 v25, v99, s62, v9\n"
"v_exp_f32 v24, v24\n"
"v_mfma_f32_16x16x32_bf16 a[124:127], v[40:43], v[124:127], a[124:127]\n"
"v_add_f32 v21, v22, v23\n"
"v_fma_f32 v26, v92, s62, v9\n"
"v_mfma_f32_16x16x32_bf16 a[128:131], v[40:43], v[84:87], a[128:131]\n"
"v_exp_f32 v25, v25\n"
"v_cvt_pk_bf16_f32 v112, v22, v23\n"
"v_mfma_f32_16x16x32_bf16 a[132:135], v[40:43], v[52:55], a[132:135]\n"
"v_fma_f32 v27, v93, s62, v9\n"
"v_mfma_f32_16x16x32_bf16 a[136:139], v[40:43], v[76:79], a[136:139]\n"
"v_exp_f32 v26, v26\n"
"v_add_f32 v21, v21, v24\n"
"v_fma_f32 v22, v94, s62, v9\n"
"v_mfma_f32_16x16x32_bf16 a[140:143], v[40:43], v[100:103], a[140:143]\n"
"s_waitcnt lgkmcnt(4)\n"
"ds_read_b64_tr_b16 v[40:41], v6 offset:1024\n"
"ds_read_b64_tr_b16 v[42:43], v6 offset:7168\n"
"v_mfma_f32_16x16x32_bf16 a[144:147], v[44:47], v[108:111], a[144:147]\n"
"v_exp_f32 v27, v27\n"
"v_mfma_f32_16x16x32_bf16 a[148:151], v[44:47], v[124:127], a[148:151]\n"
"v_add_f32 v21, v21, v25\n"
"v_cvt_pk_bf16_f32 v113, v24, v25\n"
"v_mfma_f32_16x16x32_bf16 a[152:155], v[44:47], v[84:87], a[152:155]\n"
"v_fma_f32 v23, v95, s62, v9\n"
"v_exp_f32 v22, v22\n"
"v_mfma_f32_16x16x32_bf16 a[156:159], v[44:47], v[52:55], a[156:159]\n"
"v_add_f32 v21, v21, v26\n"
"v_mfma_f32_16x16x32_bf16 a[160:163], v[44:47], v[76:79], a[160:163]\n"
"v_exp_f32 v23, v23\n"
"v_cvt_pk_bf16_f32 v114, v26, v27\n"
"v_add_f32 v21, v21, v27\n"
"v_mfma_f32_16x16x32_bf16 a[164:167], v[44:47], v[100:103], a[164:167]\n"
"s_waitcnt lgkmcnt(4)\n"
"ds_read_b64_tr_b16 v[44:45], v5 offset:1536\n"
"ds_read_b64_tr_b16 v[46:47], v5 offset:7680\n"
"v_mfma_f32_16x16x32_bf16 a[168:171], v[36:39], v[108:111], a[168:171]\n"
"v_add_f32 v21, v21, v22\n"
"v_cvt_pk_bf16_f32 v115, v22, v23\n"
"v_mfma_f32_16x16x32_bf16 a[172:175], v[36:39], v[124:127], a[172:175]\n"
"v_add_f32 v21, v21, v23\n"
"v_fma_f32 v22, v88, s62, v10\n"
"v_fma_f32 v23, v89, s62, v10\n"
"v_mfma_f32_16x16x32_bf16 a[176:179], v[36:39], v[84:87], a[176:179]\n"
"v_exp_f32 v22, v22\n"
"v_mfma_f32_16x16x32_bf16 a[180:183], v[36:39], v[52:55], a[180:183]\n"
"v_fma_f32 v24, v90, s62, v10\n"
"v_exp_f32 v23, v23\n"
"v_mfma_f32_16x16x32_bf16 a[184:187], v[36:39], v[76:79], a[184:187]\n"
"v_fma_f32 v25, v91, s62, v10\n"
"v_mfma_f32_16x16x32_bf16 a[188:191], v[36:39], v[100:103], a[188:191]\n"
"s_waitcnt lgkmcnt(4)\n"
"ds_read_b64_tr_b16 v[36:37], v6 offset:1536\n"
"ds_read_b64_tr_b16 v[38:39], v6 offset:7680\n"
"v_mfma_f32_16x16x32_bf16 a[192:195], v[32:35], v[108:111], a[192:195]\n"
"v_exp_f32 v24, v24\n"
"v_add_f32 v3, v22, v23\n"
"v_mfma_f32_16x16x32_bf16 a[196:199], v[32:35], v[124:127], a[196:199]\n"
"v_fma_f32 v26, v80, s62, v10\n"
"v_exp_f32 v25, v25\n"
"v_mfma_f32_16x16x32_bf16 a[200:203], v[32:35], v[84:87], a[200:203]\n"
"v_cvt_pk_bf16_f32 v120, v22, v23\n"
"v_fma_f32 v27, v81, s62, v10\n"
"v_mfma_f32_16x16x32_bf16 a[204:207], v[32:35], v[52:55], a[204:207]\n"
"v_exp_f32 v26, v26\n"
"v_add_f32 v3, v3, v24\n"
"v_mfma_f32_16x16x32_bf16 a[208:211], v[32:35], v[76:79], a[208:211]\n"
"v_fma_f32 v22, v82, s62, v10\n"
"v_mfma_f32_16x16x32_bf16 a[212:215], v[32:35], v[100:103], a[212:215]\n"
"s_waitcnt lgkmcnt(4)\n"
"ds_read_b64_tr_b16 v[32:33], v5 offset:2048\n"
"ds_read_b64_tr_b16 v[34:35], v5 offset:8192\n"
"v_mfma_f32_16x16x32_bf16 a[216:219], v[40:43], v[108:111], a[216:219]\n"
"v_exp_f32 v27, v27\n"
"v_add_f32 v3, v3, v25\n"
"v_cvt_pk_bf16_f32 v121, v24, v25\n"
"v_mfma_f32_16x16x32_bf16 a[220:223], v[40:43], v[124:127], a[220:223]\n"
"v_fma_f32 v23, v83, s62, v10\n"
"v_mfma_f32_16x16x32_bf16 a[224:227], v[40:43], v[84:87], a[224:227]\n"
"v_exp_f32 v22, v22\n"
"v_add_f32 v3, v3, v26\n"
"v_mfma_f32_16x16x32_bf16 a[228:231], v[40:43], v[52:55], a[228:231]\n"
"v_exp_f32 v23, v23\n"
"v_cvt_pk_bf16_f32 v122, v26, v27\n"
"v_mfma_f32_16x16x32_bf16 a[232:235], v[40:43], v[76:79], a[232:235]\n"
"v_add_f32 v3, v3, v27\n"
"v_add_f32 v3, v3, v22\n"
"v_mfma_f32_16x16x32_bf16 a[236:239], v[40:43], v[100:103], a[236:239]\n"
"s_waitcnt lgkmcnt(4)\n"
"ds_read_b64_tr_b16 v[40:41], v6 offset:2048\n"
"ds_read_b64_tr_b16 v[42:43], v6 offset:8192\n"
"v_mfma_f32_16x16x32_bf16 a[240:243], v[44:47], v[108:111], a[240:243]\n"
"v_cvt_pk_bf16_f32 v123, v22, v23\n"
"v_add_f32 v3, v3, v23\n"
"v_max_f32 v27, v21, v3\n"
"v_mfma_f32_16x16x32_bf16 a[244:247], v[44:47], v[124:127], a[244:247]\n"
"v_cmp_nge_f32 vcc, s63, v27\n"
"v_fma_f32 v22, v104, s62, v11\n"
"v_mfma_f32_16x16x32_bf16 a[248:251], v[44:47], v[84:87], a[248:251]\n"
"v_fma_f32 v23, v105, s62, v11\n"
"v_mfma_f32_16x16x32_bf16 a[252:255], v[44:47], v[52:55], a[252:255]\n"
"s_cbranch_vccnz .Lrare_4_a\n"
".Lret_4_a:\n"
"v_add_f32 v15, v15, v21\n"
"v_add_f32 v16, v16, v3\n"
"v_add_u32 v3, s68, v1\n"
"v_mfma_f32_16x16x32_bf16 v[128:131], v[44:47], v[76:79], v[128:131]\n"
"v_exp_f32 v22, v22\n"
"v_fma_f32 v24, v106, s62, v11\n"
"v_mfma_f32_16x16x32_bf16 v[132:135], v[44:47], v[100:103], v[132:135]\n"
"s_waitcnt lgkmcnt(4)\n"
"ds_read_b64_tr_b16 v[44:45], v5 offset:2560\n"
"ds_read_b64_tr_b16 v[46:47], v5 offset:8704\n"
"v_mfma_f32_16x16x32_bf16 v[136:139], v[36:39], v[108:111], v[136:139]\n"
"v_exp_f32 v23, v23\n"
"v_fma_f32 v25, v107, s62, v11\n"
"v_mfma_f32_16x16x32_bf16 v[140:143], v[36:39], v[124:127], v[140:143]\n"
"v_exp_f32 v24, v24\n"
"v_mfma_f32_16x16x32_bf16 v[144:147], v[36:39], v[84:87], v[144:147]\n"
"v_add_f32 v92, v22, v23\n"
"v_fma_f32 v26, v48, s62, v11\n"
"v_mfma_f32_16x16x32_bf16 v[148:151], v[36:39], v[52:55], v[148:151]\n"
"v_exp_f32 v25, v25\n"
"v_cvt_pk_bf16_f32 v64, v22, v23\n"
"v_mfma_f32_16x16x32_bf16 v[152:155], v[36:39], v[76:79], v[152:155]\n"
"v_fma_f32 v27, v49, s62, v11\n"
"v_exp_f32 v26, v26\n"
"v_mfma_f32_16x16x32_bf16 v[156:159], v[36:39], v[100:103], v[156:159]\n"
"s_waitcnt lgkmcnt(4)\n"
"ds_read_b64_tr_b16 v[36:37], v6 offset:2560\n"
"ds_read_b64_tr_b16 v[38:39], v6 offset:8704\n"
"v_mfma_f32_16x16x32_bf16 v[160:163], v[32:35], v[108:111], v[160:163]\n"
"v_add_f32 v92, v92, v24\n"
"v_fma_f32 v22, v50, s62, v11\n"
"v_mfma_f32_16x16x32_bf16 v[164:167], v[32:35], v[124:127], v[164:167]\n"
"v_exp_f32 v27, v27\n"
"v_add_f32 v92, v92, v25\n"
"v_mfma_f32_16x16x32_bf16 v[168:171], v[32:35], v[84:87], v[168:171]\n"
"v_cvt_pk_bf16_f32 v65, v24, v25\n"
"v_fma_f32 v23, v51, s62, v11\n"
"v_mfma_f32_16x16x32_bf16 v[172:175], v[32:35], v[52:55], v[172:175]\n"
"v_exp_f32 v22, v22\n"
"v_add_f32 v92, v92, v26\n"
"v_mfma_f32_16x16x32_bf16 v[176:179], v[32:35], v[76:79], v[176:179]\n"
"v_exp_f32 v23, v23\n"
"v_mfma_f32_16x16x32_bf16 v[180:183], v[32:35], v[100:103], v[180:183]\n"
"s_waitcnt lgkmcnt(4)\n"
"v_cvt_pk_bf16_f32 v66, v26, v27\n"
"v_add_f32 v92, v92, v27\n"
"v_mfma_f32_16x16x32_bf16 v[184:187], v[40:43], v[108:111], v[184:187]\n"
"v_add_f32 v92, v92, v22\n"
"v_cvt_pk_bf16_f32 v67, v22, v23\n"
"v_add_f32 v92, v92, v23\n"
"v_mfma_f32_16x16x32_bf16 v[188:191], v[40:43], v[124:127], v[188:191]\n"
"ds_read_b128 v[32:35], v1 offset:0\n"
"v_fma_f32 v22, v56, s62, v12\n"
"v_fma_f32 v23, v57, s62, v12\n"
"v_mfma_f32_16x16x32_bf16 v[192:195], v[40:43], v[84:87], v[192:195]\n"
"v_exp_f32 v22, v22\n"
"v_fma_f32 v24, v58, s62, v12\n"
"v_mfma_f32_16x16x32_bf16 v[196:199], v[40:43], v[52:55], v[196:199]\n"
"v_exp_f32 v23, v23\n"
"v_mfma_f32_16x16x32_bf16 v[200:203], v[40:43], v[76:79], v[200:203]\n"
"v_fma_f32 v25, v59, s62, v12\n"
"v_exp_f32 v24, v24\n"
"v_mfma_f32_16x16x32_bf16 v[204:207], v[40:43], v[100:103], v[204:207]\n"
"s_waitcnt lgkmcnt(3)\n"
"v_add_f32 v93, v22, v23\n"
"v_fma_f32 v26, v60, s62, v12\n"
"v_mfma_f32_16x16x32_bf16 v[208:211], v[44:47], v[108:111], v[208:211]\n"
"v_exp_f32 v25, v25\n"
"v_cvt_pk_bf16_f32 v96, v22, v23\n"
"v_mfma_f32_16x16x32_bf16 v[212:215], v[44:47], v[124:127], v[212:215]\n"
"ds_read_b128 v[40:43], v3 offset:0\n"
"v_fma_f32 v27, v61, s62, v12\n"
"v_mfma_f32_16x16x32_bf16 v[216:219], v[44:47], v[84:87], v[216:219]\n"
"v_exp_f32 v26, v26\n"
"v_add_f32 v93, v93, v24\n"
"v_mfma_f32_16x16x32_bf16 v[220:223], v[44:47], v[52:55], v[220:223]\n"
"v_fma_f32 v22, v62, s62, v12\n"
"v_exp_f32 v27, v27\n"
"v_mfma_f32_16x16x32_bf16 v[224:227], v[44:47], v[76:79], v[224:227]\n"
"v_add_f32 v93, v93, v25\n"
"v_cvt_pk_bf16_f32 v97, v24, v25\n"
"v_mfma_f32_16x16x32_bf16 v[228:231], v[44:47], v[100:103], v[228:231]\n"
"s_waitcnt lgkmcnt(2)\n"
"v_fma_f32 v23, v63, s62, v12\n"
"v_exp_f32 v22, v22\n"
"v_mfma_f32_16x16x32_bf16 v[232:235], v[36:39], v[108:111], v[232:235]\n"
"ds_read_b128 v[44:47], v3 offset:6144\n"
"v_add_f32 v93, v93, v26\n"
"v_mfma_f32_16x16x32_bf16 v[236:239], v[36:39], v[124:127], v[236:239]\n"
"v_exp_f32 v23, v23\n"
"v_cvt_pk_bf16_f32 v98, v26, v27\n"
"v_add_f32 v93, v93, v27\n"
"v_mfma_f32_16x16x32_bf16 v[240:243], v[36:39], v[84:87], v[240:243]\n"
"v_add_f32 v93, v93, v22\n"
"v_cvt_pk_bf16_f32 v99, v22, v23\n"
"v_mfma_f32_16x16x32_bf16 v[244:247], v[36:39], v[52:55], v[244:247]\n"
"v_add_f32 v93, v93, v23\n"
"v_max_f32 v27, v92, v93\n"
"v_cmp_nge_f32 vcc, s63, v27\n"
"v_mfma_f32_16x16x32_bf16 v[248:251], v[36:39], v[76:79], v[248:251]\n"
"v_fma_f32 v22, v68, s62, v13\n"
"v_fma_f32 v23, v69, s62, v13\n"
"v_mfma_f32_16x16x32_bf16 v[252:255], v[36:39], v[100:103], v[252:255]\n"
"s_waitcnt lgkmcnt(2)\n"
"s_cbranch_vccnz .Lrare_4_b\n"
".Lret_4_b:\n"
"ds_read_b128 v[36:39], v1 offset:6144\n"
"v_add_f32 v17, v17, v92\n"
"v_add_f32 v18, v18, v93\n"
"v_mfma_f32_16x16x32_bf16 v[88:91], v[32:35], a[0:3], 0\n"
"v_mfma_f32_16x16x32_bf16 v[80:83], v[32:35], a[24:27], 0\n"
"s_add_u32 m0, s56, 86016\n"
"v_exp_f32 v22, v22\n"
"v_mfma_f32_16x16x32_bf16 v[92:95], v[32:35], a[48:51], 0\n"
"s_add_u32 s61, s57, 73728\n"
"v_fma_f32 v24, v70, s62, v13\n"
"v_mfma_f32_16x16x32_bf16 v[104:107], v[32:35], a[72:75], 0\n"
"s_waitcnt lgkmcnt(2)\n"
"buffer_load_dwordx4 v8, s[48:51], s61 offen nt lds\n"
"v_exp_f32 v23, v23\n"
"v_fma_f32 v25, v71, s62, v13\n"
"v_mfma_f32_16x16x32_bf16 v[48:51], v[32:35], v[40:43], 0\n"
"s_waitcnt lgkmcnt(1)\n"
"v_exp_f32 v24, v24\n"
"v_mfma_f32_16x16x32_bf16 v[56:59], v[32:35], v[44:47], 0\n"
"s_waitcnt lgkmcnt(0)\n"
"ds_read_b128 v[32:35], v2 offset:0\n"
"v_mfma_f32_16x16x32_bf16 v[52:55], v[36:39], v[40:43], 0\n"
"ds_read_b128 v[40:43], v4 offset:0\n"
"v_mfma_f32_16x16x32_bf16 v[76:79], v[36:39], v[44:47], 0\n"
"ds_read_b128 v[44:47], v4 offset:6144\n"
"v_mfma_f32_16x16x32_bf16 v[60:63], v[36:39], a[0:3], 0\n"
"v_add_f32 v21, v22, v23\n"
"v_fma_f32 v26, v116, s62, v13\n"
"v_mfma_f32_16x16x32_bf16 v[108:111], v[36:39], a[24:27], 0\n"
"v_exp_f32 v25, v25\n"
"v_mfma_f32_16x16x32_bf16 v[124:127], v[36:39], a[48:51], 0\n"
"v_cvt_pk_bf16_f32 v100, v22, v23\n"
"v_fma_f32 v27, v117, s62, v13\n"
"v_mfma_f32_16x16x32_bf16 v[84:87], v[36:39], a[72:75], 0\n"
"s_waitcnt lgkmcnt(2)\n"
"ds_read_b128 v[36:39], v2 offset:6144\n"
"v_mfma_f32_16x16x32_bf16 v[88:91], v[32:35], a[4:7], v[88:91]\n"
"v_exp_f32 v26, v26\n"
"v_mfma_f32_16x16x32_bf16 v[80:83], v[32:35], a[28:31], v[80:83]\n"
"s_add_u32 m0, s56, 87040\n"
"v_add_f32 v21, v21, v24\n"
"v_fma_f32 v22, v118, s62, v13\n"
"v_mfma_f32_16x16x32_bf16 v[92:95], v[32:35], a[52:55], v[92:95]\n"
"s_add_u32 s61, s57, 73856\n"
"v_exp_f32 v27, v27\n"
"v_mfma_f32_16x16x32_bf16 v[104:107], v[32:35], a[76:79], v[104:107]\n"
"s_waitcnt lgkmcnt(2)\n"
"buffer_load_dwordx4 v8, s[48:51], s61 offen nt lds\n"
"v_add_f32 v21, v21, v25\n"
"v_cvt_pk_bf16_f32 v101, v24, v25\n"
"v_mfma_f32_16x16x32_bf16 v[48:51], v[32:35], v[40:43], v[48:51]\n"
"s_waitcnt lgkmcnt(1)\n"
"v_fma_f32 v23, v119, s62, v13\n"
"v_mfma_f32_16x16x32_bf16 v[56:59], v[32:35], v[44:47], v[56:59]\n"
"s_waitcnt lgkmcnt(0)\n"
"ds_read_b128 v[32:35], v1 offset:1024\n"
"v_mfma_f32_16x16x32_bf16 v[52:55], v[36:39], v[40:43], v[52:55]\n"
"ds_read_b128 v[40:43], v3 offset:1024\n"
"v_mfma_f32_16x16x32_bf16 v[76:79], v[36:39], v[44:47], v[76:79]\n"
"ds_read_b128 v[44:47], v3 offset:7168\n"
"s_add_u32 m0, s56, 88064\n"
"v_mfma_f32_16x16x32_bf16 v[60:63], v[36:39], a[4:7], v[60:63]\n"
"s_add_u32 s61, s57, 73984\n"
"v_exp_f32 v22, v22\n"
"v_add_f32 v21, v21, v26\n"
"v_mfma_f32_16x16x32_bf16 v[108:111], v[36:39], a[28:31], v[108:111]\n"
"buffer_load_dwordx4 v8, s[48:51], s61 offen nt lds\n"
"v_exp_f32 v23, v23\n"
"v_mfma_f32_16x16x32_bf16 v[124:127], v[36:39], a[52:55], v[124:127]\n"
"v_cvt_pk_bf16_f32 v102, v26, v27\n"
"v_add_f32 v21, v21, v27\n"
"v_mfma_f32_16x16x32_bf16 v[84:87], v[36:39], a[76:79], v[84:87]\n"
"s_waitcnt lgkmcnt(2)\n"
"ds_read_b128 v[36:39], v1 offset:7168\n"
"v_mfma_f32_16x16x32_bf16 v[88:91], v[32:35], a[8:11], v[88:91]\n"
"v_add_f32 v21, v21, v22\n"
"v_cvt_pk_bf16_f32 v103, v22, v23\n"
"v_mfma_f32_16x16x32_bf16 v[80:83], v[32:35], a[32:35], v[80:83]\n"
"v_add_f32 v21, v21, v23\n"
"v_cmp_nge_f32 vcc, s63, v21\n"
"v_mfma_f32_16x16x32_bf16 v[92:95], v[32:35], a[56:59], v[92:95]\n"
"v_fma_f32 v22, v72, s62, v14\n"
"v_fma_f32 v23, v73, s62, v14\n"
"v_mfma_f32_16x16x32_bf16 v[104:107], v[32:35], a[80:83], v[104:107]\n"
"s_waitcnt lgkmcnt(2)\n"
"s_cbranch_vccnz .Lrare_4_d4\n"
".Lret_4_d4:\n"
"v_add_f32 v19, v19, v21\n"
"v_mfma_f32_16x16x32_bf16 v[48:51], v[32:35], v[40:43], v[48:51]\n"
"s_waitcnt lgkmcnt(1)\n"
"v_exp_f32 v22, v22\n"
"v_mfma_f32_16x16x32_bf16 v[56:59], v[32:35], v[44:47], v[56:59]\n"
"s_waitcnt lgkmcnt(0)\n"
"ds_read_b128 v[32:35], v2 offset:1024\n"
"v_mfma_f32_16x16x32_bf16 v[52:55], v[36:39], v[40:43], v[52:55]\n"
"ds_read_b128 v[40:43], v4 offset:1024\n"
"v_mfma_f32_16x16x32_bf16 v[76:79], v[36:39], v[44:47], v[76:79]\n"
"ds_read_b128 v[44:47], v4 offset:7168\n"
"s_add_u32 m0, s56, 36864\n"
"v_mfma_f32_16x16x32_bf16 v[60:63], v[36:39], a[8:11], v[60:63]\n"
"s_add_u32 s61, s57, 98304\n"
"v_fma_f32 v24, v74, s62, v14\n"
"v_mfma_f32_16x16x32_bf16 v[108:111], v[36:39], a[32:35], v[108:111]\n"
"buffer_load_dwordx4 v7, s[44:47], s61 offen nt lds\n"
"v_exp_f32 v23, v23\n"
"v_fma_f32 v25, v75, s62, v14\n"
"v_mfma_f32_16x16x32_bf16 v[124:127], v[36:39], a[56:59], v[124:127]\n"
"v_exp_f32 v24, v24\n"
"v_mfma_f32_16x16x32_bf16 v[84:87], v[36:39], a[80:83], v[84:87]\n"
"s_waitcnt lgkmcnt(2)\n"
"ds_read_b128 v[36:39], v2 offset:7168\n"
"v_mfma_f32_16x16x32_bf16 v[88:91], v[32:35], a[12:15], v[88:91]\n"
"v_add_f32 v21, v22, v23\n"
"v_fma_f32 v26, v28, s62, v14\n"
"v_mfma_f32_16x16x32_bf16 v[80:83], v[32:35], a[36:39], v[80:83]\n"
"v_exp_f32 v25, v25\n"
"v_mfma_f32_16x16x32_bf16 v[92:95], v[32:35], a[60:63], v[92:95]\n"
"v_cvt_pk_bf16_f32 v68, v22, v23\n"
"v_fma_f32 v27, v29, s62, v14\n"
"v_mfma_f32_16x16x32_bf16 v[104:107], v[32:35], a[84:87], v[104:107]\n"
"s_waitcnt lgkmcnt(2)\n"
"v_exp_f32 v26, v26\n"
"v_mfma_f32_16x16x32_bf16 v[48:51], v[32:35], v[40:43], v[48:51]\n"
"s_waitcnt lgkmcnt(1)\n"
"v_add_f32 v21, v21, v24\n"
"v_fma_f32 v22, v30, s62, v14\n"
"v_mfma_f32_16x16x32_bf16 v[56:59], v[32:35], v[44:47], v[56:59]\n"
"s_waitcnt lgkmcnt(0)\n"
"ds_read_b128 v[32:35], v1 offset:2048\n"
"v_mfma_f32_16x16x32_bf16 v[52:55], v[36:39], v[40:43], v[52:55]\n"
"ds_read_b128 v[40:43], v3 offset:2048\n"
"v_mfma_f32_16x16x32_bf16 v[76:79], v[36:39], v[44:47], v[76:79]\n"
"ds_read_b128 v[44:47], v3 offset:8192\n"
"s_add_u32 m0, s56, 37888\n"
"v_mfma_f32_16x16x32_bf16 v[60:63], v[36:39], a[12:15], v[60:63]\n"
"s_add_u32 s61, s57, 98432\n"
"v_exp_f32 v27, v27\n"
"v_mfma_f32_16x16x32_bf16 v[108:111], v[36:39], a[36:39], v[108:111]\n"
"buffer_load_dwordx4 v7, s[44:47], s61 offen nt lds\n"
"v_add_f32 v21, v21, v25\n"
"v_cvt_pk_bf16_f32 v69, v24, v25\n"
"v_mfma_f32_16x16x32_bf16 v[124:127], v[36:39], a[60:63], v[124:127]\n"
"v_fma_f32 v23, v31, s62, v14\n"
"v_mfma_f32_16x16x32_bf16 v[84:87], v[36:39], a[84:87], v[84:87]\n"
"s_waitcnt lgkmcnt(2)\n"
"ds_read_b128 v[36:39], v1 offset:8192\n"
"v_mfma_f32_16x16x32_bf16 v[88:91], v[32:35], a[16:19], v[88:91]\n"
"v_exp_f32 v22, v22\n"
"v_add_f32 v21, v21, v26\n"
"v_mfma_f32_16x16x32_bf16 v[80:83], v[32:35], a[40:43], v[80:83]\n"
"s_add_u32 m0, s56, 38912\n"
"v_exp_f32 v23, v23\n"
"v_mfma_f32_16x16x32_bf16 v[92:95], v[32:35], a[64:67], v[92:95]\n"
"s_add_u32 s61, s57, 98560\n"
"v_cvt_pk_bf16_f32 v70, v26, v27\n"
"v_add_f32 v21, v21, v27\n"
"v_mfma_f32_16x16x32_bf16 v[104:107], v[32:35], a[88:91], v[104:107]\n"
"s_waitcnt lgkmcnt(2)\n"
"buffer_load_dwordx4 v7, s[44:47], s61 offen nt lds\n"
"v_add_f32 v21, v21, v22\n"
"v_cvt_pk_bf16_f32 v71, v22, v23\n"
"v_mfma_f32_16x16x32_bf16 v[48:51], v[32:35], v[40:43], v[48:51]\n"
"s_waitcnt lgkmcnt(1)\n"
"v_add_f32 v21, v21, v23\n"
"v_cmp_nge_f32 vcc, s63, v21\n"
"v_mfma_f32_16x16x32_bf16 v[56:59], v[32:35], v[44:47], v[56:59]\n"
"s_waitcnt lgkmcnt(0)\n"
"ds_read_b128 v[32:35], v2 offset:2048\n"
"v_mfma_f32_16x16x32_bf16 v[52:55], v[36:39], v[40:43], v[52:55]\n"
"ds_read_b128 v[40:43], v4 offset:2048\n"
"v_mfma_f32_16x16x32_bf16 v[76:79], v[36:39], v[44:47], v[76:79]\n"
"ds_read_b128 v[44:47], v4 offset:8192\n"
"s_cbranch_vccnz .Lrare_4_d5\n"
".Lret_4_d5:\n"
"v_add_f32 v20, v20, v21\n"
"v_mfma_f32_16x16x32_bf16 v[60:63], v[36:39], a[16:19], v[60:63]\n"
"v_mbcnt_lo_u32_b32 v22, -1, 0\n"
"v_mbcnt_hi_u32_b32 v22, -1, v22\n"
"v_mfma_f32_16x16x32_bf16 v[108:111], v[36:39], a[40:43], v[108:111]\n"
"v_and_b32 v22, 31, v22\n"
"v_mfma_f32_16x16x32_bf16 v[124:127], v[36:39], a[64:67], v[124:127]\n"
"v_lshlrev_b32 v22, 7, v22\n"
"v_mfma_f32_16x16x32_bf16 v[84:87], v[36:39], a[88:91], v[84:87]\n"
"s_waitcnt lgkmcnt(2)\n"
"ds_read_b128 v[36:39], v2 offset:8192\n"
"s_add_u32 s61, s60, 24576\n"
"v_mfma_f32_16x16x32_bf16 v[88:91], v[32:35], a[20:23], v[88:91]\n"
"s_mov_b32 m0, s73\n"
"v_mfma_f32_16x16x32_bf16 v[80:83], v[32:35], a[44:47], v[80:83]\n"
"buffer_load_dword v22, s[40:43], s61 offen lds\n"
"v_mfma_f32_16x16x32_bf16 v[92:95], v[32:35], a[68:71], v[92:95]\n"
"v_mfma_f32_16x16x32_bf16 v[104:107], v[32:35], a[92:95], v[104:107]\n"
"s_waitcnt lgkmcnt(2)\n"
"v_mfma_f32_16x16x32_bf16 v[48:51], v[32:35], v[40:43], v[48:51]\n"
"s_waitcnt vmcnt(15)\n"
"s_waitcnt lgkmcnt(0)\n"
"s_barrier\n"
"v_mfma_f32_16x16x32_bf16 v[56:59], v[32:35], v[44:47], v[56:59]\n"
"v_mfma_f32_16x16x32_bf16 v[52:55], v[36:39], v[40:43], v[52:55]\n"
"ds_read_b64_tr_b16 v[32:33], v5 offset:12288\n"
"ds_read_b64_tr_b16 v[34:35], v5 offset:18432\n"
"v_mfma_f32_16x16x32_bf16 v[76:79], v[36:39], v[44:47], v[76:79]\n"
"v_mfma_f32_16x16x32_bf16 v[60:63], v[36:39], a[20:23], v[60:63]\n"
"ds_read_b64_tr_b16 v[40:41], v6 offset:12288\n"
"ds_read_b64_tr_b16 v[42:43], v6 offset:18432\n"
"v_mfma_f32_16x16x32_bf16 v[108:111], v[36:39], a[44:47], v[108:111]\n"
"v_mfma_f32_16x16x32_bf16 v[124:127], v[36:39], a[68:71], v[124:127]\n"
"ds_read_b64_tr_b16 v[44:45], v5 offset:12800\n"
"ds_read_b64_tr_b16 v[46:47], v5 offset:18944\n"
"v_mfma_f32_16x16x32_bf16 v[84:87], v[36:39], a[92:95], v[84:87]\n"
"s_waitcnt lgkmcnt(4)\n"
"v_mfma_f32_16x16x32_bf16 a[96:99], v[32:35], v[112:115], a[96:99]\n"
"ds_read_b64_tr_b16 v[36:37], v6 offset:12800\n"
"ds_read_b64_tr_b16 v[38:39], v6 offset:18944\n"
"v_mfma_f32_16x16x32_bf16 a[100:103], v[32:35], v[120:123], a[100:103]\n"
"v_mfma_f32_16x16x32_bf16 a[104:107], v[32:35], v[64:67], a[104:107]\n"
"v_fma_f32 v22, v88, s62, v9\n"
"v_fma_f32 v23, v89, s62, v9\n"
"v_mfma_f32_16x16x32_bf16 a[108:111], v[32:35], v[96:99], a[108:111]\n"
"v_exp_f32 v22, v22\n"
"v_fma_f32 v24, v90, s62, v9\n"
"v_mfma_f32_16x16x32_bf16 a[112:115], v[32:35], v[100:103], a[112:115]\n"
"v_exp_f32 v23, v23\n"
"v_mfma_f32_16x16x32_bf16 a[116:119], v[32:35], v[68:71], a[116:119]\n"
"s_waitcnt lgkmcnt(4)\n"
"ds_read_b64_tr_b16 v[32:33], v5 offset:13312\n"
"ds_read_b64_tr_b16 v[34:35], v5 offset:19456\n"
"v_mfma_f32_16x16x32_bf16 a[120:123], v[40:43], v[112:115], a[120:123]\n"
"v_fma_f32 v25, v91, s62, v9\n"
"v_exp_f32 v24, v24\n"
"v_mfma_f32_16x16x32_bf16 a[124:127], v[40:43], v[120:123], a[124:127]\n"
"v_add_f32 v21, v22, v23\n"
"v_fma_f32 v26, v60, s62, v9\n"
"v_mfma_f32_16x16x32_bf16 a[128:131], v[40:43], v[64:67], a[128:131]\n"
"v_exp_f32 v25, v25\n"
"v_cvt_pk_bf16_f32 v116, v22, v23\n"
"v_mfma_f32_16x16x32_bf16 a[132:135], v[40:43], v[96:99], a[132:135]\n"
"v_fma_f32 v27, v61, s62, v9\n"
"v_mfma_f32_16x16x32_bf16 a[136:139], v[40:43], v[100:103], a[136:139]\n"
"v_exp_f32 v26, v26\n"
"v_add_f32 v21, v21, v24\n"
"v_fma_f32 v22, v62, s62, v9\n"
"v_mfma_f32_16x16x32_bf16 a[140:143], v[40:43], v[68:71], a[140:143]\n"
"s_waitcnt lgkmcnt(4)\n"
"ds_read_b64_tr_b16 v[40:41], v6 offset:13312\n"
"ds_read_b64_tr_b16 v[42:43], v6 offset:19456\n"
"v_mfma_f32_16x16x32_bf16 a[144:147], v[44:47], v[112:115], a[144:147]\n"
"v_exp_f32 v27, v27\n"
"v_mfma_f32_16x16x32_bf16 a[148:151], v[44:47], v[120:123], a[148:151]\n"
"v_add_f32 v21, v21, v25\n"
"v_cvt_pk_bf16_f32 v117, v24, v25\n"
"v_mfma_f32_16x16x32_bf16 a[152:155], v[44:47], v[64:67], a[152:155]\n"
"v_fma_f32 v23, v63, s62, v9\n"
"v_exp_f32 v22, v22\n"
"v_mfma_f32_16x16x32_bf16 a[156:159], v[44:47], v[96:99], a[156:159]\n"
"v_add_f32 v21, v21, v26\n"
"v_mfma_f32_16x16x32_bf16 a[160:163], v[44:47], v[100:103], a[160:163]\n"
"v_exp_f32 v23, v23\n"
"v_cvt_pk_bf16_f32 v118, v26, v27\n"
"v_add_f32 v21, v21, v27\n"
"v_mfma_f32_16x16x32_bf16 a[164:167], v[44:47], v[68:71], a[164:167]\n"
"s_waitcnt lgkmcnt(4)\n"
"ds_read_b64_tr_b16 v[44:45], v5 offset:13824\n"
"ds_read_b64_tr_b16 v[46:47], v5 offset:19968\n"
"v_mfma_f32_16x16x32_bf16 a[168:171], v[36:39], v[112:115], a[168:171]\n"
"v_add_f32 v21, v21, v22\n"
"v_cvt_pk_bf16_f32 v119, v22, v23\n"
"v_mfma_f32_16x16x32_bf16 a[172:175], v[36:39], v[120:123], a[172:175]\n"
"v_add_f32 v21, v21, v23\n"
"v_fma_f32 v22, v80, s62, v10\n"
"v_fma_f32 v23, v81, s62, v10\n"
"v_mfma_f32_16x16x32_bf16 a[176:179], v[36:39], v[64:67], a[176:179]\n"
"v_exp_f32 v22, v22\n"
"v_mfma_f32_16x16x32_bf16 a[180:183], v[36:39], v[96:99], a[180:183]\n"
"v_fma_f32 v24, v82, s62, v10\n"
"v_exp_f32 v23, v23\n"
"v_mfma_f32_16x16x32_bf16 a[184:187], v[36:39], v[100:103], a[184:187]\n"
"v_fma_f32 v25, v83, s62, v10\n"
"v_mfma_f32_16x16x32_bf16 a[188:191], v[36:39], v[68:71], a[188:191]\n"
"s_waitcnt lgkmcnt(4)\n"
"ds_read_b64_tr_b16 v[36:37], v6 offset:13824\n"
"ds_read_b64_tr_b16 v[38:39], v6 offset:19968\n"
"v_mfma_f32_16x16x32_bf16 a[192:195], v[32:35], v[112:115], a[192:195]\n"
"v_exp_f32 v24, v24\n"
"v_add_f32 v3, v22, v23\n"
"v_mfma_f32_16x16x32_bf16 a[196:199], v[32:35], v[120:123], a[196:199]\n"
"v_fma_f32 v26, v108, s62, v10\n"
"v_exp_f32 v25, v25\n"
"v_mfma_f32_16x16x32_bf16 a[200:203], v[32:35], v[64:67], a[200:203]\n"
"v_cvt_pk_bf16_f32 v72, v22, v23\n"
"v_fma_f32 v27, v109, s62, v10\n"
"v_mfma_f32_16x16x32_bf16 a[204:207], v[32:35], v[96:99], a[204:207]\n"
"v_exp_f32 v26, v26\n"
"v_add_f32 v3, v3, v24\n"
"v_mfma_f32_16x16x32_bf16 a[208:211], v[32:35], v[100:103], a[208:211]\n"
"v_fma_f32 v22, v110, s62, v10\n"
"v_mfma_f32_16x16x32_bf16 a[212:215], v[32:35], v[68:71], a[212:215]\n"
"s_waitcnt lgkmcnt(4)\n"
"ds_read_b64_tr_b16 v[32:33], v5 offset:14336\n"
"ds_read_b64_tr_b16 v[34:35], v5 offset:20480\n"
"v_mfma_f32_16x16x32_bf16 a[216:219], v[40:43], v[112:115], a[216:219]\n"
"v_exp_f32 v27, v27\n"
"v_add_f32 v3, v3, v25\n"
"v_cvt_pk_bf16_f32 v73, v24, v25\n"
"v_mfma_f32_16x16x32_bf16 a[220:223], v[40:43], v[120:123], a[220:223]\n"
"v_fma_f32 v23, v111, s62, v10\n"
"v_mfma_f32_16x16x32_bf16 a[224:227], v[40:43], v[64:67], a[224:227]\n"
"v_exp_f32 v22, v22\n"
"v_add_f32 v3, v3, v26\n"
"v_mfma_f32_16x16x32_bf16 a[228:231], v[40:43], v[96:99], a[228:231]\n"
"v_exp_f32 v23, v23\n"
"v_cvt_pk_bf16_f32 v74, v26, v27\n"
"v_mfma_f32_16x16x32_bf16 a[232:235], v[40:43], v[100:103], a[232:235]\n"
"v_add_f32 v3, v3, v27\n"
"v_add_f32 v3, v3, v22\n"
"v_mfma_f32_16x16x32_bf16 a[236:239], v[40:43], v[68:71], a[236:239]\n"
"s_waitcnt lgkmcnt(4)\n"
"ds_read_b64_tr_b16 v[40:41], v6 offset:14336\n"
"ds_read_b64_tr_b16 v[42:43], v6 offset:20480\n"
"v_mfma_f32_16x16x32_bf16 a[240:243], v[44:47], v[112:115], a[240:243]\n"
"v_cvt_pk_bf16_f32 v75, v22, v23\n"
"v_add_f32 v3, v3, v23\n"
"v_max_f32 v27, v21, v3\n"
"v_mfma_f32_16x16x32_bf16 a[244:247], v[44:47], v[120:123], a[244:247]\n"
"v_cmp_nge_f32 vcc, s63, v27\n"
"v_fma_f32 v22, v92, s62, v11\n"
"v_mfma_f32_16x16x32_bf16 a[248:251], v[44:47], v[64:67], a[248:251]\n"
"v_fma_f32 v23, v93, s62, v11\n"
"v_mfma_f32_16x16x32_bf16 a[252:255], v[44:47], v[96:99], a[252:255]\n"
"s_cbranch_vccnz .Lrare_5_a\n"
".Lret_5_a:\n"
"v_add_f32 v15, v15, v21\n"
"v_add_f32 v16, v16, v3\n"
"v_add_u32 v3, s68, v1\n"
"v_mfma_f32_16x16x32_bf16 v[128:131], v[44:47], v[100:103], v[128:131]\n"
"v_exp_f32 v22, v22\n"
"v_fma_f32 v24, v94, s62, v11\n"
"v_mfma_f32_16x16x32_bf16 v[132:135], v[44:47], v[68:71], v[132:135]\n"
"s_waitcnt lgkmcnt(4)\n"
"ds_read_b64_tr_b16 v[44:45], v5 offset:14848\n"
"ds_read_b64_tr_b16 v[46:47], v5 offset:20992\n"
"v_mfma_f32_16x16x32_bf16 v[136:139], v[36:39], v[112:115], v[136:139]\n"
"v_exp_f32 v23, v23\n"
"v_fma_f32 v25, v95, s62, v11\n"
"v_mfma_f32_16x16x32_bf16 v[140:143], v[36:39], v[120:123], v[140:143]\n"
"v_exp_f32 v24, v24\n"
"v_mfma_f32_16x16x32_bf16 v[144:147], v[36:39], v[64:67], v[144:147]\n"
"v_add_f32 v60, v22, v23\n"
"v_fma_f32 v26, v124, s62, v11\n"
"v_mfma_f32_16x16x32_bf16 v[148:151], v[36:39], v[96:99], v[148:151]\n"
"v_exp_f32 v25, v25\n"
"v_cvt_pk_bf16_f32 v28, v22, v23\n"
"v_mfma_f32_16x16x32_bf16 v[152:155], v[36:39], v[100:103], v[152:155]\n"
"v_fma_f32 v27, v125, s62, v11\n"
"v_exp_f32 v26, v26\n"
"v_mfma_f32_16x16x32_bf16 v[156:159], v[36:39], v[68:71], v[156:159]\n"
"s_waitcnt lgkmcnt(4)\n"
"ds_read_b64_tr_b16 v[36:37], v6 offset:14848\n"
"ds_read_b64_tr_b16 v[38:39], v6 offset:20992\n"
"v_mfma_f32_16x16x32_bf16 v[160:163], v[32:35], v[112:115], v[160:163]\n"
"v_add_f32 v60, v60, v24\n"
"v_fma_f32 v22, v126, s62, v11\n"
"v_mfma_f32_16x16x32_bf16 v[164:167], v[32:35], v[120:123], v[164:167]\n"
"v_exp_f32 v27, v27\n"
"v_add_f32 v60, v60, v25\n"
"v_mfma_f32_16x16x32_bf16 v[168:171], v[32:35], v[64:67], v[168:171]\n"
"v_cvt_pk_bf16_f32 v29, v24, v25\n"
"v_fma_f32 v23, v127, s62, v11\n"
"v_mfma_f32_16x16x32_bf16 v[172:175], v[32:35], v[96:99], v[172:175]\n"
"v_exp_f32 v22, v22\n"
"v_add_f32 v60, v60, v26\n"
"v_mfma_f32_16x16x32_bf16 v[176:179], v[32:35], v[100:103], v[176:179]\n"
"v_exp_f32 v23, v23\n"
"v_mfma_f32_16x16x32_bf16 v[180:183], v[32:35], v[68:71], v[180:183]\n"
"s_waitcnt lgkmcnt(4)\n"
"v_cvt_pk_bf16_f32 v30, v26, v27\n"
"v_add_f32 v60, v60, v27\n"
"v_mfma_f32_16x16x32_bf16 v[184:187], v[40:43], v[112:115], v[184:187]\n"
"v_add_f32 v60, v60, v22\n"
"v_cvt_pk_bf16_f32 v31, v22, v23\n"
"v_add_f32 v60, v60, v23\n"
"v_mfma_f32_16x16x32_bf16 v[188:191], v[40:43], v[120:123], v[188:191]\n"
"ds_read_b128 v[32:35], v1 offset:12288\n"
"v_fma_f32 v22, v104, s62, v12\n"
"v_fma_f32 v23, v105, s62, v12\n"
"v_mfma_f32_16x16x32_bf16 v[192:195], v[40:43], v[64:67], v[192:195]\n"
"v_exp_f32 v22, v22\n"
"v_fma_f32 v24, v106, s62, v12\n"
"v_mfma_f32_16x16x32_bf16 v[196:199], v[40:43], v[96:99], v[196:199]\n"
"v_exp_f32 v23, v23\n"
"v_mfma_f32_16x16x32_bf16 v[200:203], v[40:43], v[100:103], v[200:203]\n"
"v_fma_f32 v25, v107, s62, v12\n"
"v_exp_f32 v24, v24\n"
"v_mfma_f32_16x16x32_bf16 v[204:207], v[40:43], v[68:71], v[204:207]\n"
"s_waitcnt lgkmcnt(3)\n"
"v_add_f32 v61, v22, v23\n"
"v_fma_f32 v26, v84, s62, v12\n"
"v_mfma_f32_16x16x32_bf16 v[208:211], v[44:47], v[112:115], v[208:211]\n"
"v_exp_f32 v25, v25\n"
"v_cvt_pk_bf16_f32 v88, v22, v23\n"
"v_mfma_f32_16x16x32_bf16 v[212:215], v[44:47], v[120:123], v[212:215]\n"
"ds_read_b128 v[40:43], v3 offset:0\n"
"v_fma_f32 v27, v85, s62, v12\n"
"v_mfma_f32_16x16x32_bf16 v[216:219], v[44:47], v[64:67], v[216:219]\n"
"v_exp_f32 v26, v26\n"
"v_add_f32 v61, v61, v24\n"
"v_mfma_f32_16x16x32_bf16 v[220:223], v[44:47], v[96:99], v[220:223]\n"
"v_fma_f32 v22, v86, s62, v12\n"
"v_exp_f32 v27, v27\n"
"v_mfma_f32_16x16x32_bf16 v[224:227], v[44:47], v[100:103], v[224:227]\n"
"v_add_f32 v61, v61, v25\n"
"v_cvt_pk_bf16_f32 v89, v24, v25\n"
"v_mfma_f32_16x16x32_bf16 v[228:231], v[44:47], v[68:71], v[228:231]\n"
"s_waitcnt lgkmcnt(2)\n"
"v_fma_f32 v23, v87, s62, v12\n"
"v_exp_f32 v22, v22\n"
"v_mfma_f32_16x16x32_bf16 v[232:235], v[36:39], v[112:115], v[232:235]\n"
"ds_read_b128 v[44:47], v3 offset:6144\n"
"v_add_f32 v61, v61, v26\n"
"v_mfma_f32_16x16x32_bf16 v[236:239], v[36:39], v[120:123], v[236:239]\n"
"v_exp_f32 v23, v23\n"
"v_cvt_pk_bf16_f32 v90, v26, v27\n"
"v_add_f32 v61, v61, v27\n"
"v_mfma_f32_16x16x32_bf16 v[240:243], v[36:39], v[64:67], v[240:243]\n"
"v_add_f32 v61, v61, v22\n"
"v_cvt_pk_bf16_f32 v91, v22, v23\n"
"v_mfma_f32_16x16x32_bf16 v[244:247], v[36:39], v[96:99], v[244:247]\n"
"v_add_f32 v61, v61, v23\n"
"v_max_f32 v27, v60, v61\n"
"v_cmp_nge_f32 vcc, s63, v27\n"
"v_mfma_f32_16x16x32_bf16 v[248:251], v[36:39], v[100:103], v[248:251]\n"
"v_fma_f32 v22, v48, s62, v13\n"
"v_fma_f32 v23, v49, s62, v13\n"
"v_mfma_f32_16x16x32_bf16 v[252:255], v[36:39], v[68:71], v[252:255]\n"
"s_waitcnt lgkmcnt(2)\n"
"s_cbranch_vccnz .Lrare_5_b\n"
".Lret_5_b:\n"
"ds_read_b128 v[36:39], v1 offset:18432\n"
"v_add_f32 v17, v17, v60\n"
"v_add_f32 v18, v18, v61\n"
"v_mfma_f32_16x16x32_bf16 v[80:83], v[32:35], a[0:3], 0\n"
"v_mfma_f32_16x16x32_bf16 v[108:111], v[32:35], a[24:27], 0\n"
"s_add_u32 m0, s56, 49152\n"
"v_exp_f32 v22, v22\n"
"v_mfma_f32_16x16x32_bf16 v[60:63], v[32:35], a[48:51], 0\n"
"s_add_u32 s61, s57, 86016\n"
"v_fma_f32 v24, v50, s62, v13\n"
"v_mfma_f32_16x16x32_bf16 v[92:95], v[32:35], a[72:75], 0\n"
"s_waitcnt lgkmcnt(2)\n"
"buffer_load_dwordx4 v8, s[48:51], s61 offen nt lds\n"
"v_exp_f32 v23, v23\n"
"v_fma_f32 v25, v51, s62, v13\n"
"v_mfma_f32_16x16x32_bf16 v[124:127], v[32:35], v[40:43], 0\n"
"s_waitcnt lgkmcnt(1)\n"
"v_exp_f32 v24, v24\n"
"v_mfma_f32_16x16x32_bf16 v[104:107], v[32:35], v[44:47], 0\n"
"s_waitcnt lgkmcnt(0)\n"
"ds_read_b128 v[32:35], v2 offset:12288\n"
"v_mfma_f32_16x16x32_bf16 v[96:99], v[36:39], v[40:43], 0\n"
"ds_read_b128 v[40:43], v4 offset:0\n"
"v_mfma_f32_16x16x32_bf16 v[100:103], v[36:39], v[44:47], 0\n"
"ds_read_b128 v[44:47], v4 offset:6144\n"
"v_mfma_f32_16x16x32_bf16 v[84:87], v[36:39], a[0:3], 0\n"
"v_add_f32 v21, v22, v23\n"
"v_fma_f32 v26, v52, s62, v13\n"
"v_mfma_f32_16x16x32_bf16 v[112:115], v[36:39], a[24:27], 0\n"
"v_exp_f32 v25, v25\n"
"v_mfma_f32_16x16x32_bf16 v[120:123], v[36:39], a[48:51], 0\n"
"v_cvt_pk_bf16_f32 v68, v22, v23\n"
"v_fma_f32 v27, v53, s62, v13\n"
"v_mfma_f32_16x16x32_bf16 v[64:67], v[36:39], a[72:75], 0\n"
"s_waitcnt lgkmcnt(2)\n"
"ds_read_b128 v[36:39], v2 offset:18432\n"
"v_mfma_f32_16x16x32_bf16 v[80:83], v[32:35], a[4:7], v[80:83]\n"
"v_exp_f32 v26, v26\n"
"v_mfma_f32_16x16x32_bf16 v[108:111], v[32:35], a[28:31], v[108:111]\n"
"s_add_u32 m0, s56, 50176\n"
"v_add_f32 v21, v21, v24\n"
"v_fma_f32 v22, v54, s62, v13\n"
"v_mfma_f32_16x16x32_bf16 v[60:63], v[32:35], a[52:55], v[60:63]\n"
"s_add_u32 s61, s57, 86144\n"
"v_exp_f32 v27, v27\n"
"v_mfma_f32_16x16x32_bf16 v[92:95], v[32:35], a[76:79], v[92:95]\n"
"s_waitcnt lgkmcnt(2)\n"
"buffer_load_dwordx4 v8, s[48:51], s61 offen nt lds\n"
"v_add_f32 v21, v21, v25\n"
"v_cvt_pk_bf16_f32 v69, v24, v25\n"
"v_mfma_f32_16x16x32_bf16 v[124:127], v[32:35], v[40:43], v[124:127]\n"
"s_waitcnt lgkmcnt(1)\n"
"v_fma_f32 v23, v55, s62, v13\n"
"v_mfma_f32_16x16x32_bf16 v[104:107], v[32:35], v[44:47], v[104:107]\n"
"s_waitcnt lgkmcnt(0)\n"
"ds_read_b128 v[32:35], v1 offset:13312\n"
"v_mfma_f32_16x16x32_bf16 v[96:99], v[36:39], v[40:43], v[96:99]\n"
"ds_read_b128 v[40:43], v3 offset:1024\n"
"v_mfma_f32_16x16x32_bf16 v[100:103], v[36:39], v[44:47], v[100:103]\n"
"ds_read_b128 v[44:47], v3 offset:7168\n"
"s_add_u32 m0, s56, 51200\n"
"v_mfma_f32_16x16x32_bf16 v[84:87], v[36:39], a[4:7], v[84:87]\n"
"s_add_u32 s61, s57, 86272\n"
"v_exp_f32 v22, v22\n"
"v_add_f32 v21, v21, v26\n"
"v_mfma_f32_16x16x32_bf16 v[112:115], v[36:39], a[28:31], v[112:115]\n"
"buffer_load_dwordx4 v8, s[48:51], s61 offen nt lds\n"
"v_exp_f32 v23, v23\n"
"v_mfma_f32_16x16x32_bf16 v[120:123], v[36:39], a[52:55], v[120:123]\n"
"v_cvt_pk_bf16_f32 v70, v26, v27\n"
"v_add_f32 v21, v21, v27\n"
"v_mfma_f32_16x16x32_bf16 v[64:67], v[36:39], a[76:79], v[64:67]\n"
"s_waitcnt lgkmcnt(2)\n"
"ds_read_b128 v[36:39], v1 offset:19456\n"
"v_mfma_f32_16x16x32_bf16 v[80:83], v[32:35], a[8:11], v[80:83]\n"
"v_add_f32 v21, v21, v22\n"
"v_cvt_pk_bf16_f32 v71, v22, v23\n"
"v_mfma_f32_16x16x32_bf16 v[108:111], v[32:35], a[32:35], v[108:111]\n"
"v_add_f32 v21, v21, v23\n"
"v_cmp_nge_f32 vcc, s63, v21\n"
"v_mfma_f32_16x16x32_bf16 v[60:63], v[32:35], a[56:59], v[60:63]\n"
"v_fma_f32 v22, v56, s62, v14\n"
"v_fma_f32 v23, v57, s62, v14\n"
"v_mfma_f32_16x16x32_bf16 v[92:95], v[32:35], a[80:83], v[92:95]\n"
"s_waitcnt lgkmcnt(2)\n"
"s_cbranch_vccnz .Lrare_5_d4\n"
".Lret_5_d4:\n"
"v_add_f32 v19, v19, v21\n"
"v_mfma_f32_16x16x32_bf16 v[124:127], v[32:35], v[40:43], v[124:127]\n"
"s_waitcnt lgkmcnt(1)\n"
"v_exp_f32 v22, v22\n"
"v_mfma_f32_16x16x32_bf16 v[104:107], v[32:35], v[44:47], v[104:107]\n"
"s_waitcnt lgkmcnt(0)\n"
"ds_read_b128 v[32:35], v2 offset:13312\n"
"v_mfma_f32_16x16x32_bf16 v[96:99], v[36:39], v[40:43], v[96:99]\n"
"ds_read_b128 v[40:43], v4 offset:1024\n"
"v_mfma_f32_16x16x32_bf16 v[100:103], v[36:39], v[44:47], v[100:103]\n"
"ds_read_b128 v[44:47], v4 offset:7168\n"
"s_add_u32 m0, s56, 0\n"
"v_mfma_f32_16x16x32_bf16 v[84:87], v[36:39], a[8:11], v[84:87]\n"
"s_add_u32 s61, s57, 110592\n"
"v_fma_f32 v24, v58, s62, v14\n"
"v_mfma_f32_16x16x32_bf16 v[112:115], v[36:39], a[32:35], v[112:115]\n"
"buffer_load_dwordx4 v7, s[44:47], s61 offen nt lds\n"
"v_exp_f32 v23, v23\n"
"v_fma_f32 v25, v59, s62, v14\n"
"v_mfma_f32_16x16x32_bf16 v[120:123], v[36:39], a[56:59], v[120:123]\n"
"v_exp_f32 v24, v24\n"
"v_mfma_f32_16x16x32_bf16 v[64:67], v[36:39], a[80:83], v[64:67]\n"
"s_waitcnt lgkmcnt(2)\n"
"ds_read_b128 v[36:39], v2 offset:19456\n"
"v_mfma_f32_16x16x32_bf16 v[80:83], v[32:35], a[12:15], v[80:83]\n"
"v_add_f32 v21, v22, v23\n"
"v_fma_f32 v26, v76, s62, v14\n"
"v_mfma_f32_16x16x32_bf16 v[108:111], v[32:35], a[36:39], v[108:111]\n"
"v_exp_f32 v25, v25\n"
"v_mfma_f32_16x16x32_bf16 v[60:63], v[32:35], a[60:63], v[60:63]\n"
"v_cvt_pk_bf16_f32 v48, v22, v23\n"
"v_fma_f32 v27, v77, s62, v14\n"
"v_mfma_f32_16x16x32_bf16 v[92:95], v[32:35], a[84:87], v[92:95]\n"
"s_waitcnt lgkmcnt(2)\n"
"v_exp_f32 v26, v26\n"
"v_mfma_f32_16x16x32_bf16 v[124:127], v[32:35], v[40:43], v[124:127]\n"
"s_waitcnt lgkmcnt(1)\n"
"v_add_f32 v21, v21, v24\n"
"v_fma_f32 v22, v78, s62, v14\n"
"v_mfma_f32_16x16x32_bf16 v[104:107], v[32:35], v[44:47], v[104:107]\n"
"s_waitcnt lgkmcnt(0)\n"
"ds_read_b128 v[32:35], v1 offset:14336\n"
"v_mfma_f32_16x16x32_bf16 v[96:99], v[36:39], v[40:43], v[96:99]\n"
"ds_read_b128 v[40:43], v3 offset:2048\n"
"v_mfma_f32_16x16x32_bf16 v[100:103], v[36:39], v[44:47], v[100:103]\n"
"ds_read_b128 v[44:47], v3 offset:8192\n"
"s_add_u32 m0, s56, 1024\n"
"v_mfma_f32_16x16x32_bf16 v[84:87], v[36:39], a[12:15], v[84:87]\n"
"s_add_u32 s61, s57, 110720\n"
"v_exp_f32 v27, v27\n"
"v_mfma_f32_16x16x32_bf16 v[112:115], v[36:39], a[36:39], v[112:115]\n"
"buffer_load_dwordx4 v7, s[44:47], s61 offen nt lds\n"
"v_add_f32 v21, v21, v25\n"
"v_cvt_pk_bf16_f32 v49, v24, v25\n"
"v_mfma_f32_16x16x32_bf16 v[120:123], v[36:39], a[60:63], v[120:123]\n"
"v_fma_f32 v23, v79, s62, v14\n"
"v_mfma_f32_16x16x32_bf16 v[64:67], v[36:39], a[84:87], v[64:67]\n"
"s_waitcnt lgkmcnt(2)\n"
"ds_read_b128 v[36:39], v1 offset:20480\n"
"v_mfma_f32_16x16x32_bf16 v[80:83], v[32:35], a[16:19], v[80:83]\n"
"v_exp_f32 v22, v22\n"
"v_add_f32 v21, v21, v26\n"
"v_mfma_f32_16x16x32_bf16 v[108:111], v[32:35], a[40:43], v[108:111]\n"
"s_add_u32 m0, s56, 2048\n"
"v_exp_f32 v23, v23\n"
"v_mfma_f32_16x16x32_bf16 v[60:63], v[32:35], a[64:67], v[60:63]\n"
"s_add_u32 s61, s57, 110848\n"
"v_cvt_pk_bf16_f32 v50, v26, v27\n"
"v_add_f32 v21, v21, v27\n"
"v_mfma_f32_16x16x32_bf16 v[92:95], v[32:35], a[88:91], v[92:95]\n"
"s_waitcnt lgkmcnt(2)\n"
"buffer_load_dwordx4 v7, s[44:47], s61 offen nt lds\n"
"v_add_f32 v21, v21, v22\n"
"v_cvt_pk_bf16_f32 v51, v22, v23\n"
"v_mfma_f32_16x16x32_bf16 v[124:127], v[32:35], v[40:43], v[124:127]\n"
"s_waitcnt lgkmcnt(1)\n"
"v_add_f32 v21, v21, v23\n"
"v_cmp_nge_f32 vcc, s63, v21\n"
"v_mfma_f32_16x16x32_bf16 v[104:107], v[32:35], v[44:47], v[104:107]\n"
"s_waitcnt lgkmcnt(0)\n"
"ds_read_b128 v[32:35], v2 offset:14336\n"
"v_mfma_f32_16x16x32_bf16 v[96:99], v[36:39], v[40:43], v[96:99]\n"
"ds_read_b128 v[40:43], v4 offset:2048\n"
"v_mfma_f32_16x16x32_bf16 v[100:103], v[36:39], v[44:47], v[100:103]\n"
"ds_read_b128 v[44:47], v4 offset:8192\n"
"s_cbranch_vccnz .Lrare_5_d5\n"
".Lret_5_d5:\n"
"v_add_f32 v20, v20, v21\n"
"v_mfma_f32_16x16x32_bf16 v[84:87], v[36:39], a[16:19], v[84:87]\n"
"v_mbcnt_lo_u32_b32 v22, -1, 0\n"
"v_mbcnt_hi_u32_b32 v22, -1, v22\n"
"v_mfma_f32_16x16x32_bf16 v[112:115], v[36:39], a[40:43], v[112:115]\n"
"v_and_b32 v22, 31, v22\n"
"v_mfma_f32_16x16x32_bf16 v[120:123], v[36:39], a[64:67], v[120:123]\n"
"v_lshlrev_b32 v22, 7, v22\n"
"v_mfma_f32_16x16x32_bf16 v[64:67], v[36:39], a[88:91], v[64:67]\n"
"s_waitcnt lgkmcnt(2)\n"
"ds_read_b128 v[36:39], v2 offset:20480\n"
"s_add_u32 s61, s60, 28672\n"
"v_mfma_f32_16x16x32_bf16 v[80:83], v[32:35], a[20:23], v[80:83]\n"
"s_mov_b32 m0, s73\n"
"v_mfma_f32_16x16x32_bf16 v[108:111], v[32:35], a[44:47], v[108:111]\n"
"buffer_load_dword v22, s[40:43], s61 offen lds\n"
"v_mfma_f32_16x16x32_bf16 v[60:63], v[32:35], a[68:71], v[60:63]\n"
"v_mfma_f32_16x16x32_bf16 v[92:95], v[32:35], a[92:95], v[92:95]\n"
"s_waitcnt lgkmcnt(2)\n"
"v_mfma_f32_16x16x32_bf16 v[124:127], v[32:35], v[40:43], v[124:127]\n"
"s_waitcnt vmcnt(15)\n"
"s_waitcnt lgkmcnt(0)\n"
"s_barrier\n"
"v_mfma_f32_16x16x32_bf16 v[104:107], v[32:35], v[44:47], v[104:107]\n"
"v_mfma_f32_16x16x32_bf16 v[96:99], v[36:39], v[40:43], v[96:99]\n"
"ds_read_b64_tr_b16 v[32:33], v5 offset:24576\n"
"ds_read_b64_tr_b16 v[34:35], v5 offset:30720\n"
"v_mfma_f32_16x16x32_bf16 v[100:103], v[36:39], v[44:47], v[100:103]\n"
"v_mfma_f32_16x16x32_bf16 v[84:87], v[36:39], a[20:23], v[84:87]\n"
"ds_read_b64_tr_b16 v[40:41], v6 offset:24576\n"
"ds_read_b64_tr_b16 v[42:43], v6 offset:30720\n"
"v_mfma_f32_16x16x32_bf16 v[112:115], v[36:39], a[44:47], v[112:115]\n"
"v_mfma_f32_16x16x32_bf16 v[120:123], v[36:39], a[68:71], v[120:123]\n"
"ds_read_b64_tr_b16 v[44:45], v5 offset:25088\n"
"ds_read_b64_tr_b16 v[46:47], v5 offset:31232\n"
"v_mfma_f32_16x16x32_bf16 v[64:67], v[36:39], a[92:95], v[64:67]\n"
"s_waitcnt lgkmcnt(4)\n"
"v_mfma_f32_16x16x32_bf16 a[96:99], v[32:35], v[116:119], a[96:99]\n"
"ds_read_b64_tr_b16 v[36:37], v6 offset:25088\n"
"ds_read_b64_tr_b16 v[38:39], v6 offset:31232\n"
"v_mfma_f32_16x16x32_bf16 a[100:103], v[32:35], v[72:75], a[100:103]\n"
"v_mfma_f32_16x16x32_bf16 a[104:107], v[32:35], v[28:31], a[104:107]\n"
"v_fma_f32 v22, v80, s62, v9\n"
"v_fma_f32 v23, v81, s62, v9\n"
"v_mfma_f32_16x16x32_bf16 a[108:111], v[32:35], v[88:91], a[108:111]\n"
"v_exp_f32 v22, v22\n"
"v_fma_f32 v24, v82, s62, v9\n"
"v_mfma_f32_16x16x32_bf16 a[112:115], v[32:35], v[68:71], a[112:115]\n"
"v_exp_f32 v23, v23\n"
"v_mfma_f32_16x16x32_bf16 a[116:119], v[32:35], v[48:51], a[116:119]\n"
"s_waitcnt lgkmcnt(4)\n"
"ds_read_b64_tr_b16 v[32:33], v5 offset:25600\n"
"ds_read_b64_tr_b16 v[34:35], v5 offset:31744\n"
"v_mfma_f32_16x16x32_bf16 a[120:123], v[40:43], v[116:119], a[120:123]\n"
"v_fma_f32 v25, v83, s62, v9\n"
"v_exp_f32 v24, v24\n"
"v_mfma_f32_16x16x32_bf16 a[124:127], v[40:43], v[72:75], a[124:127]\n"
"v_add_f32 v21, v22, v23\n"
"v_fma_f32 v26, v84, s62, v9\n"
"v_mfma_f32_16x16x32_bf16 a[128:131], v[40:43], v[28:31], a[128:131]\n"
"v_exp_f32 v25, v25\n"
"v_cvt_pk_bf16_f32 v52, v22, v23\n"
"v_mfma_f32_16x16x32_bf16 a[132:135], v[40:43], v[88:91], a[132:135]\n"
"v_fma_f32 v27, v85, s62, v9\n"
"v_mfma_f32_16x16x32_bf16 a[136:139], v[40:43], v[68:71], a[136:139]\n"
"v_exp_f32 v26, v26\n"
"v_add_f32 v21, v21, v24\n"
"v_fma_f32 v22, v86, s62, v9\n"
"v_mfma_f32_16x16x32_bf16 a[140:143], v[40:43], v[48:51], a[140:143]\n"
"s_waitcnt lgkmcnt(4)\n"
"ds_read_b64_tr_b16 v[40:41], v6 offset:25600\n"
"ds_read_b64_tr_b16 v[42:43], v6 offset:31744\n"
"v_mfma_f32_16x16x32_bf16 a[144:147], v[44:47], v[116:119], a[144:147]\n"
"v_exp_f32 v27, v27\n"
"v_mfma_f32_16x16x32_bf16 a[148:151], v[44:47], v[72:75], a[148:151]\n"
"v_add_f32 v21, v21, v25\n"
"v_cvt_pk_bf16_f32 v53, v24, v25\n"
"v_mfma_f32_16x16x32_bf16 a[152:155], v[44:47], v[28:31], a[152:155]\n"
"v_fma_f32 v23, v87, s62, v9\n"
"v_exp_f32 v22, v22\n"
"v_mfma_f32_16x16x32_bf16 a[156:159], v[44:47], v[88:91], a[156:159]\n"
"v_add_f32 v21, v21, v26\n"
"v_mfma_f32_16x16x32_bf16 a[160:163], v[44:47], v[68:71], a[160:163]\n"
"v_exp_f32 v23, v23\n"
"v_cvt_pk_bf16_f32 v54, v26, v27\n"
"v_add_f32 v21, v21, v27\n"
"v_mfma_f32_16x16x32_bf16 a[164:167], v[44:47], v[48:51], a[164:167]\n"
"s_waitcnt lgkmcnt(4)\n"
"ds_read_b64_tr_b16 v[44:45], v5 offset:26112\n"
"ds_read_b64_tr_b16 v[46:47], v5 offset:32256\n"
"v_mfma_f32_16x16x32_bf16 a[168:171], v[36:39], v[116:119], a[168:171]\n"
"v_add_f32 v21, v21, v22\n"
"v_cvt_pk_bf16_f32 v55, v22, v23\n"
"v_mfma_f32_16x16x32_bf16 a[172:175], v[36:39], v[72:75], a[172:175]\n"
"v_add_f32 v21, v21, v23\n"
"v_fma_f32 v22, v108, s62, v10\n"
"v_fma_f32 v23, v109, s62, v10\n"
"v_mfma_f32_16x16x32_bf16 a[176:179], v[36:39], v[28:31], a[176:179]\n"
"v_exp_f32 v22, v22\n"
"v_mfma_f32_16x16x32_bf16 a[180:183], v[36:39], v[88:91], a[180:183]\n"
"v_fma_f32 v24, v110, s62, v10\n"
"v_exp_f32 v23, v23\n"
"v_mfma_f32_16x16x32_bf16 a[184:187], v[36:39], v[68:71], a[184:187]\n"
"v_fma_f32 v25, v111, s62, v10\n"
"v_mfma_f32_16x16x32_bf16 a[188:191], v[36:39], v[48:51], a[188:191]\n"
"s_waitcnt lgkmcnt(4)\n"
"ds_read_b64_tr_b16 v[36:37], v6 offset:26112\n"
"ds_read_b64_tr_b16 v[38:39], v6 offset:32256\n"
"v_mfma_f32_16x16x32_bf16 a[192:195], v[32:35], v[116:119], a[192:195]\n"
"v_exp_f32 v24, v24\n"
"v_add_f32 v3, v22, v23\n"
"v_mfma_f32_16x16x32_bf16 a[196:199], v[32:35], v[72:75], a[196:199]\n"
"v_fma_f32 v26, v112, s62, v10\n"
"v_exp_f32 v25, v25\n"
"v_mfma_f32_16x16x32_bf16 a[200:203], v[32:35], v[28:31], a[200:203]\n"
"v_cvt_pk_bf16_f32 v56, v22, v23\n"
"v_fma_f32 v27, v113, s62, v10\n"
"v_mfma_f32_16x16x32_bf16 a[204:207], v[32:35], v[88:91], a[204:207]\n"
"v_exp_f32 v26, v26\n"
"v_add_f32 v3, v3, v24\n"
"v_mfma_f32_16x16x32_bf16 a[208:211], v[32:35], v[68:71], a[208:211]\n"
"v_fma_f32 v22, v114, s62, v10\n"
"v_mfma_f32_16x16x32_bf16 a[212:215], v[32:35], v[48:51], a[212:215]\n"
"s_waitcnt lgkmcnt(4)\n"
"ds_read_b64_tr_b16 v[32:33], v5 offset:26624\n"
"ds_read_b64_tr_b16 v[34:35], v5 offset:32768\n"
"v_mfma_f32_16x16x32_bf16 a[216:219], v[40:43], v[116:119], a[216:219]\n"
"v_exp_f32 v27, v27\n"
"v_add_f32 v3, v3, v25\n"
"v_cvt_pk_bf16_f32 v57, v24, v25\n"
"v_mfma_f32_16x16x32_bf16 a[220:223], v[40:43], v[72:75], a[220:223]\n"
"v_fma_f32 v23, v115, s62, v10\n"
"v_mfma_f32_16x16x32_bf16 a[224:227], v[40:43], v[28:31], a[224:227]\n"
"v_exp_f32 v22, v22\n"
"v_add_f32 v3, v3, v26\n"
"v_mfma_f32_16x16x32_bf16 a[228:231], v[40:43], v[88:91], a[228:231]\n"
"v_exp_f32 v23, v23\n"
"v_cvt_pk_bf16_f32 v58, v26, v27\n"
"v_mfma_f32_16x16x32_bf16 a[232:235], v[40:43], v[68:71], a[232:235]\n"
"v_add_f32 v3, v3, v27\n"
"v_add_f32 v3, v3, v22\n"
"v_mfma_f32_16x16x32_bf16 a[236:239], v[40:43], v[48:51], a[236:239]\n"
"s_waitcnt lgkmcnt(4)\n"
"ds_read_b64_tr_b16 v[40:41], v6 offset:26624\n"
"ds_read_b64_tr_b16 v[42:43], v6 offset:32768\n"
"v_mfma_f32_16x16x32_bf16 a[240:243], v[44:47], v[116:119], a[240:243]\n"
"v_cvt_pk_bf16_f32 v59, v22, v23\n"
"v_add_f32 v3, v3, v23\n"
"v_max_f32 v27, v21, v3\n"
"v_mfma_f32_16x16x32_bf16 a[244:247], v[44:47], v[72:75], a[244:247]\n"
"v_cmp_nge_f32 vcc, s63, v27\n"
"v_fma_f32 v22, v60, s62, v11\n"
"v_mfma_f32_16x16x32_bf16 a[248:251], v[44:47], v[28:31], a[248:251]\n"
"v_fma_f32 v23, v61, s62, v11\n"
"v_mfma_f32_16x16x32_bf16 a[252:255], v[44:47], v[88:91], a[252:255]\n"
"s_cbranch_vccnz .Lrare_6_a\n"
".Lret_6_a:\n"
"v_add_f32 v15, v15, v21\n"
"v_add_f32 v16, v16, v3\n"
"v_add_u32 v3, s68, v1\n"
"v_mfma_f32_16x16x32_bf16 v[128:131], v[44:47], v[68:71], v[128:131]\n"
"v_exp_f32 v22, v22\n"
"v_fma_f32 v24, v62, s62, v11\n"
"v_mfma_f32_16x16x32_bf16 v[132:135], v[44:47], v[48:51], v[132:135]\n"
"s_waitcnt lgkmcnt(4)\n"
"ds_read_b64_tr_b16 v[44:45], v5 offset:27136\n"
"ds_read_b64_tr_b16 v[46:47], v5 offset:33280\n"
"v_mfma_f32_16x16x32_bf16 v[136:139], v[36:39], v[116:119], v[136:139]\n"
"v_exp_f32 v23, v23\n"
"v_fma_f32 v25, v63, s62, v11\n"
"v_mfma_f32_16x16x32_bf16 v[140:143], v[36:39], v[72:75], v[140:143]\n"
"v_exp_f32 v24, v24\n"
"v_mfma_f32_16x16x32_bf16 v[144:147], v[36:39], v[28:31], v[144:147]\n"
"v_add_f32 v84, v22, v23\n"
"v_fma_f32 v26, v120, s62, v11\n"
"v_mfma_f32_16x16x32_bf16 v[148:151], v[36:39], v[88:91], v[148:151]\n"
"v_exp_f32 v25, v25\n"
"v_cvt_pk_bf16_f32 v76, v22, v23\n"
"v_mfma_f32_16x16x32_bf16 v[152:155], v[36:39], v[68:71], v[152:155]\n"
"v_fma_f32 v27, v121, s62, v11\n"
"v_exp_f32 v26, v26\n"
"v_mfma_f32_16x16x32_bf16 v[156:159], v[36:39], v[48:51], v[156:159]\n"
"s_waitcnt lgkmcnt(4)\n"
"ds_read_b64_tr_b16 v[36:37], v6 offset:27136\n"
"ds_read_b64_tr_b16 v[38:39], v6 offset:33280\n"
"v_mfma_f32_16x16x32_bf16 v[160:163], v[32:35], v[116:119], v[160:163]\n"
"v_add_f32 v84, v84, v24\n"
"v_fma_f32 v22, v122, s62, v11\n"
"v_mfma_f32_16x16x32_bf16 v[164:167], v[32:35], v[72:75], v[164:167]\n"
"v_exp_f32 v27, v27\n"
"v_add_f32 v84, v84, v25\n"
"v_mfma_f32_16x16x32_bf16 v[168:171], v[32:35], v[28:31], v[168:171]\n"
"v_cvt_pk_bf16_f32 v77, v24, v25\n"
"v_fma_f32 v23, v123, s62, v11\n"
"v_mfma_f32_16x16x32_bf16 v[172:175], v[32:35], v[88:91], v[172:175]\n"
"v_exp_f32 v22, v22\n"
"v_add_f32 v84, v84, v26\n"
"v_mfma_f32_16x16x32_bf16 v[176:179], v[32:35], v[68:71], v[176:179]\n"
"v_exp_f32 v23, v23\n"
"v_mfma_f32_16x16x32_bf16 v[180:183], v[32:35], v[48:51], v[180:183]\n"
"s_waitcnt lgkmcnt(4)\n"
"v_cvt_pk_bf16_f32 v78, v26, v27\n"
"v_add_f32 v84, v84, v27\n"
"v_mfma_f32_16x16x32_bf16 v[184:187], v[40:43], v[116:119], v[184:187]\n"
"v_add_f32 v84, v84, v22\n"
"v_cvt_pk_bf16_f32 v79, v22, v23\n"
"v_add_f32 v84, v84, v23\n"
"v_mfma_f32_16x16x32_bf16 v[188:191], v[40:43], v[72:75], v[188:191]\n"
"ds_read_b128 v[32:35], v1 offset:24576\n"
"v_fma_f32 v22, v92, s62, v12\n"
"v_fma_f32 v23, v93, s62, v12\n"
"v_mfma_f32_16x16x32_bf16 v[192:195], v[40:43], v[28:31], v[192:195]\n"
"v_exp_f32 v22, v22\n"
"v_fma_f32 v24, v94, s62, v12\n"
"v_mfma_f32_16x16x32_bf16 v[196:199], v[40:43], v[88:91], v[196:199]\n"
"v_exp_f32 v23, v23\n"
"v_mfma_f32_16x16x32_bf16 v[200:203], v[40:43], v[68:71], v[200:203]\n"
"v_fma_f32 v25, v95, s62, v12\n"
"v_exp_f32 v24, v24\n"
"v_mfma_f32_16x16x32_bf16 v[204:207], v[40:43], v[48:51], v[204:207]\n"
"s_waitcnt lgkmcnt(3)\n"
"v_add_f32 v85, v22, v23\n"
"v_fma_f32 v26, v64, s62, v12\n"
"v_mfma_f32_16x16x32_bf16 v[208:211], v[44:47], v[116:119], v[208:211]\n"
"v_exp_f32 v25, v25\n"
"v_cvt_pk_bf16_f32 v80, v22, v23\n"
"v_mfma_f32_16x16x32_bf16 v[212:215], v[44:47], v[72:75], v[212:215]\n"
"ds_read_b128 v[40:43], v3 offset:0\n"
"v_fma_f32 v27, v65, s62, v12\n"
"v_mfma_f32_16x16x32_bf16 v[216:219], v[44:47], v[28:31], v[216:219]\n"
"v_exp_f32 v26, v26\n"
"v_add_f32 v85, v85, v24\n"
"v_mfma_f32_16x16x32_bf16 v[220:223], v[44:47], v[88:91], v[220:223]\n"
"v_fma_f32 v22, v66, s62, v12\n"
"v_exp_f32 v27, v27\n"
"v_mfma_f32_16x16x32_bf16 v[224:227], v[44:47], v[68:71], v[224:227]\n"
"v_add_f32 v85, v85, v25\n"
"v_cvt_pk_bf16_f32 v81, v24, v25\n"
"v_mfma_f32_16x16x32_bf16 v[228:231], v[44:47], v[48:51], v[228:231]\n"
"s_waitcnt lgkmcnt(2)\n"
"v_fma_f32 v23, v67, s62, v12\n"
"v_exp_f32 v22, v22\n"
"v_mfma_f32_16x16x32_bf16 v[232:235], v[36:39], v[116:119], v[232:235]\n"
"ds_read_b128 v[44:47], v3 offset:6144\n"
"v_add_f32 v85, v85, v26\n"
"v_mfma_f32_16x16x32_bf16 v[236:239], v[36:39], v[72:75], v[236:239]\n"
"v_exp_f32 v23, v23\n"
"v_cvt_pk_bf16_f32 v82, v26, v27\n"
"v_add_f32 v85, v85, v27\n"
"v_mfma_f32_16x16x32_bf16 v[240:243], v[36:39], v[28:31], v[240:243]\n"
"v_add_f32 v85, v85, v22\n"
"v_cvt_pk_bf16_f32 v83, v22, v23\n"
"v_mfma_f32_16x16x32_bf16 v[244:247], v[36:39], v[88:91], v[244:247]\n"
"v_add_f32 v85, v85, v23\n"
"v_max_f32 v27, v84, v85\n"
"v_cmp_nge_f32 vcc, s63, v27\n"
"v_mfma_f32_16x16x32_bf16 v[248:251], v[36:39], v[68:71], v[248:251]\n"
"v_fma_f32 v22, v124, s62, v13\n"
"v_fma_f32 v23, v125, s62, v13\n"
"v_mfma_f32_16x16x32_bf16 v[252:255], v[36:39], v[48:51], v[252:255]\n"
"s_waitcnt lgkmcnt(2)\n"
"s_cbranch_vccnz .Lrare_6_b\n"
".Lret_6_b:\n"
"ds_read_b128 v[36:39], v1 offset:30720\n"
"v_add_f32 v17, v17, v84\n"
"v_add_f32 v18, v18, v85\n"
"v_mfma_f32_16x16x32_bf16 v[108:111], v[32:35], a[0:3], 0\n"
"v_mfma_f32_16x16x32_bf16 v[112:115], v[32:35], a[24:27], 0\n"
"s_add_u32 m0, s56, 61440\n"
"v_exp_f32 v22, v22\n"
"v_mfma_f32_16x16x32_bf16 v[84:87], v[32:35], a[48:51], 0\n"
"s_add_u32 s61, s57, 98304\n"
"v_fma_f32 v24, v126, s62, v13\n"
"v_mfma_f32_16x16x32_bf16 v[60:63], v[32:35], a[72:75], 0\n"
"s_waitcnt lgkmcnt(2)\n"
"buffer_load_dwordx4 v8, s[48:51], s61 offen nt lds\n"
"v_exp_f32 v23, v23\n"
"v_fma_f32 v25, v127, s62, v13\n"
"v_mfma_f32_16x16x32_bf16 v[120:123], v[32:35], v[40:43], 0\n"
"s_waitcnt lgkmcnt(1)\n"
"v_exp_f32 v24, v24\n"
"v_mfma_f32_16x16x32_bf16 v[92:95], v[32:35], v[44:47], 0\n"
"s_waitcnt lgkmcnt(0)\n"
"ds_read_b128 v[32:35], v2 offset:24576\n"
"v_mfma_f32_16x16x32_bf16 v[88:91], v[36:39], v[40:43], 0\n"
"ds_read_b128 v[40:43], v4 offset:0\n"
"v_mfma_f32_16x16x32_bf16 v[68:71], v[36:39], v[44:47], 0\n"
"ds_read_b128 v[44:47], v4 offset:6144\n"
"v_mfma_f32_16x16x32_bf16 v[64:67], v[36:39], a[0:3], 0\n"
"v_add_f32 v21, v22, v23\n"
"v_fma_f32 v26, v96, s62, v13\n"
"v_mfma_f32_16x16x32_bf16 v[116:119], v[36:39], a[24:27], 0\n"
"v_exp_f32 v25, v25\n"
"v_mfma_f32_16x16x32_bf16 v[72:75], v[36:39], a[48:51], 0\n"
"v_cvt_pk_bf16_f32 v48, v22, v23\n"
"v_fma_f32 v27, v97, s62, v13\n"
"v_mfma_f32_16x16x32_bf16 v[28:31], v[36:39], a[72:75], 0\n"
"s_waitcnt lgkmcnt(2)\n"
"ds_read_b128 v[36:39], v2 offset:30720\n"
"v_mfma_f32_16x16x32_bf16 v[108:111], v[32:35], a[4:7], v[108:111]\n"
"v_exp_f32 v26, v26\n"
"v_mfma_f32_16x16x32_bf16 v[112:115], v[32:35], a[28:31], v[112:115]\n"
"s_add_u32 m0, s56, 62464\n"
"v_add_f32 v21, v21, v24\n"
"v_fma_f32 v22, v98, s62, v13\n"
"v_mfma_f32_16x16x32_bf16 v[84:87], v[32:35], a[52:55], v[84:87]\n"
"s_add_u32 s61, s57, 98432\n"
"v_exp_f32 v27, v27\n"
"v_mfma_f32_16x16x32_bf16 v[60:63], v[32:35], a[76:79], v[60:63]\n"
"s_waitcnt lgkmcnt(2)\n"
"buffer_load_dwordx4 v8, s[48:51], s61 offen nt lds\n"
"v_add_f32 v21, v21, v25\n"
"v_cvt_pk_bf16_f32 v49, v24, v25\n"
"v_mfma_f32_16x16x32_bf16 v[120:123], v[32:35], v[40:43], v[120:123]\n"
"s_waitcnt lgkmcnt(1)\n"
"v_fma_f32 v23, v99, s62, v13\n"
"v_mfma_f32_16x16x32_bf16 v[92:95], v[32:35], v[44:47], v[92:95]\n"
"s_waitcnt lgkmcnt(0)\n"
"ds_read_b128 v[32:35], v1 offset:25600\n"
"v_mfma_f32_16x16x32_bf16 v[88:91], v[36:39], v[40:43], v[88:91]\n"
"ds_read_b128 v[40:43], v3 offset:1024\n"
"v_mfma_f32_16x16x32_bf16 v[68:71], v[36:39], v[44:47], v[68:71]\n"
"ds_read_b128 v[44:47], v3 offset:7168\n"
"s_add_u32 m0, s56, 63488\n"
"v_mfma_f32_16x16x32_bf16 v[64:67], v[36:39], a[4:7], v[64:67]\n"
"s_add_u32 s61, s57, 98560\n"
"v_exp_f32 v22, v22\n"
"v_add_f32 v21, v21, v26\n"
"v_mfma_f32_16x16x32_bf16 v[116:119], v[36:39], a[28:31], v[116:119]\n"
"buffer_load_dwordx4 v8, s[48:51], s61 offen nt lds\n"
"v_exp_f32 v23, v23\n"
"v_mfma_f32_16x16x32_bf16 v[72:75], v[36:39], a[52:55], v[72:75]\n"
"v_cvt_pk_bf16_f32 v50, v26, v27\n"
"v_add_f32 v21, v21, v27\n"
"v_mfma_f32_16x16x32_bf16 v[28:31], v[36:39], a[76:79], v[28:31]\n"
"s_waitcnt lgkmcnt(2)\n"
"ds_read_b128 v[36:39], v1 offset:31744\n"
"v_mfma_f32_16x16x32_bf16 v[108:111], v[32:35], a[8:11], v[108:111]\n"
"v_add_f32 v21, v21, v22\n"
"v_cvt_pk_bf16_f32 v51, v22, v23\n"
"v_mfma_f32_16x16x32_bf16 v[112:115], v[32:35], a[32:35], v[112:115]\n"
"v_add_f32 v21, v21, v23\n"
"v_cmp_nge_f32 vcc, s63, v21\n"
"v_mfma_f32_16x16x32_bf16 v[84:87], v[32:35], a[56:59], v[84:87]\n"
"v_fma_f32 v22, v104, s62, v14\n"
"v_fma_f32 v23, v105, s62, v14\n"
"v_mfma_f32_16x16x32_bf16 v[60:63], v[32:35], a[80:83], v[60:63]\n"
"s_waitcnt lgkmcnt(2)\n"
"s_cbranch_vccnz .Lrare_6_d4\n"
".Lret_6_d4:\n"
"v_add_f32 v19, v19, v21\n"
"v_mfma_f32_16x16x32_bf16 v[120:123], v[32:35], v[40:43], v[120:123]\n"
"s_waitcnt lgkmcnt(1)\n"
"v_exp_f32 v22, v22\n"
"v_mfma_f32_16x16x32_bf16 v[92:95], v[32:35], v[44:47], v[92:95]\n"
"s_waitcnt lgkmcnt(0)\n"
"ds_read_b128 v[32:35], v2 offset:25600\n"
"v_mfma_f32_16x16x32_bf16 v[88:91], v[36:39], v[40:43], v[88:91]\n"
"ds_read_b128 v[40:43], v4 offset:1024\n"
"v_mfma_f32_16x16x32_bf16 v[68:71], v[36:39], v[44:47], v[68:71]\n"
"ds_read_b128 v[44:47], v4 offset:7168\n"
"s_add_u32 m0, s56, 12288\n"
"v_mfma_f32_16x16x32_bf16 v[64:67], v[36:39], a[8:11], v[64:67]\n"
"s_add_u32 s61, s57, 122880\n"
"v_fma_f32 v24, v106, s62, v14\n"
"v_mfma_f32_16x16x32_bf16 v[116:119], v[36:39], a[32:35], v[116:119]\n"
"buffer_load_dwordx4 v7, s[44:47], s61 offen nt lds\n"
"v_exp_f32 v23, v23\n"
"v_fma_f32 v25, v107, s62, v14\n"
"v_mfma_f32_16x16x32_bf16 v[72:75], v[36:39], a[56:59], v[72:75]\n"
"v_exp_f32 v24, v24\n"
"v_mfma_f32_16x16x32_bf16 v[28:31], v[36:39], a[80:83], v[28:31]\n"
"s_waitcnt lgkmcnt(2)\n"
"ds_read_b128 v[36:39], v2 offset:31744\n"
"v_mfma_f32_16x16x32_bf16 v[108:111], v[32:35], a[12:15], v[108:111]\n"
"v_add_f32 v21, v22, v23\n"
"v_fma_f32 v26, v100, s62, v14\n"
"v_mfma_f32_16x16x32_bf16 v[112:115], v[32:35], a[36:39], v[112:115]\n"
"v_exp_f32 v25, v25\n"
"v_mfma_f32_16x16x32_bf16 v[84:87], v[32:35], a[60:63], v[84:87]\n"
"v_cvt_pk_bf16_f32 v124, v22, v23\n"
"v_fma_f32 v27, v101, s62, v14\n"
"v_mfma_f32_16x16x32_bf16 v[60:63], v[32:35], a[84:87], v[60:63]\n"
"s_waitcnt lgkmcnt(2)\n"
"v_exp_f32 v26, v26\n"
"v_mfma_f32_16x16x32_bf16 v[120:123], v[32:35], v[40:43], v[120:123]\n"
"s_waitcnt lgkmcnt(1)\n"
"v_add_f32 v21, v21, v24\n"
"v_fma_f32 v22, v102, s62, v14\n"
"v_mfma_f32_16x16x32_bf16 v[92:95], v[32:35], v[44:47], v[92:95]\n"
"s_waitcnt lgkmcnt(0)\n"
"ds_read_b128 v[32:35], v1 offset:26624\n"
"v_mfma_f32_16x16x32_bf16 v[88:91], v[36:39], v[40:43], v[88:91]\n"
"ds_read_b128 v[40:43], v3 offset:2048\n"
"v_mfma_f32_16x16x32_bf16 v[68:71], v[36:39], v[44:47], v[68:71]\n"
"ds_read_b128 v[44:47], v3 offset:8192\n"
"s_add_u32 m0, s56, 13312\n"
"v_mfma_f32_16x16x32_bf16 v[64:67], v[36:39], a[12:15], v[64:67]\n"
"s_add_u32 s61, s57, 123008\n"
"v_exp_f32 v27, v27\n"
"v_mfma_f32_16x16x32_bf16 v[116:119], v[36:39], a[36:39], v[116:119]\n"
"buffer_load_dwordx4 v7, s[44:47], s61 offen nt lds\n"
"v_add_f32 v21, v21, v25\n"
"v_cvt_pk_bf16_f32 v125, v24, v25\n"
"v_mfma_f32_16x16x32_bf16 v[72:75], v[36:39], a[60:63], v[72:75]\n"
"v_fma_f32 v23, v103, s62, v14\n"
"v_mfma_f32_16x16x32_bf16 v[28:31], v[36:39], a[84:87], v[28:31]\n"
"s_waitcnt lgkmcnt(2)\n"
"ds_read_b128 v[36:39], v1 offset:32768\n"
"v_mfma_f32_16x16x32_bf16 v[108:111], v[32:35], a[16:19], v[108:111]\n"
"v_exp_f32 v22, v22\n"
"v_add_f32 v21, v21, v26\n"
"v_mfma_f32_16x16x32_bf16 v[112:115], v[32:35], a[40:43], v[112:115]\n"
"s_add_u32 m0, s56, 14336\n"
"v_exp_f32 v23, v23\n"
"v_mfma_f32_16x16x32_bf16 v[84:87], v[32:35], a[64:67], v[84:87]\n"
"s_add_u32 s61, s57, 123136\n"
"v_cvt_pk_bf16_f32 v126, v26, v27\n"
"v_add_f32 v21, v21, v27\n"
"v_mfma_f32_16x16x32_bf16 v[60:63], v[32:35], a[88:91], v[60:63]\n"
"s_waitcnt lgkmcnt(2)\n"
"buffer_load_dwordx4 v7, s[44:47], s61 offen nt lds\n"
"v_add_f32 v21, v21, v22\n"
"v_cvt_pk_bf16_f32 v127, v22, v23\n"
"v_mfma_f32_16x16x32_bf16 v[120:123], v[32:35], v[40:43], v[120:123]\n"
"s_waitcnt lgkmcnt(1)\n"
"v_add_f32 v21, v21, v23\n"
"v_cmp_nge_f32 vcc, s63, v21\n"
"v_mfma_f32_16x16x32_bf16 v[92:95], v[32:35], v[44:47], v[92:95]\n"
"s_waitcnt lgkmcnt(0)\n"
"ds_read_b128 v[32:35], v2 offset:26624\n"
"v_mfma_f32_16x16x32_bf16 v[88:91], v[36:39], v[40:43], v[88:91]\n"
"ds_read_b128 v[40:43], v4 offset:2048\n"
"v_mfma_f32_16x16x32_bf16 v[68:71], v[36:39], v[44:47], v[68:71]\n"
"ds_read_b128 v[44:47], v4 offset:8192\n"
"s_cbranch_vccnz .Lrare_6_d5\n"
".Lret_6_d5:\n"
"v_add_f32 v20, v20, v21\n"
"v_mfma_f32_16x16x32_bf16 v[64:67], v[36:39], a[16:19], v[64:67]\n"
"v_mbcnt_lo_u32_b32 v22, -1, 0\n"
"v_mbcnt_hi_u32_b32 v22, -1, v22\n"
"v_mfma_f32_16x16x32_bf16 v[116:119], v[36:39], a[40:43], v[116:119]\n"
"v_and_b32 v22, 31, v22\n"
"v_mfma_f32_16x16x32_bf16 v[72:75], v[36:39], a[64:67], v[72:75]\n"
"v_lshlrev_b32 v22, 7, v22\n"
"v_mfma_f32_16x16x32_bf16 v[28:31], v[36:39], a[88:91], v[28:31]\n"
"s_waitcnt lgkmcnt(2)\n"
"ds_read_b128 v[36:39], v2 offset:32768\n"
"s_add_u32 s61, s60, 32768\n"
"v_mfma_f32_16x16x32_bf16 v[108:111], v[32:35], a[20:23], v[108:111]\n"
"s_mov_b32 m0, s73\n"
"v_mfma_f32_16x16x32_bf16 v[112:115], v[32:35], a[44:47], v[112:115]\n"
"buffer_load_dword v22, s[40:43], s61 offen lds\n"
"v_mfma_f32_16x16x32_bf16 v[84:87], v[32:35], a[68:71], v[84:87]\n"
"v_mfma_f32_16x16x32_bf16 v[60:63], v[32:35], a[92:95], v[60:63]\n"
"s_waitcnt lgkmcnt(2)\n"
"v_mfma_f32_16x16x32_bf16 v[120:123], v[32:35], v[40:43], v[120:123]\n"
"s_waitcnt vmcnt(15)\n"
"s_waitcnt lgkmcnt(0)\n"
"s_barrier\n"
"v_mfma_f32_16x16x32_bf16 v[92:95], v[32:35], v[44:47], v[92:95]\n"
"v_mfma_f32_16x16x32_bf16 v[88:91], v[36:39], v[40:43], v[88:91]\n"
"ds_read_b64_tr_b16 v[32:33], v5 offset:36864\n"
"ds_read_b64_tr_b16 v[34:35], v5 offset:43008\n"
"v_mfma_f32_16x16x32_bf16 v[68:71], v[36:39], v[44:47], v[68:71]\n"
"v_mfma_f32_16x16x32_bf16 v[64:67], v[36:39], a[20:23], v[64:67]\n"
"ds_read_b64_tr_b16 v[40:41], v6 offset:36864\n"
"ds_read_b64_tr_b16 v[42:43], v6 offset:43008\n"
"v_mfma_f32_16x16x32_bf16 v[116:119], v[36:39], a[44:47], v[116:119]\n"
"v_mfma_f32_16x16x32_bf16 v[72:75], v[36:39], a[68:71], v[72:75]\n"
"ds_read_b64_tr_b16 v[44:45], v5 offset:37376\n"
"ds_read_b64_tr_b16 v[46:47], v5 offset:43520\n"
"v_mfma_f32_16x16x32_bf16 v[28:31], v[36:39], a[92:95], v[28:31]\n"
"s_waitcnt lgkmcnt(4)\n"
"v_mfma_f32_16x16x32_bf16 a[96:99], v[32:35], v[52:55], a[96:99]\n"
"ds_read_b64_tr_b16 v[36:37], v6 offset:37376\n"
"ds_read_b64_tr_b16 v[38:39], v6 offset:43520\n"
"v_mfma_f32_16x16x32_bf16 a[100:103], v[32:35], v[56:59], a[100:103]\n"
"v_mfma_f32_16x16x32_bf16 a[104:107], v[32:35], v[76:79], a[104:107]\n"
"v_fma_f32 v22, v108, s62, v9\n"
"v_fma_f32 v23, v109, s62, v9\n"
"v_mfma_f32_16x16x32_bf16 a[108:111], v[32:35], v[80:83], a[108:111]\n"
"v_exp_f32 v22, v22\n"
"v_fma_f32 v24, v110, s62, v9\n"
"v_mfma_f32_16x16x32_bf16 a[112:115], v[32:35], v[48:51], a[112:115]\n"
"v_exp_f32 v23, v23\n"
"v_mfma_f32_16x16x32_bf16 a[116:119], v[32:35], v[124:127], a[116:119]\n"
"s_waitcnt lgkmcnt(4)\n"
"ds_read_b64_tr_b16 v[32:33], v5 offset:37888\n"
"ds_read_b64_tr_b16 v[34:35], v5 offset:44032\n"
"v_mfma_f32_16x16x32_bf16 a[120:123], v[40:43], v[52:55], a[120:123]\n"
"v_fma_f32 v25, v111, s62, v9\n"
"v_exp_f32 v24, v24\n"
"v_mfma_f32_16x16x32_bf16 a[124:127], v[40:43], v[56:59], a[124:127]\n"
"v_add_f32 v21, v22, v23\n"
"v_fma_f32 v26, v64, s62, v9\n"
"v_mfma_f32_16x16x32_bf16 a[128:131], v[40:43], v[76:79], a[128:131]\n"
"v_exp_f32 v25, v25\n"
"v_cvt_pk_bf16_f32 v96, v22, v23\n"
"v_mfma_f32_16x16x32_bf16 a[132:135], v[40:43], v[80:83], a[132:135]\n"
"v_fma_f32 v27, v65, s62, v9\n"
"v_mfma_f32_16x16x32_bf16 a[136:139], v[40:43], v[48:51], a[136:139]\n"
"v_exp_f32 v26, v26\n"
"v_add_f32 v21, v21, v24\n"
"v_fma_f32 v22, v66, s62, v9\n"
"v_mfma_f32_16x16x32_bf16 a[140:143], v[40:43], v[124:127], a[140:143]\n"
"s_waitcnt lgkmcnt(4)\n"
"ds_read_b64_tr_b16 v[40:41], v6 offset:37888\n"
"ds_read_b64_tr_b16 v[42:43], v6 offset:44032\n"
"v_mfma_f32_16x16x32_bf16 a[144:147], v[44:47], v[52:55], a[144:147]\n"
"v_exp_f32 v27, v27\n"
"v_mfma_f32_16x16x32_bf16 a[148:151], v[44:47], v[56:59], a[148:151]\n"
"v_add_f32 v21, v21, v25\n"
"v_cvt_pk_bf16_f32 v97, v24, v25\n"
"v_mfma_f32_16x16x32_bf16 a[152:155], v[44:47], v[76:79], a[152:155]\n"
"v_fma_f32 v23, v67, s62, v9\n"
"v_exp_f32 v22, v22\n"
"v_mfma_f32_16x16x32_bf16 a[156:159], v[44:47], v[80:83], a[156:159]\n"
"v_add_f32 v21, v21, v26\n"
"v_mfma_f32_16x16x32_bf16 a[160:163], v[44:47], v[48:51], a[160:163]\n"
"v_exp_f32 v23, v23\n"
"v_cvt_pk_bf16_f32 v98, v26, v27\n"
"v_add_f32 v21, v21, v27\n"
"v_mfma_f32_16x16x32_bf16 a[164:167], v[44:47], v[124:127], a[164:167]\n"
"s_waitcnt lgkmcnt(4)\n"
"ds_read_b64_tr_b16 v[44:45], v5 offset:38400\n"
"ds_read_b64_tr_b16 v[46:47], v5 offset:44544\n"
"v_mfma_f32_16x16x32_bf16 a[168:171], v[36:39], v[52:55], a[168:171]\n"
"v_add_f32 v21, v21, v22\n"
"v_cvt_pk_bf16_f32 v99, v22, v23\n"
"v_mfma_f32_16x16x32_bf16 a[172:175], v[36:39], v[56:59], a[172:175]\n"
"v_add_f32 v21, v21, v23\n"
"v_fma_f32 v22, v112, s62, v10\n"
"v_fma_f32 v23, v113, s62, v10\n"
"v_mfma_f32_16x16x32_bf16 a[176:179], v[36:39], v[76:79], a[176:179]\n"
"v_exp_f32 v22, v22\n"
"v_mfma_f32_16x16x32_bf16 a[180:183], v[36:39], v[80:83], a[180:183]\n"
"v_fma_f32 v24, v114, s62, v10\n"
"v_exp_f32 v23, v23\n"
"v_mfma_f32_16x16x32_bf16 a[184:187], v[36:39], v[48:51], a[184:187]\n"
"v_fma_f32 v25, v115, s62, v10\n"
"v_mfma_f32_16x16x32_bf16 a[188:191], v[36:39], v[124:127], a[188:191]\n"
"s_waitcnt lgkmcnt(4)\n"
"ds_read_b64_tr_b16 v[36:37], v6 offset:38400\n"
"ds_read_b64_tr_b16 v[38:39], v6 offset:44544\n"
"v_mfma_f32_16x16x32_bf16 a[192:195], v[32:35], v[52:55], a[192:195]\n"
"v_exp_f32 v24, v24\n"
"v_add_f32 v3, v22, v23\n"
"v_mfma_f32_16x16x32_bf16 a[196:199], v[32:35], v[56:59], a[196:199]\n"
"v_fma_f32 v26, v116, s62, v10\n"
"v_exp_f32 v25, v25\n"
"v_mfma_f32_16x16x32_bf16 a[200:203], v[32:35], v[76:79], a[200:203]\n"
"v_cvt_pk_bf16_f32 v104, v22, v23\n"
"v_fma_f32 v27, v117, s62, v10\n"
"v_mfma_f32_16x16x32_bf16 a[204:207], v[32:35], v[80:83], a[204:207]\n"
"v_exp_f32 v26, v26\n"
"v_add_f32 v3, v3, v24\n"
"v_mfma_f32_16x16x32_bf16 a[208:211], v[32:35], v[48:51], a[208:211]\n"
"v_fma_f32 v22, v118, s62, v10\n"
"v_mfma_f32_16x16x32_bf16 a[212:215], v[32:35], v[124:127], a[212:215]\n"
"s_waitcnt lgkmcnt(4)\n"
"ds_read_b64_tr_b16 v[32:33], v5 offset:38912\n"
"ds_read_b64_tr_b16 v[34:35], v5 offset:45056\n"
"v_mfma_f32_16x16x32_bf16 a[216:219], v[40:43], v[52:55], a[216:219]\n"
"v_exp_f32 v27, v27\n"
"v_add_f32 v3, v3, v25\n"
"v_cvt_pk_bf16_f32 v105, v24, v25\n"
"v_mfma_f32_16x16x32_bf16 a[220:223], v[40:43], v[56:59], a[220:223]\n"
"v_fma_f32 v23, v119, s62, v10\n"
"v_mfma_f32_16x16x32_bf16 a[224:227], v[40:43], v[76:79], a[224:227]\n"
"v_exp_f32 v22, v22\n"
"v_add_f32 v3, v3, v26\n"
"v_mfma_f32_16x16x32_bf16 a[228:231], v[40:43], v[80:83], a[228:231]\n"
"v_exp_f32 v23, v23\n"
"v_cvt_pk_bf16_f32 v106, v26, v27\n"
"v_mfma_f32_16x16x32_bf16 a[232:235], v[40:43], v[48:51], a[232:235]\n"
"v_add_f32 v3, v3, v27\n"
"v_add_f32 v3, v3, v22\n"
"v_mfma_f32_16x16x32_bf16 a[236:239], v[40:43], v[124:127], a[236:239]\n"
"s_waitcnt lgkmcnt(4)\n"
"ds_read_b64_tr_b16 v[40:41], v6 offset:38912\n"
"ds_read_b64_tr_b16 v[42:43], v6 offset:45056\n"
"v_mfma_f32_16x16x32_bf16 a[240:243], v[44:47], v[52:55], a[240:243]\n"
"v_cvt_pk_bf16_f32 v107, v22, v23\n"
"v_add_f32 v3, v3, v23\n"
"v_max_f32 v27, v21, v3\n"
"v_mfma_f32_16x16x32_bf16 a[244:247], v[44:47], v[56:59], a[244:247]\n"
"v_cmp_nge_f32 vcc, s63, v27\n"
"v_fma_f32 v22, v84, s62, v11\n"
"v_mfma_f32_16x16x32_bf16 a[248:251], v[44:47], v[76:79], a[248:251]\n"
"v_fma_f32 v23, v85, s62, v11\n"
"v_mfma_f32_16x16x32_bf16 a[252:255], v[44:47], v[80:83], a[252:255]\n"
"s_cbranch_vccnz .Lrare_7_a\n"
".Lret_7_a:\n"
"v_add_f32 v15, v15, v21\n"
"v_add_f32 v16, v16, v3\n"
"v_add_u32 v3, s68, v1\n"
"v_mfma_f32_16x16x32_bf16 v[128:131], v[44:47], v[48:51], v[128:131]\n"
"v_exp_f32 v22, v22\n"
"v_fma_f32 v24, v86, s62, v11\n"
"v_mfma_f32_16x16x32_bf16 v[132:135], v[44:47], v[124:127], v[132:135]\n"
"s_waitcnt lgkmcnt(4)\n"
"ds_read_b64_tr_b16 v[44:45], v5 offset:39424\n"
"ds_read_b64_tr_b16 v[46:47], v5 offset:45568\n"
"v_mfma_f32_16x16x32_bf16 v[136:139], v[36:39], v[52:55], v[136:139]\n"
"v_exp_f32 v23, v23\n"
"v_fma_f32 v25, v87, s62, v11\n"
"v_mfma_f32_16x16x32_bf16 v[140:143], v[36:39], v[56:59], v[140:143]\n"
"v_exp_f32 v24, v24\n"
"v_mfma_f32_16x16x32_bf16 v[144:147], v[36:39], v[76:79], v[144:147]\n"
"v_add_f32 v64, v22, v23\n"
"v_fma_f32 v26, v72, s62, v11\n"
"v_mfma_f32_16x16x32_bf16 v[148:151], v[36:39], v[80:83], v[148:151]\n"
"v_exp_f32 v25, v25\n"
"v_cvt_pk_bf16_f32 v100, v22, v23\n"
"v_mfma_f32_16x16x32_bf16 v[152:155], v[36:39], v[48:51], v[152:155]\n"
"v_fma_f32 v27, v73, s62, v11\n"
"v_exp_f32 v26, v26\n"
"v_mfma_f32_16x16x32_bf16 v[156:159], v[36:39], v[124:127], v[156:159]\n"
"s_waitcnt lgkmcnt(4)\n"
"ds_read_b64_tr_b16 v[36:37], v6 offset:39424\n"
"ds_read_b64_tr_b16 v[38:39], v6 offset:45568\n"
"v_mfma_f32_16x16x32_bf16 v[160:163], v[32:35], v[52:55], v[160:163]\n"
"v_add_f32 v64, v64, v24\n"
"v_fma_f32 v22, v74, s62, v11\n"
"v_mfma_f32_16x16x32_bf16 v[164:167], v[32:35], v[56:59], v[164:167]\n"
"v_exp_f32 v27, v27\n"
"v_add_f32 v64, v64, v25\n"
"v_mfma_f32_16x16x32_bf16 v[168:171], v[32:35], v[76:79], v[168:171]\n"
"v_cvt_pk_bf16_f32 v101, v24, v25\n"
"v_fma_f32 v23, v75, s62, v11\n"
"v_mfma_f32_16x16x32_bf16 v[172:175], v[32:35], v[80:83], v[172:175]\n"
"v_exp_f32 v22, v22\n"
"v_add_f32 v64, v64, v26\n"
"v_mfma_f32_16x16x32_bf16 v[176:179], v[32:35], v[48:51], v[176:179]\n"
"v_exp_f32 v23, v23\n"
"v_mfma_f32_16x16x32_bf16 v[180:183], v[32:35], v[124:127], v[180:183]\n"
"s_waitcnt lgkmcnt(4)\n"
"v_cvt_pk_bf16_f32 v102, v26, v27\n"
"v_add_f32 v64, v64, v27\n"
"v_mfma_f32_16x16x32_bf16 v[184:187], v[40:43], v[52:55], v[184:187]\n"
"v_add_f32 v64, v64, v22\n"
"v_cvt_pk_bf16_f32 v103, v22, v23\n"
"v_add_f32 v64, v64, v23\n"
"v_mfma_f32_16x16x32_bf16 v[188:191], v[40:43], v[56:59], v[188:191]\n"
"ds_read_b128 v[32:35], v1 offset:36864\n"
"v_fma_f32 v22, v60, s62, v12\n"
"v_fma_f32 v23, v61, s62, v12\n"
"v_mfma_f32_16x16x32_bf16 v[192:195], v[40:43], v[76:79], v[192:195]\n"
"v_exp_f32 v22, v22\n"
"v_fma_f32 v24, v62, s62, v12\n"
"v_mfma_f32_16x16x32_bf16 v[196:199], v[40:43], v[80:83], v[196:199]\n"
"v_exp_f32 v23, v23\n"
"v_mfma_f32_16x16x32_bf16 v[200:203], v[40:43], v[48:51], v[200:203]\n"
"v_fma_f32 v25, v63, s62, v12\n"
"v_exp_f32 v24, v24\n"
"v_mfma_f32_16x16x32_bf16 v[204:207], v[40:43], v[124:127], v[204:207]\n"
"s_waitcnt lgkmcnt(3)\n"
"v_add_f32 v65, v22, v23\n"
"v_fma_f32 v26, v28, s62, v12\n"
"v_mfma_f32_16x16x32_bf16 v[208:211], v[44:47], v[52:55], v[208:211]\n"
"v_exp_f32 v25, v25\n"
"v_cvt_pk_bf16_f32 v108, v22, v23\n"
"v_mfma_f32_16x16x32_bf16 v[212:215], v[44:47], v[56:59], v[212:215]\n"
"ds_read_b128 v[40:43], v3 offset:0\n"
"v_fma_f32 v27, v29, s62, v12\n"
"v_mfma_f32_16x16x32_bf16 v[216:219], v[44:47], v[76:79], v[216:219]\n"
"v_exp_f32 v26, v26\n"
"v_add_f32 v65, v65, v24\n"
"v_mfma_f32_16x16x32_bf16 v[220:223], v[44:47], v[80:83], v[220:223]\n"
"v_fma_f32 v22, v30, s62, v12\n"
"v_exp_f32 v27, v27\n"
"v_mfma_f32_16x16x32_bf16 v[224:227], v[44:47], v[48:51], v[224:227]\n"
"v_add_f32 v65, v65, v25\n"
"v_cvt_pk_bf16_f32 v109, v24, v25\n"
"v_mfma_f32_16x16x32_bf16 v[228:231], v[44:47], v[124:127], v[228:231]\n"
"s_waitcnt lgkmcnt(2)\n"
"v_fma_f32 v23, v31, s62, v12\n"
"v_exp_f32 v22, v22\n"
"v_mfma_f32_16x16x32_bf16 v[232:235], v[36:39], v[52:55], v[232:235]\n"
"ds_read_b128 v[44:47], v3 offset:6144\n"
"v_add_f32 v65, v65, v26\n"
"v_mfma_f32_16x16x32_bf16 v[236:239], v[36:39], v[56:59], v[236:239]\n"
"v_exp_f32 v23, v23\n"
"v_cvt_pk_bf16_f32 v110, v26, v27\n"
"v_add_f32 v65, v65, v27\n"
"v_mfma_f32_16x16x32_bf16 v[240:243], v[36:39], v[76:79], v[240:243]\n"
"v_add_f32 v65, v65, v22\n"
"v_cvt_pk_bf16_f32 v111, v22, v23\n"
"v_mfma_f32_16x16x32_bf16 v[244:247], v[36:39], v[80:83], v[244:247]\n"
"v_add_f32 v65, v65, v23\n"
"v_max_f32 v27, v64, v65\n"
"v_cmp_nge_f32 vcc, s63, v27\n"
"v_mfma_f32_16x16x32_bf16 v[248:251], v[36:39], v[48:51], v[248:251]\n"
"v_fma_f32 v22, v120, s62, v13\n"
"v_fma_f32 v23, v121, s62, v13\n"
"v_mfma_f32_16x16x32_bf16 v[252:255], v[36:39], v[124:127], v[252:255]\n"
"s_waitcnt lgkmcnt(2)\n"
"s_cbranch_vccnz .Lrare_7_b\n"
".Lret_7_b:\n"
"ds_read_b128 v[36:39], v1 offset:43008\n"
"v_add_f32 v17, v17, v64\n"
"v_add_f32 v18, v18, v65\n"
"v_mfma_f32_16x16x32_bf16 v[112:115], v[32:35], a[0:3], 0\n"
"v_mfma_f32_16x16x32_bf16 v[116:119], v[32:35], a[24:27], 0\n"
"s_add_u32 m0, s56, 73728\n"
"v_exp_f32 v22, v22\n"
"v_mfma_f32_16x16x32_bf16 v[64:67], v[32:35], a[48:51], 0\n"
"s_add_u32 s61, s57, 110592\n"
"v_fma_f32 v24, v122, s62, v13\n"
"v_mfma_f32_16x16x32_bf16 v[84:87], v[32:35], a[72:75], 0\n"
"s_waitcnt lgkmcnt(2)\n"
"buffer_load_dwordx4 v8, s[48:51], s61 offen nt lds\n"
"v_exp_f32 v23, v23\n"
"v_fma_f32 v25, v123, s62, v13\n"
"v_mfma_f32_16x16x32_bf16 v[72:75], v[32:35], v[40:43], 0\n"
"s_waitcnt lgkmcnt(1)\n"
"v_exp_f32 v24, v24\n"
"v_mfma_f32_16x16x32_bf16 v[60:63], v[32:35], v[44:47], 0\n"
"s_waitcnt lgkmcnt(0)\n"
"ds_read_b128 v[32:35], v2 offset:36864\n"
"v_mfma_f32_16x16x32_bf16 v[80:83], v[36:39], v[40:43], 0\n"
"ds_read_b128 v[40:43], v4 offset:0\n"
"v_mfma_f32_16x16x32_bf16 v[48:51], v[36:39], v[44:47], 0\n"
"ds_read_b128 v[44:47], v4 offset:6144\n"
"v_mfma_f32_16x16x32_bf16 v[28:31], v[36:39], a[0:3], 0\n"
"v_add_f32 v21, v22, v23\n"
"v_fma_f32 v26, v88, s62, v13\n"
"v_mfma_f32_16x16x32_bf16 v[52:55], v[36:39], a[24:27], 0\n"
"v_exp_f32 v25, v25\n"
"v_mfma_f32_16x16x32_bf16 v[56:59], v[36:39], a[48:51], 0\n"
"v_cvt_pk_bf16_f32 v124, v22, v23\n"
"v_fma_f32 v27, v89, s62, v13\n"
"v_mfma_f32_16x16x32_bf16 v[76:79], v[36:39], a[72:75], 0\n"
"s_waitcnt lgkmcnt(2)\n"
"ds_read_b128 v[36:39], v2 offset:43008\n"
"v_mfma_f32_16x16x32_bf16 v[112:115], v[32:35], a[4:7], v[112:115]\n"
"v_exp_f32 v26, v26\n"
"v_mfma_f32_16x16x32_bf16 v[116:119], v[32:35], a[28:31], v[116:119]\n"
"s_add_u32 m0, s56, 74752\n"
"v_add_f32 v21, v21, v24\n"
"v_fma_f32 v22, v90, s62, v13\n"
"v_mfma_f32_16x16x32_bf16 v[64:67], v[32:35], a[52:55], v[64:67]\n"
"s_add_u32 s61, s57, 110720\n"
"v_exp_f32 v27, v27\n"
"v_mfma_f32_16x16x32_bf16 v[84:87], v[32:35], a[76:79], v[84:87]\n"
"s_waitcnt lgkmcnt(2)\n"
"buffer_load_dwordx4 v8, s[48:51], s61 offen nt lds\n"
"v_add_f32 v21, v21, v25\n"
"v_cvt_pk_bf16_f32 v125, v24, v25\n"
"v_mfma_f32_16x16x32_bf16 v[72:75], v[32:35], v[40:43], v[72:75]\n"
"s_waitcnt lgkmcnt(1)\n"
"v_fma_f32 v23, v91, s62, v13\n"
"v_mfma_f32_16x16x32_bf16 v[60:63], v[32:35], v[44:47], v[60:63]\n"
"s_waitcnt lgkmcnt(0)\n"
"ds_read_b128 v[32:35], v1 offset:37888\n"
"v_mfma_f32_16x16x32_bf16 v[80:83], v[36:39], v[40:43], v[80:83]\n"
"ds_read_b128 v[40:43], v3 offset:1024\n"
"v_mfma_f32_16x16x32_bf16 v[48:51], v[36:39], v[44:47], v[48:51]\n"
"ds_read_b128 v[44:47], v3 offset:7168\n"
"s_add_u32 m0, s56, 75776\n"
"v_mfma_f32_16x16x32_bf16 v[28:31], v[36:39], a[4:7], v[28:31]\n"
"s_add_u32 s61, s57, 110848\n"
"v_exp_f32 v22, v22\n"
"v_add_f32 v21, v21, v26\n"
"v_mfma_f32_16x16x32_bf16 v[52:55], v[36:39], a[28:31], v[52:55]\n"
"buffer_load_dwordx4 v8, s[48:51], s61 offen nt lds\n"
"v_exp_f32 v23, v23\n"
"v_mfma_f32_16x16x32_bf16 v[56:59], v[36:39], a[52:55], v[56:59]\n"
"v_cvt_pk_bf16_f32 v126, v26, v27\n"
"v_add_f32 v21, v21, v27\n"
"v_mfma_f32_16x16x32_bf16 v[76:79], v[36:39], a[76:79], v[76:79]\n"
"s_waitcnt lgkmcnt(2)\n"
"ds_read_b128 v[36:39], v1 offset:44032\n"
"v_mfma_f32_16x16x32_bf16 v[112:115], v[32:35], a[8:11], v[112:115]\n"
"v_add_f32 v21, v21, v22\n"
"v_cvt_pk_bf16_f32 v127, v22, v23\n"
"v_mfma_f32_16x16x32_bf16 v[116:119], v[32:35], a[32:35], v[116:119]\n"
"v_add_f32 v21, v21, v23\n"
"v_cmp_nge_f32 vcc, s63, v21\n"
"v_mfma_f32_16x16x32_bf16 v[64:67], v[32:35], a[56:59], v[64:67]\n"
"v_fma_f32 v22, v92, s62, v14\n"
"v_fma_f32 v23, v93, s62, v14\n"
"v_mfma_f32_16x16x32_bf16 v[84:87], v[32:35], a[80:83], v[84:87]\n"
"s_waitcnt lgkmcnt(2)\n"
"s_cbranch_vccnz .Lrare_7_d4\n"
".Lret_7_d4:\n"
"v_add_f32 v19, v19, v21\n"
"v_mfma_f32_16x16x32_bf16 v[72:75], v[32:35], v[40:43], v[72:75]\n"
"s_waitcnt lgkmcnt(1)\n"
"v_exp_f32 v22, v22\n"
"v_mfma_f32_16x16x32_bf16 v[60:63], v[32:35], v[44:47], v[60:63]\n"
"s_waitcnt lgkmcnt(0)\n"
"ds_read_b128 v[32:35], v2 offset:37888\n"
"v_mfma_f32_16x16x32_bf16 v[80:83], v[36:39], v[40:43], v[80:83]\n"
"ds_read_b128 v[40:43], v4 offset:1024\n"
"v_mfma_f32_16x16x32_bf16 v[48:51], v[36:39], v[44:47], v[48:51]\n"
"ds_read_b128 v[44:47], v4 offset:7168\n"
"s_add_u32 m0, s56, 24576\n"
"v_mfma_f32_16x16x32_bf16 v[28:31], v[36:39], a[8:11], v[28:31]\n"
"s_add_u32 s61, s57, 135168\n"
"v_fma_f32 v24, v94, s62, v14\n"
"v_mfma_f32_16x16x32_bf16 v[52:55], v[36:39], a[32:35], v[52:55]\n"
"buffer_load_dwordx4 v7, s[44:47], s61 offen nt lds\n"
"v_exp_f32 v23, v23\n"
"v_fma_f32 v25, v95, s62, v14\n"
"v_mfma_f32_16x16x32_bf16 v[56:59], v[36:39], a[56:59], v[56:59]\n"
"v_exp_f32 v24, v24\n"
"v_mfma_f32_16x16x32_bf16 v[76:79], v[36:39], a[80:83], v[76:79]\n"
"s_waitcnt lgkmcnt(2)\n"
"ds_read_b128 v[36:39], v2 offset:44032\n"
"v_mfma_f32_16x16x32_bf16 v[112:115], v[32:35], a[12:15], v[112:115]\n"
"v_add_f32 v21, v22, v23\n"
"v_fma_f32 v26, v68, s62, v14\n"
"v_mfma_f32_16x16x32_bf16 v[116:119], v[32:35], a[36:39], v[116:119]\n"
"v_exp_f32 v25, v25\n"
"v_mfma_f32_16x16x32_bf16 v[64:67], v[32:35], a[60:63], v[64:67]\n"
"v_cvt_pk_bf16_f32 v120, v22, v23\n"
"v_fma_f32 v27, v69, s62, v14\n"
"v_mfma_f32_16x16x32_bf16 v[84:87], v[32:35], a[84:87], v[84:87]\n"
"s_waitcnt lgkmcnt(2)\n"
"v_exp_f32 v26, v26\n"
"v_mfma_f32_16x16x32_bf16 v[72:75], v[32:35], v[40:43], v[72:75]\n"
"s_waitcnt lgkmcnt(1)\n"
"v_add_f32 v21, v21, v24\n"
"v_fma_f32 v22, v70, s62, v14\n"
"v_mfma_f32_16x16x32_bf16 v[60:63], v[32:35], v[44:47], v[60:63]\n"
"s_waitcnt lgkmcnt(0)\n"
"ds_read_b128 v[32:35], v1 offset:38912\n"
"v_mfma_f32_16x16x32_bf16 v[80:83], v[36:39], v[40:43], v[80:83]\n"
"ds_read_b128 v[40:43], v3 offset:2048\n"
"v_mfma_f32_16x16x32_bf16 v[48:51], v[36:39], v[44:47], v[48:51]\n"
"ds_read_b128 v[44:47], v3 offset:8192\n"
"s_add_u32 m0, s56, 25600\n"
"v_mfma_f32_16x16x32_bf16 v[28:31], v[36:39], a[12:15], v[28:31]\n"
"s_add_u32 s61, s57, 135296\n"
"v_exp_f32 v27, v27\n"
"v_mfma_f32_16x16x32_bf16 v[52:55], v[36:39], a[36:39], v[52:55]\n"
"buffer_load_dwordx4 v7, s[44:47], s61 offen nt lds\n"
"v_add_f32 v21, v21, v25\n"
"v_cvt_pk_bf16_f32 v121, v24, v25\n"
"v_mfma_f32_16x16x32_bf16 v[56:59], v[36:39], a[60:63], v[56:59]\n"
"v_fma_f32 v23, v71, s62, v14\n"
"v_mfma_f32_16x16x32_bf16 v[76:79], v[36:39], a[84:87], v[76:79]\n"
"s_waitcnt lgkmcnt(2)\n"
"ds_read_b128 v[36:39], v1 offset:45056\n"
"v_mfma_f32_16x16x32_bf16 v[112:115], v[32:35], a[16:19], v[112:115]\n"
"v_exp_f32 v22, v22\n"
"v_add_f32 v21, v21, v26\n"
"v_mfma_f32_16x16x32_bf16 v[116:119], v[32:35], a[40:43], v[116:119]\n"
"s_add_u32 m0, s56, 26624\n"
"v_exp_f32 v23, v23\n"
"v_mfma_f32_16x16x32_bf16 v[64:67], v[32:35], a[64:67], v[64:67]\n"
"s_add_u32 s61, s57, 135424\n"
"v_cvt_pk_bf16_f32 v122, v26, v27\n"
"v_add_f32 v21, v21, v27\n"
"v_mfma_f32_16x16x32_bf16 v[84:87], v[32:35], a[88:91], v[84:87]\n"
"s_waitcnt lgkmcnt(2)\n"
"buffer_load_dwordx4 v7, s[44:47], s61 offen nt lds\n"
"v_add_f32 v21, v21, v22\n"
"v_cvt_pk_bf16_f32 v123, v22, v23\n"
"v_mfma_f32_16x16x32_bf16 v[72:75], v[32:35], v[40:43], v[72:75]\n"
"s_waitcnt lgkmcnt(1)\n"
"v_add_f32 v21, v21, v23\n"
"v_cmp_nge_f32 vcc, s63, v21\n"
"v_mfma_f32_16x16x32_bf16 v[60:63], v[32:35], v[44:47], v[60:63]\n"
"s_waitcnt lgkmcnt(0)\n"
"ds_read_b128 v[32:35], v2 offset:38912\n"
"v_mfma_f32_16x16x32_bf16 v[80:83], v[36:39], v[40:43], v[80:83]\n"
"ds_read_b128 v[40:43], v4 offset:2048\n"
"v_mfma_f32_16x16x32_bf16 v[48:51], v[36:39], v[44:47], v[48:51]\n"
"ds_read_b128 v[44:47], v4 offset:8192\n"
"s_cbranch_vccnz .Lrare_7_d5\n"
".Lret_7_d5:\n"
"v_add_f32 v20, v20, v21\n"
"v_mfma_f32_16x16x32_bf16 v[28:31], v[36:39], a[16:19], v[28:31]\n"
"v_mbcnt_lo_u32_b32 v22, -1, 0\n"
"v_mbcnt_hi_u32_b32 v22, -1, v22\n"
"v_mfma_f32_16x16x32_bf16 v[52:55], v[36:39], a[40:43], v[52:55]\n"
"v_and_b32 v22, 31, v22\n"
"v_mfma_f32_16x16x32_bf16 v[56:59], v[36:39], a[64:67], v[56:59]\n"
"s_add_u32 s61, s58, 0\n"
"v_lshlrev_b32 v22, 7, v22\n"
"v_mfma_f32_16x16x32_bf16 v[76:79], v[36:39], a[88:91], v[76:79]\n"
"s_waitcnt lgkmcnt(2)\n"
"ds_read_b128 v[36:39], v2 offset:45056\n"
"s_add_u32 s61, s61, s79\n"
"v_mfma_f32_16x16x32_bf16 v[112:115], v[32:35], a[20:23], v[112:115]\n"
"s_mov_b32 m0, s73\n"
"v_mfma_f32_16x16x32_bf16 v[116:119], v[32:35], a[44:47], v[116:119]\n"
"buffer_load_dword v22, s[44:47], s61 offen lds\n"
"v_mfma_f32_16x16x32_bf16 v[64:67], v[32:35], a[68:71], v[64:67]\n"
"v_mfma_f32_16x16x32_bf16 v[84:87], v[32:35], a[92:95], v[84:87]\n"
"s_waitcnt lgkmcnt(2)\n"
"v_mfma_f32_16x16x32_bf16 v[72:75], v[32:35], v[40:43], v[72:75]\n"
"s_waitcnt vmcnt(15)\n"
"s_waitcnt lgkmcnt(0)\n"
"s_barrier\n"
"v_mfma_f32_16x16x32_bf16 v[60:63], v[32:35], v[44:47], v[60:63]\n"
"v_mfma_f32_16x16x32_bf16 v[80:83], v[36:39], v[40:43], v[80:83]\n"
"ds_read_b64_tr_b16 v[32:33], v5 offset:0\n"
"ds_read_b64_tr_b16 v[34:35], v5 offset:6144\n"
"v_mfma_f32_16x16x32_bf16 v[48:51], v[36:39], v[44:47], v[48:51]\n"
"v_mfma_f32_16x16x32_bf16 v[28:31], v[36:39], a[20:23], v[28:31]\n"
"ds_read_b64_tr_b16 v[40:41], v6 offset:0\n"
"ds_read_b64_tr_b16 v[42:43], v6 offset:6144\n"
"v_mfma_f32_16x16x32_bf16 v[52:55], v[36:39], a[44:47], v[52:55]\n"
"v_mfma_f32_16x16x32_bf16 v[56:59], v[36:39], a[68:71], v[56:59]\n"
"ds_read_b64_tr_b16 v[44:45], v5 offset:512\n"
"ds_read_b64_tr_b16 v[46:47], v5 offset:6656\n"
"v_mfma_f32_16x16x32_bf16 v[76:79], v[36:39], a[92:95], v[76:79]\n"
"s_waitcnt lgkmcnt(4)\n"
"v_mfma_f32_16x16x32_bf16 a[96:99], v[32:35], v[96:99], a[96:99]\n"
"ds_read_b64_tr_b16 v[36:37], v6 offset:512\n"
"ds_read_b64_tr_b16 v[38:39], v6 offset:6656\n"
"v_mfma_f32_16x16x32_bf16 a[100:103], v[32:35], v[104:107], a[100:103]\n"
"v_mfma_f32_16x16x32_bf16 a[104:107], v[32:35], v[100:103], a[104:107]\n"
"v_fma_f32 v22, v112, s62, v9\n"
"v_fma_f32 v23, v113, s62, v9\n"
"v_mfma_f32_16x16x32_bf16 a[108:111], v[32:35], v[108:111], a[108:111]\n"
"v_exp_f32 v22, v22\n"
"v_fma_f32 v24, v114, s62, v9\n"
"v_mfma_f32_16x16x32_bf16 a[112:115], v[32:35], v[124:127], a[112:115]\n"
"v_exp_f32 v23, v23\n"
"v_mfma_f32_16x16x32_bf16 a[116:119], v[32:35], v[120:123], a[116:119]\n"
"s_waitcnt lgkmcnt(4)\n"
"ds_read_b64_tr_b16 v[32:33], v5 offset:1024\n"
"ds_read_b64_tr_b16 v[34:35], v5 offset:7168\n"
"v_mfma_f32_16x16x32_bf16 a[120:123], v[40:43], v[96:99], a[120:123]\n"
"v_fma_f32 v25, v115, s62, v9\n"
"v_exp_f32 v24, v24\n"
"v_mfma_f32_16x16x32_bf16 a[124:127], v[40:43], v[104:107], a[124:127]\n"
"v_add_f32 v21, v22, v23\n"
"v_fma_f32 v26, v28, s62, v9\n"
"v_mfma_f32_16x16x32_bf16 a[128:131], v[40:43], v[100:103], a[128:131]\n"
"v_exp_f32 v25, v25\n"
"v_cvt_pk_bf16_f32 v88, v22, v23\n"
"v_mfma_f32_16x16x32_bf16 a[132:135], v[40:43], v[108:111], a[132:135]\n"
"v_fma_f32 v27, v29, s62, v9\n"
"v_mfma_f32_16x16x32_bf16 a[136:139], v[40:43], v[124:127], a[136:139]\n"
"v_exp_f32 v26, v26\n"
"v_add_f32 v21, v21, v24\n"
"v_fma_f32 v22, v30, s62, v9\n"
"v_mfma_f32_16x16x32_bf16 a[140:143], v[40:43], v[120:123], a[140:143]\n"
"s_waitcnt lgkmcnt(4)\n"
"ds_read_b64_tr_b16 v[40:41], v6 offset:1024\n"
"ds_read_b64_tr_b16 v[42:43], v6 offset:7168\n"
"v_mfma_f32_16x16x32_bf16 a[144:147], v[44:47], v[96:99], a[144:147]\n"
"v_exp_f32 v27, v27\n"
"v_mfma_f32_16x16x32_bf16 a[148:151], v[44:47], v[104:107], a[148:151]\n"
"v_add_f32 v21, v21, v25\n"
"v_cvt_pk_bf16_f32 v89, v24, v25\n"
"v_mfma_f32_16x16x32_bf16 a[152:155], v[44:47], v[100:103], a[152:155]\n"
"v_fma_f32 v23, v31, s62, v9\n"
"v_exp_f32 v22, v22\n"
"v_mfma_f32_16x16x32_bf16 a[156:159], v[44:47], v[108:111], a[156:159]\n"
"v_add_f32 v21, v21, v26\n"
"v_mfma_f32_16x16x32_bf16 a[160:163], v[44:47], v[124:127], a[160:163]\n"
"v_exp_f32 v23, v23\n"
"v_cvt_pk_bf16_f32 v90, v26, v27\n"
"v_add_f32 v21, v21, v27\n"
"v_mfma_f32_16x16x32_bf16 a[164:167], v[44:47], v[120:123], a[164:167]\n"
"s_waitcnt lgkmcnt(4)\n"
"ds_read_b64_tr_b16 v[44:45], v5 offset:1536\n"
"ds_read_b64_tr_b16 v[46:47], v5 offset:7680\n"
"v_mfma_f32_16x16x32_bf16 a[168:171], v[36:39], v[96:99], a[168:171]\n"
"v_add_f32 v21, v21, v22\n"
"v_cvt_pk_bf16_f32 v91, v22, v23\n"
"v_mfma_f32_16x16x32_bf16 a[172:175], v[36:39], v[104:107], a[172:175]\n"
"v_add_f32 v21, v21, v23\n"
"v_fma_f32 v22, v116, s62, v10\n"
"v_fma_f32 v23, v117, s62, v10\n"
"v_mfma_f32_16x16x32_bf16 a[176:179], v[36:39], v[100:103], a[176:179]\n"
"v_exp_f32 v22, v22\n"
"v_mfma_f32_16x16x32_bf16 a[180:183], v[36:39], v[108:111], a[180:183]\n"
"v_fma_f32 v24, v118, s62, v10\n"
"v_exp_f32 v23, v23\n"
"v_mfma_f32_16x16x32_bf16 a[184:187], v[36:39], v[124:127], a[184:187]\n"
"v_fma_f32 v25, v119, s62, v10\n"
"v_mfma_f32_16x16x32_bf16 a[188:191], v[36:39], v[120:123], a[188:191]\n"
"s_waitcnt lgkmcnt(4)\n"
"ds_read_b64_tr_b16 v[36:37], v6 offset:1536\n"
"ds_read_b64_tr_b16 v[38:39], v6 offset:7680\n"
"v_mfma_f32_16x16x32_bf16 a[192:195], v[32:35], v[96:99], a[192:195]\n"
"v_exp_f32 v24, v24\n"
"v_add_f32 v3, v22, v23\n"
"v_mfma_f32_16x16x32_bf16 a[196:199], v[32:35], v[104:107], a[196:199]\n"
"v_fma_f32 v26, v52, s62, v10\n"
"v_exp_f32 v25, v25\n"
"v_mfma_f32_16x16x32_bf16 a[200:203], v[32:35], v[100:103], a[200:203]\n"
"v_cvt_pk_bf16_f32 v92, v22, v23\n"
"v_fma_f32 v27, v53, s62, v10\n"
"v_mfma_f32_16x16x32_bf16 a[204:207], v[32:35], v[108:111], a[204:207]\n"
"v_exp_f32 v26, v26\n"
"v_add_f32 v3, v3, v24\n"
"v_mfma_f32_16x16x32_bf16 a[208:211], v[32:35], v[124:127], a[208:211]\n"
"v_fma_f32 v22, v54, s62, v10\n"
"v_mfma_f32_16x16x32_bf16 a[212:215], v[32:35], v[120:123], a[212:215]\n"
"s_waitcnt lgkmcnt(4)\n"
"ds_read_b64_tr_b16 v[32:33], v5 offset:2048\n"
"ds_read_b64_tr_b16 v[34:35], v5 offset:8192\n"
"v_mfma_f32_16x16x32_bf16 a[216:219], v[40:43], v[96:99], a[216:219]\n"
"v_exp_f32 v27, v27\n"
"v_add_f32 v3, v3, v25\n"
"v_cvt_pk_bf16_f32 v93, v24, v25\n"
"v_mfma_f32_16x16x32_bf16 a[220:223], v[40:43], v[104:107], a[220:223]\n"
"v_fma_f32 v23, v55, s62, v10\n"
"v_mfma_f32_16x16x32_bf16 a[224:227], v[40:43], v[100:103], a[224:227]\n"
"v_exp_f32 v22, v22\n"
"v_add_f32 v3, v3, v26\n"
"v_mfma_f32_16x16x32_bf16 a[228:231], v[40:43], v[108:111], a[228:231]\n"
"v_exp_f32 v23, v23\n"
"v_cvt_pk_bf16_f32 v94, v26, v27\n"
"v_mfma_f32_16x16x32_bf16 a[232:235], v[40:43], v[124:127], a[232:235]\n"
"v_add_f32 v3, v3, v27\n"
"v_add_f32 v3, v3, v22\n"
"v_mfma_f32_16x16x32_bf16 a[236:239], v[40:43], v[120:123], a[236:239]\n"
"s_waitcnt lgkmcnt(4)\n"
"ds_read_b64_tr_b16 v[40:41], v6 offset:2048\n"
"ds_read_b64_tr_b16 v[42:43], v6 offset:8192\n"
"v_mfma_f32_16x16x32_bf16 a[240:243], v[44:47], v[96:99], a[240:243]\n"
"v_cvt_pk_bf16_f32 v95, v22, v23\n"
"v_add_f32 v3, v3, v23\n"
"v_max_f32 v27, v21, v3\n"
"v_mfma_f32_16x16x32_bf16 a[244:247], v[44:47], v[104:107], a[244:247]\n"
"v_cmp_nge_f32 vcc, s63, v27\n"
"v_fma_f32 v22, v64, s62, v11\n"
"v_mfma_f32_16x16x32_bf16 a[248:251], v[44:47], v[100:103], a[248:251]\n"
"v_fma_f32 v23, v65, s62, v11\n"
"v_mfma_f32_16x16x32_bf16 a[252:255], v[44:47], v[108:111], a[252:255]\n"
"s_cbranch_vccnz .Lrare_8_a\n"
".Lret_8_a:\n"
"v_add_f32 v15, v15, v21\n"
"v_add_f32 v16, v16, v3\n"
"v_add_u32 v3, s68, v1\n"
"v_mfma_f32_16x16x32_bf16 v[128:131], v[44:47], v[124:127], v[128:131]\n"
"v_exp_f32 v22, v22\n"
"v_fma_f32 v24, v66, s62, v11\n"
"v_mfma_f32_16x16x32_bf16 v[132:135], v[44:47], v[120:123], v[132:135]\n"
"s_waitcnt lgkmcnt(4)\n"
"ds_read_b64_tr_b16 v[44:45], v5 offset:2560\n"
"ds_read_b64_tr_b16 v[46:47], v5 offset:8704\n"
"v_mfma_f32_16x16x32_bf16 v[136:139], v[36:39], v[96:99], v[136:139]\n"
"v_exp_f32 v23, v23\n"
"v_fma_f32 v25, v67, s62, v11\n"
"v_mfma_f32_16x16x32_bf16 v[140:143], v[36:39], v[104:107], v[140:143]\n"
"v_exp_f32 v24, v24\n"
"v_mfma_f32_16x16x32_bf16 v[144:147], v[36:39], v[100:103], v[144:147]\n"
"v_add_f32 v28, v22, v23\n"
"v_fma_f32 v26, v56, s62, v11\n"
"v_mfma_f32_16x16x32_bf16 v[148:151], v[36:39], v[108:111], v[148:151]\n"
"v_exp_f32 v25, v25\n"
"v_cvt_pk_bf16_f32 v68, v22, v23\n"
"v_mfma_f32_16x16x32_bf16 v[152:155], v[36:39], v[124:127], v[152:155]\n"
"v_fma_f32 v27, v57, s62, v11\n"
"v_exp_f32 v26, v26\n"
"v_mfma_f32_16x16x32_bf16 v[156:159], v[36:39], v[120:123], v[156:159]\n"
"s_waitcnt lgkmcnt(4)\n"
"ds_read_b64_tr_b16 v[36:37], v6 offset:2560\n"
"ds_read_b64_tr_b16 v[38:39], v6 offset:8704\n"
"v_mfma_f32_16x16x32_bf16 v[160:163], v[32:35], v[96:99], v[160:163]\n"
"v_add_f32 v28, v28, v24\n"
"v_fma_f32 v22, v58, s62, v11\n"
"v_mfma_f32_16x16x32_bf16 v[164:167], v[32:35], v[104:107], v[164:167]\n"
"v_exp_f32 v27, v27\n"
"v_add_f32 v28, v28, v25\n"
"v_mfma_f32_16x16x32_bf16 v[168:171], v[32:35], v[100:103], v[168:171]\n"
"v_cvt_pk_bf16_f32 v69, v24, v25\n"
"v_fma_f32 v23, v59, s62, v11\n"
"v_mfma_f32_16x16x32_bf16 v[172:175], v[32:35], v[108:111], v[172:175]\n"
"v_exp_f32 v22, v22\n"
"v_add_f32 v28, v28, v26\n"
"v_mfma_f32_16x16x32_bf16 v[176:179], v[32:35], v[124:127], v[176:179]\n"
"v_exp_f32 v23, v23\n"
"v_mfma_f32_16x16x32_bf16 v[180:183], v[32:35], v[120:123], v[180:183]\n"
"s_waitcnt lgkmcnt(4)\n"
"v_cvt_pk_bf16_f32 v70, v26, v27\n"
"v_add_f32 v28, v28, v27\n"
"v_mfma_f32_16x16x32_bf16 v[184:187], v[40:43], v[96:99], v[184:187]\n"
"v_add_f32 v28, v28, v22\n"
"v_cvt_pk_bf16_f32 v71, v22, v23\n"
"v_add_f32 v28, v28, v23\n"
"v_mfma_f32_16x16x32_bf16 v[188:191], v[40:43], v[104:107], v[188:191]\n"
"ds_read_b128 v[32:35], v1 offset:0\n"
"v_fma_f32 v22, v84, s62, v12\n"
"v_fma_f32 v23, v85, s62, v12\n"
"v_mfma_f32_16x16x32_bf16 v[192:195], v[40:43], v[100:103], v[192:195]\n"
"v_exp_f32 v22, v22\n"
"v_fma_f32 v24, v86, s62, v12\n"
"v_mfma_f32_16x16x32_bf16 v[196:199], v[40:43], v[108:111], v[196:199]\n"
"v_exp_f32 v23, v23\n"
"v_mfma_f32_16x16x32_bf16 v[200:203], v[40:43], v[124:127], v[200:203]\n"
"v_fma_f32 v25, v87, s62, v12\n"
"v_exp_f32 v24, v24\n"
"v_mfma_f32_16x16x32_bf16 v[204:207], v[40:43], v[120:123], v[204:207]\n"
"s_waitcnt lgkmcnt(3)\n"
"v_add_f32 v29, v22, v23\n"
"v_fma_f32 v26, v76, s62, v12\n"
"v_mfma_f32_16x16x32_bf16 v[208:211], v[44:47], v[96:99], v[208:211]\n"
"v_exp_f32 v25, v25\n"
"v_cvt_pk_bf16_f32 v112, v22, v23\n"
"v_mfma_f32_16x16x32_bf16 v[212:215], v[44:47], v[104:107], v[212:215]\n"
"ds_read_b128 v[40:43], v3 offset:0\n"
"v_fma_f32 v27, v77, s62, v12\n"
"v_mfma_f32_16x16x32_bf16 v[216:219], v[44:47], v[100:103], v[216:219]\n"
"v_exp_f32 v26, v26\n"
"v_add_f32 v29, v29, v24\n"
"v_mfma_f32_16x16x32_bf16 v[220:223], v[44:47], v[108:111], v[220:223]\n"
"v_fma_f32 v22, v78, s62, v12\n"
"v_exp_f32 v27, v27\n"
"v_mfma_f32_16x16x32_bf16 v[224:227], v[44:47], v[124:127], v[224:227]\n"
"v_add_f32 v29, v29, v25\n"
"v_cvt_pk_bf16_f32 v113, v24, v25\n"
"v_mfma_f32_16x16x32_bf16 v[228:231], v[44:47], v[120:123], v[228:231]\n"
"s_waitcnt lgkmcnt(2)\n"
"v_fma_f32 v23, v79, s62, v12\n"
"v_exp_f32 v22, v22\n"
"v_mfma_f32_16x16x32_bf16 v[232:235], v[36:39], v[96:99], v[232:235]\n"
"ds_read_b128 v[44:47], v3 offset:6144\n"
"v_add_f32 v29, v29, v26\n"
"v_mfma_f32_16x16x32_bf16 v[236:239], v[36:39], v[104:107], v[236:239]\n"
"v_exp_f32 v23, v23\n"
"v_cvt_pk_bf16_f32 v114, v26, v27\n"
"v_add_f32 v29, v29, v27\n"
"v_mfma_f32_16x16x32_bf16 v[240:243], v[36:39], v[100:103], v[240:243]\n"
"v_add_f32 v29, v29, v22\n"
"v_cvt_pk_bf16_f32 v115, v22, v23\n"
"v_mfma_f32_16x16x32_bf16 v[244:247], v[36:39], v[108:111], v[244:247]\n"
"v_add_f32 v29, v29, v23\n"
"v_max_f32 v27, v28, v29\n"
"v_cmp_nge_f32 vcc, s63, v27\n"
"v_mfma_f32_16x16x32_bf16 v[248:251], v[36:39], v[124:127], v[248:251]\n"
"v_fma_f32 v22, v72, s62, v13\n"
"v_fma_f32 v23, v73, s62, v13\n"
"v_mfma_f32_16x16x32_bf16 v[252:255], v[36:39], v[120:123], v[252:255]\n"
"s_waitcnt lgkmcnt(2)\n"
"s_cbranch_vccnz .Lrare_8_b\n"
".Lret_8_b:\n"
"ds_read_b128 v[36:39], v1 offset:6144\n"
"v_add_f32 v17, v17, v28\n"
"v_add_f32 v18, v18, v29\n"
"v_mfma_f32_16x16x32_bf16 v[116:119], v[32:35], a[0:3], 0\n"
"v_mfma_f32_16x16x32_bf16 v[52:55], v[32:35], a[24:27], 0\n"
"s_add_u32 m0, s56, 86016\n"
"v_exp_f32 v22, v22\n"
"v_mfma_f32_16x16x32_bf16 v[28:31], v[32:35], a[48:51], 0\n"
"s_add_u32 s61, s57, 122880\n"
"v_fma_f32 v24, v74, s62, v13\n"
"v_mfma_f32_16x16x32_bf16 v[64:67], v[32:35], a[72:75], 0\n"
"s_waitcnt lgkmcnt(2)\n"
"buffer_load_dwordx4 v8, s[48:51], s61 offen nt lds\n"
"v_exp_f32 v23, v23\n"
"v_fma_f32 v25, v75, s62, v13\n"
"v_mfma_f32_16x16x32_bf16 v[56:59], v[32:35], v[40:43], 0\n"
"s_waitcnt lgkmcnt(1)\n"
"v_exp_f32 v24, v24\n"
"v_mfma_f32_16x16x32_bf16 v[84:87], v[32:35], v[44:47], 0\n"
"s_waitcnt lgkmcnt(0)\n"
"ds_read_b128 v[32:35], v2 offset:0\n"
"v_mfma_f32_16x16x32_bf16 v[108:111], v[36:39], v[40:43], 0\n"
"ds_read_b128 v[40:43], v4 offset:0\n"
"v_mfma_f32_16x16x32_bf16 v[124:127], v[36:39], v[44:47], 0\n"
"ds_read_b128 v[44:47], v4 offset:6144\n"
"v_mfma_f32_16x16x32_bf16 v[76:79], v[36:39], a[0:3], 0\n"
"v_add_f32 v21, v22, v23\n"
"v_fma_f32 v26, v80, s62, v13\n"
"v_mfma_f32_16x16x32_bf16 v[96:99], v[36:39], a[24:27], 0\n"
"v_exp_f32 v25, v25\n"
"v_mfma_f32_16x16x32_bf16 v[104:107], v[36:39], a[48:51], 0\n"
"v_cvt_pk_bf16_f32 v120, v22, v23\n"
"v_fma_f32 v27, v81, s62, v13\n"
"v_mfma_f32_16x16x32_bf16 v[100:103], v[36:39], a[72:75], 0\n"
"s_waitcnt lgkmcnt(2)\n"
"ds_read_b128 v[36:39], v2 offset:6144\n"
"v_mfma_f32_16x16x32_bf16 v[116:119], v[32:35], a[4:7], v[116:119]\n"
"v_exp_f32 v26, v26\n"
"v_mfma_f32_16x16x32_bf16 v[52:55], v[32:35], a[28:31], v[52:55]\n"
"s_add_u32 m0, s56, 87040\n"
"v_add_f32 v21, v21, v24\n"
"v_fma_f32 v22, v82, s62, v13\n"
"v_mfma_f32_16x16x32_bf16 v[28:31], v[32:35], a[52:55], v[28:31]\n"
"s_add_u32 s61, s57, 123008\n"
"v_exp_f32 v27, v27\n"
"v_mfma_f32_16x16x32_bf16 v[64:67], v[32:35], a[76:79], v[64:67]\n"
"s_waitcnt lgkmcnt(2)\n"
"buffer_load_dwordx4 v8, s[48:51], s61 offen nt lds\n"
"v_add_f32 v21, v21, v25\n"
"v_cvt_pk_bf16_f32 v121, v24, v25\n"
"v_mfma_f32_16x16x32_bf16 v[56:59], v[32:35], v[40:43], v[56:59]\n"
"s_waitcnt lgkmcnt(1)\n"
"v_fma_f32 v23, v83, s62, v13\n"
"v_mfma_f32_16x16x32_bf16 v[84:87], v[32:35], v[44:47], v[84:87]\n"
"s_waitcnt lgkmcnt(0)\n"
"ds_read_b128 v[32:35], v1 offset:1024\n"
"v_mfma_f32_16x16x32_bf16 v[108:111], v[36:39], v[40:43], v[108:111]\n"
"ds_read_b128 v[40:43], v3 offset:1024\n"
"v_mfma_f32_16x16x32_bf16 v[124:127], v[36:39], v[44:47], v[124:127]\n"
"ds_read_b128 v[44:47], v3 offset:7168\n"
"s_add_u32 m0, s56, 88064\n"
"v_mfma_f32_16x16x32_bf16 v[76:79], v[36:39], a[4:7], v[76:79]\n"
"s_add_u32 s61, s57, 123136\n"
"v_exp_f32 v22, v22\n"
"v_add_f32 v21, v21, v26\n"
"v_mfma_f32_16x16x32_bf16 v[96:99], v[36:39], a[28:31], v[96:99]\n"
"buffer_load_dwordx4 v8, s[48:51], s61 offen nt lds\n"
"v_exp_f32 v23, v23\n"
"v_mfma_f32_16x16x32_bf16 v[104:107], v[36:39], a[52:55], v[104:107]\n"
"v_cvt_pk_bf16_f32 v122, v26, v27\n"
"v_add_f32 v21, v21, v27\n"
"v_mfma_f32_16x16x32_bf16 v[100:103], v[36:39], a[76:79], v[100:103]\n"
"s_waitcnt lgkmcnt(2)\n"
"ds_read_b128 v[36:39], v1 offset:7168\n"
"v_mfma_f32_16x16x32_bf16 v[116:119], v[32:35], a[8:11], v[116:119]\n"
"v_add_f32 v21, v21, v22\n"
"v_cvt_pk_bf16_f32 v123, v22, v23\n"
"v_mfma_f32_16x16x32_bf16 v[52:55], v[32:35], a[32:35], v[52:55]\n"
"v_add_f32 v21, v21, v23\n"
"v_cmp_nge_f32 vcc, s63, v21\n"
"v_mfma_f32_16x16x32_bf16 v[28:31], v[32:35], a[56:59], v[28:31]\n"
"v_fma_f32 v22, v60, s62, v14\n"
"v_fma_f32 v23, v61, s62, v14\n"
"v_mfma_f32_16x16x32_bf16 v[64:67], v[32:35], a[80:83], v[64:67]\n"
"s_waitcnt lgkmcnt(2)\n"
"s_cbranch_vccnz .Lrare_8_d4\n"
".Lret_8_d4:\n"
"v_add_f32 v19, v19, v21\n"
"v_mfma_f32_16x16x32_bf16 v[56:59], v[32:35], v[40:43], v[56:59]\n"
"s_waitcnt lgkmcnt(1)\n"
"v_exp_f32 v22, v22\n"
"v_mfma_f32_16x16x32_bf16 v[84:87], v[32:35], v[44:47], v[84:87]\n"
"s_waitcnt lgkmcnt(0)\n"
"ds_read_b128 v[32:35], v2 offset:1024\n"
"v_mfma_f32_16x16x32_bf16 v[108:111], v[36:39], v[40:43], v[108:111]\n"
"ds_read_b128 v[40:43], v4 offset:1024\n"
"v_mfma_f32_16x16x32_bf16 v[124:127], v[36:39], v[44:47], v[124:127]\n"
"ds_read_b128 v[44:47], v4 offset:7168\n"
"s_add_u32 m0, s56, 36864\n"
"v_mfma_f32_16x16x32_bf16 v[76:79], v[36:39], a[8:11], v[76:79]\n"
"s_add_u32 s61, s57, 135168\n"
"v_fma_f32 v24, v62, s62, v14\n"
"v_mfma_f32_16x16x32_bf16 v[96:99], v[36:39], a[32:35], v[96:99]\n"
"buffer_load_dwordx4 v8, s[48:51], s61 offen nt lds\n"
"v_exp_f32 v23, v23\n"
"v_fma_f32 v25, v63, s62, v14\n"
"v_mfma_f32_16x16x32_bf16 v[104:107], v[36:39], a[56:59], v[104:107]\n"
"v_exp_f32 v24, v24\n"
"v_mfma_f32_16x16x32_bf16 v[100:103], v[36:39], a[80:83], v[100:103]\n"
"s_waitcnt lgkmcnt(2)\n"
"ds_read_b128 v[36:39], v2 offset:7168\n"
"v_mfma_f32_16x16x32_bf16 v[116:119], v[32:35], a[12:15], v[116:119]\n"
"v_add_f32 v21, v22, v23\n"
"v_fma_f32 v26, v48, s62, v14\n"
"v_mfma_f32_16x16x32_bf16 v[52:55], v[32:35], a[36:39], v[52:55]\n"
"v_exp_f32 v25, v25\n"
"v_mfma_f32_16x16x32_bf16 v[28:31], v[32:35], a[60:63], v[28:31]\n"
"v_cvt_pk_bf16_f32 v72, v22, v23\n"
"v_fma_f32 v27, v49, s62, v14\n"
"v_mfma_f32_16x16x32_bf16 v[64:67], v[32:35], a[84:87], v[64:67]\n"
"s_waitcnt lgkmcnt(2)\n"
"v_exp_f32 v26, v26\n"
"v_mfma_f32_16x16x32_bf16 v[56:59], v[32:35], v[40:43], v[56:59]\n"
"s_waitcnt lgkmcnt(1)\n"
"v_add_f32 v21, v21, v24\n"
"v_fma_f32 v22, v50, s62, v14\n"
"v_mfma_f32_16x16x32_bf16 v[84:87], v[32:35], v[44:47], v[84:87]\n"
"s_waitcnt lgkmcnt(0)\n"
"ds_read_b128 v[32:35], v1 offset:2048\n"
"v_mfma_f32_16x16x32_bf16 v[108:111], v[36:39], v[40:43], v[108:111]\n"
"ds_read_b128 v[40:43], v3 offset:2048\n"
"v_mfma_f32_16x16x32_bf16 v[124:127], v[36:39], v[44:47], v[124:127]\n"
"ds_read_b128 v[44:47], v3 offset:8192\n"
"s_add_u32 m0, s56, 37888\n"
"v_mfma_f32_16x16x32_bf16 v[76:79], v[36:39], a[12:15], v[76:79]\n"
"s_add_u32 s61, s57, 135296\n"
"v_exp_f32 v27, v27\n"
"v_mfma_f32_16x16x32_bf16 v[96:99], v[36:39], a[36:39], v[96:99]\n"
"buffer_load_dwordx4 v8, s[48:51], s61 offen nt lds\n"
"v_add_f32 v21, v21, v25\n"
"v_cvt_pk_bf16_f32 v73, v24, v25\n"
"v_mfma_f32_16x16x32_bf16 v[104:107], v[36:39], a[60:63], v[104:107]\n"
"v_fma_f32 v23, v51, s62, v14\n"
"v_mfma_f32_16x16x32_bf16 v[100:103], v[36:39], a[84:87], v[100:103]\n"
"s_waitcnt lgkmcnt(2)\n"
"ds_read_b128 v[36:39], v1 offset:8192\n"
"v_mfma_f32_16x16x32_bf16 v[116:119], v[32:35], a[16:19], v[116:119]\n"
"v_exp_f32 v22, v22\n"
"v_add_f32 v21, v21, v26\n"
"v_mfma_f32_16x16x32_bf16 v[52:55], v[32:35], a[40:43], v[52:55]\n"
"s_add_u32 m0, s56, 38912\n"
"v_exp_f32 v23, v23\n"
"v_mfma_f32_16x16x32_bf16 v[28:31], v[32:35], a[64:67], v[28:31]\n"
"s_add_u32 s61, s57, 135424\n"
"v_cvt_pk_bf16_f32 v74, v26, v27\n"
"v_add_f32 v21, v21, v27\n"
"v_mfma_f32_16x16x32_bf16 v[64:67], v[32:35], a[88:91], v[64:67]\n"
"s_waitcnt lgkmcnt(2)\n"
"buffer_load_dwordx4 v8, s[48:51], s61 offen nt lds\n"
"v_add_f32 v21, v21, v22\n"
"v_cvt_pk_bf16_f32 v75, v22, v23\n"
"v_mfma_f32_16x16x32_bf16 v[56:59], v[32:35], v[40:43], v[56:59]\n"
"s_waitcnt lgkmcnt(1)\n"
"v_add_f32 v21, v21, v23\n"
"v_cmp_nge_f32 vcc, s63, v21\n"
"v_mfma_f32_16x16x32_bf16 v[84:87], v[32:35], v[44:47], v[84:87]\n"
"s_waitcnt lgkmcnt(0)\n"
"ds_read_b128 v[32:35], v2 offset:2048\n"
"v_mfma_f32_16x16x32_bf16 v[108:111], v[36:39], v[40:43], v[108:111]\n"
"ds_read_b128 v[40:43], v4 offset:2048\n"
"v_mfma_f32_16x16x32_bf16 v[124:127], v[36:39], v[44:47], v[124:127]\n"
"ds_read_b128 v[44:47], v4 offset:8192\n"
"s_cbranch_vccnz .Lrare_8_d5\n"
".Lret_8_d5:\n"
"v_add_f32 v20, v20, v21\n"
"v_mfma_f32_16x16x32_bf16 v[76:79], v[36:39], a[16:19], v[76:79]\n"
"v_mbcnt_lo_u32_b32 v22, -1, 0\n"
"v_mbcnt_hi_u32_b32 v22, -1, v22\n"
"v_mfma_f32_16x16x32_bf16 v[96:99], v[36:39], a[40:43], v[96:99]\n"
"v_and_b32 v22, 31, v22\n"
"v_mfma_f32_16x16x32_bf16 v[104:107], v[36:39], a[64:67], v[104:107]\n"
"s_add_u32 s61, s58, 0\n"
"v_lshlrev_b32 v22, 7, v22\n"
"v_mfma_f32_16x16x32_bf16 v[100:103], v[36:39], a[88:91], v[100:103]\n"
"s_waitcnt lgkmcnt(2)\n"
"ds_read_b128 v[36:39], v2 offset:8192\n"
"s_add_u32 s61, s61, s79\n"
"v_mfma_f32_16x16x32_bf16 v[116:119], v[32:35], a[20:23], v[116:119]\n"
"s_mov_b32 m0, s73\n"
"v_mfma_f32_16x16x32_bf16 v[52:55], v[32:35], a[44:47], v[52:55]\n"
"buffer_load_dword v22, s[48:51], s61 offen lds\n"
"v_mfma_f32_16x16x32_bf16 v[28:31], v[32:35], a[68:71], v[28:31]\n"
"v_mfma_f32_16x16x32_bf16 v[64:67], v[32:35], a[92:95], v[64:67]\n"
"s_waitcnt lgkmcnt(2)\n"
"v_mfma_f32_16x16x32_bf16 v[56:59], v[32:35], v[40:43], v[56:59]\n"
"s_waitcnt vmcnt(15)\n"
"s_waitcnt lgkmcnt(0)\n"
"s_barrier\n"
"v_mfma_f32_16x16x32_bf16 v[84:87], v[32:35], v[44:47], v[84:87]\n"
"v_mfma_f32_16x16x32_bf16 v[108:111], v[36:39], v[40:43], v[108:111]\n"
"ds_read_b64_tr_b16 v[32:33], v5 offset:12288\n"
"ds_read_b64_tr_b16 v[34:35], v5 offset:18432\n"
"v_mfma_f32_16x16x32_bf16 v[124:127], v[36:39], v[44:47], v[124:127]\n"
"v_mfma_f32_16x16x32_bf16 v[76:79], v[36:39], a[20:23], v[76:79]\n"
"ds_read_b64_tr_b16 v[40:41], v6 offset:12288\n"
"ds_read_b64_tr_b16 v[42:43], v6 offset:18432\n"
"v_mfma_f32_16x16x32_bf16 v[96:99], v[36:39], a[44:47], v[96:99]\n"
"v_mfma_f32_16x16x32_bf16 v[104:107], v[36:39], a[68:71], v[104:107]\n"
"ds_read_b64_tr_b16 v[44:45], v5 offset:12800\n"
"ds_read_b64_tr_b16 v[46:47], v5 offset:18944\n"
"v_mfma_f32_16x16x32_bf16 v[100:103], v[36:39], a[92:95], v[100:103]\n"
"s_waitcnt lgkmcnt(4)\n"
"v_mfma_f32_16x16x32_bf16 a[96:99], v[32:35], v[88:91], a[96:99]\n"
"ds_read_b64_tr_b16 v[36:37], v6 offset:12800\n"
"ds_read_b64_tr_b16 v[38:39], v6 offset:18944\n"
"v_mfma_f32_16x16x32_bf16 a[100:103], v[32:35], v[92:95], a[100:103]\n"
"v_mfma_f32_16x16x32_bf16 a[104:107], v[32:35], v[68:71], a[104:107]\n"
"v_fma_f32 v22, v116, s62, v9\n"
"v_fma_f32 v23, v117, s62, v9\n"
"v_mfma_f32_16x16x32_bf16 a[108:111], v[32:35], v[112:115], a[108:111]\n"
"v_exp_f32 v22, v22\n"
"v_fma_f32 v24, v118, s62, v9\n"
"v_mfma_f32_16x16x32_bf16 a[112:115], v[32:35], v[120:123], a[112:115]\n"
"v_exp_f32 v23, v23\n"
"v_mfma_f32_16x16x32_bf16 a[116:119], v[32:35], v[72:75], a[116:119]\n"
"s_waitcnt lgkmcnt(4)\n"
"ds_read_b64_tr_b16 v[32:33], v5 offset:13312\n"
"ds_read_b64_tr_b16 v[34:35], v5 offset:19456\n"
"v_mfma_f32_16x16x32_bf16 a[120:123], v[40:43], v[88:91], a[120:123]\n"
"v_fma_f32 v25, v119, s62, v9\n"
"v_exp_f32 v24, v24\n"
"v_mfma_f32_16x16x32_bf16 a[124:127], v[40:43], v[92:95], a[124:127]\n"
"v_add_f32 v21, v22, v23\n"
"v_fma_f32 v26, v76, s62, v9\n"
"v_mfma_f32_16x16x32_bf16 a[128:131], v[40:43], v[68:71], a[128:131]\n"
"v_exp_f32 v25, v25\n"
"v_cvt_pk_bf16_f32 v80, v22, v23\n"
"v_mfma_f32_16x16x32_bf16 a[132:135], v[40:43], v[112:115], a[132:135]\n"
"v_fma_f32 v27, v77, s62, v9\n"
"v_mfma_f32_16x16x32_bf16 a[136:139], v[40:43], v[120:123], a[136:139]\n"
"v_exp_f32 v26, v26\n"
"v_add_f32 v21, v21, v24\n"
"v_fma_f32 v22, v78, s62, v9\n"
"v_mfma_f32_16x16x32_bf16 a[140:143], v[40:43], v[72:75], a[140:143]\n"
"s_waitcnt lgkmcnt(4)\n"
"ds_read_b64_tr_b16 v[40:41], v6 offset:13312\n"
"ds_read_b64_tr_b16 v[42:43], v6 offset:19456\n"
"v_mfma_f32_16x16x32_bf16 a[144:147], v[44:47], v[88:91], a[144:147]\n"
"v_exp_f32 v27, v27\n"
"v_mfma_f32_16x16x32_bf16 a[148:151], v[44:47], v[92:95], a[148:151]\n"
"v_add_f32 v21, v21, v25\n"
"v_cvt_pk_bf16_f32 v81, v24, v25\n"
"v_mfma_f32_16x16x32_bf16 a[152:155], v[44:47], v[68:71], a[152:155]\n"
"v_fma_f32 v23, v79, s62, v9\n"
"v_exp_f32 v22, v22\n"
"v_mfma_f32_16x16x32_bf16 a[156:159], v[44:47], v[112:115], a[156:159]\n"
"v_add_f32 v21, v21, v26\n"
"v_mfma_f32_16x16x32_bf16 a[160:163], v[44:47], v[120:123], a[160:163]\n"
"v_exp_f32 v23, v23\n"
"v_cvt_pk_bf16_f32 v82, v26, v27\n"
"v_add_f32 v21, v21, v27\n"
"v_mfma_f32_16x16x32_bf16 a[164:167], v[44:47], v[72:75], a[164:167]\n"
"s_waitcnt lgkmcnt(4)\n"
"ds_read_b64_tr_b16 v[44:45], v5 offset:13824\n"
"ds_read_b64_tr_b16 v[46:47], v5 offset:19968\n"
"v_mfma_f32_16x16x32_bf16 a[168:171], v[36:39], v[88:91], a[168:171]\n"
"v_add_f32 v21, v21, v22\n"
"v_cvt_pk_bf16_f32 v83, v22, v23\n"
"v_mfma_f32_16x16x32_bf16 a[172:175], v[36:39], v[92:95], a[172:175]\n"
"v_add_f32 v21, v21, v23\n"
"v_fma_f32 v22, v52, s62, v10\n"
"v_fma_f32 v23, v53, s62, v10\n"
"v_mfma_f32_16x16x32_bf16 a[176:179], v[36:39], v[68:71], a[176:179]\n"
"v_exp_f32 v22, v22\n"
"v_mfma_f32_16x16x32_bf16 a[180:183], v[36:39], v[112:115], a[180:183]\n"
"v_fma_f32 v24, v54, s62, v10\n"
"v_exp_f32 v23, v23\n"
"v_mfma_f32_16x16x32_bf16 a[184:187], v[36:39], v[120:123], a[184:187]\n"
"v_fma_f32 v25, v55, s62, v10\n"
"v_mfma_f32_16x16x32_bf16 a[188:191], v[36:39], v[72:75], a[188:191]\n"
"s_waitcnt lgkmcnt(4)\n"
"ds_read_b64_tr_b16 v[36:37], v6 offset:13824\n"
"ds_read_b64_tr_b16 v[38:39], v6 offset:19968\n"
"v_mfma_f32_16x16x32_bf16 a[192:195], v[32:35], v[88:91], a[192:195]\n"
"v_exp_f32 v24, v24\n"
"v_add_f32 v3, v22, v23\n"
"v_mfma_f32_16x16x32_bf16 a[196:199], v[32:35], v[92:95], a[196:199]\n"
"v_fma_f32 v26, v96, s62, v10\n"
"v_exp_f32 v25, v25\n"
"v_mfma_f32_16x16x32_bf16 a[200:203], v[32:35], v[68:71], a[200:203]\n"
"v_cvt_pk_bf16_f32 v60, v22, v23\n"
"v_fma_f32 v27, v97, s62, v10\n"
"v_mfma_f32_16x16x32_bf16 a[204:207], v[32:35], v[112:115], a[204:207]\n"
"v_exp_f32 v26, v26\n"
"v_add_f32 v3, v3, v24\n"
"v_mfma_f32_16x16x32_bf16 a[208:211], v[32:35], v[120:123], a[208:211]\n"
"v_fma_f32 v22, v98, s62, v10\n"
"v_mfma_f32_16x16x32_bf16 a[212:215], v[32:35], v[72:75], a[212:215]\n"
"s_waitcnt lgkmcnt(4)\n"
"ds_read_b64_tr_b16 v[32:33], v5 offset:14336\n"
"ds_read_b64_tr_b16 v[34:35], v5 offset:20480\n"
"v_mfma_f32_16x16x32_bf16 a[216:219], v[40:43], v[88:91], a[216:219]\n"
"v_exp_f32 v27, v27\n"
"v_add_f32 v3, v3, v25\n"
"v_cvt_pk_bf16_f32 v61, v24, v25\n"
"v_mfma_f32_16x16x32_bf16 a[220:223], v[40:43], v[92:95], a[220:223]\n"
"v_fma_f32 v23, v99, s62, v10\n"
"v_mfma_f32_16x16x32_bf16 a[224:227], v[40:43], v[68:71], a[224:227]\n"
"v_exp_f32 v22, v22\n"
"v_add_f32 v3, v3, v26\n"
"v_mfma_f32_16x16x32_bf16 a[228:231], v[40:43], v[112:115], a[228:231]\n"
"v_exp_f32 v23, v23\n"
"v_cvt_pk_bf16_f32 v62, v26, v27\n"
"v_mfma_f32_16x16x32_bf16 a[232:235], v[40:43], v[120:123], a[232:235]\n"
"v_add_f32 v3, v3, v27\n"
"v_add_f32 v3, v3, v22\n"
"v_mfma_f32_16x16x32_bf16 a[236:239], v[40:43], v[72:75], a[236:239]\n"
"s_waitcnt lgkmcnt(4)\n"
"ds_read_b64_tr_b16 v[40:41], v6 offset:14336\n"
"ds_read_b64_tr_b16 v[42:43], v6 offset:20480\n"
"v_mfma_f32_16x16x32_bf16 a[240:243], v[44:47], v[88:91], a[240:243]\n"
"v_cvt_pk_bf16_f32 v63, v22, v23\n"
"v_add_f32 v3, v3, v23\n"
"v_max_f32 v27, v21, v3\n"
"v_mfma_f32_16x16x32_bf16 a[244:247], v[44:47], v[92:95], a[244:247]\n"
"v_cmp_nge_f32 vcc, s63, v27\n"
"v_fma_f32 v22, v28, s62, v11\n"
"v_mfma_f32_16x16x32_bf16 a[248:251], v[44:47], v[68:71], a[248:251]\n"
"v_fma_f32 v23, v29, s62, v11\n"
"v_mfma_f32_16x16x32_bf16 a[252:255], v[44:47], v[112:115], a[252:255]\n"
"s_cbranch_vccnz .Lrare_9_a\n"
".Lret_9_a:\n"
"v_add_f32 v15, v15, v21\n"
"v_add_f32 v16, v16, v3\n"
"v_add_u32 v3, s68, v1\n"
"v_mfma_f32_16x16x32_bf16 v[128:131], v[44:47], v[120:123], v[128:131]\n"
"v_exp_f32 v22, v22\n"
"v_fma_f32 v24, v30, s62, v11\n"
"v_mfma_f32_16x16x32_bf16 v[132:135], v[44:47], v[72:75], v[132:135]\n"
"s_waitcnt lgkmcnt(4)\n"
"ds_read_b64_tr_b16 v[44:45], v5 offset:14848\n"
"ds_read_b64_tr_b16 v[46:47], v5 offset:20992\n"
"v_mfma_f32_16x16x32_bf16 v[136:139], v[36:39], v[88:91], v[136:139]\n"
"v_exp_f32 v23, v23\n"
"v_fma_f32 v25, v31, s62, v11\n"
"v_mfma_f32_16x16x32_bf16 v[140:143], v[36:39], v[92:95], v[140:143]\n"
"v_exp_f32 v24, v24\n"
"v_mfma_f32_16x16x32_bf16 v[144:147], v[36:39], v[68:71], v[144:147]\n"
"v_add_f32 v76, v22, v23\n"
"v_fma_f32 v26, v104, s62, v11\n"
"v_mfma_f32_16x16x32_bf16 v[148:151], v[36:39], v[112:115], v[148:151]\n"
"v_exp_f32 v25, v25\n"
"v_cvt_pk_bf16_f32 v48, v22, v23\n"
"v_mfma_f32_16x16x32_bf16 v[152:155], v[36:39], v[120:123], v[152:155]\n"
"v_fma_f32 v27, v105, s62, v11\n"
"v_exp_f32 v26, v26\n"
"v_mfma_f32_16x16x32_bf16 v[156:159], v[36:39], v[72:75], v[156:159]\n"
"s_waitcnt lgkmcnt(4)\n"
"ds_read_b64_tr_b16 v[36:37], v6 offset:14848\n"
"ds_read_b64_tr_b16 v[38:39], v6 offset:20992\n"
"v_mfma_f32_16x16x32_bf16 v[160:163], v[32:35], v[88:91], v[160:163]\n"
"v_add_f32 v76, v76, v24\n"
"v_fma_f32 v22, v106, s62, v11\n"
"v_mfma_f32_16x16x32_bf16 v[164:167], v[32:35], v[92:95], v[164:167]\n"
"v_exp_f32 v27, v27\n"
"v_add_f32 v76, v76, v25\n"
"v_mfma_f32_16x16x32_bf16 v[168:171], v[32:35], v[68:71], v[168:171]\n"
"v_cvt_pk_bf16_f32 v49, v24, v25\n"
"v_fma_f32 v23, v107, s62, v11\n"
"v_mfma_f32_16x16x32_bf16 v[172:175], v[32:35], v[112:115], v[172:175]\n"
"v_exp_f32 v22, v22\n"
"v_add_f32 v76, v76, v26\n"
"v_mfma_f32_16x16x32_bf16 v[176:179], v[32:35], v[120:123], v[176:179]\n"
"v_exp_f32 v23, v23\n"
"v_mfma_f32_16x16x32_bf16 v[180:183], v[32:35], v[72:75], v[180:183]\n"
"s_waitcnt lgkmcnt(4)\n"
"v_cvt_pk_bf16_f32 v50, v26, v27\n"
"v_add_f32 v76, v76, v27\n"
"v_mfma_f32_16x16x32_bf16 v[184:187], v[40:43], v[88:91], v[184:187]\n"
"v_add_f32 v76, v76, v22\n"
"v_cvt_pk_bf16_f32 v51, v22, v23\n"
"v_add_f32 v76, v76, v23\n"
"v_mfma_f32_16x16x32_bf16 v[188:191], v[40:43], v[92:95], v[188:191]\n"
"ds_read_b128 v[32:35], v1 offset:12288\n"
"v_fma_f32 v22, v64, s62, v12\n"
"v_fma_f32 v23, v65, s62, v12\n"
"v_mfma_f32_16x16x32_bf16 v[192:195], v[40:43], v[68:71], v[192:195]\n"
"v_exp_f32 v22, v22\n"
"v_fma_f32 v24, v66, s62, v12\n"
"v_mfma_f32_16x16x32_bf16 v[196:199], v[40:43], v[112:115], v[196:199]\n"
"v_exp_f32 v23, v23\n"
"v_mfma_f32_16x16x32_bf16 v[200:203], v[40:43], v[120:123], v[200:203]\n"
"v_fma_f32 v25, v67, s62, v12\n"
"v_exp_f32 v24, v24\n"
"v_mfma_f32_16x16x32_bf16 v[204:207], v[40:43], v[72:75], v[204:207]\n"
"s_waitcnt lgkmcnt(3)\n"
"v_add_f32 v77, v22, v23\n"
"v_fma_f32 v26, v100, s62, v12\n"
"v_mfma_f32_16x16x32_bf16 v[208:211], v[44:47], v[88:91], v[208:211]\n"
"v_exp_f32 v25, v25\n"
"v_cvt_pk_bf16_f32 v116, v22, v23\n"
"v_mfma_f32_16x16x32_bf16 v[212:215], v[44:47], v[92:95], v[212:215]\n"
"ds_read_b128 v[40:43], v3 offset:0\n"
"v_fma_f32 v27, v101, s62, v12\n"
"v_mfma_f32_16x16x32_bf16 v[216:219], v[44:47], v[68:71], v[216:219]\n"
"v_exp_f32 v26, v26\n"
"v_add_f32 v77, v77, v24\n"
"v_mfma_f32_16x16x32_bf16 v[220:223], v[44:47], v[112:115], v[220:223]\n"
"v_fma_f32 v22, v102, s62, v12\n"
"v_exp_f32 v27, v27\n"
"v_mfma_f32_16x16x32_bf16 v[224:227], v[44:47], v[120:123], v[224:227]\n"
"v_add_f32 v77, v77, v25\n"
"v_cvt_pk_bf16_f32 v117, v24, v25\n"
"v_mfma_f32_16x16x32_bf16 v[228:231], v[44:47], v[72:75], v[228:231]\n"
"s_waitcnt lgkmcnt(2)\n"
"v_fma_f32 v23, v103, s62, v12\n"
"v_exp_f32 v22, v22\n"
"v_mfma_f32_16x16x32_bf16 v[232:235], v[36:39], v[88:91], v[232:235]\n"
"ds_read_b128 v[44:47], v3 offset:6144\n"
"v_add_f32 v77, v77, v26\n"
"v_mfma_f32_16x16x32_bf16 v[236:239], v[36:39], v[92:95], v[236:239]\n"
"v_exp_f32 v23, v23\n"
"v_cvt_pk_bf16_f32 v118, v26, v27\n"
"v_add_f32 v77, v77, v27\n"
"v_mfma_f32_16x16x32_bf16 v[240:243], v[36:39], v[68:71], v[240:243]\n"
"v_add_f32 v77, v77, v22\n"
"v_cvt_pk_bf16_f32 v119, v22, v23\n"
"v_mfma_f32_16x16x32_bf16 v[244:247], v[36:39], v[112:115], v[244:247]\n"
"v_add_f32 v77, v77, v23\n"
"v_max_f32 v27, v76, v77\n"
"v_cmp_nge_f32 vcc, s63, v27\n"
"v_mfma_f32_16x16x32_bf16 v[248:251], v[36:39], v[120:123], v[248:251]\n"
"v_fma_f32 v22, v56, s62, v13\n"
"v_fma_f32 v23, v57, s62, v13\n"
"v_mfma_f32_16x16x32_bf16 v[252:255], v[36:39], v[72:75], v[252:255]\n"
"s_waitcnt lgkmcnt(2)\n"
"s_cbranch_vccnz .Lrare_9_b\n"
".Lret_9_b:\n"
"ds_read_b128 v[36:39], v1 offset:18432\n"
"v_add_f32 v17, v17, v76\n"
"v_add_f32 v18, v18, v77\n"
"v_mfma_f32_16x16x32_bf16 v[52:55], v[32:35], a[0:3], 0\n"
"v_mfma_f32_16x16x32_bf16 v[96:99], v[32:35], a[24:27], 0\n"
"v_xor_b32 v7, s70, v7\n"
"s_add_u32 m0, s56, 49152\n"
"v_exp_f32 v22, v22\n"
"v_mfma_f32_16x16x32_bf16 v[76:79], v[32:35], a[48:51], 0\n"
"s_add_u32 s61, s60, 0\n"
"v_fma_f32 v24, v58, s62, v13\n"
"v_mfma_f32_16x16x32_bf16 v[28:31], v[32:35], a[72:75], 0\n"
"s_waitcnt lgkmcnt(2)\n"
"buffer_load_dwordx4 v7, s[40:43], s61 offen nt lds\n"
"v_exp_f32 v23, v23\n"
"v_fma_f32 v25, v59, s62, v13\n"
"v_mfma_f32_16x16x32_bf16 v[104:107], v[32:35], v[40:43], 0\n"
"s_waitcnt lgkmcnt(1)\n"
"v_exp_f32 v24, v24\n"
"v_mfma_f32_16x16x32_bf16 v[64:67], v[32:35], v[44:47], 0\n"
"s_waitcnt lgkmcnt(0)\n"
"ds_read_b128 v[32:35], v2 offset:12288\n"
"v_mfma_f32_16x16x32_bf16 v[112:115], v[36:39], v[40:43], 0\n"
"ds_read_b128 v[40:43], v4 offset:0\n"
"v_mfma_f32_16x16x32_bf16 v[120:123], v[36:39], v[44:47], 0\n"
"ds_read_b128 v[44:47], v4 offset:6144\n"
"v_mfma_f32_16x16x32_bf16 v[100:103], v[36:39], a[0:3], 0\n"
"v_add_f32 v21, v22, v23\n"
"v_fma_f32 v26, v108, s62, v13\n"
"v_mfma_f32_16x16x32_bf16 v[88:91], v[36:39], a[24:27], 0\n"
"v_exp_f32 v25, v25\n"
"v_mfma_f32_16x16x32_bf16 v[92:95], v[36:39], a[48:51], 0\n"
"v_cvt_pk_bf16_f32 v72, v22, v23\n"
"v_fma_f32 v27, v109, s62, v13\n"
"v_mfma_f32_16x16x32_bf16 v[68:71], v[36:39], a[72:75], 0\n"
"s_waitcnt lgkmcnt(2)\n"
"ds_read_b128 v[36:39], v2 offset:18432\n"
"v_mfma_f32_16x16x32_bf16 v[52:55], v[32:35], a[4:7], v[52:55]\n"
"v_exp_f32 v26, v26\n"
"v_mfma_f32_16x16x32_bf16 v[96:99], v[32:35], a[28:31], v[96:99]\n"
"s_add_u32 m0, s56, 50176\n"
"v_add_f32 v21, v21, v24\n"
"v_fma_f32 v22, v110, s62, v13\n"
"v_mfma_f32_16x16x32_bf16 v[76:79], v[32:35], a[52:55], v[76:79]\n"
"s_add_u32 s61, s60, 128\n"
"v_exp_f32 v27, v27\n"
"v_mfma_f32_16x16x32_bf16 v[28:31], v[32:35], a[76:79], v[28:31]\n"
"s_waitcnt lgkmcnt(2)\n"
"buffer_load_dwordx4 v7, s[40:43], s61 offen nt lds\n"
"v_add_f32 v21, v21, v25\n"
"v_cvt_pk_bf16_f32 v73, v24, v25\n"
"v_mfma_f32_16x16x32_bf16 v[104:107], v[32:35], v[40:43], v[104:107]\n"
"s_waitcnt lgkmcnt(1)\n"
"v_fma_f32 v23, v111, s62, v13\n"
"v_mfma_f32_16x16x32_bf16 v[64:67], v[32:35], v[44:47], v[64:67]\n"
"s_waitcnt lgkmcnt(0)\n"
"ds_read_b128 v[32:35], v1 offset:13312\n"
"v_mfma_f32_16x16x32_bf16 v[112:115], v[36:39], v[40:43], v[112:115]\n"
"ds_read_b128 v[40:43], v3 offset:1024\n"
"v_mfma_f32_16x16x32_bf16 v[120:123], v[36:39], v[44:47], v[120:123]\n"
"ds_read_b128 v[44:47], v3 offset:7168\n"
"s_add_u32 m0, s56, 51200\n"
"v_mfma_f32_16x16x32_bf16 v[100:103], v[36:39], a[4:7], v[100:103]\n"
"s_add_u32 s61, s60, 256\n"
"v_exp_f32 v22, v22\n"
"v_add_f32 v21, v21, v26\n"
"v_mfma_f32_16x16x32_bf16 v[88:91], v[36:39], a[28:31], v[88:91]\n"
"buffer_load_dwordx4 v7, s[40:43], s61 offen nt lds\n"
"v_exp_f32 v23, v23\n"
"v_mfma_f32_16x16x32_bf16 v[92:95], v[36:39], a[52:55], v[92:95]\n"
"v_cvt_pk_bf16_f32 v74, v26, v27\n"
"v_add_f32 v21, v21, v27\n"
"v_mfma_f32_16x16x32_bf16 v[68:71], v[36:39], a[76:79], v[68:71]\n"
"s_waitcnt lgkmcnt(2)\n"
"ds_read_b128 v[36:39], v1 offset:19456\n"
"v_mfma_f32_16x16x32_bf16 v[52:55], v[32:35], a[8:11], v[52:55]\n"
"v_add_f32 v21, v21, v22\n"
"v_cvt_pk_bf16_f32 v75, v22, v23\n"
"v_mfma_f32_16x16x32_bf16 v[96:99], v[32:35], a[32:35], v[96:99]\n"
"v_add_f32 v21, v21, v23\n"
"v_cmp_nge_f32 vcc, s63, v21\n"
"v_mfma_f32_16x16x32_bf16 v[76:79], v[32:35], a[56:59], v[76:79]\n"
"v_fma_f32 v22, v84, s62, v14\n"
"v_fma_f32 v23, v85, s62, v14\n"
"v_mfma_f32_16x16x32_bf16 v[28:31], v[32:35], a[80:83], v[28:31]\n"
"s_waitcnt lgkmcnt(2)\n"
"s_cbranch_vccnz .Lrare_9_d4\n"
".Lret_9_d4:\n"
"v_add_f32 v19, v19, v21\n"
"v_mfma_f32_16x16x32_bf16 v[104:107], v[32:35], v[40:43], v[104:107]\n"
"s_waitcnt lgkmcnt(1)\n"
"v_exp_f32 v22, v22\n"
"v_mfma_f32_16x16x32_bf16 v[64:67], v[32:35], v[44:47], v[64:67]\n"
"s_waitcnt lgkmcnt(0)\n"
"ds_read_b128 v[32:35], v2 offset:13312\n"
"v_mfma_f32_16x16x32_bf16 v[112:115], v[36:39], v[40:43], v[112:115]\n"
"ds_read_b128 v[40:43], v4 offset:1024\n"
"v_mfma_f32_16x16x32_bf16 v[120:123], v[36:39], v[44:47], v[120:123]\n"
"ds_read_b128 v[44:47], v4 offset:7168\n"
"v_xor_b32 v7, 64, v7\n"
"s_add_u32 m0, s56, 0\n"
"v_mfma_f32_16x16x32_bf16 v[100:103], v[36:39], a[8:11], v[100:103]\n"
"s_add_u32 s61, s60, 3072\n"
"v_fma_f32 v24, v86, s62, v14\n"
"v_mfma_f32_16x16x32_bf16 v[88:91], v[36:39], a[32:35], v[88:91]\n"
"buffer_load_dwordx4 v7, s[40:43], s61 offen nt lds\n"
"v_exp_f32 v23, v23\n"
"v_fma_f32 v25, v87, s62, v14\n"
"v_mfma_f32_16x16x32_bf16 v[92:95], v[36:39], a[56:59], v[92:95]\n"
"v_exp_f32 v24, v24\n"
"v_mfma_f32_16x16x32_bf16 v[68:71], v[36:39], a[80:83], v[68:71]\n"
"s_waitcnt lgkmcnt(2)\n"
"ds_read_b128 v[36:39], v2 offset:19456\n"
"v_mfma_f32_16x16x32_bf16 v[52:55], v[32:35], a[12:15], v[52:55]\n"
"v_add_f32 v21, v22, v23\n"
"v_fma_f32 v26, v124, s62, v14\n"
"v_mfma_f32_16x16x32_bf16 v[96:99], v[32:35], a[36:39], v[96:99]\n"
"v_exp_f32 v25, v25\n"
"v_mfma_f32_16x16x32_bf16 v[76:79], v[32:35], a[60:63], v[76:79]\n"
"v_cvt_pk_bf16_f32 v56, v22, v23\n"
"v_fma_f32 v27, v125, s62, v14\n"
"v_mfma_f32_16x16x32_bf16 v[28:31], v[32:35], a[84:87], v[28:31]\n"
"s_waitcnt lgkmcnt(2)\n"
"v_exp_f32 v26, v26\n"
"v_mfma_f32_16x16x32_bf16 v[104:107], v[32:35], v[40:43], v[104:107]\n"
"s_waitcnt lgkmcnt(1)\n"
"v_add_f32 v21, v21, v24\n"
"v_fma_f32 v22, v126, s62, v14\n"
"v_mfma_f32_16x16x32_bf16 v[64:67], v[32:35], v[44:47], v[64:67]\n"
"s_waitcnt lgkmcnt(0)\n"
"ds_read_b128 v[32:35], v1 offset:14336\n"
"v_mfma_f32_16x16x32_bf16 v[112:115], v[36:39], v[40:43], v[112:115]\n"
"ds_read_b128 v[40:43], v3 offset:2048\n"
"v_mfma_f32_16x16x32_bf16 v[120:123], v[36:39], v[44:47], v[120:123]\n"
"ds_read_b128 v[44:47], v3 offset:8192\n"
"s_add_u32 m0, s56, 1024\n"
"v_mfma_f32_16x16x32_bf16 v[100:103], v[36:39], a[12:15], v[100:103]\n"
"s_add_u32 s61, s60, 3200\n"
"v_exp_f32 v27, v27\n"
"v_mfma_f32_16x16x32_bf16 v[88:91], v[36:39], a[36:39], v[88:91]\n"
"buffer_load_dwordx4 v7, s[40:43], s61 offen nt lds\n"
"v_add_f32 v21, v21, v25\n"
"v_cvt_pk_bf16_f32 v57, v24, v25\n"
"v_mfma_f32_16x16x32_bf16 v[92:95], v[36:39], a[60:63], v[92:95]\n"
"v_fma_f32 v23, v127, s62, v14\n"
"v_mfma_f32_16x16x32_bf16 v[68:71], v[36:39], a[84:87], v[68:71]\n"
"s_waitcnt lgkmcnt(2)\n"
"ds_read_b128 v[36:39], v1 offset:20480\n"
"v_mfma_f32_16x16x32_bf16 v[52:55], v[32:35], a[16:19], v[52:55]\n"
"v_exp_f32 v22, v22\n"
"v_add_f32 v21, v21, v26\n"
"v_mfma_f32_16x16x32_bf16 v[96:99], v[32:35], a[40:43], v[96:99]\n"
"s_add_u32 m0, s56, 2048\n"
"v_exp_f32 v23, v23\n"
"v_mfma_f32_16x16x32_bf16 v[76:79], v[32:35], a[64:67], v[76:79]\n"
"s_add_u32 s61, s60, 3328\n"
"v_cvt_pk_bf16_f32 v58, v26, v27\n"
"v_add_f32 v21, v21, v27\n"
"v_mfma_f32_16x16x32_bf16 v[28:31], v[32:35], a[88:91], v[28:31]\n"
"s_waitcnt lgkmcnt(2)\n"
"buffer_load_dwordx4 v7, s[40:43], s61 offen nt lds\n"
"v_add_f32 v21, v21, v22\n"
"v_cvt_pk_bf16_f32 v59, v22, v23\n"
"v_mfma_f32_16x16x32_bf16 v[104:107], v[32:35], v[40:43], v[104:107]\n"
"s_waitcnt lgkmcnt(1)\n"
"v_add_f32 v21, v21, v23\n"
"v_cmp_nge_f32 vcc, s63, v21\n"
"v_mfma_f32_16x16x32_bf16 v[64:67], v[32:35], v[44:47], v[64:67]\n"
"s_waitcnt lgkmcnt(0)\n"
"ds_read_b128 v[32:35], v2 offset:14336\n"
"v_mfma_f32_16x16x32_bf16 v[112:115], v[36:39], v[40:43], v[112:115]\n"
"ds_read_b128 v[40:43], v4 offset:2048\n"
"v_mfma_f32_16x16x32_bf16 v[120:123], v[36:39], v[44:47], v[120:123]\n"
"ds_read_b128 v[44:47], v4 offset:8192\n"
"s_cbranch_vccnz .Lrare_9_d5\n"
".Lret_9_d5:\n"
"v_add_f32 v20, v20, v21\n"
"v_mfma_f32_16x16x32_bf16 v[100:103], v[36:39], a[16:19], v[100:103]\n"
"v_mbcnt_lo_u32_b32 v22, -1, 0\n"
"v_mbcnt_hi_u32_b32 v22, -1, v22\n"
"v_mfma_f32_16x16x32_bf16 v[88:91], v[36:39], a[40:43], v[88:91]\n"
"v_and_b32 v22, 31, v22\n"
"v_mfma_f32_16x16x32_bf16 v[92:95], v[36:39], a[64:67], v[92:95]\n"
"s_add_u32 s61, s58, 16384\n"
"v_lshlrev_b32 v22, 7, v22\n"
"v_mfma_f32_16x16x32_bf16 v[68:71], v[36:39], a[88:91], v[68:71]\n"
"s_waitcnt lgkmcnt(2)\n"
"ds_read_b128 v[36:39], v2 offset:20480\n"
"s_add_u32 s61, s61, s79\n"
"v_mfma_f32_16x16x32_bf16 v[52:55], v[32:35], a[20:23], v[52:55]\n"
"s_mov_b32 m0, s73\n"
"v_mfma_f32_16x16x32_bf16 v[96:99], v[32:35], a[44:47], v[96:99]\n"
"buffer_load_dword v22, s[44:47], s61 offen lds\n"
"v_mfma_f32_16x16x32_bf16 v[76:79], v[32:35], a[68:71], v[76:79]\n"
"v_mfma_f32_16x16x32_bf16 v[28:31], v[32:35], a[92:95], v[28:31]\n"
"s_waitcnt lgkmcnt(2)\n"
"v_mfma_f32_16x16x32_bf16 v[104:107], v[32:35], v[40:43], v[104:107]\n"
"s_waitcnt vmcnt(15)\n"
"s_waitcnt lgkmcnt(0)\n"
"s_barrier\n"
"v_mfma_f32_16x16x32_bf16 v[64:67], v[32:35], v[44:47], v[64:67]\n"
"v_mfma_f32_16x16x32_bf16 v[112:115], v[36:39], v[40:43], v[112:115]\n"
"ds_read_b64_tr_b16 v[32:33], v5 offset:24576\n"
"ds_read_b64_tr_b16 v[34:35], v5 offset:30720\n"
"v_mfma_f32_16x16x32_bf16 v[120:123], v[36:39], v[44:47], v[120:123]\n"
"v_mfma_f32_16x16x32_bf16 v[100:103], v[36:39], a[20:23], v[100:103]\n"
"ds_read_b64_tr_b16 v[40:41], v6 offset:24576\n"
"ds_read_b64_tr_b16 v[42:43], v6 offset:30720\n"
"v_mfma_f32_16x16x32_bf16 v[88:91], v[36:39], a[44:47], v[88:91]\n"
"v_mfma_f32_16x16x32_bf16 v[92:95], v[36:39], a[68:71], v[92:95]\n"
"ds_read_b64_tr_b16 v[44:45], v5 offset:25088\n"
"ds_read_b64_tr_b16 v[46:47], v5 offset:31232\n"
"v_mfma_f32_16x16x32_bf16 v[68:71], v[36:39], a[92:95], v[68:71]\n"
"s_waitcnt lgkmcnt(4)\n"
"v_mfma_f32_16x16x32_bf16 a[96:99], v[32:35], v[80:83], a[96:99]\n"
"ds_read_b64_tr_b16 v[36:37], v6 offset:25088\n"
"ds_read_b64_tr_b16 v[38:39], v6 offset:31232\n"
"v_mfma_f32_16x16x32_bf16 a[100:103], v[32:35], v[60:63], a[100:103]\n"
"v_mfma_f32_16x16x32_bf16 a[104:107], v[32:35], v[48:51], a[104:107]\n"
"v_fma_f32 v22, v52, s62, v9\n"
"v_fma_f32 v23, v53, s62, v9\n"
"v_mfma_f32_16x16x32_bf16 a[108:111], v[32:35], v[116:119], a[108:111]\n"
"v_exp_f32 v22, v22\n"
"v_fma_f32 v24, v54, s62, v9\n"
"v_mfma_f32_16x16x32_bf16 a[112:115], v[32:35], v[72:75], a[112:115]\n"
"v_exp_f32 v23, v23\n"
"v_mfma_f32_16x16x32_bf16 a[116:119], v[32:35], v[56:59], a[116:119]\n"
"s_waitcnt lgkmcnt(4)\n"
"ds_read_b64_tr_b16 v[32:33], v5 offset:25600\n"
"ds_read_b64_tr_b16 v[34:35], v5 offset:31744\n"
"v_mfma_f32_16x16x32_bf16 a[120:123], v[40:43], v[80:83], a[120:123]\n"
"v_fma_f32 v25, v55, s62, v9\n"
"v_exp_f32 v24, v24\n"
"v_mfma_f32_16x16x32_bf16 a[124:127], v[40:43], v[60:63], a[124:127]\n"
"v_add_f32 v21, v22, v23\n"
"v_fma_f32 v26, v100, s62, v9\n"
"v_mfma_f32_16x16x32_bf16 a[128:131], v[40:43], v[48:51], a[128:131]\n"
"v_exp_f32 v25, v25\n"
"v_cvt_pk_bf16_f32 v108, v22, v23\n"
"v_mfma_f32_16x16x32_bf16 a[132:135], v[40:43], v[116:119], a[132:135]\n"
"v_fma_f32 v27, v101, s62, v9\n"
"v_mfma_f32_16x16x32_bf16 a[136:139], v[40:43], v[72:75], a[136:139]\n"
"v_exp_f32 v26, v26\n"
"v_add_f32 v21, v21, v24\n"
"v_fma_f32 v22, v102, s62, v9\n"
"v_mfma_f32_16x16x32_bf16 a[140:143], v[40:43], v[56:59], a[140:143]\n"
"s_waitcnt lgkmcnt(4)\n"
"ds_read_b64_tr_b16 v[40:41], v6 offset:25600\n"
"ds_read_b64_tr_b16 v[42:43], v6 offset:31744\n"
"v_mfma_f32_16x16x32_bf16 a[144:147], v[44:47], v[80:83], a[144:147]\n"
"v_exp_f32 v27, v27\n"
"v_mfma_f32_16x16x32_bf16 a[148:151], v[44:47], v[60:63], a[148:151]\n"
"v_add_f32 v21, v21, v25\n"
"v_cvt_pk_bf16_f32 v109, v24, v25\n"
"v_mfma_f32_16x16x32_bf16 a[152:155], v[44:47], v[48:51], a[152:155]\n"
"v_fma_f32 v23, v103, s62, v9\n"
"v_exp_f32 v22, v22\n"
"v_mfma_f32_16x16x32_bf16 a[156:159], v[44:47], v[116:119], a[156:159]\n"
"v_add_f32 v21, v21, v26\n"
"v_mfma_f32_16x16x32_bf16 a[160:163], v[44:47], v[72:75], a[160:163]\n"
"v_exp_f32 v23, v23\n"
"v_cvt_pk_bf16_f32 v110, v26, v27\n"
"v_add_f32 v21, v21, v27\n"
"v_mfma_f32_16x16x32_bf16 a[164:167], v[44:47], v[56:59], a[164:167]\n"
"s_waitcnt lgkmcnt(4)\n"
"ds_read_b64_tr_b16 v[44:45], v5 offset:26112\n"
"ds_read_b64_tr_b16 v[46:47], v5 offset:32256\n"
"v_mfma_f32_16x16x32_bf16 a[168:171], v[36:39], v[80:83], a[168:171]\n"
"v_add_f32 v21, v21, v22\n"
"v_cvt_pk_bf16_f32 v111, v22, v23\n"
"v_mfma_f32_16x16x32_bf16 a[172:175], v[36:39], v[60:63], a[172:175]\n"
"v_add_f32 v21, v21, v23\n"
"v_fma_f32 v22, v96, s62, v10\n"
"v_fma_f32 v23, v97, s62, v10\n"
"v_mfma_f32_16x16x32_bf16 a[176:179], v[36:39], v[48:51], a[176:179]\n"
"v_exp_f32 v22, v22\n"
"v_mfma_f32_16x16x32_bf16 a[180:183], v[36:39], v[116:119], a[180:183]\n"
"v_fma_f32 v24, v98, s62, v10\n"
"v_exp_f32 v23, v23\n"
"v_mfma_f32_16x16x32_bf16 a[184:187], v[36:39], v[72:75], a[184:187]\n"
"v_fma_f32 v25, v99, s62, v10\n"
"v_mfma_f32_16x16x32_bf16 a[188:191], v[36:39], v[56:59], a[188:191]\n"
"s_waitcnt lgkmcnt(4)\n"
"ds_read_b64_tr_b16 v[36:37], v6 offset:26112\n"
"ds_read_b64_tr_b16 v[38:39], v6 offset:32256\n"
"v_mfma_f32_16x16x32_bf16 a[192:195], v[32:35], v[80:83], a[192:195]\n"
"v_exp_f32 v24, v24\n"
"v_add_f32 v3, v22, v23\n"
"v_mfma_f32_16x16x32_bf16 a[196:199], v[32:35], v[60:63], a[196:199]\n"
"v_fma_f32 v26, v88, s62, v10\n"
"v_exp_f32 v25, v25\n"
"v_mfma_f32_16x16x32_bf16 a[200:203], v[32:35], v[48:51], a[200:203]\n"
"v_cvt_pk_bf16_f32 v84, v22, v23\n"
"v_fma_f32 v27, v89, s62, v10\n"
"v_mfma_f32_16x16x32_bf16 a[204:207], v[32:35], v[116:119], a[204:207]\n"
"v_exp_f32 v26, v26\n"
"v_add_f32 v3, v3, v24\n"
"v_mfma_f32_16x16x32_bf16 a[208:211], v[32:35], v[72:75], a[208:211]\n"
"v_fma_f32 v22, v90, s62, v10\n"
"v_mfma_f32_16x16x32_bf16 a[212:215], v[32:35], v[56:59], a[212:215]\n"
"s_waitcnt lgkmcnt(4)\n"
"ds_read_b64_tr_b16 v[32:33], v5 offset:26624\n"
"ds_read_b64_tr_b16 v[34:35], v5 offset:32768\n"
"v_mfma_f32_16x16x32_bf16 a[216:219], v[40:43], v[80:83], a[216:219]\n"
"v_exp_f32 v27, v27\n"
"v_add_f32 v3, v3, v25\n"
"v_cvt_pk_bf16_f32 v85, v24, v25\n"
"v_mfma_f32_16x16x32_bf16 a[220:223], v[40:43], v[60:63], a[220:223]\n"
"v_fma_f32 v23, v91, s62, v10\n"
"v_mfma_f32_16x16x32_bf16 a[224:227], v[40:43], v[48:51], a[224:227]\n"
"v_exp_f32 v22, v22\n"
"v_add_f32 v3, v3, v26\n"
"v_mfma_f32_16x16x32_bf16 a[228:231], v[40:43], v[116:119], a[228:231]\n"
"v_exp_f32 v23, v23\n"
"v_cvt_pk_bf16_f32 v86, v26, v27\n"
"v_mfma_f32_16x16x32_bf16 a[232:235], v[40:43], v[72:75], a[232:235]\n"
"v_add_f32 v3, v3, v27\n"
"v_add_f32 v3, v3, v22\n"
"v_mfma_f32_16x16x32_bf16 a[236:239], v[40:43], v[56:59], a[236:239]\n"
"s_waitcnt lgkmcnt(4)\n"
"ds_read_b64_tr_b16 v[40:41], v6 offset:26624\n"
"ds_read_b64_tr_b16 v[42:43], v6 offset:32768\n"
"v_mfma_f32_16x16x32_bf16 a[240:243], v[44:47], v[80:83], a[240:243]\n"
"v_cvt_pk_bf16_f32 v87, v22, v23\n"
"v_add_f32 v3, v3, v23\n"
"v_max_f32 v27, v21, v3\n"
"v_mfma_f32_16x16x32_bf16 a[244:247], v[44:47], v[60:63], a[244:247]\n"
"v_cmp_nge_f32 vcc, s63, v27\n"
"v_fma_f32 v22, v76, s62, v11\n"
"v_mfma_f32_16x16x32_bf16 a[248:251], v[44:47], v[48:51], a[248:251]\n"
"v_fma_f32 v23, v77, s62, v11\n"
"v_mfma_f32_16x16x32_bf16 a[252:255], v[44:47], v[116:119], a[252:255]\n"
"s_cbranch_vccnz .Lrare_10_a\n"
".Lret_10_a:\n"
"v_add_f32 v15, v15, v21\n"
"v_add_f32 v16, v16, v3\n"
"v_add_u32 v3, s68, v1\n"
"v_mfma_f32_16x16x32_bf16 v[128:131], v[44:47], v[72:75], v[128:131]\n"
"v_exp_f32 v22, v22\n"
"v_fma_f32 v24, v78, s62, v11\n"
"v_mfma_f32_16x16x32_bf16 v[132:135], v[44:47], v[56:59], v[132:135]\n"
"s_waitcnt lgkmcnt(4)\n"
"ds_read_b64_tr_b16 v[44:45], v5 offset:27136\n"
"ds_read_b64_tr_b16 v[46:47], v5 offset:33280\n"
"v_mfma_f32_16x16x32_bf16 v[136:139], v[36:39], v[80:83], v[136:139]\n"
"v_exp_f32 v23, v23\n"
"v_fma_f32 v25, v79, s62, v11\n"
"v_mfma_f32_16x16x32_bf16 v[140:143], v[36:39], v[60:63], v[140:143]\n"
"v_exp_f32 v24, v24\n"
"v_mfma_f32_16x16x32_bf16 v[144:147], v[36:39], v[48:51], v[144:147]\n"
"v_add_f32 v100, v22, v23\n"
"v_fma_f32 v26, v92, s62, v11\n"
"v_mfma_f32_16x16x32_bf16 v[148:151], v[36:39], v[116:119], v[148:151]\n"
"v_exp_f32 v25, v25\n"
"v_cvt_pk_bf16_f32 v124, v22, v23\n"
"v_mfma_f32_16x16x32_bf16 v[152:155], v[36:39], v[72:75], v[152:155]\n"
"v_fma_f32 v27, v93, s62, v11\n"
"v_exp_f32 v26, v26\n"
"v_mfma_f32_16x16x32_bf16 v[156:159], v[36:39], v[56:59], v[156:159]\n"
"s_waitcnt lgkmcnt(4)\n"
"ds_read_b64_tr_b16 v[36:37], v6 offset:27136\n"
"ds_read_b64_tr_b16 v[38:39], v6 offset:33280\n"
"v_mfma_f32_16x16x32_bf16 v[160:163], v[32:35], v[80:83], v[160:163]\n"
"v_add_f32 v100, v100, v24\n"
"v_fma_f32 v22, v94, s62, v11\n"
"v_mfma_f32_16x16x32_bf16 v[164:167], v[32:35], v[60:63], v[164:167]\n"
"v_exp_f32 v27, v27\n"
"v_add_f32 v100, v100, v25\n"
"v_mfma_f32_16x16x32_bf16 v[168:171], v[32:35], v[48:51], v[168:171]\n"
"v_cvt_pk_bf16_f32 v125, v24, v25\n"
"v_fma_f32 v23, v95, s62, v11\n"
"v_mfma_f32_16x16x32_bf16 v[172:175], v[32:35], v[116:119], v[172:175]\n"
"v_exp_f32 v22, v22\n"
"v_add_f32 v100, v100, v26\n"
"v_mfma_f32_16x16x32_bf16 v[176:179], v[32:35], v[72:75], v[176:179]\n"
"v_exp_f32 v23, v23\n"
"v_mfma_f32_16x16x32_bf16 v[180:183], v[32:35], v[56:59], v[180:183]\n"
"s_waitcnt lgkmcnt(4)\n"
"v_cvt_pk_bf16_f32 v126, v26, v27\n"
"v_add_f32 v100, v100, v27\n"
"v_mfma_f32_16x16x32_bf16 v[184:187], v[40:43], v[80:83], v[184:187]\n"
"v_add_f32 v100, v100, v22\n"
"v_cvt_pk_bf16_f32 v127, v22, v23\n"
"v_add_f32 v100, v100, v23\n"
"v_mfma_f32_16x16x32_bf16 v[188:191], v[40:43], v[60:63], v[188:191]\n"
"ds_read_b128 v[32:35], v1 offset:24576\n"
"v_fma_f32 v22, v28, s62, v12\n"
"v_fma_f32 v23, v29, s62, v12\n"
"v_mfma_f32_16x16x32_bf16 v[192:195], v[40:43], v[48:51], v[192:195]\n"
"v_exp_f32 v22, v22\n"
"v_fma_f32 v24, v30, s62, v12\n"
"v_mfma_f32_16x16x32_bf16 v[196:199], v[40:43], v[116:119], v[196:199]\n"
"v_exp_f32 v23, v23\n"
"v_mfma_f32_16x16x32_bf16 v[200:203], v[40:43], v[72:75], v[200:203]\n"
"v_fma_f32 v25, v31, s62, v12\n"
"v_exp_f32 v24, v24\n"
"v_mfma_f32_16x16x32_bf16 v[204:207], v[40:43], v[56:59], v[204:207]\n"
"s_waitcnt lgkmcnt(3)\n"
"v_add_f32 v101, v22, v23\n"
"v_fma_f32 v26, v68, s62, v12\n"
"v_mfma_f32_16x16x32_bf16 v[208:211], v[44:47], v[80:83], v[208:211]\n"
"v_exp_f32 v25, v25\n"
"v_cvt_pk_bf16_f32 v52, v22, v23\n"
"v_mfma_f32_16x16x32_bf16 v[212:215], v[44:47], v[60:63], v[212:215]\n"
"ds_read_b128 v[40:43], v3 offset:0\n"
"v_fma_f32 v27, v69, s62, v12\n"
"v_mfma_f32_16x16x32_bf16 v[216:219], v[44:47], v[48:51], v[216:219]\n"
"v_exp_f32 v26, v26\n"
"v_add_f32 v101, v101, v24\n"
"v_mfma_f32_16x16x32_bf16 v[220:223], v[44:47], v[116:119], v[220:223]\n"
"v_fma_f32 v22, v70, s62, v12\n"
"v_exp_f32 v27, v27\n"
"v_mfma_f32_16x16x32_bf16 v[224:227], v[44:47], v[72:75], v[224:227]\n"
"v_add_f32 v101, v101, v25\n"
"v_cvt_pk_bf16_f32 v53, v24, v25\n"
"v_mfma_f32_16x16x32_bf16 v[228:231], v[44:47], v[56:59], v[228:231]\n"
"s_waitcnt lgkmcnt(2)\n"
"v_fma_f32 v23, v71, s62, v12\n"
"v_exp_f32 v22, v22\n"
"v_mfma_f32_16x16x32_bf16 v[232:235], v[36:39], v[80:83], v[232:235]\n"
"ds_read_b128 v[44:47], v3 offset:6144\n"
"v_add_f32 v101, v101, v26\n"
"v_mfma_f32_16x16x32_bf16 v[236:239], v[36:39], v[60:63], v[236:239]\n"
"v_exp_f32 v23, v23\n"
"v_cvt_pk_bf16_f32 v54, v26, v27\n"
"v_add_f32 v101, v101, v27\n"
"v_mfma_f32_16x16x32_bf16 v[240:243], v[36:39], v[48:51], v[240:243]\n"
"v_add_f32 v101, v101, v22\n"
"v_cvt_pk_bf16_f32 v55, v22, v23\n"
"v_mfma_f32_16x16x32_bf16 v[244:247], v[36:39], v[116:119], v[244:247]\n"
"v_add_f32 v101, v101, v23\n"
"v_max_f32 v27, v100, v101\n"
"v_cmp_nge_f32 vcc, s63, v27\n"
"v_mfma_f32_16x16x32_bf16 v[248:251], v[36:39], v[72:75], v[248:251]\n"
"v_fma_f32 v22, v104, s62, v13\n"
"v_fma_f32 v23, v105, s62, v13\n"
"v_mfma_f32_16x16x32_bf16 v[252:255], v[36:39], v[56:59], v[252:255]\n"
"s_waitcnt lgkmcnt(2)\n"
"s_cbranch_vccnz .Lrare_10_b\n"
".Lret_10_b:\n"
"ds_read_b128 v[36:39], v1 offset:30720\n"
"v_add_f32 v17, v17, v100\n"
"v_add_f32 v18, v18, v101\n"
"v_mfma_f32_16x16x32_bf16 v[96:99], v[32:35], a[0:3], 0\n"
"v_mfma_f32_16x16x32_bf16 v[88:91], v[32:35], a[24:27], 0\n"
"v_xor_b32 v7, 64, v7\n"
"s_add_u32 m0, s56, 61440\n"
"v_exp_f32 v22, v22\n"
"v_mfma_f32_16x16x32_bf16 v[100:103], v[32:35], a[48:51], 0\n"
"s_add_u32 s61, s60, 6144\n"
"v_fma_f32 v24, v106, s62, v13\n"
"v_mfma_f32_16x16x32_bf16 v[76:79], v[32:35], a[72:75], 0\n"
"s_waitcnt lgkmcnt(2)\n"
"buffer_load_dwordx4 v7, s[40:43], s61 offen nt lds\n"
"v_exp_f32 v23, v23\n"
"v_fma_f32 v25, v107, s62, v13\n"
"v_mfma_f32_16x16x32_bf16 v[92:95], v[32:35], v[40:43], 0\n"
"s_waitcnt lgkmcnt(1)\n"
"v_exp_f32 v24, v24\n"
"v_mfma_f32_16x16x32_bf16 v[28:31], v[32:35], v[44:47], 0\n"
"s_waitcnt lgkmcnt(0)\n"
"ds_read_b128 v[32:35], v2 offset:24576\n"
"v_mfma_f32_16x16x32_bf16 v[116:119], v[36:39], v[40:43], 0\n"
"ds_read_b128 v[40:43], v4 offset:0\n"
"v_mfma_f32_16x16x32_bf16 v[72:75], v[36:39], v[44:47], 0\n"
"ds_read_b128 v[44:47], v4 offset:6144\n"
"v_mfma_f32_16x16x32_bf16 v[68:71], v[36:39], a[0:3], 0\n"
"v_add_f32 v21, v22, v23\n"
"v_fma_f32 v26, v112, s62, v13\n"
"v_mfma_f32_16x16x32_bf16 v[80:83], v[36:39], a[24:27], 0\n"
"v_exp_f32 v25, v25\n"
"v_mfma_f32_16x16x32_bf16 v[60:63], v[36:39], a[48:51], 0\n"
"v_cvt_pk_bf16_f32 v56, v22, v23\n"
"v_fma_f32 v27, v113, s62, v13\n"
"v_mfma_f32_16x16x32_bf16 v[48:51], v[36:39], a[72:75], 0\n"
"s_waitcnt lgkmcnt(2)\n"
"ds_read_b128 v[36:39], v2 offset:30720\n"
"v_mfma_f32_16x16x32_bf16 v[96:99], v[32:35], a[4:7], v[96:99]\n"
"v_exp_f32 v26, v26\n"
"v_mfma_f32_16x16x32_bf16 v[88:91], v[32:35], a[28:31], v[88:91]\n"
"s_add_u32 m0, s56, 62464\n"
"v_add_f32 v21, v21, v24\n"
"v_fma_f32 v22, v114, s62, v13\n"
"v_mfma_f32_16x16x32_bf16 v[100:103], v[32:35], a[52:55], v[100:103]\n"
"s_add_u32 s61, s60, 6272\n"
"v_exp_f32 v27, v27\n"
"v_mfma_f32_16x16x32_bf16 v[76:79], v[32:35], a[76:79], v[76:79]\n"
"s_waitcnt lgkmcnt(2)\n"
"buffer_load_dwordx4 v7, s[40:43], s61 offen nt lds\n"
"v_add_f32 v21, v21, v25\n"
"v_cvt_pk_bf16_f32 v57, v24, v25\n"
"v_mfma_f32_16x16x32_bf16 v[92:95], v[32:35], v[40:43], v[92:95]\n"
"s_waitcnt lgkmcnt(1)\n"
"v_fma_f32 v23, v115, s62, v13\n"
"v_mfma_f32_16x16x32_bf16 v[28:31], v[32:35], v[44:47], v[28:31]\n"
"s_waitcnt lgkmcnt(0)\n"
"ds_read_b128 v[32:35], v1 offset:25600\n"
"v_mfma_f32_16x16x32_bf16 v[116:119], v[36:39], v[40:43], v[116:119]\n"
"ds_read_b128 v[40:43], v3 offset:1024\n"
"v_mfma_f32_16x16x32_bf16 v[72:75], v[36:39], v[44:47], v[72:75]\n"
"ds_read_b128 v[44:47], v3 offset:7168\n"
"s_add_u32 m0, s56, 63488\n"
"v_mfma_f32_16x16x32_bf16 v[68:71], v[36:39], a[4:7], v[68:71]\n"
"s_add_u32 s61, s60, 6400\n"
"v_exp_f32 v22, v22\n"
"v_add_f32 v21, v21, v26\n"
"v_mfma_f32_16x16x32_bf16 v[80:83], v[36:39], a[28:31], v[80:83]\n"
"buffer_load_dwordx4 v7, s[40:43], s61 offen nt lds\n"
"v_exp_f32 v23, v23\n"
"v_mfma_f32_16x16x32_bf16 v[60:63], v[36:39], a[52:55], v[60:63]\n"
"v_cvt_pk_bf16_f32 v58, v26, v27\n"
"v_add_f32 v21, v21, v27\n"
"v_mfma_f32_16x16x32_bf16 v[48:51], v[36:39], a[76:79], v[48:51]\n"
"s_waitcnt lgkmcnt(2)\n"
"ds_read_b128 v[36:39], v1 offset:31744\n"
"v_mfma_f32_16x16x32_bf16 v[96:99], v[32:35], a[8:11], v[96:99]\n"
"v_add_f32 v21, v21, v22\n"
"v_cvt_pk_bf16_f32 v59, v22, v23\n"
"v_mfma_f32_16x16x32_bf16 v[88:91], v[32:35], a[32:35], v[88:91]\n"
"v_add_f32 v21, v21, v23\n"
"v_cmp_nge_f32 vcc, s63, v21\n"
"v_mfma_f32_16x16x32_bf16 v[100:103], v[32:35], a[56:59], v[100:103]\n"
"v_fma_f32 v22, v64, s62, v14\n"
"v_fma_f32 v23, v65, s62, v14\n"
"v_mfma_f32_16x16x32_bf16 v[76:79], v[32:35], a[80:83], v[76:79]\n"
"s_waitcnt lgkmcnt(2)\n"
"s_cbranch_vccnz .Lrare_10_d4\n"
".Lret_10_d4:\n"
"v_add_f32 v19, v19, v21\n"
"v_mfma_f32_16x16x32_bf16 v[92:95], v[32:35], v[40:43], v[92:95]\n"
"s_waitcnt lgkmcnt(1)\n"
"v_exp_f32 v22, v22\n"
"v_mfma_f32_16x16x32_bf16 v[28:31], v[32:35], v[44:47], v[28:31]\n"
"s_waitcnt lgkmcnt(0)\n"
"ds_read_b128 v[32:35], v2 offset:25600\n"
"v_mfma_f32_16x16x32_bf16 v[116:119], v[36:39], v[40:43], v[116:119]\n"
"ds_read_b128 v[40:43], v4 offset:1024\n"
"v_mfma_f32_16x16x32_bf16 v[72:75], v[36:39], v[44:47], v[72:75]\n"
"ds_read_b128 v[44:47], v4 offset:7168\n"
"v_xor_b32 v7, 64, v7\n"
"s_add_u32 m0, s56, 12288\n"
"v_mfma_f32_16x16x32_bf16 v[68:71], v[36:39], a[8:11], v[68:71]\n"
"s_add_u32 s61, s60, 9216\n"
"v_fma_f32 v24, v66, s62, v14\n"
"v_mfma_f32_16x16x32_bf16 v[80:83], v[36:39], a[32:35], v[80:83]\n"
"buffer_load_dwordx4 v7, s[40:43], s61 offen nt lds\n"
"v_exp_f32 v23, v23\n"
"v_fma_f32 v25, v67, s62, v14\n"
"v_mfma_f32_16x16x32_bf16 v[60:63], v[36:39], a[56:59], v[60:63]\n"
"v_exp_f32 v24, v24\n"
"v_mfma_f32_16x16x32_bf16 v[48:51], v[36:39], a[80:83], v[48:51]\n"
"s_waitcnt lgkmcnt(2)\n"
"ds_read_b128 v[36:39], v2 offset:31744\n"
"v_mfma_f32_16x16x32_bf16 v[96:99], v[32:35], a[12:15], v[96:99]\n"
"v_add_f32 v21, v22, v23\n"
"v_fma_f32 v26, v120, s62, v14\n"
"v_mfma_f32_16x16x32_bf16 v[88:91], v[32:35], a[36:39], v[88:91]\n"
"v_exp_f32 v25, v25\n"
"v_mfma_f32_16x16x32_bf16 v[100:103], v[32:35], a[60:63], v[100:103]\n"
"v_cvt_pk_bf16_f32 v104, v22, v23\n"
"v_fma_f32 v27, v121, s62, v14\n"
"v_mfma_f32_16x16x32_bf16 v[76:79], v[32:35], a[84:87], v[76:79]\n"
"s_waitcnt lgkmcnt(2)\n"
"v_exp_f32 v26, v26\n"
"v_mfma_f32_16x16x32_bf16 v[92:95], v[32:35], v[40:43], v[92:95]\n"
"s_waitcnt lgkmcnt(1)\n"
"v_add_f32 v21, v21, v24\n"
"v_fma_f32 v22, v122, s62, v14\n"
"v_mfma_f32_16x16x32_bf16 v[28:31], v[32:35], v[44:47], v[28:31]\n"
"s_waitcnt lgkmcnt(0)\n"
"ds_read_b128 v[32:35], v1 offset:26624\n"
"v_mfma_f32_16x16x32_bf16 v[116:119], v[36:39], v[40:43], v[116:119]\n"
"ds_read_b128 v[40:43], v3 offset:2048\n"
"v_mfma_f32_16x16x32_bf16 v[72:75], v[36:39], v[44:47], v[72:75]\n"
"ds_read_b128 v[44:47], v3 offset:8192\n"
"s_add_u32 m0, s56, 13312\n"
"v_mfma_f32_16x16x32_bf16 v[68:71], v[36:39], a[12:15], v[68:71]\n"
"s_add_u32 s61, s60, 9344\n"
"v_exp_f32 v27, v27\n"
"v_mfma_f32_16x16x32_bf16 v[80:83], v[36:39], a[36:39], v[80:83]\n"
"buffer_load_dwordx4 v7, s[40:43], s61 offen nt lds\n"
"v_add_f32 v21, v21, v25\n"
"v_cvt_pk_bf16_f32 v105, v24, v25\n"
"v_mfma_f32_16x16x32_bf16 v[60:63], v[36:39], a[60:63], v[60:63]\n"
"v_fma_f32 v23, v123, s62, v14\n"
"v_mfma_f32_16x16x32_bf16 v[48:51], v[36:39], a[84:87], v[48:51]\n"
"s_waitcnt lgkmcnt(2)\n"
"ds_read_b128 v[36:39], v1 offset:32768\n"
"v_mfma_f32_16x16x32_bf16 v[96:99], v[32:35], a[16:19], v[96:99]\n"
"v_exp_f32 v22, v22\n"
"v_add_f32 v21, v21, v26\n"
"v_mfma_f32_16x16x32_bf16 v[88:91], v[32:35], a[40:43], v[88:91]\n"
"s_add_u32 m0, s56, 14336\n"
"v_exp_f32 v23, v23\n"
"v_mfma_f32_16x16x32_bf16 v[100:103], v[32:35], a[64:67], v[100:103]\n"
"s_add_u32 s61, s60, 9472\n"
"v_cvt_pk_bf16_f32 v106, v26, v27\n"
"v_add_f32 v21, v21, v27\n"
"v_mfma_f32_16x16x32_bf16 v[76:79], v[32:35], a[88:91], v[76:79]\n"
"s_waitcnt lgkmcnt(2)\n"
"buffer_load_dwordx4 v7, s[40:43], s61 offen nt lds\n"
"v_add_f32 v21, v21, v22\n"
"v_cvt_pk_bf16_f32 v107, v22, v23\n"
"v_mfma_f32_16x16x32_bf16 v[92:95], v[32:35], v[40:43], v[92:95]\n"
"s_waitcnt lgkmcnt(1)\n"
"v_add_f32 v21, v21, v23\n"
"v_cmp_nge_f32 vcc, s63, v21\n"
"v_mfma_f32_16x16x32_bf16 v[28:31], v[32:35], v[44:47], v[28:31]\n"
"s_waitcnt lgkmcnt(0)\n"
"ds_read_b128 v[32:35], v2 offset:26624\n"
"v_mfma_f32_16x16x32_bf16 v[116:119], v[36:39], v[40:43], v[116:119]\n"
"ds_read_b128 v[40:43], v4 offset:2048\n"
"v_mfma_f32_16x16x32_bf16 v[72:75], v[36:39], v[44:47], v[72:75]\n"
"ds_read_b128 v[44:47], v4 offset:8192\n"
"s_cbranch_vccnz .Lrare_10_d5\n"
".Lret_10_d5:\n"
"v_add_f32 v20, v20, v21\n"
"v_mfma_f32_16x16x32_bf16 v[68:71], v[36:39], a[16:19], v[68:71]\n"
"v_mfma_f32_16x16x32_bf16 v[80:83], v[36:39], a[40:43], v[80:83]\n"
"v_mfma_f32_16x16x32_bf16 v[60:63], v[36:39], a[64:67], v[60:63]\n"
"v_mfma_f32_16x16x32_bf16 v[48:51], v[36:39], a[88:91], v[48:51]\n"
"s_waitcnt lgkmcnt(2)\n"
"ds_read_b128 v[36:39], v2 offset:32768\n"
"v_mfma_f32_16x16x32_bf16 v[96:99], v[32:35], a[20:23], v[96:99]\n"
"v_mfma_f32_16x16x32_bf16 v[88:91], v[32:35], a[44:47], v[88:91]\n"
"v_mfma_f32_16x16x32_bf16 v[100:103], v[32:35], a[68:71], v[100:103]\n"
"v_mfma_f32_16x16x32_bf16 v[76:79], v[32:35], a[92:95], v[76:79]\n"
"s_waitcnt lgkmcnt(2)\n"
"v_mfma_f32_16x16x32_bf16 v[92:95], v[32:35], v[40:43], v[92:95]\n"
"s_waitcnt vmcnt(17)\n"
"s_waitcnt lgkmcnt(0)\n"
"s_barrier\n"
"v_mfma_f32_16x16x32_bf16 v[28:31], v[32:35], v[44:47], v[28:31]\n"
"v_mfma_f32_16x16x32_bf16 v[116:119], v[36:39], v[40:43], v[116:119]\n"
"ds_read_b64_tr_b16 v[32:33], v5 offset:36864\n"
"ds_read_b64_tr_b16 v[34:35], v5 offset:43008\n"
"v_mfma_f32_16x16x32_bf16 v[72:75], v[36:39], v[44:47], v[72:75]\n"
"v_mfma_f32_16x16x32_bf16 v[68:71], v[36:39], a[20:23], v[68:71]\n"
"ds_read_b64_tr_b16 v[40:41], v6 offset:36864\n"
"ds_read_b64_tr_b16 v[42:43], v6 offset:43008\n"
"v_mfma_f32_16x16x32_bf16 v[80:83], v[36:39], a[44:47], v[80:83]\n"
"v_mfma_f32_16x16x32_bf16 v[60:63], v[36:39], a[68:71], v[60:63]\n"
"ds_read_b64_tr_b16 v[44:45], v5 offset:37376\n"
"ds_read_b64_tr_b16 v[46:47], v5 offset:43520\n"
"v_mfma_f32_16x16x32_bf16 v[48:51], v[36:39], a[92:95], v[48:51]\n"
"s_waitcnt lgkmcnt(4)\n"
"v_mfma_f32_16x16x32_bf16 a[96:99], v[32:35], v[108:111], a[96:99]\n"
"ds_read_b64_tr_b16 v[36:37], v6 offset:37376\n"
"ds_read_b64_tr_b16 v[38:39], v6 offset:43520\n"
"v_mfma_f32_16x16x32_bf16 a[100:103], v[32:35], v[84:87], a[100:103]\n"
"v_xor_b32 v7, 64, v7\n"
"s_add_u32 m0, s56, 73728\n"
"v_mfma_f32_16x16x32_bf16 a[104:107], v[32:35], v[124:127], a[104:107]\n"
"s_add_u32 s61, s60, 12288\n"
"v_fma_f32 v22, v96, s62, v9\n"
"v_fma_f32 v23, v97, s62, v9\n"
"v_mfma_f32_16x16x32_bf16 a[108:111], v[32:35], v[52:55], a[108:111]\n"
"buffer_load_dwordx4 v7, s[40:43], s61 offen nt lds\n"
"v_exp_f32 v22, v22\n"
"v_fma_f32 v24, v98, s62, v9\n"
"v_mfma_f32_16x16x32_bf16 a[112:115], v[32:35], v[56:59], a[112:115]\n"
"v_exp_f32 v23, v23\n"
"v_mfma_f32_16x16x32_bf16 a[116:119], v[32:35], v[104:107], a[116:119]\n"
"s_waitcnt lgkmcnt(4)\n"
"ds_read_b64_tr_b16 v[32:33], v5 offset:37888\n"
"ds_read_b64_tr_b16 v[34:35], v5 offset:44032\n"
"v_mfma_f32_16x16x32_bf16 a[120:123], v[40:43], v[108:111], a[120:123]\n"
"v_fma_f32 v25, v99, s62, v9\n"
"v_exp_f32 v24, v24\n"
"v_mfma_f32_16x16x32_bf16 a[124:127], v[40:43], v[84:87], a[124:127]\n"
"s_add_u32 m0, s56, 74752\n"
"v_add_f32 v21, v22, v23\n"
"v_fma_f32 v26, v68, s62, v9\n"
"v_mfma_f32_16x16x32_bf16 a[128:131], v[40:43], v[124:127], a[128:131]\n"
"s_add_u32 s61, s60, 12416\n"
"v_exp_f32 v25, v25\n"
"v_cvt_pk_bf16_f32 v112, v22, v23\n"
"v_mfma_f32_16x16x32_bf16 a[132:135], v[40:43], v[52:55], a[132:135]\n"
"buffer_load_dwordx4 v7, s[40:43], s61 offen nt lds\n"
"v_fma_f32 v27, v69, s62, v9\n"
"v_mfma_f32_16x16x32_bf16 a[136:139], v[40:43], v[56:59], a[136:139]\n"
"v_exp_f32 v26, v26\n"
"v_add_f32 v21, v21, v24\n"
"v_fma_f32 v22, v70, s62, v9\n"
"v_mfma_f32_16x16x32_bf16 a[140:143], v[40:43], v[104:107], a[140:143]\n"
"s_waitcnt lgkmcnt(4)\n"
"ds_read_b64_tr_b16 v[40:41], v6 offset:37888\n"
"ds_read_b64_tr_b16 v[42:43], v6 offset:44032\n"
"v_mfma_f32_16x16x32_bf16 a[144:147], v[44:47], v[108:111], a[144:147]\n"
"v_exp_f32 v27, v27\n"
"v_mfma_f32_16x16x32_bf16 a[148:151], v[44:47], v[84:87], a[148:151]\n"
"s_add_u32 m0, s56, 75776\n"
"v_add_f32 v21, v21, v25\n"
"v_cvt_pk_bf16_f32 v113, v24, v25\n"
"v_mfma_f32_16x16x32_bf16 a[152:155], v[44:47], v[124:127], a[152:155]\n"
"s_add_u32 s61, s60, 12544\n"
"v_fma_f32 v23, v71, s62, v9\n"
"v_exp_f32 v22, v22\n"
"v_mfma_f32_16x16x32_bf16 a[156:159], v[44:47], v[52:55], a[156:159]\n"
"buffer_load_dwordx4 v7, s[40:43], s61 offen nt lds\n"
"v_add_f32 v21, v21, v26\n"
"v_mfma_f32_16x16x32_bf16 a[160:163], v[44:47], v[56:59], a[160:163]\n"
"v_exp_f32 v23, v23\n"
"v_cvt_pk_bf16_f32 v114, v26, v27\n"
"v_add_f32 v21, v21, v27\n"
"v_mfma_f32_16x16x32_bf16 a[164:167], v[44:47], v[104:107], a[164:167]\n"
"s_waitcnt lgkmcnt(4)\n"
"ds_read_b64_tr_b16 v[44:45], v5 offset:38400\n"
"ds_read_b64_tr_b16 v[46:47], v5 offset:44544\n"
"v_mfma_f32_16x16x32_bf16 a[168:171], v[36:39], v[108:111], a[168:171]\n"
"v_add_f32 v21, v21, v22\n"
"v_cvt_pk_bf16_f32 v115, v22, v23\n"
"v_mfma_f32_16x16x32_bf16 a[172:175], v[36:39], v[84:87], a[172:175]\n"
"v_xor_b32 v7, 64, v7\n"
"s_add_u32 m0, s56, 24576\n"
"v_add_f32 v21, v21, v23\n"
"v_fma_f32 v22, v88, s62, v10\n"
"v_fma_f32 v23, v89, s62, v10\n"
"v_mfma_f32_16x16x32_bf16 a[176:179], v[36:39], v[124:127], a[176:179]\n"
"s_add_u32 s61, s60, 15360\n"
"v_exp_f32 v22, v22\n"
"v_mfma_f32_16x16x32_bf16 a[180:183], v[36:39], v[52:55], a[180:183]\n"
"buffer_load_dwordx4 v7, s[40:43], s61 offen nt lds\n"
"v_fma_f32 v24, v90, s62, v10\n"
"v_exp_f32 v23, v23\n"
"v_mfma_f32_16x16x32_bf16 a[184:187], v[36:39], v[56:59], a[184:187]\n"
"v_fma_f32 v25, v91, s62, v10\n"
"v_mfma_f32_16x16x32_bf16 a[188:191], v[36:39], v[104:107], a[188:191]\n"
"s_waitcnt lgkmcnt(4)\n"
"ds_read_b64_tr_b16 v[36:37], v6 offset:38400\n"
"ds_read_b64_tr_b16 v[38:39], v6 offset:44544\n"
"v_mfma_f32_16x16x32_bf16 a[192:195], v[32:35], v[108:111], a[192:195]\n"
"v_exp_f32 v24, v24\n"
"v_add_f32 v3, v22, v23\n"
"v_mfma_f32_16x16x32_bf16 a[196:199], v[32:35], v[84:87], a[196:199]\n"
"s_add_u32 m0, s56, 25600\n"
"v_fma_f32 v26, v80, s62, v10\n"
"v_exp_f32 v25, v25\n"
"v_mfma_f32_16x16x32_bf16 a[200:203], v[32:35], v[124:127], a[200:203]\n"
"s_add_u32 s61, s60, 15488\n"
"v_cvt_pk_bf16_f32 v64, v22, v23\n"
"v_fma_f32 v27, v81, s62, v10\n"
"v_mfma_f32_16x16x32_bf16 a[204:207], v[32:35], v[52:55], a[204:207]\n"
"buffer_load_dwordx4 v7, s[40:43], s61 offen nt lds\n"
"v_exp_f32 v26, v26\n"
"v_add_f32 v3, v3, v24\n"
"v_mfma_f32_16x16x32_bf16 a[208:211], v[32:35], v[56:59], a[208:211]\n"
"v_fma_f32 v22, v82, s62, v10\n"
"v_mfma_f32_16x16x32_bf16 a[212:215], v[32:35], v[104:107], a[212:215]\n"
"s_waitcnt lgkmcnt(4)\n"
"ds_read_b64_tr_b16 v[32:33], v5 offset:38912\n"
"ds_read_b64_tr_b16 v[34:35], v5 offset:45056\n"
"v_mfma_f32_16x16x32_bf16 a[216:219], v[40:43], v[108:111], a[216:219]\n"
"v_exp_f32 v27, v27\n"
"v_add_f32 v3, v3, v25\n"
"v_cvt_pk_bf16_f32 v65, v24, v25\n"
"v_mfma_f32_16x16x32_bf16 a[220:223], v[40:43], v[84:87], a[220:223]\n"
"s_add_u32 m0, s56, 26624\n"
"v_fma_f32 v23, v83, s62, v10\n"
"v_mfma_f32_16x16x32_bf16 a[224:227], v[40:43], v[124:127], a[224:227]\n"
"s_add_u32 s61, s60, 15616\n"
"v_exp_f32 v22, v22\n"
"v_add_f32 v3, v3, v26\n"
"v_mfma_f32_16x16x32_bf16 a[228:231], v[40:43], v[52:55], a[228:231]\n"
"buffer_load_dwordx4 v7, s[40:43], s61 offen nt lds\n"
"v_exp_f32 v23, v23\n"
"v_cvt_pk_bf16_f32 v66, v26, v27\n"
"v_mfma_f32_16x16x32_bf16 a[232:235], v[40:43], v[56:59], a[232:235]\n"
"v_add_f32 v3, v3, v27\n"
"v_add_f32 v3, v3, v22\n"
"v_mfma_f32_16x16x32_bf16 a[236:239], v[40:43], v[104:107], a[236:239]\n"
"s_waitcnt lgkmcnt(4)\n"
"ds_read_b64_tr_b16 v[40:41], v6 offset:38912\n"
"ds_read_b64_tr_b16 v[42:43], v6 offset:45056\n"
"v_mfma_f32_16x16x32_bf16 a[240:243], v[44:47], v[108:111], a[240:243]\n"
"v_cvt_pk_bf16_f32 v67, v22, v23\n"
"v_add_f32 v3, v3, v23\n"
"v_max_f32 v27, v21, v3\n"
"v_mfma_f32_16x16x32_bf16 a[244:247], v[44:47], v[84:87], a[244:247]\n"
"v_xor_b32 v7, 64, v7\n"
"s_add_u32 m0, s69, 0\n"
"v_cmp_nge_f32 vcc, s63, v27\n"
"v_fma_f32 v22, v100, s62, v11\n"
"v_mfma_f32_16x16x32_bf16 a[248:251], v[44:47], v[124:127], a[248:251]\n"
"s_add_u32 s61, s60, 24576\n"
"v_fma_f32 v23, v101, s62, v11\n"
"v_mfma_f32_16x16x32_bf16 a[252:255], v[44:47], v[52:55], a[252:255]\n"
"buffer_load_dwordx4 v7, s[40:43], s61 offen nt lds\n"
"s_cbranch_vccnz .Lrare_11_a\n"
".Lret_11_a:\n"
"v_add_f32 v15, v15, v21\n"
"v_add_f32 v16, v16, v3\n"
"v_add_u32 v3, s68, v1\n"
"v_mfma_f32_16x16x32_bf16 v[128:131], v[44:47], v[56:59], v[128:131]\n"
"v_exp_f32 v22, v22\n"
"v_fma_f32 v24, v102, s62, v11\n"
"v_mfma_f32_16x16x32_bf16 v[132:135], v[44:47], v[104:107], v[132:135]\n"
"s_waitcnt lgkmcnt(4)\n"
"ds_read_b64_tr_b16 v[44:45], v5 offset:39424\n"
"ds_read_b64_tr_b16 v[46:47], v5 offset:45568\n"
"v_mfma_f32_16x16x32_bf16 v[136:139], v[36:39], v[108:111], v[136:139]\n"
"v_exp_f32 v23, v23\n"
"v_fma_f32 v25, v103, s62, v11\n"
"v_mfma_f32_16x16x32_bf16 v[140:143], v[36:39], v[84:87], v[140:143]\n"
"s_add_u32 m0, s69, 1024\n"
"v_exp_f32 v24, v24\n"
"v_mfma_f32_16x16x32_bf16 v[144:147], v[36:39], v[124:127], v[144:147]\n"
"s_add_u32 s61, s60, 24704\n"
"v_add_f32 v68, v22, v23\n"
"v_fma_f32 v26, v60, s62, v11\n"
"v_mfma_f32_16x16x32_bf16 v[148:151], v[36:39], v[52:55], v[148:151]\n"
"buffer_load_dwordx4 v7, s[40:43], s61 offen nt lds\n"
"v_exp_f32 v25, v25\n"
"v_cvt_pk_bf16_f32 v120, v22, v23\n"
"v_mfma_f32_16x16x32_bf16 v[152:155], v[36:39], v[56:59], v[152:155]\n"
"v_fma_f32 v27, v61, s62, v11\n"
"v_exp_f32 v26, v26\n"
"v_mfma_f32_16x16x32_bf16 v[156:159], v[36:39], v[104:107], v[156:159]\n"
"s_waitcnt lgkmcnt(4)\n"
"ds_read_b64_tr_b16 v[36:37], v6 offset:39424\n"
"ds_read_b64_tr_b16 v[38:39], v6 offset:45568\n"
"v_mfma_f32_16x16x32_bf16 v[160:163], v[32:35], v[108:111], v[160:163]\n"
"v_add_f32 v68, v68, v24\n"
"v_fma_f32 v22, v62, s62, v11\n"
"v_mfma_f32_16x16x32_bf16 v[164:167], v[32:35], v[84:87], v[164:167]\n"
"v_exp_f32 v27, v27\n"
"v_add_f32 v68, v68, v25\n"
"v_mfma_f32_16x16x32_bf16 v[168:171], v[32:35], v[124:127], v[168:171]\n"
"s_add_u32 m0, s69, 2048\n"
"v_cvt_pk_bf16_f32 v121, v24, v25\n"
"v_fma_f32 v23, v63, s62, v11\n"
"v_mfma_f32_16x16x32_bf16 v[172:175], v[32:35], v[52:55], v[172:175]\n"
"s_add_u32 s61, s60, 24832\n"
"v_exp_f32 v22, v22\n"
"v_add_f32 v68, v68, v26\n"
"v_mfma_f32_16x16x32_bf16 v[176:179], v[32:35], v[56:59], v[176:179]\n"
"buffer_load_dwordx4 v7, s[40:43], s61 offen nt lds\n"
"v_exp_f32 v23, v23\n"
"v_mfma_f32_16x16x32_bf16 v[180:183], v[32:35], v[104:107], v[180:183]\n"
"s_waitcnt lgkmcnt(4)\n"
"v_cvt_pk_bf16_f32 v122, v26, v27\n"
"v_add_f32 v68, v68, v27\n"
"v_mfma_f32_16x16x32_bf16 v[184:187], v[40:43], v[108:111], v[184:187]\n"
"v_add_f32 v68, v68, v22\n"
"v_cvt_pk_bf16_f32 v123, v22, v23\n"
"v_add_f32 v68, v68, v23\n"
"v_mfma_f32_16x16x32_bf16 v[188:191], v[40:43], v[84:87], v[188:191]\n"
"v_fma_f32 v22, v76, s62, v12\n"
"v_fma_f32 v23, v77, s62, v12\n"
"v_mfma_f32_16x16x32_bf16 v[192:195], v[40:43], v[124:127], v[192:195]\n"
"v_xor_b32 v7, 64, v7\n"
"s_add_u32 m0, s69, 3072\n"
"v_exp_f32 v22, v22\n"
"v_fma_f32 v24, v78, s62, v12\n"
"v_mfma_f32_16x16x32_bf16 v[196:199], v[40:43], v[52:55], v[196:199]\n"
"s_add_u32 s61, s60, 27648\n"
"v_exp_f32 v23, v23\n"
"v_mfma_f32_16x16x32_bf16 v[200:203], v[40:43], v[56:59], v[200:203]\n"
"buffer_load_dwordx4 v7, s[40:43], s61 offen nt lds\n"
"v_fma_f32 v25, v79, s62, v12\n"
"v_exp_f32 v24, v24\n"
"v_mfma_f32_16x16x32_bf16 v[204:207], v[40:43], v[104:107], v[204:207]\n"
"s_waitcnt lgkmcnt(2)\n"
"v_add_f32 v69, v22, v23\n"
"v_fma_f32 v26, v48, s62, v12\n"
"v_mfma_f32_16x16x32_bf16 v[208:211], v[44:47], v[108:111], v[208:211]\n"
"v_exp_f32 v25, v25\n"
"v_cvt_pk_bf16_f32 v96, v22, v23\n"
"v_mfma_f32_16x16x32_bf16 v[212:215], v[44:47], v[84:87], v[212:215]\n"
"v_fma_f32 v27, v49, s62, v12\n"
"v_mfma_f32_16x16x32_bf16 v[216:219], v[44:47], v[124:127], v[216:219]\n"
"s_add_u32 m0, s69, 4096\n"
"v_exp_f32 v26, v26\n"
"v_add_f32 v69, v69, v24\n"
"v_mfma_f32_16x16x32_bf16 v[220:223], v[44:47], v[52:55], v[220:223]\n"
"s_add_u32 s61, s60, 27776\n"
"v_fma_f32 v22, v50, s62, v12\n"
"v_exp_f32 v27, v27\n"
"v_mfma_f32_16x16x32_bf16 v[224:227], v[44:47], v[56:59], v[224:227]\n"
"buffer_load_dwordx4 v7, s[40:43], s61 offen nt lds\n"
"v_add_f32 v69, v69, v25\n"
"v_cvt_pk_bf16_f32 v97, v24, v25\n"
"v_mfma_f32_16x16x32_bf16 v[228:231], v[44:47], v[104:107], v[228:231]\n"
"s_waitcnt lgkmcnt(0)\n"
"v_fma_f32 v23, v51, s62, v12\n"
"v_exp_f32 v22, v22\n"
"v_mfma_f32_16x16x32_bf16 v[232:235], v[36:39], v[108:111], v[232:235]\n"
"v_add_f32 v69, v69, v26\n"
"v_mfma_f32_16x16x32_bf16 v[236:239], v[36:39], v[84:87], v[236:239]\n"
"v_exp_f32 v23, v23\n"
"v_cvt_pk_bf16_f32 v98, v26, v27\n"
"v_add_f32 v69, v69, v27\n"
"v_mfma_f32_16x16x32_bf16 v[240:243], v[36:39], v[124:127], v[240:243]\n"
"s_add_u32 m0, s69, 5120\n"
"v_add_f32 v69, v69, v22\n"
"v_cvt_pk_bf16_f32 v99, v22, v23\n"
"v_mfma_f32_16x16x32_bf16 v[244:247], v[36:39], v[52:55], v[244:247]\n"
"s_add_u32 s61, s60, 27904\n"
"v_add_f32 v69, v69, v23\n"
"v_max_f32 v27, v68, v69\n"
"v_cmp_nge_f32 vcc, s63, v27\n"
"v_mfma_f32_16x16x32_bf16 v[248:251], v[36:39], v[56:59], v[248:251]\n"
"buffer_load_dwordx4 v7, s[40:43], s61 offen nt lds\n"
"v_fma_f32 v22, v92, s62, v13\n"
"v_fma_f32 v23, v93, s62, v13\n"
"v_mfma_f32_16x16x32_bf16 v[252:255], v[36:39], v[104:107], v[252:255]\n"
"s_cbranch_vccnz .Lrare_11_b\n"
".Lret_11_b:\n"
"v_add_f32 v17, v17, v68\n"
"v_add_f32 v18, v18, v69\n"
"s_waitcnt vmcnt(12)\n"
"v_mbcnt_lo_u32_b32 v70, -1, 0\n"
"v_mbcnt_hi_u32_b32 v70, -1, v70\n"
"v_bfe_u32 v70, v70, 3, 1\n"
"v_mul_u32_u24 v71, 0xc00, v70\n"
"v_xor_b32 v70, 1, v70\n"
"v_mul_u32_u24 v70, 0xc000, v70\n"
"v_sub_u32 v70, v70, v71\n"
"v_add_u32 v70, s67, v70\n"
"v_add_u32 v68, v1, v70\n"
"v_add_u32 v69, v2, v70\n"
"ds_read_b128 a[0:3], v68 offset:0\n"
"ds_read_b128 a[4:7], v69 offset:0\n"
"ds_read_b128 a[8:11], v68 offset:1024\n"
"ds_read_b128 a[12:15], v69 offset:1024\n"
"ds_read_b128 a[16:19], v68 offset:2048\n"
"ds_read_b128 a[20:23], v69 offset:2048\n"
"ds_read_b128 a[24:27], v68 offset:12288\n"
"ds_read_b128 a[28:31], v69 offset:12288\n"
"ds_read_b128 a[32:35], v68 offset:13312\n"
"ds_read_b128 a[36:39], v69 offset:13312\n"
"ds_read_b128 a[40:43], v68 offset:14336\n"
"ds_read_b128 a[44:47], v69 offset:14336\n"
"v_subrev_u32 v100, 0x3000, v5\n"
"v_subrev_u32 v101, 0x3000, v6\n"
"s_waitcnt lgkmcnt(0)\n"
"s_barrier\n"
"ds_read_b64_tr_b16 v[32:33], v100 offset:0\n"
"ds_read_b64_tr_b16 v[34:35], v100 offset:6144\n"
"ds_read_b64_tr_b16 v[40:41], v101 offset:0\n"
"ds_read_b64_tr_b16 v[42:43], v101 offset:6144\n"
"ds_read_b64_tr_b16 v[44:45], v100 offset:512\n"
"ds_read_b64_tr_b16 v[46:47], v100 offset:6656\n"
"s_waitcnt lgkmcnt(4)\n"
"v_mfma_f32_16x16x32_bf16 a[96:99], v[32:35], v[112:115], a[96:99]\n"
"ds_read_b64_tr_b16 v[36:37], v101 offset:512\n"
"ds_read_b64_tr_b16 v[38:39], v101 offset:6656\n"
"v_xor_b32 v7, 64, v7\n"
"s_add_u32 m0, s56, 86016\n"
"v_mfma_f32_16x16x32_bf16 a[100:103], v[32:35], v[64:67], a[100:103]\n"
"s_add_u32 s61, s60, 18432\n"
"v_exp_f32 v22, v22\n"
"v_mfma_f32_16x16x32_bf16 a[104:107], v[32:35], v[120:123], a[104:107]\n"
"buffer_load_dwordx4 v7, s[40:43], s61 offen nt lds\n"
"v_fma_f32 v24, v94, s62, v13\n"
"v_mfma_f32_16x16x32_bf16 a[108:111], v[32:35], v[96:99], a[108:111]\n"
"s_waitcnt lgkmcnt(4)\n"
"ds_read_b64_tr_b16 v[32:33], v100 offset:1024\n"
"ds_read_b64_tr_b16 v[34:35], v100 offset:7168\n"
"s_add_u32 m0, s56, 87040\n"
"v_exp_f32 v23, v23\n"
"v_mfma_f32_16x16x32_bf16 a[120:123], v[40:43], v[112:115], a[120:123]\n"
"s_add_u32 s61, s60, 18560\n"
"v_fma_f32 v25, v95, s62, v13\n"
"v_mfma_f32_16x16x32_bf16 a[124:127], v[40:43], v[64:67], a[124:127]\n"
"buffer_load_dwordx4 v7, s[40:43], s61 offen nt lds\n"
"v_exp_f32 v24, v24\n"
"v_add_f32 v21, v22, v23\n"
"v_mfma_f32_16x16x32_bf16 a[128:131], v[40:43], v[120:123], a[128:131]\n"
"s_add_u32 m0, s56, 88064\n"
"v_fma_f32 v26, v116, s62, v13\n"
"v_mfma_f32_16x16x32_bf16 a[132:135], v[40:43], v[96:99], a[132:135]\n"
"s_waitcnt lgkmcnt(4)\n"
"ds_read_b64_tr_b16 v[40:41], v101 offset:1024\n"
"ds_read_b64_tr_b16 v[42:43], v101 offset:7168\n"
"s_add_u32 s61, s60, 18688\n"
"v_exp_f32 v25, v25\n"
"v_mfma_f32_16x16x32_bf16 a[144:147], v[44:47], v[112:115], a[144:147]\n"
"buffer_load_dwordx4 v7, s[40:43], s61 offen nt lds\n"
"v_cvt_pk_bf16_f32 v88, v22, v23\n"
"v_fma_f32 v27, v117, s62, v13\n"
"v_mfma_f32_16x16x32_bf16 a[148:151], v[44:47], v[64:67], a[148:151]\n"
"s_add_u32 m0, s69, 6144\n"
"v_exp_f32 v26, v26\n"
"v_mfma_f32_16x16x32_bf16 a[152:155], v[44:47], v[120:123], a[152:155]\n"
"s_add_u32 s61, s60, 30720\n"
"v_add_f32 v21, v21, v24\n"
"v_fma_f32 v22, v118, s62, v13\n"
"v_mfma_f32_16x16x32_bf16 a[156:159], v[44:47], v[96:99], a[156:159]\n"
"s_waitcnt lgkmcnt(4)\n"
"ds_read_b64_tr_b16 v[44:45], v100 offset:1536\n"
"ds_read_b64_tr_b16 v[46:47], v100 offset:7680\n"
"buffer_load_dwordx4 v7, s[40:43], s61 offen nt lds\n"
"v_exp_f32 v27, v27\n"
"v_mfma_f32_16x16x32_bf16 a[168:171], v[36:39], v[112:115], a[168:171]\n"
"v_add_f32 v21, v21, v25\n"
"v_mfma_f32_16x16x32_bf16 a[172:175], v[36:39], v[64:67], a[172:175]\n"
"s_add_u32 m0, s69, 7168\n"
"v_cvt_pk_bf16_f32 v89, v24, v25\n"
"v_fma_f32 v23, v119, s62, v13\n"
"v_mfma_f32_16x16x32_bf16 a[176:179], v[36:39], v[120:123], a[176:179]\n"
"s_add_u32 s61, s60, 30848\n"
"v_exp_f32 v22, v22\n"
"v_mfma_f32_16x16x32_bf16 a[180:183], v[36:39], v[96:99], a[180:183]\n"
"s_waitcnt lgkmcnt(4)\n"
"ds_read_b64_tr_b16 v[36:37], v101 offset:1536\n"
"ds_read_b64_tr_b16 v[38:39], v101 offset:7680\n"
"buffer_load_dwordx4 v7, s[40:43], s61 offen nt lds\n"
"v_add_f32 v21, v21, v26\n"
"v_mfma_f32_16x16x32_bf16 a[192:195], v[32:35], v[112:115], a[192:195]\n"
"s_add_u32 m0, s69, 8192\n"
"v_exp_f32 v23, v23\n"
"v_mfma_f32_16x16x32_bf16 a[196:199], v[32:35], v[64:67], a[196:199]\n"
"s_add_u32 s61, s60, 30976\n"
"v_cvt_pk_bf16_f32 v90, v26, v27\n"
"v_add_f32 v21, v21, v27\n"
"v_mfma_f32_16x16x32_bf16 a[200:203], v[32:35], v[120:123], a[200:203]\n"
"buffer_load_dwordx4 v7, s[40:43], s61 offen nt lds\n"
"v_add_f32 v21, v21, v22\n"
"v_cvt_pk_bf16_f32 v91, v22, v23\n"
"v_mfma_f32_16x16x32_bf16 a[204:207], v[32:35], v[96:99], a[204:207]\n"
"s_waitcnt lgkmcnt(4)\n"
"ds_read_b64_tr_b16 v[32:33], v100 offset:2048\n"
"ds_read_b64_tr_b16 v[34:35], v100 offset:8192\n"
"v_xor_b32 v7, 64, v7\n"
"s_add_u32 m0, s69, 9216\n"
"v_add_f32 v21, v21, v23\n"
"v_cmp_nge_f32 vcc, s63, v21\n"
"v_mfma_f32_16x16x32_bf16 a[216:219], v[40:43], v[112:115], a[216:219]\n"
"s_add_u32 s61, s60, 33792\n"
"v_fma_f32 v22, v28, s62, v14\n"
"v_fma_f32 v23, v29, s62, v14\n"
"v_mfma_f32_16x16x32_bf16 a[220:223], v[40:43], v[64:67], a[220:223]\n"
"buffer_load_dwordx4 v7, s[40:43], s61 offen nt lds\n"
"s_cbranch_vccnz .Lrare_11_d4\n"
".Lret_11_d4:\n"
"v_add_f32 v19, v19, v21\n"
"v_mfma_f32_16x16x32_bf16 a[224:227], v[40:43], v[120:123], a[224:227]\n"
"v_exp_f32 v22, v22\n"
"v_mfma_f32_16x16x32_bf16 a[228:231], v[40:43], v[96:99], a[228:231]\n"
"s_waitcnt lgkmcnt(4)\n"
"ds_read_b64_tr_b16 v[40:41], v101 offset:2048\n"
"ds_read_b64_tr_b16 v[42:43], v101 offset:8192\n"
"s_add_u32 m0, s69, 10240\n"
"v_fma_f32 v24, v30, s62, v14\n"
"v_mfma_f32_16x16x32_bf16 a[240:243], v[44:47], v[112:115], a[240:243]\n"
"s_add_u32 s61, s60, 33920\n"
"v_exp_f32 v23, v23\n"
"v_fma_f32 v25, v31, s62, v14\n"
"v_mfma_f32_16x16x32_bf16 a[244:247], v[44:47], v[64:67], a[244:247]\n"
"buffer_load_dwordx4 v7, s[40:43], s61 offen nt lds\n"
"v_mfma_f32_16x16x32_bf16 a[248:251], v[44:47], v[120:123], a[248:251]\n"
"s_add_u32 m0, s69, 11264\n"
"v_exp_f32 v24, v24\n"
"v_add_f32 v21, v22, v23\n"
"v_mfma_f32_16x16x32_bf16 a[252:255], v[44:47], v[96:99], a[252:255]\n"
"s_waitcnt lgkmcnt(4)\n"
"ds_read_b64_tr_b16 v[44:45], v100 offset:2560\n"
"ds_read_b64_tr_b16 v[46:47], v100 offset:8704\n"
"s_add_u32 s61, s60, 34048\n"
"v_fma_f32 v26, v72, s62, v14\n"
"v_mfma_f32_16x16x32_bf16 v[136:139], v[36:39], v[112:115], v[136:139]\n"
"buffer_load_dwordx4 v7, s[40:43], s61 offen nt lds\n"
"v_exp_f32 v25, v25\n"
"v_cvt_pk_bf16_f32 v80, v22, v23\n"
"v_mfma_f32_16x16x32_bf16 v[140:143], v[36:39], v[64:67], v[140:143]\n"
"v_xor_b32 v7, s74, v7\n"
"s_add_u32 m0, s56, 49152\n"
"v_fma_f32 v27, v73, s62, v14\n"
"v_mfma_f32_16x16x32_bf16 v[144:147], v[36:39], v[120:123], v[144:147]\n"
"s_add_u32 s61, s58, 0\n"
"v_exp_f32 v26, v26\n"
"v_mfma_f32_16x16x32_bf16 v[148:151], v[36:39], v[96:99], v[148:151]\n"
"s_waitcnt lgkmcnt(4)\n"
"ds_read_b64_tr_b16 v[36:37], v101 offset:2560\n"
"ds_read_b64_tr_b16 v[38:39], v101 offset:8704\n"
"buffer_load_dwordx4 v7, s[44:47], s61 offen nt lds\n"
"v_add_f32 v21, v21, v24\n"
"v_fma_f32 v22, v74, s62, v14\n"
"v_mfma_f32_16x16x32_bf16 v[160:163], v[32:35], v[112:115], v[160:163]\n"
"s_add_u32 m0, s56, 50176\n"
"v_exp_f32 v27, v27\n"
"v_mfma_f32_16x16x32_bf16 v[164:167], v[32:35], v[64:67], v[164:167]\n"
"s_add_u32 s61, s58, 128\n"
"v_add_f32 v21, v21, v25\n"
"v_cvt_pk_bf16_f32 v81, v24, v25\n"
"v_mfma_f32_16x16x32_bf16 v[168:171], v[32:35], v[120:123], v[168:171]\n"
"buffer_load_dwordx4 v7, s[44:47], s61 offen nt lds\n"
"v_fma_f32 v23, v75, s62, v14\n"
"v_mfma_f32_16x16x32_bf16 v[172:175], v[32:35], v[96:99], v[172:175]\n"
"s_waitcnt lgkmcnt(4)\n"
"ds_read_b64_tr_b16 v[32:33], v100 offset:0\n"
"ds_read_b64_tr_b16 v[34:35], v100 offset:6144\n"
"v_exp_f32 v22, v22\n"
"v_mfma_f32_16x16x32_bf16 v[184:187], v[40:43], v[112:115], v[184:187]\n"
"s_add_u32 m0, s56, 51200\n"
"v_add_f32 v21, v21, v26\n"
"v_mfma_f32_16x16x32_bf16 v[188:191], v[40:43], v[64:67], v[188:191]\n"
"s_add_u32 s61, s58, 256\n"
"v_exp_f32 v23, v23\n"
"v_cvt_pk_bf16_f32 v82, v26, v27\n"
"v_mfma_f32_16x16x32_bf16 v[192:195], v[40:43], v[120:123], v[192:195]\n"
"buffer_load_dwordx4 v7, s[44:47], s61 offen nt lds\n"
"v_add_f32 v21, v21, v27\n"
"v_mfma_f32_16x16x32_bf16 v[196:199], v[40:43], v[96:99], v[196:199]\n"
"s_waitcnt lgkmcnt(4)\n"
"ds_read_b64_tr_b16 v[40:41], v101 offset:0\n"
"ds_read_b64_tr_b16 v[42:43], v101 offset:6144\n"
"s_add_u32 m0, s56, 0\n"
"v_add_f32 v21, v21, v22\n"
"v_cvt_pk_bf16_f32 v83, v22, v23\n"
"v_mfma_f32_16x16x32_bf16 v[208:211], v[44:47], v[112:115], v[208:211]\n"
"s_add_u32 s61, s58, 12288\n"
"v_add_f32 v21, v21, v23\n"
"v_cmp_nge_f32 vcc, s63, v21\n"
"v_mfma_f32_16x16x32_bf16 v[212:215], v[44:47], v[64:67], v[212:215]\n"
"buffer_load_dwordx4 v7, s[44:47], s61 offen nt lds\n"
"v_mfma_f32_16x16x32_bf16 v[216:219], v[44:47], v[120:123], v[216:219]\n"
"s_add_u32 m0, s56, 1024\n"
"v_mfma_f32_16x16x32_bf16 v[220:223], v[44:47], v[96:99], v[220:223]\n"
"s_waitcnt lgkmcnt(4)\n"
"ds_read_b64_tr_b16 v[44:45], v100 offset:512\n"
"ds_read_b64_tr_b16 v[46:47], v100 offset:6656\n"
"s_add_u32 s61, s58, 12416\n"
"s_cbranch_vccnz .Lrare_11_d5\n"
".Lret_11_d5:\n"
"v_add_f32 v20, v20, v21\n"
"v_mfma_f32_16x16x32_bf16 v[232:235], v[36:39], v[112:115], v[232:235]\n"
"buffer_load_dwordx4 v7, s[44:47], s61 offen nt lds\n"
"v_mfma_f32_16x16x32_bf16 v[236:239], v[36:39], v[64:67], v[236:239]\n"
"v_mfma_f32_16x16x32_bf16 v[240:243], v[36:39], v[120:123], v[240:243]\n"
"s_add_u32 m0, s56, 2048\n"
"v_mfma_f32_16x16x32_bf16 v[244:247], v[36:39], v[96:99], v[244:247]\n"
"s_waitcnt lgkmcnt(4)\n"
"ds_read_b64_tr_b16 v[36:37], v101 offset:512\n"
"ds_read_b64_tr_b16 v[38:39], v101 offset:6656\n"
"s_add_u32 s61, s58, 12544\n"
"v_mfma_f32_16x16x32_bf16 a[112:115], v[32:35], v[88:91], a[112:115]\n"
"buffer_load_dwordx4 v7, s[44:47], s61 offen nt lds\n"
"v_mfma_f32_16x16x32_bf16 a[116:119], v[32:35], v[80:83], a[116:119]\n"
"s_waitcnt lgkmcnt(4)\n"
"ds_read_b64_tr_b16 v[32:33], v100 offset:1024\n"
"ds_read_b64_tr_b16 v[34:35], v100 offset:7168\n"
"s_add_u32 m0, s56, 61440\n"
"v_mfma_f32_16x16x32_bf16 a[136:139], v[40:43], v[88:91], a[136:139]\n"
"s_add_u32 s61, s58, 0\n"
"v_mfma_f32_16x16x32_bf16 a[140:143], v[40:43], v[80:83], a[140:143]\n"
"s_waitcnt lgkmcnt(4)\n"
"ds_read_b64_tr_b16 v[40:41], v101 offset:1024\n"
"ds_read_b64_tr_b16 v[42:43], v101 offset:7168\n"
"buffer_load_dwordx4 v8, s[48:51], s61 offen nt lds\n"
"v_mfma_f32_16x16x32_bf16 a[160:163], v[44:47], v[88:91], a[160:163]\n"
"s_add_u32 m0, s56, 62464\n"
"v_mfma_f32_16x16x32_bf16 a[164:167], v[44:47], v[80:83], a[164:167]\n"
"s_waitcnt lgkmcnt(4)\n"
"ds_read_b64_tr_b16 v[44:45], v100 offset:1536\n"
"ds_read_b64_tr_b16 v[46:47], v100 offset:7680\n"
"s_add_u32 s61, s58, 128\n"
"v_mfma_f32_16x16x32_bf16 a[184:187], v[36:39], v[88:91], a[184:187]\n"
"buffer_load_dwordx4 v8, s[48:51], s61 offen nt lds\n"
"v_mfma_f32_16x16x32_bf16 a[188:191], v[36:39], v[80:83], a[188:191]\n"
"s_waitcnt lgkmcnt(4)\n"
"ds_read_b64_tr_b16 v[36:37], v101 offset:1536\n"
"ds_read_b64_tr_b16 v[38:39], v101 offset:7680\n"
"s_add_u32 m0, s56, 63488\n"
"v_mfma_f32_16x16x32_bf16 a[208:211], v[32:35], v[88:91], a[208:211]\n"
"s_add_u32 s61, s58, 256\n"
"v_mfma_f32_16x16x32_bf16 a[212:215], v[32:35], v[80:83], a[212:215]\n"
"s_waitcnt lgkmcnt(4)\n"
"ds_read_b64_tr_b16 v[32:33], v100 offset:2048\n"
"ds_read_b64_tr_b16 v[34:35], v100 offset:8192\n"
"buffer_load_dwordx4 v8, s[48:51], s61 offen nt lds\n"
"v_mfma_f32_16x16x32_bf16 a[232:235], v[40:43], v[88:91], a[232:235]\n"
"v_mfma_f32_16x16x32_bf16 a[236:239], v[40:43], v[80:83], a[236:239]\n"
"s_waitcnt lgkmcnt(4)\n"
"ds_read_b64_tr_b16 v[40:41], v101 offset:2048\n"
"ds_read_b64_tr_b16 v[42:43], v101 offset:8192\n"
"s_add_u32 m0, s56, 12288\n"
"v_mfma_f32_16x16x32_bf16 v[128:131], v[44:47], v[88:91], v[128:131]\n"
"s_add_u32 s61, s58, 24576\n"
"v_mfma_f32_16x16x32_bf16 v[132:135], v[44:47], v[80:83], v[132:135]\n"
"s_waitcnt lgkmcnt(4)\n"
"ds_read_b64_tr_b16 v[44:45], v100 offset:2560\n"
"ds_read_b64_tr_b16 v[46:47], v100 offset:8704\n"
"buffer_load_dwordx4 v7, s[44:47], s61 offen nt lds\n"
"v_mfma_f32_16x16x32_bf16 v[152:155], v[36:39], v[88:91], v[152:155]\n"
"s_add_u32 m0, s56, 13312\n"
"v_mfma_f32_16x16x32_bf16 v[156:159], v[36:39], v[80:83], v[156:159]\n"
"s_waitcnt lgkmcnt(4)\n"
"ds_read_b64_tr_b16 v[36:37], v101 offset:2560\n"
"ds_read_b64_tr_b16 v[38:39], v101 offset:8704\n"
"s_add_u32 s61, s58, 24704\n"
"v_mfma_f32_16x16x32_bf16 v[176:179], v[32:35], v[88:91], v[176:179]\n"
"buffer_load_dwordx4 v7, s[44:47], s61 offen nt lds\n"
"v_mfma_f32_16x16x32_bf16 v[180:183], v[32:35], v[80:83], v[180:183]\n"
"s_waitcnt lgkmcnt(4)\n"
"s_add_u32 m0, s56, 14336\n"
"v_mfma_f32_16x16x32_bf16 v[200:203], v[40:43], v[88:91], v[200:203]\n"
"s_add_u32 s61, s58, 24832\n"
"v_mfma_f32_16x16x32_bf16 v[204:207], v[40:43], v[80:83], v[204:207]\n"
"s_waitcnt lgkmcnt(2)\n"
"buffer_load_dwordx4 v7, s[44:47], s61 offen nt lds\n"
"v_mfma_f32_16x16x32_bf16 v[224:227], v[44:47], v[88:91], v[224:227]\n"
"v_mfma_f32_16x16x32_bf16 v[228:231], v[44:47], v[80:83], v[228:231]\n"
"s_waitcnt lgkmcnt(0)\n"
"v_mfma_f32_16x16x32_bf16 v[248:251], v[36:39], v[88:91], v[248:251]\n"
"v_mfma_f32_16x16x32_bf16 v[252:255], v[36:39], v[80:83], v[252:255]\n"
"s_waitcnt lgkmcnt(0)\n"
"s_barrier\n"
"v_xor_b32 v7, s74, v7\n"
"s_add_u32 m0, s56, 36864\n"
"s_add_u32 s61, s60, 21504\n"
"buffer_load_dwordx4 v7, s[40:43], s61 offen nt lds\n"
"s_add_u32 m0, s56, 37888\n"
"s_add_u32 s61, s60, 21632\n"
"buffer_load_dwordx4 v7, s[40:43], s61 offen nt lds\n"
"s_add_u32 m0, s56, 38912\n"
"s_add_u32 s61, s60, 21760\n"
"buffer_load_dwordx4 v7, s[40:43], s61 offen nt lds\n"
"v_mov_b32 v104, v15\n"
"v_mov_b32 v105, v16\n"
"v_mov_b32 v106, v17\n"
"v_mov_b32 v107, v18\n"
"v_mov_b32 v108, v19\n"
"v_mov_b32 v109, v20\n"
"v_permlane32_swap_b32 v15, v104\n"
"v_permlane32_swap_b32 v16, v105\n"
"v_permlane32_swap_b32 v17, v106\n"
"v_permlane32_swap_b32 v18, v107\n"
"v_permlane32_swap_b32 v19, v108\n"
"v_permlane32_swap_b32 v20, v109\n"
"v_add_f32 v15, v15, v104\n"
"v_add_f32 v16, v16, v105\n"
"v_add_f32 v17, v17, v106\n"
"v_add_f32 v18, v18, v107\n"
"v_add_f32 v19, v19, v108\n"
"v_add_f32 v20, v20, v109\n"
"v_mov_b32 v104, v15\n"
"v_mov_b32 v105, v16\n"
"v_mov_b32 v106, v17\n"
"v_mov_b32 v107, v18\n"
"v_mov_b32 v108, v19\n"
"v_mov_b32 v109, v20\n"
"v_permlane16_swap_b32 v15, v104\n"
"v_permlane16_swap_b32 v16, v105\n"
"v_permlane16_swap_b32 v17, v106\n"
"v_permlane16_swap_b32 v18, v107\n"
"v_permlane16_swap_b32 v19, v108\n"
"v_permlane16_swap_b32 v20, v109\n"
"v_add_f32 v15, v15, v104\n"
"v_add_f32 v16, v16, v105\n"
"v_add_f32 v17, v17, v106\n"
"v_add_f32 v18, v18, v107\n"
"v_add_f32 v19, v19, v108\n"
"v_add_f32 v20, v20, v109\n"
"v_rcp_f32 v52, v15\n"
"v_rcp_f32 v54, v16\n"
"v_rcp_f32 v56, v17\n"
"v_rcp_f32 v58, v18\n"
"v_rcp_f32 v60, v19\n"
"v_rcp_f32 v62, v20\n"
"v_mbcnt_lo_u32_b32 v68, -1, 0\n"
"v_mbcnt_hi_u32_b32 v68, -1, v68\n"
"v_lshlrev_b32 v66, 4, v68\n"
"v_add_u32 v66, s73, v66\n"
"v_lshrrev_b32 v69, 3, v68\n"
"v_and_b32 v67, 7, v68\n"
"v_xor_b32 v67, v67, v69\n"
"v_lshlrev_b32 v67, 4, v67\n"
"v_mul_u32_u24 v69, 0x180, v69\n"
"v_add_u32 v67, v67, v69\n"
"v_lshrrev_b32 v69, 5, v68\n"
"v_and_b32 v65, 7, v68\n"
"v_xor_b32 v69, v69, v65\n"
"v_lshlrev_b32 v69, 4, v69\n"
"v_bfe_u32 v65, v68, 4, 1\n"
"v_lshlrev_b32 v65, 3, v65\n"
"v_add_u32 v69, v69, v65\n"
"v_and_b32 v68, 15, v68\n"
"v_lshlrev_b32 v68, 7, v68\n"
"v_add_u32 v68, v68, v69\n"
"v_add_u32 v120, s73, v68\n"
"v_xor_b32 v121, 32, v120\n"
"v_xor_b32 v122, 64, v120\n"
"v_xor_b32 v123, 96, v120\n"
"v_accvgpr_read_b32 v72, a96\n"
"v_accvgpr_read_b32 v73, a97\n"
"v_accvgpr_read_b32 v74, a98\n"
"v_accvgpr_read_b32 v75, a99\n"
"v_accvgpr_read_b32 v76, a120\n"
"v_accvgpr_read_b32 v77, a121\n"
"v_accvgpr_read_b32 v78, a122\n"
"v_accvgpr_read_b32 v79, a123\n"
"v_pk_mul_f32 v[72:73], v[72:73], v[52:53] op_sel_hi:[1,0]\n"
"v_pk_mul_f32 v[74:75], v[74:75], v[52:53] op_sel_hi:[1,0]\n"
"v_pk_mul_f32 v[76:77], v[76:77], v[52:53] op_sel_hi:[1,0]\n"
"v_pk_mul_f32 v[78:79], v[78:79], v[52:53] op_sel_hi:[1,0]\n"
"v_cvt_pk_bf16_f32 v88, v72, v73\n"
"v_cvt_pk_bf16_f32 v89, v74, v75\n"
"v_cvt_pk_bf16_f32 v90, v76, v77\n"
"v_cvt_pk_bf16_f32 v91, v78, v79\n"
"v_accvgpr_read_b32 v80, a144\n"
"v_accvgpr_read_b32 v81, a145\n"
"v_accvgpr_read_b32 v82, a146\n"
"v_accvgpr_read_b32 v83, a147\n"
"v_accvgpr_read_b32 v84, a168\n"
"v_accvgpr_read_b32 v85, a169\n"
"v_accvgpr_read_b32 v86, a170\n"
"v_accvgpr_read_b32 v87, a171\n"
"v_pk_mul_f32 v[80:81], v[80:81], v[52:53] op_sel_hi:[1,0]\n"
"v_pk_mul_f32 v[82:83], v[82:83], v[52:53] op_sel_hi:[1,0]\n"
"v_pk_mul_f32 v[84:85], v[84:85], v[52:53] op_sel_hi:[1,0]\n"
"v_pk_mul_f32 v[86:87], v[86:87], v[52:53] op_sel_hi:[1,0]\n"
"v_cvt_pk_bf16_f32 v92, v80, v81\n"
"v_cvt_pk_bf16_f32 v93, v82, v83\n"
"v_cvt_pk_bf16_f32 v94, v84, v85\n"
"v_cvt_pk_bf16_f32 v95, v86, v87\n"
"ds_write_b64 v120, v[88:89] offset:0\n"
"ds_write_b64 v121, v[90:91] offset:0\n"
"v_accvgpr_read_b32 v72, a100\n"
"v_accvgpr_read_b32 v73, a101\n"
"v_accvgpr_read_b32 v74, a102\n"
"v_accvgpr_read_b32 v75, a103\n"
"v_accvgpr_read_b32 v76, a124\n"
"v_accvgpr_read_b32 v77, a125\n"
"v_accvgpr_read_b32 v78, a126\n"
"v_accvgpr_read_b32 v79, a127\n"
"v_pk_mul_f32 v[72:73], v[72:73], v[54:55] op_sel_hi:[1,0]\n"
"v_pk_mul_f32 v[74:75], v[74:75], v[54:55] op_sel_hi:[1,0]\n"
"v_pk_mul_f32 v[76:77], v[76:77], v[54:55] op_sel_hi:[1,0]\n"
"v_pk_mul_f32 v[78:79], v[78:79], v[54:55] op_sel_hi:[1,0]\n"
"v_cvt_pk_bf16_f32 v96, v72, v73\n"
"v_cvt_pk_bf16_f32 v97, v74, v75\n"
"v_cvt_pk_bf16_f32 v98, v76, v77\n"
"v_cvt_pk_bf16_f32 v99, v78, v79\n"
"ds_write_b64 v122, v[92:93] offset:0\n"
"ds_write_b64 v123, v[94:95] offset:0\n"
"v_accvgpr_read_b32 v80, a148\n"
"v_accvgpr_read_b32 v81, a149\n"
"v_accvgpr_read_b32 v82, a150\n"
"v_accvgpr_read_b32 v83, a151\n"
"v_accvgpr_read_b32 v84, a172\n"
"v_accvgpr_read_b32 v85, a173\n"
"v_accvgpr_read_b32 v86, a174\n"
"v_accvgpr_read_b32 v87, a175\n"
"v_pk_mul_f32 v[80:81], v[80:81], v[54:55] op_sel_hi:[1,0]\n"
"v_pk_mul_f32 v[82:83], v[82:83], v[54:55] op_sel_hi:[1,0]\n"
"v_pk_mul_f32 v[84:85], v[84:85], v[54:55] op_sel_hi:[1,0]\n"
"v_pk_mul_f32 v[86:87], v[86:87], v[54:55] op_sel_hi:[1,0]\n"
"v_cvt_pk_bf16_f32 v100, v80, v81\n"
"v_cvt_pk_bf16_f32 v101, v82, v83\n"
"v_cvt_pk_bf16_f32 v102, v84, v85\n"
"v_cvt_pk_bf16_f32 v103, v86, v87\n"
"ds_write_b64 v120, v[96:97] offset:2048\n"
"ds_write_b64 v121, v[98:99] offset:2048\n"
"v_accvgpr_read_b32 v72, a192\n"
"v_accvgpr_read_b32 v73, a193\n"
"v_accvgpr_read_b32 v74, a194\n"
"v_accvgpr_read_b32 v75, a195\n"
"v_accvgpr_read_b32 v76, a216\n"
"v_accvgpr_read_b32 v77, a217\n"
"v_accvgpr_read_b32 v78, a218\n"
"v_accvgpr_read_b32 v79, a219\n"
"v_pk_mul_f32 v[72:73], v[72:73], v[52:53] op_sel_hi:[1,0]\n"
"v_pk_mul_f32 v[74:75], v[74:75], v[52:53] op_sel_hi:[1,0]\n"
"v_pk_mul_f32 v[76:77], v[76:77], v[52:53] op_sel_hi:[1,0]\n"
"v_pk_mul_f32 v[78:79], v[78:79], v[52:53] op_sel_hi:[1,0]\n"
"v_cvt_pk_bf16_f32 v88, v72, v73\n"
"v_cvt_pk_bf16_f32 v89, v74, v75\n"
"v_cvt_pk_bf16_f32 v90, v76, v77\n"
"v_cvt_pk_bf16_f32 v91, v78, v79\n"
"ds_write_b64 v122, v[100:101] offset:2048\n"
"ds_write_b64 v123, v[102:103] offset:2048\n"
"ds_read_b128 v[104:107], v66 offset:0\n"
"ds_read_b128 v[108:111], v66 offset:1024\n"
"ds_read_b128 v[112:115], v66 offset:2048\n"
"ds_read_b128 v[116:119], v66 offset:3072\n"
"v_accvgpr_read_b32 v80, a240\n"
"v_accvgpr_read_b32 v81, a241\n"
"v_accvgpr_read_b32 v82, a242\n"
"v_accvgpr_read_b32 v83, a243\n"
"s_add_u32 s61, s59, 0\n"
"s_waitcnt lgkmcnt(3)\n"
"buffer_store_dwordx4 v[104:107], v67, s[52:55], s61 offen sc1\n"
"v_pk_mul_f32 v[80:81], v[80:81], v[52:53] op_sel_hi:[1,0]\n"
"v_pk_mul_f32 v[82:83], v[82:83], v[52:53] op_sel_hi:[1,0]\n"
"v_pk_mul_f32 v[136:137], v[136:137], v[52:53] op_sel_hi:[1,0]\n"
"v_pk_mul_f32 v[138:139], v[138:139], v[52:53] op_sel_hi:[1,0]\n"
"v_cvt_pk_bf16_f32 v92, v80, v81\n"
"v_cvt_pk_bf16_f32 v93, v82, v83\n"
"v_cvt_pk_bf16_f32 v94, v136, v137\n"
"v_cvt_pk_bf16_f32 v95, v138, v139\n"
"ds_write_b64 v120, v[88:89] offset:0\n"
"ds_write_b64 v121, v[90:91] offset:0\n"
"v_accvgpr_read_b32 v72, a196\n"
"v_accvgpr_read_b32 v73, a197\n"
"v_accvgpr_read_b32 v74, a198\n"
"v_accvgpr_read_b32 v75, a199\n"
"v_accvgpr_read_b32 v76, a220\n"
"v_accvgpr_read_b32 v77, a221\n"
"v_accvgpr_read_b32 v78, a222\n"
"v_accvgpr_read_b32 v79, a223\n"
"s_add_u32 s61, s59, 3072\n"
"s_waitcnt lgkmcnt(4)\n"
"buffer_store_dwordx4 v[108:111], v67, s[52:55], s61 offen sc1\n"
"v_pk_mul_f32 v[72:73], v[72:73], v[54:55] op_sel_hi:[1,0]\n"
"v_pk_mul_f32 v[74:75], v[74:75], v[54:55] op_sel_hi:[1,0]\n"
"v_pk_mul_f32 v[76:77], v[76:77], v[54:55] op_sel_hi:[1,0]\n"
"v_pk_mul_f32 v[78:79], v[78:79], v[54:55] op_sel_hi:[1,0]\n"
"v_cvt_pk_bf16_f32 v96, v72, v73\n"
"v_cvt_pk_bf16_f32 v97, v74, v75\n"
"v_cvt_pk_bf16_f32 v98, v76, v77\n"
"v_cvt_pk_bf16_f32 v99, v78, v79\n"
"ds_write_b64 v122, v[92:93] offset:0\n"
"ds_write_b64 v123, v[94:95] offset:0\n"
"v_accvgpr_read_b32 v80, a244\n"
"v_accvgpr_read_b32 v81, a245\n"
"v_accvgpr_read_b32 v82, a246\n"
"v_accvgpr_read_b32 v83, a247\n"
"s_add_u32 s61, s59, 6144\n"
"s_waitcnt lgkmcnt(5)\n"
"buffer_store_dwordx4 v[112:115], v67, s[52:55], s61 offen sc1\n"
"v_pk_mul_f32 v[80:81], v[80:81], v[54:55] op_sel_hi:[1,0]\n"
"v_pk_mul_f32 v[82:83], v[82:83], v[54:55] op_sel_hi:[1,0]\n"
"v_pk_mul_f32 v[140:141], v[140:141], v[54:55] op_sel_hi:[1,0]\n"
"v_pk_mul_f32 v[142:143], v[142:143], v[54:55] op_sel_hi:[1,0]\n"
"v_cvt_pk_bf16_f32 v100, v80, v81\n"
"v_cvt_pk_bf16_f32 v101, v82, v83\n"
"v_cvt_pk_bf16_f32 v102, v140, v141\n"
"v_cvt_pk_bf16_f32 v103, v142, v143\n"
"ds_write_b64 v120, v[96:97] offset:2048\n"
"ds_write_b64 v121, v[98:99] offset:2048\n"
"s_add_u32 s61, s59, 9216\n"
"s_waitcnt lgkmcnt(6)\n"
"buffer_store_dwordx4 v[116:119], v67, s[52:55], s61 offen sc1\n"
"v_pk_mul_f32 v[160:161], v[160:161], v[52:53] op_sel_hi:[1,0]\n"
"v_pk_mul_f32 v[162:163], v[162:163], v[52:53] op_sel_hi:[1,0]\n"
"v_pk_mul_f32 v[184:185], v[184:185], v[52:53] op_sel_hi:[1,0]\n"
"v_pk_mul_f32 v[186:187], v[186:187], v[52:53] op_sel_hi:[1,0]\n"
"v_cvt_pk_bf16_f32 v88, v160, v161\n"
"v_cvt_pk_bf16_f32 v89, v162, v163\n"
"v_cvt_pk_bf16_f32 v90, v184, v185\n"
"v_cvt_pk_bf16_f32 v91, v186, v187\n"
"ds_write_b64 v122, v[100:101] offset:2048\n"
"ds_write_b64 v123, v[102:103] offset:2048\n"
"ds_read_b128 v[28:31], v66 offset:0\n"
"ds_read_b128 v[32:35], v66 offset:1024\n"
"ds_read_b128 v[36:39], v66 offset:2048\n"
"ds_read_b128 v[40:43], v66 offset:3072\n"
"s_add_u32 s61, s59, 128\n"
"s_waitcnt lgkmcnt(3)\n"
"buffer_store_dwordx4 v[28:31], v67, s[52:55], s61 offen sc1\n"
"v_pk_mul_f32 v[208:209], v[208:209], v[52:53] op_sel_hi:[1,0]\n"
"v_pk_mul_f32 v[210:211], v[210:211], v[52:53] op_sel_hi:[1,0]\n"
"v_pk_mul_f32 v[232:233], v[232:233], v[52:53] op_sel_hi:[1,0]\n"
"v_pk_mul_f32 v[234:235], v[234:235], v[52:53] op_sel_hi:[1,0]\n"
"v_cvt_pk_bf16_f32 v92, v208, v209\n"
"v_cvt_pk_bf16_f32 v93, v210, v211\n"
"v_cvt_pk_bf16_f32 v94, v232, v233\n"
"v_cvt_pk_bf16_f32 v95, v234, v235\n"
"ds_write_b64 v120, v[88:89] offset:0\n"
"ds_write_b64 v121, v[90:91] offset:0\n"
"s_add_u32 s61, s59, 3200\n"
"s_waitcnt lgkmcnt(4)\n"
"buffer_store_dwordx4 v[32:35], v67, s[52:55], s61 offen sc1\n"
"v_pk_mul_f32 v[164:165], v[164:165], v[54:55] op_sel_hi:[1,0]\n"
"v_pk_mul_f32 v[166:167], v[166:167], v[54:55] op_sel_hi:[1,0]\n"
"v_pk_mul_f32 v[188:189], v[188:189], v[54:55] op_sel_hi:[1,0]\n"
"v_pk_mul_f32 v[190:191], v[190:191], v[54:55] op_sel_hi:[1,0]\n"
"v_cvt_pk_bf16_f32 v96, v164, v165\n"
"v_cvt_pk_bf16_f32 v97, v166, v167\n"
"v_cvt_pk_bf16_f32 v98, v188, v189\n"
"v_cvt_pk_bf16_f32 v99, v190, v191\n"
"ds_write_b64 v122, v[92:93] offset:0\n"
"ds_write_b64 v123, v[94:95] offset:0\n"
"s_add_u32 s61, s59, 6272\n"
"s_waitcnt lgkmcnt(5)\n"
"buffer_store_dwordx4 v[36:39], v67, s[52:55], s61 offen sc1\n"
"v_pk_mul_f32 v[212:213], v[212:213], v[54:55] op_sel_hi:[1,0]\n"
"v_pk_mul_f32 v[214:215], v[214:215], v[54:55] op_sel_hi:[1,0]\n"
"v_pk_mul_f32 v[236:237], v[236:237], v[54:55] op_sel_hi:[1,0]\n"
"v_pk_mul_f32 v[238:239], v[238:239], v[54:55] op_sel_hi:[1,0]\n"
"v_cvt_pk_bf16_f32 v100, v212, v213\n"
"v_cvt_pk_bf16_f32 v101, v214, v215\n"
"v_cvt_pk_bf16_f32 v102, v236, v237\n"
"v_cvt_pk_bf16_f32 v103, v238, v239\n"
"ds_write_b64 v120, v[96:97] offset:2048\n"
"ds_write_b64 v121, v[98:99] offset:2048\n"
"v_accvgpr_read_b32 v72, a104\n"
"v_accvgpr_read_b32 v73, a105\n"
"v_accvgpr_read_b32 v74, a106\n"
"v_accvgpr_read_b32 v75, a107\n"
"v_accvgpr_read_b32 v76, a128\n"
"v_accvgpr_read_b32 v77, a129\n"
"v_accvgpr_read_b32 v78, a130\n"
"v_accvgpr_read_b32 v79, a131\n"
"s_add_u32 s61, s59, 9344\n"
"s_waitcnt lgkmcnt(6)\n"
"buffer_store_dwordx4 v[40:43], v67, s[52:55], s61 offen sc1\n"
"v_pk_mul_f32 v[72:73], v[72:73], v[56:57] op_sel_hi:[1,0]\n"
"v_pk_mul_f32 v[74:75], v[74:75], v[56:57] op_sel_hi:[1,0]\n"
"v_pk_mul_f32 v[76:77], v[76:77], v[56:57] op_sel_hi:[1,0]\n"
"v_pk_mul_f32 v[78:79], v[78:79], v[56:57] op_sel_hi:[1,0]\n"
"v_cvt_pk_bf16_f32 v88, v72, v73\n"
"v_cvt_pk_bf16_f32 v89, v74, v75\n"
"v_cvt_pk_bf16_f32 v90, v76, v77\n"
"v_cvt_pk_bf16_f32 v91, v78, v79\n"
"ds_write_b64 v122, v[100:101] offset:2048\n"
"ds_write_b64 v123, v[102:103] offset:2048\n"
"ds_read_b128 v[104:107], v66 offset:0\n"
"ds_read_b128 v[108:111], v66 offset:1024\n"
"ds_read_b128 v[112:115], v66 offset:2048\n"
"ds_read_b128 v[116:119], v66 offset:3072\n"
"v_accvgpr_read_b32 v80, a152\n"
"v_accvgpr_read_b32 v81, a153\n"
"v_accvgpr_read_b32 v82, a154\n"
"v_accvgpr_read_b32 v83, a155\n"
"v_accvgpr_read_b32 v84, a176\n"
"v_accvgpr_read_b32 v85, a177\n"
"v_accvgpr_read_b32 v86, a178\n"
"v_accvgpr_read_b32 v87, a179\n"
"s_add_u32 s61, s59, 256\n"
"s_waitcnt lgkmcnt(3)\n"
"buffer_store_dwordx4 v[104:107], v67, s[52:55], s61 offen sc1\n"
"v_pk_mul_f32 v[80:81], v[80:81], v[56:57] op_sel_hi:[1,0]\n"
"v_pk_mul_f32 v[82:83], v[82:83], v[56:57] op_sel_hi:[1,0]\n"
"v_pk_mul_f32 v[84:85], v[84:85], v[56:57] op_sel_hi:[1,0]\n"
"v_pk_mul_f32 v[86:87], v[86:87], v[56:57] op_sel_hi:[1,0]\n"
"v_cvt_pk_bf16_f32 v92, v80, v81\n"
"v_cvt_pk_bf16_f32 v93, v82, v83\n"
"v_cvt_pk_bf16_f32 v94, v84, v85\n"
"v_cvt_pk_bf16_f32 v95, v86, v87\n"
"ds_write_b64 v120, v[88:89] offset:0\n"
"ds_write_b64 v121, v[90:91] offset:0\n"
"v_accvgpr_read_b32 v72, a108\n"
"v_accvgpr_read_b32 v73, a109\n"
"v_accvgpr_read_b32 v74, a110\n"
"v_accvgpr_read_b32 v75, a111\n"
"v_accvgpr_read_b32 v76, a132\n"
"v_accvgpr_read_b32 v77, a133\n"
"v_accvgpr_read_b32 v78, a134\n"
"v_accvgpr_read_b32 v79, a135\n"
"s_add_u32 s61, s59, 3328\n"
"s_waitcnt lgkmcnt(4)\n"
"buffer_store_dwordx4 v[108:111], v67, s[52:55], s61 offen sc1\n"
"v_pk_mul_f32 v[72:73], v[72:73], v[58:59] op_sel_hi:[1,0]\n"
"v_pk_mul_f32 v[74:75], v[74:75], v[58:59] op_sel_hi:[1,0]\n"
"v_pk_mul_f32 v[76:77], v[76:77], v[58:59] op_sel_hi:[1,0]\n"
"v_pk_mul_f32 v[78:79], v[78:79], v[58:59] op_sel_hi:[1,0]\n"
"v_cvt_pk_bf16_f32 v96, v72, v73\n"
"v_cvt_pk_bf16_f32 v97, v74, v75\n"
"v_cvt_pk_bf16_f32 v98, v76, v77\n"
"v_cvt_pk_bf16_f32 v99, v78, v79\n"
"ds_write_b64 v122, v[92:93] offset:0\n"
"ds_write_b64 v123, v[94:95] offset:0\n"
"v_accvgpr_read_b32 v80, a156\n"
"v_accvgpr_read_b32 v81, a157\n"
"v_accvgpr_read_b32 v82, a158\n"
"v_accvgpr_read_b32 v83, a159\n"
"v_accvgpr_read_b32 v84, a180\n"
"v_accvgpr_read_b32 v85, a181\n"
"v_accvgpr_read_b32 v86, a182\n"
"v_accvgpr_read_b32 v87, a183\n"
"s_add_u32 s61, s59, 6400\n"
"s_waitcnt lgkmcnt(5)\n"
"buffer_store_dwordx4 v[112:115], v67, s[52:55], s61 offen sc1\n"
"v_pk_mul_f32 v[80:81], v[80:81], v[58:59] op_sel_hi:[1,0]\n"
"v_pk_mul_f32 v[82:83], v[82:83], v[58:59] op_sel_hi:[1,0]\n"
"v_pk_mul_f32 v[84:85], v[84:85], v[58:59] op_sel_hi:[1,0]\n"
"v_pk_mul_f32 v[86:87], v[86:87], v[58:59] op_sel_hi:[1,0]\n"
"v_cvt_pk_bf16_f32 v100, v80, v81\n"
"v_cvt_pk_bf16_f32 v101, v82, v83\n"
"v_cvt_pk_bf16_f32 v102, v84, v85\n"
"v_cvt_pk_bf16_f32 v103, v86, v87\n"
"ds_write_b64 v120, v[96:97] offset:2048\n"
"ds_write_b64 v121, v[98:99] offset:2048\n"
"v_accvgpr_read_b32 v72, a200\n"
"v_accvgpr_read_b32 v73, a201\n"
"v_accvgpr_read_b32 v74, a202\n"
"v_accvgpr_read_b32 v75, a203\n"
"v_accvgpr_read_b32 v76, a224\n"
"v_accvgpr_read_b32 v77, a225\n"
"v_accvgpr_read_b32 v78, a226\n"
"v_accvgpr_read_b32 v79, a227\n"
"s_add_u32 s61, s59, 9472\n"
"s_waitcnt lgkmcnt(6)\n"
"buffer_store_dwordx4 v[116:119], v67, s[52:55], s61 offen sc1\n"
"v_pk_mul_f32 v[72:73], v[72:73], v[56:57] op_sel_hi:[1,0]\n"
"v_pk_mul_f32 v[74:75], v[74:75], v[56:57] op_sel_hi:[1,0]\n"
"v_pk_mul_f32 v[76:77], v[76:77], v[56:57] op_sel_hi:[1,0]\n"
"v_pk_mul_f32 v[78:79], v[78:79], v[56:57] op_sel_hi:[1,0]\n"
"v_cvt_pk_bf16_f32 v88, v72, v73\n"
"v_cvt_pk_bf16_f32 v89, v74, v75\n"
"v_cvt_pk_bf16_f32 v90, v76, v77\n"
"v_cvt_pk_bf16_f32 v91, v78, v79\n"
"ds_write_b64 v122, v[100:101] offset:2048\n"
"ds_write_b64 v123, v[102:103] offset:2048\n"
"ds_read_b128 v[28:31], v66 offset:0\n"
"ds_read_b128 v[32:35], v66 offset:1024\n"
"ds_read_b128 v[36:39], v66 offset:2048\n"
"ds_read_b128 v[40:43], v66 offset:3072\n"
"v_accvgpr_read_b32 v80, a248\n"
"v_accvgpr_read_b32 v81, a249\n"
"v_accvgpr_read_b32 v82, a250\n"
"v_accvgpr_read_b32 v83, a251\n"
"s_add_u32 s61, s59, 12288\n"
"s_waitcnt lgkmcnt(3)\n"
"buffer_store_dwordx4 v[28:31], v67, s[52:55], s61 offen sc1\n"
"v_pk_mul_f32 v[80:81], v[80:81], v[56:57] op_sel_hi:[1,0]\n"
"v_pk_mul_f32 v[82:83], v[82:83], v[56:57] op_sel_hi:[1,0]\n"
"v_pk_mul_f32 v[144:145], v[144:145], v[56:57] op_sel_hi:[1,0]\n"
"v_pk_mul_f32 v[146:147], v[146:147], v[56:57] op_sel_hi:[1,0]\n"
"v_cvt_pk_bf16_f32 v92, v80, v81\n"
"v_cvt_pk_bf16_f32 v93, v82, v83\n"
"v_cvt_pk_bf16_f32 v94, v144, v145\n"
"v_cvt_pk_bf16_f32 v95, v146, v147\n"
"ds_write_b64 v120, v[88:89] offset:0\n"
"ds_write_b64 v121, v[90:91] offset:0\n"
"v_accvgpr_read_b32 v72, a204\n"
"v_accvgpr_read_b32 v73, a205\n"
"v_accvgpr_read_b32 v74, a206\n"
"v_accvgpr_read_b32 v75, a207\n"
"v_accvgpr_read_b32 v76, a228\n"
"v_accvgpr_read_b32 v77, a229\n"
"v_accvgpr_read_b32 v78, a230\n"
"v_accvgpr_read_b32 v79, a231\n"
"s_add_u32 s61, s59, 15360\n"
"s_waitcnt lgkmcnt(4)\n"
"buffer_store_dwordx4 v[32:35], v67, s[52:55], s61 offen sc1\n"
"v_pk_mul_f32 v[72:73], v[72:73], v[58:59] op_sel_hi:[1,0]\n"
"v_pk_mul_f32 v[74:75], v[74:75], v[58:59] op_sel_hi:[1,0]\n"
"v_pk_mul_f32 v[76:77], v[76:77], v[58:59] op_sel_hi:[1,0]\n"
"v_pk_mul_f32 v[78:79], v[78:79], v[58:59] op_sel_hi:[1,0]\n"
"v_cvt_pk_bf16_f32 v96, v72, v73\n"
"v_cvt_pk_bf16_f32 v97, v74, v75\n"
"v_cvt_pk_bf16_f32 v98, v76, v77\n"
"v_cvt_pk_bf16_f32 v99, v78, v79\n"
"ds_write_b64 v122, v[92:93] offset:0\n"
"ds_write_b64 v123, v[94:95] offset:0\n"
"v_accvgpr_read_b32 v80, a252\n"
"v_accvgpr_read_b32 v81, a253\n"
"v_accvgpr_read_b32 v82, a254\n"
"v_accvgpr_read_b32 v83, a255\n"
"s_add_u32 s61, s59, 18432\n"
"s_waitcnt lgkmcnt(5)\n"
"buffer_store_dwordx4 v[36:39], v67, s[52:55], s61 offen sc1\n"
"v_pk_mul_f32 v[80:81], v[80:81], v[58:59] op_sel_hi:[1,0]\n"
"v_pk_mul_f32 v[82:83], v[82:83], v[58:59] op_sel_hi:[1,0]\n"
"v_pk_mul_f32 v[148:149], v[148:149], v[58:59] op_sel_hi:[1,0]\n"
"v_pk_mul_f32 v[150:151], v[150:151], v[58:59] op_sel_hi:[1,0]\n"
"v_cvt_pk_bf16_f32 v100, v80, v81\n"
"v_cvt_pk_bf16_f32 v101, v82, v83\n"
"v_cvt_pk_bf16_f32 v102, v148, v149\n"
"v_cvt_pk_bf16_f32 v103, v150, v151\n"
"ds_write_b64 v120, v[96:97] offset:2048\n"
"ds_write_b64 v121, v[98:99] offset:2048\n"
"s_add_u32 s61, s59, 21504\n"
"s_waitcnt lgkmcnt(6)\n"
"buffer_store_dwordx4 v[40:43], v67, s[52:55], s61 offen sc1\n"
"v_pk_mul_f32 v[168:169], v[168:169], v[56:57] op_sel_hi:[1,0]\n"
"v_pk_mul_f32 v[170:171], v[170:171], v[56:57] op_sel_hi:[1,0]\n"
"v_pk_mul_f32 v[192:193], v[192:193], v[56:57] op_sel_hi:[1,0]\n"
"v_pk_mul_f32 v[194:195], v[194:195], v[56:57] op_sel_hi:[1,0]\n"
"v_cvt_pk_bf16_f32 v88, v168, v169\n"
"v_cvt_pk_bf16_f32 v89, v170, v171\n"
"v_cvt_pk_bf16_f32 v90, v192, v193\n"
"v_cvt_pk_bf16_f32 v91, v194, v195\n"
"ds_write_b64 v122, v[100:101] offset:2048\n"
"ds_write_b64 v123, v[102:103] offset:2048\n"
"ds_read_b128 v[104:107], v66 offset:0\n"
"ds_read_b128 v[108:111], v66 offset:1024\n"
"ds_read_b128 v[112:115], v66 offset:2048\n"
"ds_read_b128 v[116:119], v66 offset:3072\n"
"s_add_u32 s61, s59, 12416\n"
"s_waitcnt lgkmcnt(3)\n"
"buffer_store_dwordx4 v[104:107], v67, s[52:55], s61 offen sc1\n"
"v_pk_mul_f32 v[216:217], v[216:217], v[56:57] op_sel_hi:[1,0]\n"
"v_pk_mul_f32 v[218:219], v[218:219], v[56:57] op_sel_hi:[1,0]\n"
"v_pk_mul_f32 v[240:241], v[240:241], v[56:57] op_sel_hi:[1,0]\n"
"v_pk_mul_f32 v[242:243], v[242:243], v[56:57] op_sel_hi:[1,0]\n"
"v_cvt_pk_bf16_f32 v92, v216, v217\n"
"v_cvt_pk_bf16_f32 v93, v218, v219\n"
"v_cvt_pk_bf16_f32 v94, v240, v241\n"
"v_cvt_pk_bf16_f32 v95, v242, v243\n"
"ds_write_b64 v120, v[88:89] offset:0\n"
"ds_write_b64 v121, v[90:91] offset:0\n"
"s_add_u32 s61, s59, 15488\n"
"s_waitcnt lgkmcnt(4)\n"
"buffer_store_dwordx4 v[108:111], v67, s[52:55], s61 offen sc1\n"
"v_pk_mul_f32 v[172:173], v[172:173], v[58:59] op_sel_hi:[1,0]\n"
"v_pk_mul_f32 v[174:175], v[174:175], v[58:59] op_sel_hi:[1,0]\n"
"v_pk_mul_f32 v[196:197], v[196:197], v[58:59] op_sel_hi:[1,0]\n"
"v_pk_mul_f32 v[198:199], v[198:199], v[58:59] op_sel_hi:[1,0]\n"
"v_cvt_pk_bf16_f32 v96, v172, v173\n"
"v_cvt_pk_bf16_f32 v97, v174, v175\n"
"v_cvt_pk_bf16_f32 v98, v196, v197\n"
"v_cvt_pk_bf16_f32 v99, v198, v199\n"
"ds_write_b64 v122, v[92:93] offset:0\n"
"ds_write_b64 v123, v[94:95] offset:0\n"
"s_add_u32 s61, s59, 18560\n"
"s_waitcnt lgkmcnt(5)\n"
"buffer_store_dwordx4 v[112:115], v67, s[52:55], s61 offen sc1\n"
"v_pk_mul_f32 v[220:221], v[220:221], v[58:59] op_sel_hi:[1,0]\n"
"v_pk_mul_f32 v[222:223], v[222:223], v[58:59] op_sel_hi:[1,0]\n"
"v_pk_mul_f32 v[244:245], v[244:245], v[58:59] op_sel_hi:[1,0]\n"
"v_pk_mul_f32 v[246:247], v[246:247], v[58:59] op_sel_hi:[1,0]\n"
"v_cvt_pk_bf16_f32 v100, v220, v221\n"
"v_cvt_pk_bf16_f32 v101, v222, v223\n"
"v_cvt_pk_bf16_f32 v102, v244, v245\n"
"v_cvt_pk_bf16_f32 v103, v246, v247\n"
"ds_write_b64 v120, v[96:97] offset:2048\n"
"ds_write_b64 v121, v[98:99] offset:2048\n"
"v_accvgpr_read_b32 v72, a112\n"
"v_accvgpr_read_b32 v73, a113\n"
"v_accvgpr_read_b32 v74, a114\n"
"v_accvgpr_read_b32 v75, a115\n"
"v_accvgpr_read_b32 v76, a136\n"
"v_accvgpr_read_b32 v77, a137\n"
"v_accvgpr_read_b32 v78, a138\n"
"v_accvgpr_read_b32 v79, a139\n"
"s_add_u32 s61, s59, 21632\n"
"s_waitcnt lgkmcnt(6)\n"
"buffer_store_dwordx4 v[116:119], v67, s[52:55], s61 offen sc1\n"
"s_waitcnt vmcnt(20)\n"
"v_mbcnt_lo_u32_b32 v126, -1, 0\n"
"v_mbcnt_hi_u32_b32 v126, -1, v126\n"
"v_bfe_u32 v126, v126, 3, 1\n"
"v_mul_u32_u24 v127, 0xc00, v126\n"
"v_xor_b32 v126, 1, v126\n"
"v_mul_u32_u24 v126, 0xc000, v126\n"
"v_sub_u32 v126, v126, v127\n"
"v_add_u32 v126, s67, v126\n"
"v_add_u32 v124, v1, v126\n"
"v_add_u32 v125, v2, v126\n"
"v_pk_mul_f32 v[72:73], v[72:73], v[60:61] op_sel_hi:[1,0]\n"
"v_pk_mul_f32 v[74:75], v[74:75], v[60:61] op_sel_hi:[1,0]\n"
"v_pk_mul_f32 v[76:77], v[76:77], v[60:61] op_sel_hi:[1,0]\n"
"v_pk_mul_f32 v[78:79], v[78:79], v[60:61] op_sel_hi:[1,0]\n"
"v_cvt_pk_bf16_f32 v88, v72, v73\n"
"v_cvt_pk_bf16_f32 v89, v74, v75\n"
"v_cvt_pk_bf16_f32 v90, v76, v77\n"
"v_cvt_pk_bf16_f32 v91, v78, v79\n"
"ds_write_b64 v122, v[100:101] offset:2048\n"
"ds_write_b64 v123, v[102:103] offset:2048\n"
"ds_read_b128 v[28:31], v66 offset:0\n"
"ds_read_b128 v[32:35], v66 offset:1024\n"
"ds_read_b128 v[36:39], v66 offset:2048\n"
"ds_read_b128 v[40:43], v66 offset:3072\n"
"v_accvgpr_read_b32 v80, a160\n"
"v_accvgpr_read_b32 v81, a161\n"
"v_accvgpr_read_b32 v82, a162\n"
"v_accvgpr_read_b32 v83, a163\n"
"v_accvgpr_read_b32 v84, a184\n"
"v_accvgpr_read_b32 v85, a185\n"
"v_accvgpr_read_b32 v86, a186\n"
"v_accvgpr_read_b32 v87, a187\n"
"s_add_u32 s61, s59, 12544\n"
"s_waitcnt lgkmcnt(3)\n"
"buffer_store_dwordx4 v[28:31], v67, s[52:55], s61 offen sc1\n"
"ds_read_b128 a[48:51], v124 offset:24576\n"
"ds_read_b128 a[52:55], v125 offset:24576\n"
"v_pk_mul_f32 v[80:81], v[80:81], v[60:61] op_sel_hi:[1,0]\n"
"v_pk_mul_f32 v[82:83], v[82:83], v[60:61] op_sel_hi:[1,0]\n"
"v_pk_mul_f32 v[84:85], v[84:85], v[60:61] op_sel_hi:[1,0]\n"
"v_pk_mul_f32 v[86:87], v[86:87], v[60:61] op_sel_hi:[1,0]\n"
"v_cvt_pk_bf16_f32 v92, v80, v81\n"
"v_cvt_pk_bf16_f32 v93, v82, v83\n"
"v_cvt_pk_bf16_f32 v94, v84, v85\n"
"v_cvt_pk_bf16_f32 v95, v86, v87\n"
"ds_write_b64 v120, v[88:89] offset:0\n"
"ds_write_b64 v121, v[90:91] offset:0\n"
"v_accvgpr_read_b32 v72, a116\n"
"v_accvgpr_read_b32 v73, a117\n"
"v_accvgpr_read_b32 v74, a118\n"
"v_accvgpr_read_b32 v75, a119\n"
"v_accvgpr_read_b32 v76, a140\n"
"v_accvgpr_read_b32 v77, a141\n"
"v_accvgpr_read_b32 v78, a142\n"
"v_accvgpr_read_b32 v79, a143\n"
"s_add_u32 s61, s59, 15616\n"
"s_waitcnt lgkmcnt(6)\n"
"buffer_store_dwordx4 v[32:35], v67, s[52:55], s61 offen sc1\n"
"ds_read_b128 a[56:59], v124 offset:25600\n"
"ds_read_b128 a[60:63], v125 offset:25600\n"
"v_pk_mul_f32 v[72:73], v[72:73], v[62:63] op_sel_hi:[1,0]\n"
"v_pk_mul_f32 v[74:75], v[74:75], v[62:63] op_sel_hi:[1,0]\n"
"v_pk_mul_f32 v[76:77], v[76:77], v[62:63] op_sel_hi:[1,0]\n"
"v_pk_mul_f32 v[78:79], v[78:79], v[62:63] op_sel_hi:[1,0]\n"
"v_cvt_pk_bf16_f32 v96, v72, v73\n"
"v_cvt_pk_bf16_f32 v97, v74, v75\n"
"v_cvt_pk_bf16_f32 v98, v76, v77\n"
"v_cvt_pk_bf16_f32 v99, v78, v79\n"
"ds_write_b64 v122, v[92:93] offset:0\n"
"ds_write_b64 v123, v[94:95] offset:0\n"
"v_accvgpr_read_b32 v80, a164\n"
"v_accvgpr_read_b32 v81, a165\n"
"v_accvgpr_read_b32 v82, a166\n"
"v_accvgpr_read_b32 v83, a167\n"
"v_accvgpr_read_b32 v84, a188\n"
"v_accvgpr_read_b32 v85, a189\n"
"v_accvgpr_read_b32 v86, a190\n"
"v_accvgpr_read_b32 v87, a191\n"
"s_add_u32 s61, s59, 18688\n"
"s_waitcnt lgkmcnt(9)\n"
"buffer_store_dwordx4 v[36:39], v67, s[52:55], s61 offen sc1\n"
"ds_read_b128 a[64:67], v124 offset:26624\n"
"ds_read_b128 a[68:71], v125 offset:26624\n"
"v_pk_mul_f32 v[80:81], v[80:81], v[62:63] op_sel_hi:[1,0]\n"
"v_pk_mul_f32 v[82:83], v[82:83], v[62:63] op_sel_hi:[1,0]\n"
"v_pk_mul_f32 v[84:85], v[84:85], v[62:63] op_sel_hi:[1,0]\n"
"v_pk_mul_f32 v[86:87], v[86:87], v[62:63] op_sel_hi:[1,0]\n"
"v_cvt_pk_bf16_f32 v100, v80, v81\n"
"v_cvt_pk_bf16_f32 v101, v82, v83\n"
"v_cvt_pk_bf16_f32 v102, v84, v85\n"
"v_cvt_pk_bf16_f32 v103, v86, v87\n"
"ds_write_b64 v120, v[96:97] offset:2048\n"
"ds_write_b64 v121, v[98:99] offset:2048\n"
"v_accvgpr_read_b32 v72, a208\n"
"v_accvgpr_read_b32 v73, a209\n"
"v_accvgpr_read_b32 v74, a210\n"
"v_accvgpr_read_b32 v75, a211\n"
"v_accvgpr_read_b32 v76, a232\n"
"v_accvgpr_read_b32 v77, a233\n"
"v_accvgpr_read_b32 v78, a234\n"
"v_accvgpr_read_b32 v79, a235\n"
"s_add_u32 s61, s59, 21760\n"
"s_waitcnt lgkmcnt(12)\n"
"buffer_store_dwordx4 v[40:43], v67, s[52:55], s61 offen sc1\n"
"ds_read_b128 a[72:75], v124 offset:36864\n"
"ds_read_b128 a[76:79], v125 offset:36864\n"
"v_pk_mul_f32 v[72:73], v[72:73], v[60:61] op_sel_hi:[1,0]\n"
"v_pk_mul_f32 v[74:75], v[74:75], v[60:61] op_sel_hi:[1,0]\n"
"v_pk_mul_f32 v[76:77], v[76:77], v[60:61] op_sel_hi:[1,0]\n"
"v_pk_mul_f32 v[78:79], v[78:79], v[60:61] op_sel_hi:[1,0]\n"
"v_cvt_pk_bf16_f32 v88, v72, v73\n"
"v_cvt_pk_bf16_f32 v89, v74, v75\n"
"v_cvt_pk_bf16_f32 v90, v76, v77\n"
"v_cvt_pk_bf16_f32 v91, v78, v79\n"
"ds_write_b64 v122, v[100:101] offset:2048\n"
"ds_write_b64 v123, v[102:103] offset:2048\n"
"ds_read_b128 v[104:107], v66 offset:0\n"
"ds_read_b128 v[108:111], v66 offset:1024\n"
"ds_read_b128 v[112:115], v66 offset:2048\n"
"ds_read_b128 v[116:119], v66 offset:3072\n"
"s_add_u32 s61, s59, 24576\n"
"s_waitcnt lgkmcnt(3)\n"
"buffer_store_dwordx4 v[104:107], v67, s[52:55], s61 offen sc1\n"
"ds_read_b128 a[80:83], v124 offset:37888\n"
"ds_read_b128 a[84:87], v125 offset:37888\n"
"v_pk_mul_f32 v[128:129], v[128:129], v[60:61] op_sel_hi:[1,0]\n"
"v_pk_mul_f32 v[130:131], v[130:131], v[60:61] op_sel_hi:[1,0]\n"
"v_pk_mul_f32 v[152:153], v[152:153], v[60:61] op_sel_hi:[1,0]\n"
"v_pk_mul_f32 v[154:155], v[154:155], v[60:61] op_sel_hi:[1,0]\n"
"v_cvt_pk_bf16_f32 v92, v128, v129\n"
"v_cvt_pk_bf16_f32 v93, v130, v131\n"
"v_cvt_pk_bf16_f32 v94, v152, v153\n"
"v_cvt_pk_bf16_f32 v95, v154, v155\n"
"ds_write_b64 v120, v[88:89] offset:0\n"
"ds_write_b64 v121, v[90:91] offset:0\n"
"v_accvgpr_read_b32 v72, a212\n"
"v_accvgpr_read_b32 v73, a213\n"
"v_accvgpr_read_b32 v74, a214\n"
"v_accvgpr_read_b32 v75, a215\n"
"v_accvgpr_read_b32 v76, a236\n"
"v_accvgpr_read_b32 v77, a237\n"
"v_accvgpr_read_b32 v78, a238\n"
"v_accvgpr_read_b32 v79, a239\n"
"s_add_u32 s61, s59, 27648\n"
"s_waitcnt lgkmcnt(6)\n"
"buffer_store_dwordx4 v[108:111], v67, s[52:55], s61 offen sc1\n"
"ds_read_b128 a[88:91], v124 offset:38912\n"
"ds_read_b128 a[92:95], v125 offset:38912\n"
"v_pk_mul_f32 v[72:73], v[72:73], v[62:63] op_sel_hi:[1,0]\n"
"v_pk_mul_f32 v[74:75], v[74:75], v[62:63] op_sel_hi:[1,0]\n"
"v_pk_mul_f32 v[76:77], v[76:77], v[62:63] op_sel_hi:[1,0]\n"
"v_pk_mul_f32 v[78:79], v[78:79], v[62:63] op_sel_hi:[1,0]\n"
"v_cvt_pk_bf16_f32 v96, v72, v73\n"
"v_cvt_pk_bf16_f32 v97, v74, v75\n"
"v_cvt_pk_bf16_f32 v98, v76, v77\n"
"v_cvt_pk_bf16_f32 v99, v78, v79\n"
"ds_write_b64 v122, v[92:93] offset:0\n"
"ds_write_b64 v123, v[94:95] offset:0\n"
"s_add_u32 s61, s59, 30720\n"
"s_waitcnt lgkmcnt(9)\n"
"buffer_store_dwordx4 v[112:115], v67, s[52:55], s61 offen sc1\n"
"v_pk_mul_f32 v[132:133], v[132:133], v[62:63] op_sel_hi:[1,0]\n"
"v_pk_mul_f32 v[134:135], v[134:135], v[62:63] op_sel_hi:[1,0]\n"
"v_pk_mul_f32 v[156:157], v[156:157], v[62:63] op_sel_hi:[1,0]\n"
"v_pk_mul_f32 v[158:159], v[158:159], v[62:63] op_sel_hi:[1,0]\n"
"v_cvt_pk_bf16_f32 v100, v132, v133\n"
"v_cvt_pk_bf16_f32 v101, v134, v135\n"
"v_cvt_pk_bf16_f32 v102, v156, v157\n"
"v_cvt_pk_bf16_f32 v103, v158, v159\n"
"ds_write_b64 v120, v[96:97] offset:2048\n"
"ds_write_b64 v121, v[98:99] offset:2048\n"
"s_add_u32 s61, s59, 33792\n"
"s_waitcnt lgkmcnt(10)\n"
"buffer_store_dwordx4 v[116:119], v67, s[52:55], s61 offen sc1\n"
"v_pk_mul_f32 v[176:177], v[176:177], v[60:61] op_sel_hi:[1,0]\n"
"v_pk_mul_f32 v[178:179], v[178:179], v[60:61] op_sel_hi:[1,0]\n"
"v_pk_mul_f32 v[200:201], v[200:201], v[60:61] op_sel_hi:[1,0]\n"
"v_pk_mul_f32 v[202:203], v[202:203], v[60:61] op_sel_hi:[1,0]\n"
"v_cvt_pk_bf16_f32 v88, v176, v177\n"
"v_cvt_pk_bf16_f32 v89, v178, v179\n"
"v_cvt_pk_bf16_f32 v90, v200, v201\n"
"v_cvt_pk_bf16_f32 v91, v202, v203\n"
"ds_write_b64 v122, v[100:101] offset:2048\n"
"ds_write_b64 v123, v[102:103] offset:2048\n"
"ds_read_b128 v[28:31], v66 offset:0\n"
"ds_read_b128 v[32:35], v66 offset:1024\n"
"ds_read_b128 v[36:39], v66 offset:2048\n"
"ds_read_b128 v[40:43], v66 offset:3072\n"
"s_add_u32 s61, s59, 24704\n"
"s_waitcnt lgkmcnt(3)\n"
"buffer_store_dwordx4 v[28:31], v67, s[52:55], s61 offen sc1\n"
"v_pk_mul_f32 v[224:225], v[224:225], v[60:61] op_sel_hi:[1,0]\n"
"v_pk_mul_f32 v[226:227], v[226:227], v[60:61] op_sel_hi:[1,0]\n"
"v_pk_mul_f32 v[248:249], v[248:249], v[60:61] op_sel_hi:[1,0]\n"
"v_pk_mul_f32 v[250:251], v[250:251], v[60:61] op_sel_hi:[1,0]\n"
"v_cvt_pk_bf16_f32 v92, v224, v225\n"
"v_cvt_pk_bf16_f32 v93, v226, v227\n"
"v_cvt_pk_bf16_f32 v94, v248, v249\n"
"v_cvt_pk_bf16_f32 v95, v250, v251\n"
"ds_write_b64 v120, v[88:89] offset:0\n"
"ds_write_b64 v121, v[90:91] offset:0\n"
"s_add_u32 s61, s59, 27776\n"
"s_waitcnt lgkmcnt(4)\n"
"buffer_store_dwordx4 v[32:35], v67, s[52:55], s61 offen sc1\n"
"v_pk_mul_f32 v[180:181], v[180:181], v[62:63] op_sel_hi:[1,0]\n"
"v_pk_mul_f32 v[182:183], v[182:183], v[62:63] op_sel_hi:[1,0]\n"
"v_pk_mul_f32 v[204:205], v[204:205], v[62:63] op_sel_hi:[1,0]\n"
"v_pk_mul_f32 v[206:207], v[206:207], v[62:63] op_sel_hi:[1,0]\n"
"v_cvt_pk_bf16_f32 v96, v180, v181\n"
"v_cvt_pk_bf16_f32 v97, v182, v183\n"
"v_cvt_pk_bf16_f32 v98, v204, v205\n"
"v_cvt_pk_bf16_f32 v99, v206, v207\n"
"ds_write_b64 v122, v[92:93] offset:0\n"
"ds_write_b64 v123, v[94:95] offset:0\n"
"s_add_u32 s61, s59, 30848\n"
"s_waitcnt lgkmcnt(5)\n"
"buffer_store_dwordx4 v[36:39], v67, s[52:55], s61 offen sc1\n"
"v_pk_mul_f32 v[228:229], v[228:229], v[62:63] op_sel_hi:[1,0]\n"
"v_pk_mul_f32 v[230:231], v[230:231], v[62:63] op_sel_hi:[1,0]\n"
"v_pk_mul_f32 v[252:253], v[252:253], v[62:63] op_sel_hi:[1,0]\n"
"v_pk_mul_f32 v[254:255], v[254:255], v[62:63] op_sel_hi:[1,0]\n"
"v_cvt_pk_bf16_f32 v100, v228, v229\n"
"v_cvt_pk_bf16_f32 v101, v230, v231\n"
"v_cvt_pk_bf16_f32 v102, v252, v253\n"
"v_cvt_pk_bf16_f32 v103, v254, v255\n"
"ds_write_b64 v120, v[96:97] offset:2048\n"
"ds_write_b64 v121, v[98:99] offset:2048\n"
"s_nop 0\n"
"ds_write_b64 v122, v[100:101] offset:2048\n"
"ds_write_b64 v123, v[102:103] offset:2048\n"
"ds_read_b128 v[104:107], v66 offset:0\n"
"ds_read_b128 v[108:111], v66 offset:1024\n"
"ds_read_b128 v[112:115], v66 offset:2048\n"
"ds_read_b128 v[116:119], v66 offset:3072\n"
"s_add_u32 s61, s59, 33920\n"
"s_waitcnt lgkmcnt(12)\n"
"buffer_store_dwordx4 v[40:43], v67, s[52:55], s61 offen sc1\n"
"s_add_u32 s61, s59, 24832\n"
"s_waitcnt lgkmcnt(3)\n"
"buffer_store_dwordx4 v[104:107], v67, s[52:55], s61 offen sc1\n"
"s_add_u32 s61, s59, 27904\n"
"s_waitcnt lgkmcnt(2)\n"
"buffer_store_dwordx4 v[108:111], v67, s[52:55], s61 offen sc1\n"
"s_add_u32 s61, s59, 30976\n"
"s_waitcnt lgkmcnt(1)\n"
"buffer_store_dwordx4 v[112:115], v67, s[52:55], s61 offen sc1\n"
"s_add_u32 s61, s59, 34048\n"
"s_waitcnt lgkmcnt(0)\n"
"buffer_store_dwordx4 v[116:119], v67, s[52:55], s61 offen sc1\n"
"v_xor_b32 v7, s74, v7\n"
".Lbodyend:\n"
"s_mov_b32 s57, s58\n"
"s_mov_b32 s58, 0x10000000\n"
"s_mov_b32 s59, s60\n"
"s_mov_b32 s60, 0x10000000\n"
"s_mov_b32 s65, 1\n"
"s_add_u32 s64, s64, 1\n"
"s_cmp_lt_u32 s64, 2\n"
"s_cbranch_scc1 .Ltop\n"
"s_waitcnt vmcnt(0)\n"
"s_branch .Lend\n"
".Lrare_1_a:\n"
"s_nop 7\n"
"v_max3_f32 v25, v112, v113, v114\n"
"v_max3_f32 v26, v115, v72, v73\n"
"v_max3_f32 v25, v25, v74, v75\n"
"v_max_f32 v25, v25, v26\n"
"v_mov_b32 v26, v25\n"
"s_nop 1\n"
"v_permlane32_swap_b32 v25, v26\n"
"s_nop 1\n"
"v_max_f32 v25, v25, v26\n"
"v_mov_b32 v26, v25\n"
"s_nop 1\n"
"v_permlane16_swap_b32 v25, v26\n"
"s_nop 1\n"
"v_max_f32 v25, v25, v26\n"
"v_mul_f32 v25, s62, v25\n"
"v_sub_f32 v25, 0, v25\n"
"v_min_f32 v25, v25, v9\n"
"v_sub_f32 v26, v25, v9\n"
"v_exp_f32 v26, v26\n"
"v_mov_b32 v9, v25\n"
"s_nop 0\n"
"v_mul_f32 v15, v15, v26\n"
"v_accvgpr_read_b32 v22, a96\n"
"v_accvgpr_read_b32 v23, a97\n"
"v_mul_f32 v22, v22, v26\n"
"v_mul_f32 v23, v23, v26\n"
"v_accvgpr_write_b32 a96, v22\n"
"v_accvgpr_write_b32 a97, v23\n"
"v_accvgpr_read_b32 v22, a98\n"
"v_accvgpr_read_b32 v23, a99\n"
"v_mul_f32 v22, v22, v26\n"
"v_mul_f32 v23, v23, v26\n"
"v_accvgpr_write_b32 a98, v22\n"
"v_accvgpr_write_b32 a99, v23\n"
"v_accvgpr_read_b32 v22, a120\n"
"v_accvgpr_read_b32 v23, a121\n"
"v_mul_f32 v22, v22, v26\n"
"v_mul_f32 v23, v23, v26\n"
"v_accvgpr_write_b32 a120, v22\n"
"v_accvgpr_write_b32 a121, v23\n"
"v_accvgpr_read_b32 v22, a122\n"
"v_accvgpr_read_b32 v23, a123\n"
"v_mul_f32 v22, v22, v26\n"
"v_mul_f32 v23, v23, v26\n"
"v_accvgpr_write_b32 a122, v22\n"
"v_accvgpr_write_b32 a123, v23\n"
"v_accvgpr_read_b32 v22, a144\n"
"v_accvgpr_read_b32 v23, a145\n"
"v_mul_f32 v22, v22, v26\n"
"v_mul_f32 v23, v23, v26\n"
"v_accvgpr_write_b32 a144, v22\n"
"v_accvgpr_write_b32 a145, v23\n"
"v_accvgpr_read_b32 v22, a146\n"
"v_accvgpr_read_b32 v23, a147\n"
"v_mul_f32 v22, v22, v26\n"
"v_mul_f32 v23, v23, v26\n"
"v_accvgpr_write_b32 a146, v22\n"
"v_accvgpr_write_b32 a147, v23\n"
"v_accvgpr_read_b32 v22, a168\n"
"v_accvgpr_read_b32 v23, a169\n"
"v_mul_f32 v22, v22, v26\n"
"v_mul_f32 v23, v23, v26\n"
"v_accvgpr_write_b32 a168, v22\n"
"v_accvgpr_write_b32 a169, v23\n"
"v_accvgpr_read_b32 v22, a170\n"
"v_accvgpr_read_b32 v23, a171\n"
"v_mul_f32 v22, v22, v26\n"
"v_mul_f32 v23, v23, v26\n"
"v_accvgpr_write_b32 a170, v22\n"
"v_accvgpr_write_b32 a171, v23\n"
"v_accvgpr_read_b32 v22, a192\n"
"v_accvgpr_read_b32 v23, a193\n"
"v_mul_f32 v22, v22, v26\n"
"v_mul_f32 v23, v23, v26\n"
"v_accvgpr_write_b32 a192, v22\n"
"v_accvgpr_write_b32 a193, v23\n"
"v_accvgpr_read_b32 v22, a194\n"
"v_accvgpr_read_b32 v23, a195\n"
"v_mul_f32 v22, v22, v26\n"
"v_mul_f32 v23, v23, v26\n"
"v_accvgpr_write_b32 a194, v22\n"
"v_accvgpr_write_b32 a195, v23\n"
"v_accvgpr_read_b32 v22, a216\n"
"v_accvgpr_read_b32 v23, a217\n"
"v_mul_f32 v22, v22, v26\n"
"v_mul_f32 v23, v23, v26\n"
"v_accvgpr_write_b32 a216, v22\n"
"v_accvgpr_write_b32 a217, v23\n"
"v_accvgpr_read_b32 v22, a218\n"
"v_accvgpr_read_b32 v23, a219\n"
"v_mul_f32 v22, v22, v26\n"
"v_mul_f32 v23, v23, v26\n"
"v_accvgpr_write_b32 a218, v22\n"
"v_accvgpr_write_b32 a219, v23\n"
"v_accvgpr_read_b32 v22, a240\n"
"v_accvgpr_read_b32 v23, a241\n"
"v_mul_f32 v22, v22, v26\n"
"v_mul_f32 v23, v23, v26\n"
"v_accvgpr_write_b32 a240, v22\n"
"v_accvgpr_write_b32 a241, v23\n"
"v_accvgpr_read_b32 v22, a242\n"
"v_accvgpr_read_b32 v23, a243\n"
"v_mul_f32 v22, v22, v26\n"
"v_mul_f32 v23, v23, v26\n"
"v_accvgpr_write_b32 a242, v22\n"
"v_accvgpr_write_b32 a243, v23\n"
"v_mul_f32 v136, v136, v26\n"
"v_mul_f32 v137, v137, v26\n"
"v_mul_f32 v138, v138, v26\n"
"v_mul_f32 v139, v139, v26\n"
"v_mul_f32 v160, v160, v26\n"
"v_mul_f32 v161, v161, v26\n"
"v_mul_f32 v162, v162, v26\n"
"v_mul_f32 v163, v163, v26\n"
"v_mul_f32 v184, v184, v26\n"
"v_mul_f32 v185, v185, v26\n"
"v_mul_f32 v186, v186, v26\n"
"v_mul_f32 v187, v187, v26\n"
"v_mul_f32 v208, v208, v26\n"
"v_mul_f32 v209, v209, v26\n"
"v_mul_f32 v210, v210, v26\n"
"v_mul_f32 v211, v211, v26\n"
"v_mul_f32 v232, v232, v26\n"
"v_mul_f32 v233, v233, v26\n"
"v_mul_f32 v234, v234, v26\n"
"v_mul_f32 v235, v235, v26\n"
"v_lshlrev_b32 v22, 16, v96\n"
"v_and_b32 v23, 0xffff0000, v96\n"
"v_mul_f32 v22, v22, v26\n"
"v_mul_f32 v23, v23, v26\n"
"v_cvt_pk_bf16_f32 v96, v22, v23\n"
"v_lshlrev_b32 v22, 16, v97\n"
"v_and_b32 v23, 0xffff0000, v97\n"
"v_mul_f32 v22, v22, v26\n"
"v_mul_f32 v23, v23, v26\n"
"v_cvt_pk_bf16_f32 v97, v22, v23\n"
"v_lshlrev_b32 v22, 16, v98\n"
"v_and_b32 v23, 0xffff0000, v98\n"
"v_mul_f32 v22, v22, v26\n"
"v_mul_f32 v23, v23, v26\n"
"v_cvt_pk_bf16_f32 v98, v22, v23\n"
"v_lshlrev_b32 v22, 16, v99\n"
"v_and_b32 v23, 0xffff0000, v99\n"
"v_mul_f32 v22, v22, v26\n"
"v_mul_f32 v23, v23, v26\n"
"v_cvt_pk_bf16_f32 v99, v22, v23\n"
"v_fma_f32 v22, v112, s62, v9\n"
"v_fma_f32 v23, v113, s62, v9\n"
"v_exp_f32 v22, v22\n"
"v_exp_f32 v23, v23\n"
"s_nop 0\n"
"v_add_f32 v21, v22, v23\n"
"v_cvt_pk_bf16_f32 v88, v22, v23\n"
"v_fma_f32 v22, v114, s62, v9\n"
"v_fma_f32 v23, v115, s62, v9\n"
"v_exp_f32 v22, v22\n"
"v_exp_f32 v23, v23\n"
"s_nop 0\n"
"v_add_f32 v21, v21, v22\n"
"v_add_f32 v21, v21, v23\n"
"v_cvt_pk_bf16_f32 v89, v22, v23\n"
"v_fma_f32 v22, v72, s62, v9\n"
"v_fma_f32 v23, v73, s62, v9\n"
"v_exp_f32 v22, v22\n"
"v_exp_f32 v23, v23\n"
"s_nop 0\n"
"v_add_f32 v21, v21, v22\n"
"v_add_f32 v21, v21, v23\n"
"v_cvt_pk_bf16_f32 v90, v22, v23\n"
"v_fma_f32 v22, v74, s62, v9\n"
"v_fma_f32 v23, v75, s62, v9\n"
"v_exp_f32 v22, v22\n"
"v_exp_f32 v23, v23\n"
"s_nop 0\n"
"v_add_f32 v21, v21, v22\n"
"v_add_f32 v21, v21, v23\n"
"v_cvt_pk_bf16_f32 v91, v22, v23\n"
"v_max3_f32 v25, v116, v117, v118\n"
"v_max3_f32 v26, v119, v52, v53\n"
"v_max3_f32 v25, v25, v54, v55\n"
"v_max_f32 v25, v25, v26\n"
"v_mov_b32 v26, v25\n"
"s_nop 1\n"
"v_permlane32_swap_b32 v25, v26\n"
"s_nop 1\n"
"v_max_f32 v25, v25, v26\n"
"v_mov_b32 v26, v25\n"
"s_nop 1\n"
"v_permlane16_swap_b32 v25, v26\n"
"s_nop 1\n"
"v_max_f32 v25, v25, v26\n"
"v_mul_f32 v25, s62, v25\n"
"v_sub_f32 v25, 0, v25\n"
"v_min_f32 v25, v25, v10\n"
"v_sub_f32 v26, v25, v10\n"
"v_exp_f32 v26, v26\n"
"v_mov_b32 v10, v25\n"
"s_nop 0\n"
"v_mul_f32 v16, v16, v26\n"
"v_accvgpr_read_b32 v22, a100\n"
"v_accvgpr_read_b32 v23, a101\n"
"v_mul_f32 v22, v22, v26\n"
"v_mul_f32 v23, v23, v26\n"
"v_accvgpr_write_b32 a100, v22\n"
"v_accvgpr_write_b32 a101, v23\n"
"v_accvgpr_read_b32 v22, a102\n"
"v_accvgpr_read_b32 v23, a103\n"
"v_mul_f32 v22, v22, v26\n"
"v_mul_f32 v23, v23, v26\n"
"v_accvgpr_write_b32 a102, v22\n"
"v_accvgpr_write_b32 a103, v23\n"
"v_accvgpr_read_b32 v22, a124\n"
"v_accvgpr_read_b32 v23, a125\n"
"v_mul_f32 v22, v22, v26\n"
"v_mul_f32 v23, v23, v26\n"
"v_accvgpr_write_b32 a124, v22\n"
"v_accvgpr_write_b32 a125, v23\n"
"v_accvgpr_read_b32 v22, a126\n"
"v_accvgpr_read_b32 v23, a127\n"
"v_mul_f32 v22, v22, v26\n"
"v_mul_f32 v23, v23, v26\n"
"v_accvgpr_write_b32 a126, v22\n"
"v_accvgpr_write_b32 a127, v23\n"
"v_accvgpr_read_b32 v22, a148\n"
"v_accvgpr_read_b32 v23, a149\n"
"v_mul_f32 v22, v22, v26\n"
"v_mul_f32 v23, v23, v26\n"
"v_accvgpr_write_b32 a148, v22\n"
"v_accvgpr_write_b32 a149, v23\n"
"v_accvgpr_read_b32 v22, a150\n"
"v_accvgpr_read_b32 v23, a151\n"
"v_mul_f32 v22, v22, v26\n"
"v_mul_f32 v23, v23, v26\n"
"v_accvgpr_write_b32 a150, v22\n"
"v_accvgpr_write_b32 a151, v23\n"
"v_accvgpr_read_b32 v22, a172\n"
"v_accvgpr_read_b32 v23, a173\n"
"v_mul_f32 v22, v22, v26\n"
"v_mul_f32 v23, v23, v26\n"
"v_accvgpr_write_b32 a172, v22\n"
"v_accvgpr_write_b32 a173, v23\n"
"v_accvgpr_read_b32 v22, a174\n"
"v_accvgpr_read_b32 v23, a175\n"
"v_mul_f32 v22, v22, v26\n"
"v_mul_f32 v23, v23, v26\n"
"v_accvgpr_write_b32 a174, v22\n"
"v_accvgpr_write_b32 a175, v23\n"
"v_accvgpr_read_b32 v22, a196\n"
"v_accvgpr_read_b32 v23, a197\n"
"v_mul_f32 v22, v22, v26\n"
"v_mul_f32 v23, v23, v26\n"
"v_accvgpr_write_b32 a196, v22\n"
"v_accvgpr_write_b32 a197, v23\n"
"v_accvgpr_read_b32 v22, a198\n"
"v_accvgpr_read_b32 v23, a199\n"
"v_mul_f32 v22, v22, v26\n"
"v_mul_f32 v23, v23, v26\n"
"v_accvgpr_write_b32 a198, v22\n"
"v_accvgpr_write_b32 a199, v23\n"
"v_accvgpr_read_b32 v22, a220\n"
"v_accvgpr_read_b32 v23, a221\n"
"v_mul_f32 v22, v22, v26\n"
"v_mul_f32 v23, v23, v26\n"
"v_accvgpr_write_b32 a220, v22\n"
"v_accvgpr_write_b32 a221, v23\n"
"v_accvgpr_read_b32 v22, a222\n"
"v_accvgpr_read_b32 v23, a223\n"
"v_mul_f32 v22, v22, v26\n"
"v_mul_f32 v23, v23, v26\n"
"v_accvgpr_write_b32 a222, v22\n"
"v_accvgpr_write_b32 a223, v23\n"
"v_accvgpr_read_b32 v22, a244\n"
"v_accvgpr_read_b32 v23, a245\n"
"v_mul_f32 v22, v22, v26\n"
"v_mul_f32 v23, v23, v26\n"
"v_accvgpr_write_b32 a244, v22\n"
"v_accvgpr_write_b32 a245, v23\n"
"v_accvgpr_read_b32 v22, a246\n"
"v_accvgpr_read_b32 v23, a247\n"
"v_mul_f32 v22, v22, v26\n"
"v_mul_f32 v23, v23, v26\n"
"v_accvgpr_write_b32 a246, v22\n"
"v_accvgpr_write_b32 a247, v23\n"
"v_mul_f32 v140, v140, v26\n"
"v_mul_f32 v141, v141, v26\n"
"v_mul_f32 v142, v142, v26\n"
"v_mul_f32 v143, v143, v26\n"
"v_mul_f32 v164, v164, v26\n"
"v_mul_f32 v165, v165, v26\n"
"v_mul_f32 v166, v166, v26\n"
"v_mul_f32 v167, v167, v26\n"
"v_mul_f32 v188, v188, v26\n"
"v_mul_f32 v189, v189, v26\n"
"v_mul_f32 v190, v190, v26\n"
"v_mul_f32 v191, v191, v26\n"
"v_mul_f32 v212, v212, v26\n"
"v_mul_f32 v213, v213, v26\n"
"v_mul_f32 v214, v214, v26\n"
"v_mul_f32 v215, v215, v26\n"
"v_mul_f32 v236, v236, v26\n"
"v_mul_f32 v237, v237, v26\n"
"v_mul_f32 v238, v238, v26\n"
"v_mul_f32 v239, v239, v26\n"
"v_lshlrev_b32 v22, 16, v100\n"
"v_and_b32 v23, 0xffff0000, v100\n"
"v_mul_f32 v22, v22, v26\n"
"v_mul_f32 v23, v23, v26\n"
"v_cvt_pk_bf16_f32 v100, v22, v23\n"
"v_lshlrev_b32 v22, 16, v101\n"
"v_and_b32 v23, 0xffff0000, v101\n"
"v_mul_f32 v22, v22, v26\n"
"v_mul_f32 v23, v23, v26\n"
"v_cvt_pk_bf16_f32 v101, v22, v23\n"
"v_lshlrev_b32 v22, 16, v102\n"
"v_and_b32 v23, 0xffff0000, v102\n"
"v_mul_f32 v22, v22, v26\n"
"v_mul_f32 v23, v23, v26\n"
"v_cvt_pk_bf16_f32 v102, v22, v23\n"
"v_lshlrev_b32 v22, 16, v103\n"
"v_and_b32 v23, 0xffff0000, v103\n"
"v_mul_f32 v22, v22, v26\n"
"v_mul_f32 v23, v23, v26\n"
"v_cvt_pk_bf16_f32 v103, v22, v23\n"
"v_fma_f32 v22, v116, s62, v10\n"
"v_fma_f32 v23, v117, s62, v10\n"
"v_exp_f32 v22, v22\n"
"v_exp_f32 v23, v23\n"
"s_nop 0\n"
"v_add_f32 v3, v22, v23\n"
"v_cvt_pk_bf16_f32 v68, v22, v23\n"
"v_fma_f32 v22, v118, s62, v10\n"
"v_fma_f32 v23, v119, s62, v10\n"
"v_exp_f32 v22, v22\n"
"v_exp_f32 v23, v23\n"
"s_nop 0\n"
"v_add_f32 v3, v3, v22\n"
"v_add_f32 v3, v3, v23\n"
"v_cvt_pk_bf16_f32 v69, v22, v23\n"
"v_fma_f32 v22, v52, s62, v10\n"
"v_fma_f32 v23, v53, s62, v10\n"
"v_exp_f32 v22, v22\n"
"v_exp_f32 v23, v23\n"
"s_nop 0\n"
"v_add_f32 v3, v3, v22\n"
"v_add_f32 v3, v3, v23\n"
"v_cvt_pk_bf16_f32 v70, v22, v23\n"
"v_fma_f32 v22, v54, s62, v10\n"
"v_fma_f32 v23, v55, s62, v10\n"
"v_exp_f32 v22, v22\n"
"v_exp_f32 v23, v23\n"
"s_nop 0\n"
"v_add_f32 v3, v3, v22\n"
"v_add_f32 v3, v3, v23\n"
"v_cvt_pk_bf16_f32 v71, v22, v23\n"
"v_fma_f32 v22, v120, s62, v11\n"
"v_fma_f32 v23, v121, s62, v11\n"
"s_nop 1\n"
"s_branch .Lret_1_a\n"
".Lrare_1_b:\n"
"s_nop 7\n"
"v_max3_f32 v25, v120, v121, v122\n"
"v_max3_f32 v26, v123, v76, v77\n"
"v_max3_f32 v25, v25, v78, v79\n"
"v_max_f32 v25, v25, v26\n"
"v_mov_b32 v26, v25\n"
"s_nop 1\n"
"v_permlane32_swap_b32 v25, v26\n"
"s_nop 1\n"
"v_max_f32 v25, v25, v26\n"
"v_mov_b32 v26, v25\n"
"s_nop 1\n"
"v_permlane16_swap_b32 v25, v26\n"
"s_nop 1\n"
"v_max_f32 v25, v25, v26\n"
"v_mul_f32 v25, s62, v25\n"
"v_sub_f32 v25, 0, v25\n"
"v_min_f32 v25, v25, v11\n"
"v_sub_f32 v26, v25, v11\n"
"v_exp_f32 v26, v26\n"
"v_mov_b32 v11, v25\n"
"s_nop 0\n"
"v_mul_f32 v17, v17, v26\n"
"v_accvgpr_read_b32 v22, a104\n"
"v_accvgpr_read_b32 v23, a105\n"
"v_mul_f32 v22, v22, v26\n"
"v_mul_f32 v23, v23, v26\n"
"v_accvgpr_write_b32 a104, v22\n"
"v_accvgpr_write_b32 a105, v23\n"
"v_accvgpr_read_b32 v22, a106\n"
"v_accvgpr_read_b32 v23, a107\n"
"v_mul_f32 v22, v22, v26\n"
"v_mul_f32 v23, v23, v26\n"
"v_accvgpr_write_b32 a106, v22\n"
"v_accvgpr_write_b32 a107, v23\n"
"v_accvgpr_read_b32 v22, a128\n"
"v_accvgpr_read_b32 v23, a129\n"
"v_mul_f32 v22, v22, v26\n"
"v_mul_f32 v23, v23, v26\n"
"v_accvgpr_write_b32 a128, v22\n"
"v_accvgpr_write_b32 a129, v23\n"
"v_accvgpr_read_b32 v22, a130\n"
"v_accvgpr_read_b32 v23, a131\n"
"v_mul_f32 v22, v22, v26\n"
"v_mul_f32 v23, v23, v26\n"
"v_accvgpr_write_b32 a130, v22\n"
"v_accvgpr_write_b32 a131, v23\n"
"v_accvgpr_read_b32 v22, a152\n"
"v_accvgpr_read_b32 v23, a153\n"
"v_mul_f32 v22, v22, v26\n"
"v_mul_f32 v23, v23, v26\n"
"v_accvgpr_write_b32 a152, v22\n"
"v_accvgpr_write_b32 a153, v23\n"
"v_accvgpr_read_b32 v22, a154\n"
"v_accvgpr_read_b32 v23, a155\n"
"v_mul_f32 v22, v22, v26\n"
"v_mul_f32 v23, v23, v26\n"
"v_accvgpr_write_b32 a154, v22\n"
"v_accvgpr_write_b32 a155, v23\n"
"v_accvgpr_read_b32 v22, a176\n"
"v_accvgpr_read_b32 v23, a177\n"
"v_mul_f32 v22, v22, v26\n"
"v_mul_f32 v23, v23, v26\n"
"v_accvgpr_write_b32 a176, v22\n"
"v_accvgpr_write_b32 a177, v23\n"
"v_accvgpr_read_b32 v22, a178\n"
"v_accvgpr_read_b32 v23, a179\n"
"v_mul_f32 v22, v22, v26\n"
"v_mul_f32 v23, v23, v26\n"
"v_accvgpr_write_b32 a178, v22\n"
"v_accvgpr_write_b32 a179, v23\n"
"v_accvgpr_read_b32 v22, a200\n"
"v_accvgpr_read_b32 v23, a201\n"
"v_mul_f32 v22, v22, v26\n"
"v_mul_f32 v23, v23, v26\n"
"v_accvgpr_write_b32 a200, v22\n"
"v_accvgpr_write_b32 a201, v23\n"
"v_accvgpr_read_b32 v22, a202\n"
"v_accvgpr_read_b32 v23, a203\n"
"v_mul_f32 v22, v22, v26\n"
"v_mul_f32 v23, v23, v26\n"
"v_accvgpr_write_b32 a202, v22\n"
"v_accvgpr_write_b32 a203, v23\n"
"v_accvgpr_read_b32 v22, a224\n"
"v_accvgpr_read_b32 v23, a225\n"
"v_mul_f32 v22, v22, v26\n"
"v_mul_f32 v23, v23, v26\n"
"v_accvgpr_write_b32 a224, v22\n"
"v_accvgpr_write_b32 a225, v23\n"
"v_accvgpr_read_b32 v22, a226\n"
"v_accvgpr_read_b32 v23, a227\n"
"v_mul_f32 v22, v22, v26\n"
"v_mul_f32 v23, v23, v26\n"
"v_accvgpr_write_b32 a226, v22\n"
"v_accvgpr_write_b32 a227, v23\n"
"v_accvgpr_read_b32 v22, a248\n"
"v_accvgpr_read_b32 v23, a249\n"
"v_mul_f32 v22, v22, v26\n"
"v_mul_f32 v23, v23, v26\n"
"v_accvgpr_write_b32 a248, v22\n"
"v_accvgpr_write_b32 a249, v23\n"
"v_accvgpr_read_b32 v22, a250\n"
"v_accvgpr_read_b32 v23, a251\n"
"v_mul_f32 v22, v22, v26\n"
"v_mul_f32 v23, v23, v26\n"
"v_accvgpr_write_b32 a250, v22\n"
"v_accvgpr_write_b32 a251, v23\n"
"v_mul_f32 v144, v144, v26\n"
"v_mul_f32 v145, v145, v26\n"
"v_mul_f32 v146, v146, v26\n"
"v_mul_f32 v147, v147, v26\n"
"v_mul_f32 v168, v168, v26\n"
"v_mul_f32 v169, v169, v26\n"
"v_mul_f32 v170, v170, v26\n"
"v_mul_f32 v171, v171, v26\n"
"v_mul_f32 v192, v192, v26\n"
"v_mul_f32 v193, v193, v26\n"
"v_mul_f32 v194, v194, v26\n"
"v_mul_f32 v195, v195, v26\n"
"v_mul_f32 v216, v216, v26\n"
"v_mul_f32 v217, v217, v26\n"
"v_mul_f32 v218, v218, v26\n"
"v_mul_f32 v219, v219, v26\n"
"v_mul_f32 v240, v240, v26\n"
"v_mul_f32 v241, v241, v26\n"
"v_mul_f32 v242, v242, v26\n"
"v_mul_f32 v243, v243, v26\n"
"v_lshlrev_b32 v22, 16, v104\n"
"v_and_b32 v23, 0xffff0000, v104\n"
"v_mul_f32 v22, v22, v26\n"
"v_mul_f32 v23, v23, v26\n"
"v_cvt_pk_bf16_f32 v104, v22, v23\n"
"v_lshlrev_b32 v22, 16, v105\n"
"v_and_b32 v23, 0xffff0000, v105\n"
"v_mul_f32 v22, v22, v26\n"
"v_mul_f32 v23, v23, v26\n"
"v_cvt_pk_bf16_f32 v105, v22, v23\n"
"v_lshlrev_b32 v22, 16, v106\n"
"v_and_b32 v23, 0xffff0000, v106\n"
"v_mul_f32 v22, v22, v26\n"
"v_mul_f32 v23, v23, v26\n"
"v_cvt_pk_bf16_f32 v106, v22, v23\n"
"v_lshlrev_b32 v22, 16, v107\n"
"v_and_b32 v23, 0xffff0000, v107\n"
"v_mul_f32 v22, v22, v26\n"
"v_mul_f32 v23, v23, v26\n"
"v_cvt_pk_bf16_f32 v107, v22, v23\n"
"v_fma_f32 v22, v120, s62, v11\n"
"v_fma_f32 v23, v121, s62, v11\n"
"v_exp_f32 v22, v22\n"
"v_exp_f32 v23, v23\n"
"s_nop 0\n"
"v_add_f32 v72, v22, v23\n"
"v_cvt_pk_bf16_f32 v92, v22, v23\n"
"v_fma_f32 v22, v122, s62, v11\n"
"v_fma_f32 v23, v123, s62, v11\n"
"v_exp_f32 v22, v22\n"
"v_exp_f32 v23, v23\n"
"s_nop 0\n"
"v_add_f32 v72, v72, v22\n"
"v_add_f32 v72, v72, v23\n"
"v_cvt_pk_bf16_f32 v93, v22, v23\n"
"v_fma_f32 v22, v76, s62, v11\n"
"v_fma_f32 v23, v77, s62, v11\n"
"v_exp_f32 v22, v22\n"
"v_exp_f32 v23, v23\n"
"s_nop 0\n"
"v_add_f32 v72, v72, v22\n"
"v_add_f32 v72, v72, v23\n"
"v_cvt_pk_bf16_f32 v94, v22, v23\n"
"v_fma_f32 v22, v78, s62, v11\n"
"v_fma_f32 v23, v79, s62, v11\n"
"v_exp_f32 v22, v22\n"
"v_exp_f32 v23, v23\n"
"s_nop 0\n"
"v_add_f32 v72, v72, v22\n"
"v_add_f32 v72, v72, v23\n"
"v_cvt_pk_bf16_f32 v95, v22, v23\n"
"v_max3_f32 v25, v124, v125, v126\n"
"v_max3_f32 v26, v127, v56, v57\n"
"v_max3_f32 v25, v25, v58, v59\n"
"v_max_f32 v25, v25, v26\n"
"v_mov_b32 v26, v25\n"
"s_nop 1\n"
"v_permlane32_swap_b32 v25, v26\n"
"s_nop 1\n"
"v_max_f32 v25, v25, v26\n"
"v_mov_b32 v26, v25\n"
"s_nop 1\n"
"v_permlane16_swap_b32 v25, v26\n"
"s_nop 1\n"
"v_max_f32 v25, v25, v26\n"
"v_mul_f32 v25, s62, v25\n"
"v_sub_f32 v25, 0, v25\n"
"v_min_f32 v25, v25, v12\n"
"v_sub_f32 v26, v25, v12\n"
"v_exp_f32 v26, v26\n"
"v_mov_b32 v12, v25\n"
"s_nop 0\n"
"v_mul_f32 v18, v18, v26\n"
"v_accvgpr_read_b32 v22, a108\n"
"v_accvgpr_read_b32 v23, a109\n"
"v_mul_f32 v22, v22, v26\n"
"v_mul_f32 v23, v23, v26\n"
"v_accvgpr_write_b32 a108, v22\n"
"v_accvgpr_write_b32 a109, v23\n"
"v_accvgpr_read_b32 v22, a110\n"
"v_accvgpr_read_b32 v23, a111\n"
"v_mul_f32 v22, v22, v26\n"
"v_mul_f32 v23, v23, v26\n"
"v_accvgpr_write_b32 a110, v22\n"
"v_accvgpr_write_b32 a111, v23\n"
"v_accvgpr_read_b32 v22, a132\n"
"v_accvgpr_read_b32 v23, a133\n"
"v_mul_f32 v22, v22, v26\n"
"v_mul_f32 v23, v23, v26\n"
"v_accvgpr_write_b32 a132, v22\n"
"v_accvgpr_write_b32 a133, v23\n"
"v_accvgpr_read_b32 v22, a134\n"
"v_accvgpr_read_b32 v23, a135\n"
"v_mul_f32 v22, v22, v26\n"
"v_mul_f32 v23, v23, v26\n"
"v_accvgpr_write_b32 a134, v22\n"
"v_accvgpr_write_b32 a135, v23\n"
"v_accvgpr_read_b32 v22, a156\n"
"v_accvgpr_read_b32 v23, a157\n"
"v_mul_f32 v22, v22, v26\n"
"v_mul_f32 v23, v23, v26\n"
"v_accvgpr_write_b32 a156, v22\n"
"v_accvgpr_write_b32 a157, v23\n"
"v_accvgpr_read_b32 v22, a158\n"
"v_accvgpr_read_b32 v23, a159\n"
"v_mul_f32 v22, v22, v26\n"
"v_mul_f32 v23, v23, v26\n"
"v_accvgpr_write_b32 a158, v22\n"
"v_accvgpr_write_b32 a159, v23\n"
"v_accvgpr_read_b32 v22, a180\n"
"v_accvgpr_read_b32 v23, a181\n"
"v_mul_f32 v22, v22, v26\n"
"v_mul_f32 v23, v23, v26\n"
"v_accvgpr_write_b32 a180, v22\n"
"v_accvgpr_write_b32 a181, v23\n"
"v_accvgpr_read_b32 v22, a182\n"
"v_accvgpr_read_b32 v23, a183\n"
"v_mul_f32 v22, v22, v26\n"
"v_mul_f32 v23, v23, v26\n"
"v_accvgpr_write_b32 a182, v22\n"
"v_accvgpr_write_b32 a183, v23\n"
"v_accvgpr_read_b32 v22, a204\n"
"v_accvgpr_read_b32 v23, a205\n"
"v_mul_f32 v22, v22, v26\n"
"v_mul_f32 v23, v23, v26\n"
"v_accvgpr_write_b32 a204, v22\n"
"v_accvgpr_write_b32 a205, v23\n"
"v_accvgpr_read_b32 v22, a206\n"
"v_accvgpr_read_b32 v23, a207\n"
"v_mul_f32 v22, v22, v26\n"
"v_mul_f32 v23, v23, v26\n"
"v_accvgpr_write_b32 a206, v22\n"
"v_accvgpr_write_b32 a207, v23\n"
"v_accvgpr_read_b32 v22, a228\n"
"v_accvgpr_read_b32 v23, a229\n"
"v_mul_f32 v22, v22, v26\n"
"v_mul_f32 v23, v23, v26\n"
"v_accvgpr_write_b32 a228, v22\n"
"v_accvgpr_write_b32 a229, v23\n"
"v_accvgpr_read_b32 v22, a230\n"
"v_accvgpr_read_b32 v23, a231\n"
"v_mul_f32 v22, v22, v26\n"
"v_mul_f32 v23, v23, v26\n"
"v_accvgpr_write_b32 a230, v22\n"
"v_accvgpr_write_b32 a231, v23\n"
"v_accvgpr_read_b32 v22, a252\n"
"v_accvgpr_read_b32 v23, a253\n"
"v_mul_f32 v22, v22, v26\n"
"v_mul_f32 v23, v23, v26\n"
"v_accvgpr_write_b32 a252, v22\n"
"v_accvgpr_write_b32 a253, v23\n"
"v_accvgpr_read_b32 v22, a254\n"
"v_accvgpr_read_b32 v23, a255\n"
"v_mul_f32 v22, v22, v26\n"
"v_mul_f32 v23, v23, v26\n"
"v_accvgpr_write_b32 a254, v22\n"
"v_accvgpr_write_b32 a255, v23\n"
"v_mul_f32 v148, v148, v26\n"
"v_mul_f32 v149, v149, v26\n"
"v_mul_f32 v150, v150, v26\n"
"v_mul_f32 v151, v151, v26\n"
"v_mul_f32 v172, v172, v26\n"
"v_mul_f32 v173, v173, v26\n"
"v_mul_f32 v174, v174, v26\n"
"v_mul_f32 v175, v175, v26\n"
"v_mul_f32 v196, v196, v26\n"
"v_mul_f32 v197, v197, v26\n"
"v_mul_f32 v198, v198, v26\n"
"v_mul_f32 v199, v199, v26\n"
"v_mul_f32 v220, v220, v26\n"
"v_mul_f32 v221, v221, v26\n"
"v_mul_f32 v222, v222, v26\n"
"v_mul_f32 v223, v223, v26\n"
"v_mul_f32 v244, v244, v26\n"
"v_mul_f32 v245, v245, v26\n"
"v_mul_f32 v246, v246, v26\n"
"v_mul_f32 v247, v247, v26\n"
"v_lshlrev_b32 v22, 16, v108\n"
"v_and_b32 v23, 0xffff0000, v108\n"
"v_mul_f32 v22, v22, v26\n"
"v_mul_f32 v23, v23, v26\n"
"v_cvt_pk_bf16_f32 v108, v22, v23\n"
"v_lshlrev_b32 v22, 16, v109\n"
"v_and_b32 v23, 0xffff0000, v109\n"
"v_mul_f32 v22, v22, v26\n"
"v_mul_f32 v23, v23, v26\n"
"v_cvt_pk_bf16_f32 v109, v22, v23\n"
"v_lshlrev_b32 v22, 16, v110\n"
"v_and_b32 v23, 0xffff0000, v110\n"
"v_mul_f32 v22, v22, v26\n"
"v_mul_f32 v23, v23, v26\n"
"v_cvt_pk_bf16_f32 v110, v22, v23\n"
"v_lshlrev_b32 v22, 16, v111\n"
"v_and_b32 v23, 0xffff0000, v111\n"
"v_mul_f32 v22, v22, v26\n"
"v_mul_f32 v23, v23, v26\n"
"v_cvt_pk_bf16_f32 v111, v22, v23\n"
"v_fma_f32 v22, v124, s62, v12\n"
"v_fma_f32 v23, v125, s62, v12\n"
"v_exp_f32 v22, v22\n"
"v_exp_f32 v23, v23\n"
"s_nop 0\n"
"v_add_f32 v73, v22, v23\n"
"v_cvt_pk_bf16_f32 v112, v22, v23\n"
"v_fma_f32 v22, v126, s62, v12\n"
"v_fma_f32 v23, v127, s62, v12\n"
"v_exp_f32 v22, v22\n"
"v_exp_f32 v23, v23\n"
"s_nop 0\n"
"v_add_f32 v73, v73, v22\n"
"v_add_f32 v73, v73, v23\n"
"v_cvt_pk_bf16_f32 v113, v22, v23\n"
"v_fma_f32 v22, v56, s62, v12\n"
"v_fma_f32 v23, v57, s62, v12\n"
"v_exp_f32 v22, v22\n"
"v_exp_f32 v23, v23\n"
"s_nop 0\n"
"v_add_f32 v73, v73, v22\n"
"v_add_f32 v73, v73, v23\n"
"v_cvt_pk_bf16_f32 v114, v22, v23\n"
"v_fma_f32 v22, v58, s62, v12\n"
"v_fma_f32 v23, v59, s62, v12\n"
"v_exp_f32 v22, v22\n"
"v_exp_f32 v23, v23\n"
"s_nop 0\n"
"v_add_f32 v73, v73, v22\n"
"v_add_f32 v73, v73, v23\n"
"v_cvt_pk_bf16_f32 v115, v22, v23\n"
"v_fma_f32 v22, v28, s62, v13\n"
"v_fma_f32 v23, v29, s62, v13\n"
"s_nop 1\n"
"s_branch .Lret_1_b\n"
".Lrare_1_d4:\n"
"s_nop 7\n"
"v_max3_f32 v25, v28, v29, v30\n"
"v_max3_f32 v26, v31, v80, v81\n"
"v_max3_f32 v25, v25, v82, v83\n"
"v_max_f32 v25, v25, v26\n"
"v_mov_b32 v26, v25\n"
"s_nop 1\n"
"v_permlane32_swap_b32 v25, v26\n"
"s_nop 1\n"
"v_max_f32 v25, v25, v26\n"
"v_mov_b32 v26, v25\n"
"s_nop 1\n"
"v_permlane16_swap_b32 v25, v26\n"
"s_nop 1\n"
"v_max_f32 v25, v25, v26\n"
"v_mul_f32 v25, s62, v25\n"
"v_sub_f32 v25, 0, v25\n"
"v_min_f32 v25, v25, v13\n"
"v_sub_f32 v26, v25, v13\n"
"v_exp_f32 v26, v26\n"
"v_mov_b32 v13, v25\n"
"s_nop 0\n"
"v_mul_f32 v19, v19, v26\n"
"v_accvgpr_read_b32 v22, a112\n"
"v_accvgpr_read_b32 v23, a113\n"
"v_mul_f32 v22, v22, v26\n"
"v_mul_f32 v23, v23, v26\n"
"v_accvgpr_write_b32 a112, v22\n"
"v_accvgpr_write_b32 a113, v23\n"
"v_accvgpr_read_b32 v22, a114\n"
"v_accvgpr_read_b32 v23, a115\n"
"v_mul_f32 v22, v22, v26\n"
"v_mul_f32 v23, v23, v26\n"
"v_accvgpr_write_b32 a114, v22\n"
"v_accvgpr_write_b32 a115, v23\n"
"v_accvgpr_read_b32 v22, a136\n"
"v_accvgpr_read_b32 v23, a137\n"
"v_mul_f32 v22, v22, v26\n"
"v_mul_f32 v23, v23, v26\n"
"v_accvgpr_write_b32 a136, v22\n"
"v_accvgpr_write_b32 a137, v23\n"
"v_accvgpr_read_b32 v22, a138\n"
"v_accvgpr_read_b32 v23, a139\n"
"v_mul_f32 v22, v22, v26\n"
"v_mul_f32 v23, v23, v26\n"
"v_accvgpr_write_b32 a138, v22\n"
"v_accvgpr_write_b32 a139, v23\n"
"v_accvgpr_read_b32 v22, a160\n"
"v_accvgpr_read_b32 v23, a161\n"
"v_mul_f32 v22, v22, v26\n"
"v_mul_f32 v23, v23, v26\n"
"v_accvgpr_write_b32 a160, v22\n"
"v_accvgpr_write_b32 a161, v23\n"
"v_accvgpr_read_b32 v22, a162\n"
"v_accvgpr_read_b32 v23, a163\n"
"v_mul_f32 v22, v22, v26\n"
"v_mul_f32 v23, v23, v26\n"
"v_accvgpr_write_b32 a162, v22\n"
"v_accvgpr_write_b32 a163, v23\n"
"v_accvgpr_read_b32 v22, a184\n"
"v_accvgpr_read_b32 v23, a185\n"
"v_mul_f32 v22, v22, v26\n"
"v_mul_f32 v23, v23, v26\n"
"v_accvgpr_write_b32 a184, v22\n"
"v_accvgpr_write_b32 a185, v23\n"
"v_accvgpr_read_b32 v22, a186\n"
"v_accvgpr_read_b32 v23, a187\n"
"v_mul_f32 v22, v22, v26\n"
"v_mul_f32 v23, v23, v26\n"
"v_accvgpr_write_b32 a186, v22\n"
"v_accvgpr_write_b32 a187, v23\n"
"v_accvgpr_read_b32 v22, a208\n"
"v_accvgpr_read_b32 v23, a209\n"
"v_mul_f32 v22, v22, v26\n"
"v_mul_f32 v23, v23, v26\n"
"v_accvgpr_write_b32 a208, v22\n"
"v_accvgpr_write_b32 a209, v23\n"
"v_accvgpr_read_b32 v22, a210\n"
"v_accvgpr_read_b32 v23, a211\n"
"v_mul_f32 v22, v22, v26\n"
"v_mul_f32 v23, v23, v26\n"
"v_accvgpr_write_b32 a210, v22\n"
"v_accvgpr_write_b32 a211, v23\n"
"v_accvgpr_read_b32 v22, a232\n"
"v_accvgpr_read_b32 v23, a233\n"
"v_mul_f32 v22, v22, v26\n"
"v_mul_f32 v23, v23, v26\n"
"v_accvgpr_write_b32 a232, v22\n"
"v_accvgpr_write_b32 a233, v23\n"
"v_accvgpr_read_b32 v22, a234\n"
"v_accvgpr_read_b32 v23, a235\n"
"v_mul_f32 v22, v22, v26\n"
"v_mul_f32 v23, v23, v26\n"
"v_accvgpr_write_b32 a234, v22\n"
"v_accvgpr_write_b32 a235, v23\n"
"v_mul_f32 v128, v128, v26\n"
"v_mul_f32 v129, v129, v26\n"
"v_mul_f32 v130, v130, v26\n"
"v_mul_f32 v131, v131, v26\n"
"v_mul_f32 v152, v152, v26\n"
"v_mul_f32 v153, v153, v26\n"
"v_mul_f32 v154, v154, v26\n"
"v_mul_f32 v155, v155, v26\n"
"v_mul_f32 v176, v176, v26\n"
"v_mul_f32 v177, v177, v26\n"
"v_mul_f32 v178, v178, v26\n"
"v_mul_f32 v179, v179, v26\n"
"v_mul_f32 v200, v200, v26\n"
"v_mul_f32 v201, v201, v26\n"
"v_mul_f32 v202, v202, v26\n"
"v_mul_f32 v203, v203, v26\n"
"v_mul_f32 v224, v224, v26\n"
"v_mul_f32 v225, v225, v26\n"
"v_mul_f32 v226, v226, v26\n"
"v_mul_f32 v227, v227, v26\n"
"v_mul_f32 v248, v248, v26\n"
"v_mul_f32 v249, v249, v26\n"
"v_mul_f32 v250, v250, v26\n"
"v_mul_f32 v251, v251, v26\n"
"v_fma_f32 v22, v28, s62, v13\n"
"v_fma_f32 v23, v29, s62, v13\n"
"v_exp_f32 v22, v22\n"
"v_exp_f32 v23, v23\n"
"s_nop 0\n"
"v_add_f32 v21, v22, v23\n"
"v_cvt_pk_bf16_f32 v64, v22, v23\n"
"v_fma_f32 v22, v30, s62, v13\n"
"v_fma_f32 v23, v31, s62, v13\n"
"v_exp_f32 v22, v22\n"
"v_exp_f32 v23, v23\n"
"s_nop 0\n"
"v_add_f32 v21, v21, v22\n"
"v_add_f32 v21, v21, v23\n"
"v_cvt_pk_bf16_f32 v65, v22, v23\n"
"v_fma_f32 v22, v80, s62, v13\n"
"v_fma_f32 v23, v81, s62, v13\n"
"v_exp_f32 v22, v22\n"
"v_exp_f32 v23, v23\n"
"s_nop 0\n"
"v_add_f32 v21, v21, v22\n"
"v_add_f32 v21, v21, v23\n"
"v_cvt_pk_bf16_f32 v66, v22, v23\n"
"v_fma_f32 v22, v82, s62, v13\n"
"v_fma_f32 v23, v83, s62, v13\n"
"v_exp_f32 v22, v22\n"
"v_exp_f32 v23, v23\n"
"s_nop 0\n"
"v_add_f32 v21, v21, v22\n"
"v_add_f32 v21, v21, v23\n"
"v_cvt_pk_bf16_f32 v67, v22, v23\n"
"v_fma_f32 v22, v48, s62, v14\n"
"v_fma_f32 v23, v49, s62, v14\n"
"s_nop 1\n"
"s_branch .Lret_1_d4\n"
".Lrare_1_d5:\n"
"s_nop 7\n"
"v_max3_f32 v25, v48, v49, v50\n"
"v_max3_f32 v26, v51, v60, v61\n"
"v_max3_f32 v25, v25, v62, v63\n"
"v_max_f32 v25, v25, v26\n"
"v_mov_b32 v26, v25\n"
"s_nop 1\n"
"v_permlane32_swap_b32 v25, v26\n"
"s_nop 1\n"
"v_max_f32 v25, v25, v26\n"
"v_mov_b32 v26, v25\n"
"s_nop 1\n"
"v_permlane16_swap_b32 v25, v26\n"
"s_nop 1\n"
"v_max_f32 v25, v25, v26\n"
"v_mul_f32 v25, s62, v25\n"
"v_sub_f32 v25, 0, v25\n"
"v_min_f32 v25, v25, v14\n"
"v_sub_f32 v26, v25, v14\n"
"v_exp_f32 v26, v26\n"
"v_mov_b32 v14, v25\n"
"s_nop 0\n"
"v_mul_f32 v20, v20, v26\n"
"v_accvgpr_read_b32 v22, a116\n"
"v_accvgpr_read_b32 v23, a117\n"
"v_mul_f32 v22, v22, v26\n"
"v_mul_f32 v23, v23, v26\n"
"v_accvgpr_write_b32 a116, v22\n"
"v_accvgpr_write_b32 a117, v23\n"
"v_accvgpr_read_b32 v22, a118\n"
"v_accvgpr_read_b32 v23, a119\n"
"v_mul_f32 v22, v22, v26\n"
"v_mul_f32 v23, v23, v26\n"
"v_accvgpr_write_b32 a118, v22\n"
"v_accvgpr_write_b32 a119, v23\n"
"v_accvgpr_read_b32 v22, a140\n"
"v_accvgpr_read_b32 v23, a141\n"
"v_mul_f32 v22, v22, v26\n"
"v_mul_f32 v23, v23, v26\n"
"v_accvgpr_write_b32 a140, v22\n"
"v_accvgpr_write_b32 a141, v23\n"
"v_accvgpr_read_b32 v22, a142\n"
"v_accvgpr_read_b32 v23, a143\n"
"v_mul_f32 v22, v22, v26\n"
"v_mul_f32 v23, v23, v26\n"
"v_accvgpr_write_b32 a142, v22\n"
"v_accvgpr_write_b32 a143, v23\n"
"v_accvgpr_read_b32 v22, a164\n"
"v_accvgpr_read_b32 v23, a165\n"
"v_mul_f32 v22, v22, v26\n"
"v_mul_f32 v23, v23, v26\n"
"v_accvgpr_write_b32 a164, v22\n"
"v_accvgpr_write_b32 a165, v23\n"
"v_accvgpr_read_b32 v22, a166\n"
"v_accvgpr_read_b32 v23, a167\n"
"v_mul_f32 v22, v22, v26\n"
"v_mul_f32 v23, v23, v26\n"
"v_accvgpr_write_b32 a166, v22\n"
"v_accvgpr_write_b32 a167, v23\n"
"v_accvgpr_read_b32 v22, a188\n"
"v_accvgpr_read_b32 v23, a189\n"
"v_mul_f32 v22, v22, v26\n"
"v_mul_f32 v23, v23, v26\n"
"v_accvgpr_write_b32 a188, v22\n"
"v_accvgpr_write_b32 a189, v23\n"
"v_accvgpr_read_b32 v22, a190\n"
"v_accvgpr_read_b32 v23, a191\n"
"v_mul_f32 v22, v22, v26\n"
"v_mul_f32 v23, v23, v26\n"
"v_accvgpr_write_b32 a190, v22\n"
"v_accvgpr_write_b32 a191, v23\n"
"v_accvgpr_read_b32 v22, a212\n"
"v_accvgpr_read_b32 v23, a213\n"
"v_mul_f32 v22, v22, v26\n"
"v_mul_f32 v23, v23, v26\n"
"v_accvgpr_write_b32 a212, v22\n"
"v_accvgpr_write_b32 a213, v23\n"
"v_accvgpr_read_b32 v22, a214\n"
"v_accvgpr_read_b32 v23, a215\n"
"v_mul_f32 v22, v22, v26\n"
"v_mul_f32 v23, v23, v26\n"
"v_accvgpr_write_b32 a214, v22\n"
"v_accvgpr_write_b32 a215, v23\n"
"v_accvgpr_read_b32 v22, a236\n"
"v_accvgpr_read_b32 v23, a237\n"
"v_mul_f32 v22, v22, v26\n"
"v_mul_f32 v23, v23, v26\n"
"v_accvgpr_write_b32 a236, v22\n"
"v_accvgpr_write_b32 a237, v23\n"
"v_accvgpr_read_b32 v22, a238\n"
"v_accvgpr_read_b32 v23, a239\n"
"v_mul_f32 v22, v22, v26\n"
"v_mul_f32 v23, v23, v26\n"
"v_accvgpr_write_b32 a238, v22\n"
"v_accvgpr_write_b32 a239, v23\n"
"v_mul_f32 v132, v132, v26\n"
"v_mul_f32 v133, v133, v26\n"
"v_mul_f32 v134, v134, v26\n"
"v_mul_f32 v135, v135, v26\n"
"v_mul_f32 v156, v156, v26\n"
"v_mul_f32 v157, v157, v26\n"
"v_mul_f32 v158, v158, v26\n"
"v_mul_f32 v159, v159, v26\n"
"v_mul_f32 v180, v180, v26\n"
"v_mul_f32 v181, v181, v26\n"
"v_mul_f32 v182, v182, v26\n"
"v_mul_f32 v183, v183, v26\n"
"v_mul_f32 v204, v204, v26\n"
"v_mul_f32 v205, v205, v26\n"
"v_mul_f32 v206, v206, v26\n"
"v_mul_f32 v207, v207, v26\n"
"v_mul_f32 v228, v228, v26\n"
"v_mul_f32 v229, v229, v26\n"
"v_mul_f32 v230, v230, v26\n"
"v_mul_f32 v231, v231, v26\n"
"v_mul_f32 v252, v252, v26\n"
"v_mul_f32 v253, v253, v26\n"
"v_mul_f32 v254, v254, v26\n"
"v_mul_f32 v255, v255, v26\n"
"v_fma_f32 v22, v48, s62, v14\n"
"v_fma_f32 v23, v49, s62, v14\n"
"v_exp_f32 v22, v22\n"
"v_exp_f32 v23, v23\n"
"s_nop 0\n"
"v_add_f32 v21, v22, v23\n"
"v_cvt_pk_bf16_f32 v28, v22, v23\n"
"v_fma_f32 v22, v50, s62, v14\n"
"v_fma_f32 v23, v51, s62, v14\n"
"v_exp_f32 v22, v22\n"
"v_exp_f32 v23, v23\n"
"s_nop 0\n"
"v_add_f32 v21, v21, v22\n"
"v_add_f32 v21, v21, v23\n"
"v_cvt_pk_bf16_f32 v29, v22, v23\n"
"v_fma_f32 v22, v60, s62, v14\n"
"v_fma_f32 v23, v61, s62, v14\n"
"v_exp_f32 v22, v22\n"
"v_exp_f32 v23, v23\n"
"s_nop 0\n"
"v_add_f32 v21, v21, v22\n"
"v_add_f32 v21, v21, v23\n"
"v_cvt_pk_bf16_f32 v30, v22, v23\n"
"v_fma_f32 v22, v62, s62, v14\n"
"v_fma_f32 v23, v63, s62, v14\n"
"v_exp_f32 v22, v22\n"
"v_exp_f32 v23, v23\n"
"s_nop 0\n"
"v_add_f32 v21, v21, v22\n"
"v_add_f32 v21, v21, v23\n"
"v_cvt_pk_bf16_f32 v31, v22, v23\n"
"s_nop 1\n"
"s_branch .Lret_1_d5\n"
".Lrare_2_a:\n"
"s_nop 7\n"
"v_max3_f32 v25, v116, v117, v118\n"
"v_max3_f32 v26, v119, v56, v57\n"
"v_max3_f32 v25, v25, v58, v59\n"
"v_max_f32 v25, v25, v26\n"
"v_mov_b32 v26, v25\n"
"s_nop 1\n"
"v_permlane32_swap_b32 v25, v26\n"
"s_nop 1\n"
"v_max_f32 v25, v25, v26\n"
"v_mov_b32 v26, v25\n"
"s_nop 1\n"
"v_permlane16_swap_b32 v25, v26\n"
"s_nop 1\n"
"v_max_f32 v25, v25, v26\n"
"v_mul_f32 v25, s62, v25\n"
"v_sub_f32 v25, 0, v25\n"
"v_min_f32 v25, v25, v9\n"
"v_sub_f32 v26, v25, v9\n"
"v_exp_f32 v26, v26\n"
"v_mov_b32 v9, v25\n"
"s_nop 0\n"
"v_mul_f32 v15, v15, v26\n"
"v_accvgpr_read_b32 v22, a96\n"
"v_accvgpr_read_b32 v23, a97\n"
"v_mul_f32 v22, v22, v26\n"
"v_mul_f32 v23, v23, v26\n"
"v_accvgpr_write_b32 a96, v22\n"
"v_accvgpr_write_b32 a97, v23\n"
"v_accvgpr_read_b32 v22, a98\n"
"v_accvgpr_read_b32 v23, a99\n"
"v_mul_f32 v22, v22, v26\n"
"v_mul_f32 v23, v23, v26\n"
"v_accvgpr_write_b32 a98, v22\n"
"v_accvgpr_write_b32 a99, v23\n"
"v_accvgpr_read_b32 v22, a120\n"
"v_accvgpr_read_b32 v23, a121\n"
"v_mul_f32 v22, v22, v26\n"
"v_mul_f32 v23, v23, v26\n"
"v_accvgpr_write_b32 a120, v22\n"
"v_accvgpr_write_b32 a121, v23\n"
"v_accvgpr_read_b32 v22, a122\n"
"v_accvgpr_read_b32 v23, a123\n"
"v_mul_f32 v22, v22, v26\n"
"v_mul_f32 v23, v23, v26\n"
"v_accvgpr_write_b32 a122, v22\n"
"v_accvgpr_write_b32 a123, v23\n"
"v_accvgpr_read_b32 v22, a144\n"
"v_accvgpr_read_b32 v23, a145\n"
"v_mul_f32 v22, v22, v26\n"
"v_mul_f32 v23, v23, v26\n"
"v_accvgpr_write_b32 a144, v22\n"
"v_accvgpr_write_b32 a145, v23\n"
"v_accvgpr_read_b32 v22, a146\n"
"v_accvgpr_read_b32 v23, a147\n"
"v_mul_f32 v22, v22, v26\n"
"v_mul_f32 v23, v23, v26\n"
"v_accvgpr_write_b32 a146, v22\n"
"v_accvgpr_write_b32 a147, v23\n"
"v_accvgpr_read_b32 v22, a168\n"
"v_accvgpr_read_b32 v23, a169\n"
"v_mul_f32 v22, v22, v26\n"
"v_mul_f32 v23, v23, v26\n"
"v_accvgpr_write_b32 a168, v22\n"
"v_accvgpr_write_b32 a169, v23\n"
"v_accvgpr_read_b32 v22, a170\n"
"v_accvgpr_read_b32 v23, a171\n"
"v_mul_f32 v22, v22, v26\n"
"v_mul_f32 v23, v23, v26\n"
"v_accvgpr_write_b32 a170, v22\n"
"v_accvgpr_write_b32 a171, v23\n"
"v_accvgpr_read_b32 v22, a192\n"
"v_accvgpr_read_b32 v23, a193\n"
"v_mul_f32 v22, v22, v26\n"
"v_mul_f32 v23, v23, v26\n"
"v_accvgpr_write_b32 a192, v22\n"
"v_accvgpr_write_b32 a193, v23\n"
"v_accvgpr_read_b32 v22, a194\n"
"v_accvgpr_read_b32 v23, a195\n"
"v_mul_f32 v22, v22, v26\n"
"v_mul_f32 v23, v23, v26\n"
"v_accvgpr_write_b32 a194, v22\n"
"v_accvgpr_write_b32 a195, v23\n"
"v_accvgpr_read_b32 v22, a216\n"
"v_accvgpr_read_b32 v23, a217\n"
"v_mul_f32 v22, v22, v26\n"
"v_mul_f32 v23, v23, v26\n"
"v_accvgpr_write_b32 a216, v22\n"
"v_accvgpr_write_b32 a217, v23\n"
"v_accvgpr_read_b32 v22, a218\n"
"v_accvgpr_read_b32 v23, a219\n"
"v_mul_f32 v22, v22, v26\n"
"v_mul_f32 v23, v23, v26\n"
"v_accvgpr_write_b32 a218, v22\n"
"v_accvgpr_write_b32 a219, v23\n"
"v_accvgpr_read_b32 v22, a240\n"
"v_accvgpr_read_b32 v23, a241\n"
"v_mul_f32 v22, v22, v26\n"
"v_mul_f32 v23, v23, v26\n"
"v_accvgpr_write_b32 a240, v22\n"
"v_accvgpr_write_b32 a241, v23\n"
"v_accvgpr_read_b32 v22, a242\n"
"v_accvgpr_read_b32 v23, a243\n"
"v_mul_f32 v22, v22, v26\n"
"v_mul_f32 v23, v23, v26\n"
"v_accvgpr_write_b32 a242, v22\n"
"v_accvgpr_write_b32 a243, v23\n"
"v_mul_f32 v136, v136, v26\n"
"v_mul_f32 v137, v137, v26\n"
"v_mul_f32 v138, v138, v26\n"
"v_mul_f32 v139, v139, v26\n"
"v_mul_f32 v160, v160, v26\n"
"v_mul_f32 v161, v161, v26\n"
"v_mul_f32 v162, v162, v26\n"
"v_mul_f32 v163, v163, v26\n"
"v_mul_f32 v184, v184, v26\n"
"v_mul_f32 v185, v185, v26\n"
"v_mul_f32 v186, v186, v26\n"
"v_mul_f32 v187, v187, v26\n"
"v_mul_f32 v208, v208, v26\n"
"v_mul_f32 v209, v209, v26\n"
"v_mul_f32 v210, v210, v26\n"
"v_mul_f32 v211, v211, v26\n"
"v_mul_f32 v232, v232, v26\n"
"v_mul_f32 v233, v233, v26\n"
"v_mul_f32 v234, v234, v26\n"
"v_mul_f32 v235, v235, v26\n"
"v_lshlrev_b32 v22, 16, v88\n"
"v_and_b32 v23, 0xffff0000, v88\n"
"v_mul_f32 v22, v22, v26\n"
"v_mul_f32 v23, v23, v26\n"
"v_cvt_pk_bf16_f32 v88, v22, v23\n"
"v_lshlrev_b32 v22, 16, v89\n"
"v_and_b32 v23, 0xffff0000, v89\n"
"v_mul_f32 v22, v22, v26\n"
"v_mul_f32 v23, v23, v26\n"
"v_cvt_pk_bf16_f32 v89, v22, v23\n"
"v_lshlrev_b32 v22, 16, v90\n"
"v_and_b32 v23, 0xffff0000, v90\n"
"v_mul_f32 v22, v22, v26\n"
"v_mul_f32 v23, v23, v26\n"
"v_cvt_pk_bf16_f32 v90, v22, v23\n"
"v_lshlrev_b32 v22, 16, v91\n"
"v_and_b32 v23, 0xffff0000, v91\n"
"v_mul_f32 v22, v22, v26\n"
"v_mul_f32 v23, v23, v26\n"
"v_cvt_pk_bf16_f32 v91, v22, v23\n"
"v_fma_f32 v22, v116, s62, v9\n"
"v_fma_f32 v23, v117, s62, v9\n"
"v_exp_f32 v22, v22\n"
"v_exp_f32 v23, v23\n"
"s_nop 0\n"
"v_add_f32 v21, v22, v23\n"
"v_cvt_pk_bf16_f32 v80, v22, v23\n"
"v_fma_f32 v22, v118, s62, v9\n"
"v_fma_f32 v23, v119, s62, v9\n"
"v_exp_f32 v22, v22\n"
"v_exp_f32 v23, v23\n"
"s_nop 0\n"
"v_add_f32 v21, v21, v22\n"
"v_add_f32 v21, v21, v23\n"
"v_cvt_pk_bf16_f32 v81, v22, v23\n"
"v_fma_f32 v22, v56, s62, v9\n"
"v_fma_f32 v23, v57, s62, v9\n"
"v_exp_f32 v22, v22\n"
"v_exp_f32 v23, v23\n"
"s_nop 0\n"
"v_add_f32 v21, v21, v22\n"
"v_add_f32 v21, v21, v23\n"
"v_cvt_pk_bf16_f32 v82, v22, v23\n"
"v_fma_f32 v22, v58, s62, v9\n"
"v_fma_f32 v23, v59, s62, v9\n"
"v_exp_f32 v22, v22\n"
"v_exp_f32 v23, v23\n"
"s_nop 0\n"
"v_add_f32 v21, v21, v22\n"
"v_add_f32 v21, v21, v23\n"
"v_cvt_pk_bf16_f32 v83, v22, v23\n"
"v_max3_f32 v25, v52, v53, v54\n"
"v_max3_f32 v26, v55, v96, v97\n"
"v_max3_f32 v25, v25, v98, v99\n"
"v_max_f32 v25, v25, v26\n"
"v_mov_b32 v26, v25\n"
"s_nop 1\n"
"v_permlane32_swap_b32 v25, v26\n"
"s_nop 1\n"
"v_max_f32 v25, v25, v26\n"
"v_mov_b32 v26, v25\n"
"s_nop 1\n"
"v_permlane16_swap_b32 v25, v26\n"
"s_nop 1\n"
"v_max_f32 v25, v25, v26\n"
"v_mul_f32 v25, s62, v25\n"
"v_sub_f32 v25, 0, v25\n"
"v_min_f32 v25, v25, v10\n"
"v_sub_f32 v26, v25, v10\n"
"v_exp_f32 v26, v26\n"
"v_mov_b32 v10, v25\n"
"s_nop 0\n"
"v_mul_f32 v16, v16, v26\n"
"v_accvgpr_read_b32 v22, a100\n"
"v_accvgpr_read_b32 v23, a101\n"
"v_mul_f32 v22, v22, v26\n"
"v_mul_f32 v23, v23, v26\n"
"v_accvgpr_write_b32 a100, v22\n"
"v_accvgpr_write_b32 a101, v23\n"
"v_accvgpr_read_b32 v22, a102\n"
"v_accvgpr_read_b32 v23, a103\n"
"v_mul_f32 v22, v22, v26\n"
"v_mul_f32 v23, v23, v26\n"
"v_accvgpr_write_b32 a102, v22\n"
"v_accvgpr_write_b32 a103, v23\n"
"v_accvgpr_read_b32 v22, a124\n"
"v_accvgpr_read_b32 v23, a125\n"
"v_mul_f32 v22, v22, v26\n"
"v_mul_f32 v23, v23, v26\n"
"v_accvgpr_write_b32 a124, v22\n"
"v_accvgpr_write_b32 a125, v23\n"
"v_accvgpr_read_b32 v22, a126\n"
"v_accvgpr_read_b32 v23, a127\n"
"v_mul_f32 v22, v22, v26\n"
"v_mul_f32 v23, v23, v26\n"
"v_accvgpr_write_b32 a126, v22\n"
"v_accvgpr_write_b32 a127, v23\n"
"v_accvgpr_read_b32 v22, a148\n"
"v_accvgpr_read_b32 v23, a149\n"
"v_mul_f32 v22, v22, v26\n"
"v_mul_f32 v23, v23, v26\n"
"v_accvgpr_write_b32 a148, v22\n"
"v_accvgpr_write_b32 a149, v23\n"
"v_accvgpr_read_b32 v22, a150\n"
"v_accvgpr_read_b32 v23, a151\n"
"v_mul_f32 v22, v22, v26\n"
"v_mul_f32 v23, v23, v26\n"
"v_accvgpr_write_b32 a150, v22\n"
"v_accvgpr_write_b32 a151, v23\n"
"v_accvgpr_read_b32 v22, a172\n"
"v_accvgpr_read_b32 v23, a173\n"
"v_mul_f32 v22, v22, v26\n"
"v_mul_f32 v23, v23, v26\n"
"v_accvgpr_write_b32 a172, v22\n"
"v_accvgpr_write_b32 a173, v23\n"
"v_accvgpr_read_b32 v22, a174\n"
"v_accvgpr_read_b32 v23, a175\n"
"v_mul_f32 v22, v22, v26\n"
"v_mul_f32 v23, v23, v26\n"
"v_accvgpr_write_b32 a174, v22\n"
"v_accvgpr_write_b32 a175, v23\n"
"v_accvgpr_read_b32 v22, a196\n"
"v_accvgpr_read_b32 v23, a197\n"
"v_mul_f32 v22, v22, v26\n"
"v_mul_f32 v23, v23, v26\n"
"v_accvgpr_write_b32 a196, v22\n"
"v_accvgpr_write_b32 a197, v23\n"
"v_accvgpr_read_b32 v22, a198\n"
"v_accvgpr_read_b32 v23, a199\n"
"v_mul_f32 v22, v22, v26\n"
"v_mul_f32 v23, v23, v26\n"
"v_accvgpr_write_b32 a198, v22\n"
"v_accvgpr_write_b32 a199, v23\n"
"v_accvgpr_read_b32 v22, a220\n"
"v_accvgpr_read_b32 v23, a221\n"
"v_mul_f32 v22, v22, v26\n"
"v_mul_f32 v23, v23, v26\n"
"v_accvgpr_write_b32 a220, v22\n"
"v_accvgpr_write_b32 a221, v23\n"
"v_accvgpr_read_b32 v22, a222\n"
"v_accvgpr_read_b32 v23, a223\n"
"v_mul_f32 v22, v22, v26\n"
"v_mul_f32 v23, v23, v26\n"
"v_accvgpr_write_b32 a222, v22\n"
"v_accvgpr_write_b32 a223, v23\n"
"v_accvgpr_read_b32 v22, a244\n"
"v_accvgpr_read_b32 v23, a245\n"
"v_mul_f32 v22, v22, v26\n"
"v_mul_f32 v23, v23, v26\n"
"v_accvgpr_write_b32 a244, v22\n"
"v_accvgpr_write_b32 a245, v23\n"
"v_accvgpr_read_b32 v22, a246\n"
"v_accvgpr_read_b32 v23, a247\n"
"v_mul_f32 v22, v22, v26\n"
"v_mul_f32 v23, v23, v26\n"
"v_accvgpr_write_b32 a246, v22\n"
"v_accvgpr_write_b32 a247, v23\n"
"v_mul_f32 v140, v140, v26\n"
"v_mul_f32 v141, v141, v26\n"
"v_mul_f32 v142, v142, v26\n"
"v_mul_f32 v143, v143, v26\n"
"v_mul_f32 v164, v164, v26\n"
"v_mul_f32 v165, v165, v26\n"
"v_mul_f32 v166, v166, v26\n"
"v_mul_f32 v167, v167, v26\n"
"v_mul_f32 v188, v188, v26\n"
"v_mul_f32 v189, v189, v26\n"
"v_mul_f32 v190, v190, v26\n"
"v_mul_f32 v191, v191, v26\n"
"v_mul_f32 v212, v212, v26\n"
"v_mul_f32 v213, v213, v26\n"
"v_mul_f32 v214, v214, v26\n"
"v_mul_f32 v215, v215, v26\n"
"v_mul_f32 v236, v236, v26\n"
"v_mul_f32 v237, v237, v26\n"
"v_mul_f32 v238, v238, v26\n"
"v_mul_f32 v239, v239, v26\n"
"v_lshlrev_b32 v22, 16, v68\n"
"v_and_b32 v23, 0xffff0000, v68\n"
"v_mul_f32 v22, v22, v26\n"
"v_mul_f32 v23, v23, v26\n"
"v_cvt_pk_bf16_f32 v68, v22, v23\n"
"v_lshlrev_b32 v22, 16, v69\n"
"v_and_b32 v23, 0xffff0000, v69\n"
"v_mul_f32 v22, v22, v26\n"
"v_mul_f32 v23, v23, v26\n"
"v_cvt_pk_bf16_f32 v69, v22, v23\n"
"v_lshlrev_b32 v22, 16, v70\n"
"v_and_b32 v23, 0xffff0000, v70\n"
"v_mul_f32 v22, v22, v26\n"
"v_mul_f32 v23, v23, v26\n"
"v_cvt_pk_bf16_f32 v70, v22, v23\n"
"v_lshlrev_b32 v22, 16, v71\n"
"v_and_b32 v23, 0xffff0000, v71\n"
"v_mul_f32 v22, v22, v26\n"
"v_mul_f32 v23, v23, v26\n"
"v_cvt_pk_bf16_f32 v71, v22, v23\n"
"v_fma_f32 v22, v52, s62, v10\n"
"v_fma_f32 v23, v53, s62, v10\n"
"v_exp_f32 v22, v22\n"
"v_exp_f32 v23, v23\n"
"s_nop 0\n"
"v_add_f32 v3, v22, v23\n"
"v_cvt_pk_bf16_f32 v48, v22, v23\n"
"v_fma_f32 v22, v54, s62, v10\n"
"v_fma_f32 v23, v55, s62, v10\n"
"v_exp_f32 v22, v22\n"
"v_exp_f32 v23, v23\n"
"s_nop 0\n"
"v_add_f32 v3, v3, v22\n"
"v_add_f32 v3, v3, v23\n"
"v_cvt_pk_bf16_f32 v49, v22, v23\n"
"v_fma_f32 v22, v96, s62, v10\n"
"v_fma_f32 v23, v97, s62, v10\n"
"v_exp_f32 v22, v22\n"
"v_exp_f32 v23, v23\n"
"s_nop 0\n"
"v_add_f32 v3, v3, v22\n"
"v_add_f32 v3, v3, v23\n"
"v_cvt_pk_bf16_f32 v50, v22, v23\n"
"v_fma_f32 v22, v98, s62, v10\n"
"v_fma_f32 v23, v99, s62, v10\n"
"v_exp_f32 v22, v22\n"
"v_exp_f32 v23, v23\n"
"s_nop 0\n"
"v_add_f32 v3, v3, v22\n"
"v_add_f32 v3, v3, v23\n"
"v_cvt_pk_bf16_f32 v51, v22, v23\n"
"v_fma_f32 v22, v72, s62, v11\n"
"v_fma_f32 v23, v73, s62, v11\n"
"s_nop 1\n"
"s_branch .Lret_2_a\n"
".Lrare_2_b:\n"
"s_nop 7\n"
"v_max3_f32 v25, v72, v73, v74\n"
"v_max3_f32 v26, v75, v100, v101\n"
"v_max3_f32 v25, v25, v102, v103\n"
"v_max_f32 v25, v25, v26\n"
"v_mov_b32 v26, v25\n"
"s_nop 1\n"
"v_permlane32_swap_b32 v25, v26\n"
"s_nop 1\n"
"v_max_f32 v25, v25, v26\n"
"v_mov_b32 v26, v25\n"
"s_nop 1\n"
"v_permlane16_swap_b32 v25, v26\n"
"s_nop 1\n"
"v_max_f32 v25, v25, v26\n"
"v_mul_f32 v25, s62, v25\n"
"v_sub_f32 v25, 0, v25\n"
"v_min_f32 v25, v25, v11\n"
"v_sub_f32 v26, v25, v11\n"
"v_exp_f32 v26, v26\n"
"v_mov_b32 v11, v25\n"
"s_nop 0\n"
"v_mul_f32 v17, v17, v26\n"
"v_accvgpr_read_b32 v22, a104\n"
"v_accvgpr_read_b32 v23, a105\n"
"v_mul_f32 v22, v22, v26\n"
"v_mul_f32 v23, v23, v26\n"
"v_accvgpr_write_b32 a104, v22\n"
"v_accvgpr_write_b32 a105, v23\n"
"v_accvgpr_read_b32 v22, a106\n"
"v_accvgpr_read_b32 v23, a107\n"
"v_mul_f32 v22, v22, v26\n"
"v_mul_f32 v23, v23, v26\n"
"v_accvgpr_write_b32 a106, v22\n"
"v_accvgpr_write_b32 a107, v23\n"
"v_accvgpr_read_b32 v22, a128\n"
"v_accvgpr_read_b32 v23, a129\n"
"v_mul_f32 v22, v22, v26\n"
"v_mul_f32 v23, v23, v26\n"
"v_accvgpr_write_b32 a128, v22\n"
"v_accvgpr_write_b32 a129, v23\n"
"v_accvgpr_read_b32 v22, a130\n"
"v_accvgpr_read_b32 v23, a131\n"
"v_mul_f32 v22, v22, v26\n"
"v_mul_f32 v23, v23, v26\n"
"v_accvgpr_write_b32 a130, v22\n"
"v_accvgpr_write_b32 a131, v23\n"
"v_accvgpr_read_b32 v22, a152\n"
"v_accvgpr_read_b32 v23, a153\n"
"v_mul_f32 v22, v22, v26\n"
"v_mul_f32 v23, v23, v26\n"
"v_accvgpr_write_b32 a152, v22\n"
"v_accvgpr_write_b32 a153, v23\n"
"v_accvgpr_read_b32 v22, a154\n"
"v_accvgpr_read_b32 v23, a155\n"
"v_mul_f32 v22, v22, v26\n"
"v_mul_f32 v23, v23, v26\n"
"v_accvgpr_write_b32 a154, v22\n"
"v_accvgpr_write_b32 a155, v23\n"
"v_accvgpr_read_b32 v22, a176\n"
"v_accvgpr_read_b32 v23, a177\n"
"v_mul_f32 v22, v22, v26\n"
"v_mul_f32 v23, v23, v26\n"
"v_accvgpr_write_b32 a176, v22\n"
"v_accvgpr_write_b32 a177, v23\n"
"v_accvgpr_read_b32 v22, a178\n"
"v_accvgpr_read_b32 v23, a179\n"
"v_mul_f32 v22, v22, v26\n"
"v_mul_f32 v23, v23, v26\n"
"v_accvgpr_write_b32 a178, v22\n"
"v_accvgpr_write_b32 a179, v23\n"
"v_accvgpr_read_b32 v22, a200\n"
"v_accvgpr_read_b32 v23, a201\n"
"v_mul_f32 v22, v22, v26\n"
"v_mul_f32 v23, v23, v26\n"
"v_accvgpr_write_b32 a200, v22\n"
"v_accvgpr_write_b32 a201, v23\n"
"v_accvgpr_read_b32 v22, a202\n"
"v_accvgpr_read_b32 v23, a203\n"
"v_mul_f32 v22, v22, v26\n"
"v_mul_f32 v23, v23, v26\n"
"v_accvgpr_write_b32 a202, v22\n"
"v_accvgpr_write_b32 a203, v23\n"
"v_accvgpr_read_b32 v22, a224\n"
"v_accvgpr_read_b32 v23, a225\n"
"v_mul_f32 v22, v22, v26\n"
"v_mul_f32 v23, v23, v26\n"
"v_accvgpr_write_b32 a224, v22\n"
"v_accvgpr_write_b32 a225, v23\n"
"v_accvgpr_read_b32 v22, a226\n"
"v_accvgpr_read_b32 v23, a227\n"
"v_mul_f32 v22, v22, v26\n"
"v_mul_f32 v23, v23, v26\n"
"v_accvgpr_write_b32 a226, v22\n"
"v_accvgpr_write_b32 a227, v23\n"
"v_accvgpr_read_b32 v22, a248\n"
"v_accvgpr_read_b32 v23, a249\n"
"v_mul_f32 v22, v22, v26\n"
"v_mul_f32 v23, v23, v26\n"
"v_accvgpr_write_b32 a248, v22\n"
"v_accvgpr_write_b32 a249, v23\n"
"v_accvgpr_read_b32 v22, a250\n"
"v_accvgpr_read_b32 v23, a251\n"
"v_mul_f32 v22, v22, v26\n"
"v_mul_f32 v23, v23, v26\n"
"v_accvgpr_write_b32 a250, v22\n"
"v_accvgpr_write_b32 a251, v23\n"
"v_mul_f32 v144, v144, v26\n"
"v_mul_f32 v145, v145, v26\n"
"v_mul_f32 v146, v146, v26\n"
"v_mul_f32 v147, v147, v26\n"
"v_mul_f32 v168, v168, v26\n"
"v_mul_f32 v169, v169, v26\n"
"v_mul_f32 v170, v170, v26\n"
"v_mul_f32 v171, v171, v26\n"
"v_mul_f32 v192, v192, v26\n"
"v_mul_f32 v193, v193, v26\n"
"v_mul_f32 v194, v194, v26\n"
"v_mul_f32 v195, v195, v26\n"
"v_mul_f32 v216, v216, v26\n"
"v_mul_f32 v217, v217, v26\n"
"v_mul_f32 v218, v218, v26\n"
"v_mul_f32 v219, v219, v26\n"
"v_mul_f32 v240, v240, v26\n"
"v_mul_f32 v241, v241, v26\n"
"v_mul_f32 v242, v242, v26\n"
"v_mul_f32 v243, v243, v26\n"
"v_lshlrev_b32 v22, 16, v92\n"
"v_and_b32 v23, 0xffff0000, v92\n"
"v_mul_f32 v22, v22, v26\n"
"v_mul_f32 v23, v23, v26\n"
"v_cvt_pk_bf16_f32 v92, v22, v23\n"
"v_lshlrev_b32 v22, 16, v93\n"
"v_and_b32 v23, 0xffff0000, v93\n"
"v_mul_f32 v22, v22, v26\n"
"v_mul_f32 v23, v23, v26\n"
"v_cvt_pk_bf16_f32 v93, v22, v23\n"
"v_lshlrev_b32 v22, 16, v94\n"
"v_and_b32 v23, 0xffff0000, v94\n"
"v_mul_f32 v22, v22, v26\n"
"v_mul_f32 v23, v23, v26\n"
"v_cvt_pk_bf16_f32 v94, v22, v23\n"
"v_lshlrev_b32 v22, 16, v95\n"
"v_and_b32 v23, 0xffff0000, v95\n"
"v_mul_f32 v22, v22, v26\n"
"v_mul_f32 v23, v23, v26\n"
"v_cvt_pk_bf16_f32 v95, v22, v23\n"
"v_fma_f32 v22, v72, s62, v11\n"
"v_fma_f32 v23, v73, s62, v11\n"
"v_exp_f32 v22, v22\n"
"v_exp_f32 v23, v23\n"
"s_nop 0\n"
"v_add_f32 v56, v22, v23\n"
"v_cvt_pk_bf16_f32 v60, v22, v23\n"
"v_fma_f32 v22, v74, s62, v11\n"
"v_fma_f32 v23, v75, s62, v11\n"
"v_exp_f32 v22, v22\n"
"v_exp_f32 v23, v23\n"
"s_nop 0\n"
"v_add_f32 v56, v56, v22\n"
"v_add_f32 v56, v56, v23\n"
"v_cvt_pk_bf16_f32 v61, v22, v23\n"
"v_fma_f32 v22, v100, s62, v11\n"
"v_fma_f32 v23, v101, s62, v11\n"
"v_exp_f32 v22, v22\n"
"v_exp_f32 v23, v23\n"
"s_nop 0\n"
"v_add_f32 v56, v56, v22\n"
"v_add_f32 v56, v56, v23\n"
"v_cvt_pk_bf16_f32 v62, v22, v23\n"
"v_fma_f32 v22, v102, s62, v11\n"
"v_fma_f32 v23, v103, s62, v11\n"
"v_exp_f32 v22, v22\n"
"v_exp_f32 v23, v23\n"
"s_nop 0\n"
"v_add_f32 v56, v56, v22\n"
"v_add_f32 v56, v56, v23\n"
"v_cvt_pk_bf16_f32 v63, v22, v23\n"
"v_max3_f32 v25, v120, v121, v122\n"
"v_max3_f32 v26, v123, v104, v105\n"
"v_max3_f32 v25, v25, v106, v107\n"
"v_max_f32 v25, v25, v26\n"
"v_mov_b32 v26, v25\n"
"s_nop 1\n"
"v_permlane32_swap_b32 v25, v26\n"
"s_nop 1\n"
"v_max_f32 v25, v25, v26\n"
"v_mov_b32 v26, v25\n"
"s_nop 1\n"
"v_permlane16_swap_b32 v25, v26\n"
"s_nop 1\n"
"v_max_f32 v25, v25, v26\n"
"v_mul_f32 v25, s62, v25\n"
"v_sub_f32 v25, 0, v25\n"
"v_min_f32 v25, v25, v12\n"
"v_sub_f32 v26, v25, v12\n"
"v_exp_f32 v26, v26\n"
"v_mov_b32 v12, v25\n"
"s_nop 0\n"
"v_mul_f32 v18, v18, v26\n"
"v_accvgpr_read_b32 v22, a108\n"
"v_accvgpr_read_b32 v23, a109\n"
"v_mul_f32 v22, v22, v26\n"
"v_mul_f32 v23, v23, v26\n"
"v_accvgpr_write_b32 a108, v22\n"
"v_accvgpr_write_b32 a109, v23\n"
"v_accvgpr_read_b32 v22, a110\n"
"v_accvgpr_read_b32 v23, a111\n"
"v_mul_f32 v22, v22, v26\n"
"v_mul_f32 v23, v23, v26\n"
"v_accvgpr_write_b32 a110, v22\n"
"v_accvgpr_write_b32 a111, v23\n"
"v_accvgpr_read_b32 v22, a132\n"
"v_accvgpr_read_b32 v23, a133\n"
"v_mul_f32 v22, v22, v26\n"
"v_mul_f32 v23, v23, v26\n"
"v_accvgpr_write_b32 a132, v22\n"
"v_accvgpr_write_b32 a133, v23\n"
"v_accvgpr_read_b32 v22, a134\n"
"v_accvgpr_read_b32 v23, a135\n"
"v_mul_f32 v22, v22, v26\n"
"v_mul_f32 v23, v23, v26\n"
"v_accvgpr_write_b32 a134, v22\n"
"v_accvgpr_write_b32 a135, v23\n"
"v_accvgpr_read_b32 v22, a156\n"
"v_accvgpr_read_b32 v23, a157\n"
"v_mul_f32 v22, v22, v26\n"
"v_mul_f32 v23, v23, v26\n"
"v_accvgpr_write_b32 a156, v22\n"
"v_accvgpr_write_b32 a157, v23\n"
"v_accvgpr_read_b32 v22, a158\n"
"v_accvgpr_read_b32 v23, a159\n"
"v_mul_f32 v22, v22, v26\n"
"v_mul_f32 v23, v23, v26\n"
"v_accvgpr_write_b32 a158, v22\n"
"v_accvgpr_write_b32 a159, v23\n"
"v_accvgpr_read_b32 v22, a180\n"
"v_accvgpr_read_b32 v23, a181\n"
"v_mul_f32 v22, v22, v26\n"
"v_mul_f32 v23, v23, v26\n"
"v_accvgpr_write_b32 a180, v22\n"
"v_accvgpr_write_b32 a181, v23\n"
"v_accvgpr_read_b32 v22, a182\n"
"v_accvgpr_read_b32 v23, a183\n"
"v_mul_f32 v22, v22, v26\n"
"v_mul_f32 v23, v23, v26\n"
"v_accvgpr_write_b32 a182, v22\n"
"v_accvgpr_write_b32 a183, v23\n"
"v_accvgpr_read_b32 v22, a204\n"
"v_accvgpr_read_b32 v23, a205\n"
"v_mul_f32 v22, v22, v26\n"
"v_mul_f32 v23, v23, v26\n"
"v_accvgpr_write_b32 a204, v22\n"
"v_accvgpr_write_b32 a205, v23\n"
"v_accvgpr_read_b32 v22, a206\n"
"v_accvgpr_read_b32 v23, a207\n"
"v_mul_f32 v22, v22, v26\n"
"v_mul_f32 v23, v23, v26\n"
"v_accvgpr_write_b32 a206, v22\n"
"v_accvgpr_write_b32 a207, v23\n"
"v_accvgpr_read_b32 v22, a228\n"
"v_accvgpr_read_b32 v23, a229\n"
"v_mul_f32 v22, v22, v26\n"
"v_mul_f32 v23, v23, v26\n"
"v_accvgpr_write_b32 a228, v22\n"
"v_accvgpr_write_b32 a229, v23\n"
"v_accvgpr_read_b32 v22, a230\n"
"v_accvgpr_read_b32 v23, a231\n"
"v_mul_f32 v22, v22, v26\n"
"v_mul_f32 v23, v23, v26\n"
"v_accvgpr_write_b32 a230, v22\n"
"v_accvgpr_write_b32 a231, v23\n"
"v_accvgpr_read_b32 v22, a252\n"
"v_accvgpr_read_b32 v23, a253\n"
"v_mul_f32 v22, v22, v26\n"
"v_mul_f32 v23, v23, v26\n"
"v_accvgpr_write_b32 a252, v22\n"
"v_accvgpr_write_b32 a253, v23\n"
"v_accvgpr_read_b32 v22, a254\n"
"v_accvgpr_read_b32 v23, a255\n"
"v_mul_f32 v22, v22, v26\n"
"v_mul_f32 v23, v23, v26\n"
"v_accvgpr_write_b32 a254, v22\n"
"v_accvgpr_write_b32 a255, v23\n"
"v_mul_f32 v148, v148, v26\n"
"v_mul_f32 v149, v149, v26\n"
"v_mul_f32 v150, v150, v26\n"
"v_mul_f32 v151, v151, v26\n"
"v_mul_f32 v172, v172, v26\n"
"v_mul_f32 v173, v173, v26\n"
"v_mul_f32 v174, v174, v26\n"
"v_mul_f32 v175, v175, v26\n"
"v_mul_f32 v196, v196, v26\n"
"v_mul_f32 v197, v197, v26\n"
"v_mul_f32 v198, v198, v26\n"
"v_mul_f32 v199, v199, v26\n"
"v_mul_f32 v220, v220, v26\n"
"v_mul_f32 v221, v221, v26\n"
"v_mul_f32 v222, v222, v26\n"
"v_mul_f32 v223, v223, v26\n"
"v_mul_f32 v244, v244, v26\n"
"v_mul_f32 v245, v245, v26\n"
"v_mul_f32 v246, v246, v26\n"
"v_mul_f32 v247, v247, v26\n"
"v_lshlrev_b32 v22, 16, v112\n"
"v_and_b32 v23, 0xffff0000, v112\n"
"v_mul_f32 v22, v22, v26\n"
"v_mul_f32 v23, v23, v26\n"
"v_cvt_pk_bf16_f32 v112, v22, v23\n"
"v_lshlrev_b32 v22, 16, v113\n"
"v_and_b32 v23, 0xffff0000, v113\n"
"v_mul_f32 v22, v22, v26\n"
"v_mul_f32 v23, v23, v26\n"
"v_cvt_pk_bf16_f32 v113, v22, v23\n"
"v_lshlrev_b32 v22, 16, v114\n"
"v_and_b32 v23, 0xffff0000, v114\n"
"v_mul_f32 v22, v22, v26\n"
"v_mul_f32 v23, v23, v26\n"
"v_cvt_pk_bf16_f32 v114, v22, v23\n"
"v_lshlrev_b32 v22, 16, v115\n"
"v_and_b32 v23, 0xffff0000, v115\n"
"v_mul_f32 v22, v22, v26\n"
"v_mul_f32 v23, v23, v26\n"
"v_cvt_pk_bf16_f32 v115, v22, v23\n"
"v_fma_f32 v22, v120, s62, v12\n"
"v_fma_f32 v23, v121, s62, v12\n"
"v_exp_f32 v22, v22\n"
"v_exp_f32 v23, v23\n"
"s_nop 0\n"
"v_add_f32 v57, v22, v23\n"
"v_cvt_pk_bf16_f32 v116, v22, v23\n"
"v_fma_f32 v22, v122, s62, v12\n"
"v_fma_f32 v23, v123, s62, v12\n"
"v_exp_f32 v22, v22\n"
"v_exp_f32 v23, v23\n"
"s_nop 0\n"
"v_add_f32 v57, v57, v22\n"
"v_add_f32 v57, v57, v23\n"
"v_cvt_pk_bf16_f32 v117, v22, v23\n"
"v_fma_f32 v22, v104, s62, v12\n"
"v_fma_f32 v23, v105, s62, v12\n"
"v_exp_f32 v22, v22\n"
"v_exp_f32 v23, v23\n"
"s_nop 0\n"
"v_add_f32 v57, v57, v22\n"
"v_add_f32 v57, v57, v23\n"
"v_cvt_pk_bf16_f32 v118, v22, v23\n"
"v_fma_f32 v22, v106, s62, v12\n"
"v_fma_f32 v23, v107, s62, v12\n"
"v_exp_f32 v22, v22\n"
"v_exp_f32 v23, v23\n"
"s_nop 0\n"
"v_add_f32 v57, v57, v22\n"
"v_add_f32 v57, v57, v23\n"
"v_cvt_pk_bf16_f32 v119, v22, v23\n"
"v_fma_f32 v22, v76, s62, v13\n"
"v_fma_f32 v23, v77, s62, v13\n"
"s_nop 1\n"
"s_branch .Lret_2_b\n"
".Lrare_2_d4:\n"
"s_nop 7\n"
"v_max3_f32 v25, v76, v77, v78\n"
"v_max3_f32 v26, v79, v108, v109\n"
"v_max3_f32 v25, v25, v110, v111\n"
"v_max_f32 v25, v25, v26\n"
"v_mov_b32 v26, v25\n"
"s_nop 1\n"
"v_permlane32_swap_b32 v25, v26\n"
"s_nop 1\n"
"v_max_f32 v25, v25, v26\n"
"v_mov_b32 v26, v25\n"
"s_nop 1\n"
"v_permlane16_swap_b32 v25, v26\n"
"s_nop 1\n"
"v_max_f32 v25, v25, v26\n"
"v_mul_f32 v25, s62, v25\n"
"v_sub_f32 v25, 0, v25\n"
"v_min_f32 v25, v25, v13\n"
"v_sub_f32 v26, v25, v13\n"
"v_exp_f32 v26, v26\n"
"v_mov_b32 v13, v25\n"
"s_nop 0\n"
"v_mul_f32 v19, v19, v26\n"
"v_accvgpr_read_b32 v22, a112\n"
"v_accvgpr_read_b32 v23, a113\n"
"v_mul_f32 v22, v22, v26\n"
"v_mul_f32 v23, v23, v26\n"
"v_accvgpr_write_b32 a112, v22\n"
"v_accvgpr_write_b32 a113, v23\n"
"v_accvgpr_read_b32 v22, a114\n"
"v_accvgpr_read_b32 v23, a115\n"
"v_mul_f32 v22, v22, v26\n"
"v_mul_f32 v23, v23, v26\n"
"v_accvgpr_write_b32 a114, v22\n"
"v_accvgpr_write_b32 a115, v23\n"
"v_accvgpr_read_b32 v22, a136\n"
"v_accvgpr_read_b32 v23, a137\n"
"v_mul_f32 v22, v22, v26\n"
"v_mul_f32 v23, v23, v26\n"
"v_accvgpr_write_b32 a136, v22\n"
"v_accvgpr_write_b32 a137, v23\n"
"v_accvgpr_read_b32 v22, a138\n"
"v_accvgpr_read_b32 v23, a139\n"
"v_mul_f32 v22, v22, v26\n"
"v_mul_f32 v23, v23, v26\n"
"v_accvgpr_write_b32 a138, v22\n"
"v_accvgpr_write_b32 a139, v23\n"
"v_accvgpr_read_b32 v22, a160\n"
"v_accvgpr_read_b32 v23, a161\n"
"v_mul_f32 v22, v22, v26\n"
"v_mul_f32 v23, v23, v26\n"
"v_accvgpr_write_b32 a160, v22\n"
"v_accvgpr_write_b32 a161, v23\n"
"v_accvgpr_read_b32 v22, a162\n"
"v_accvgpr_read_b32 v23, a163\n"
"v_mul_f32 v22, v22, v26\n"
"v_mul_f32 v23, v23, v26\n"
"v_accvgpr_write_b32 a162, v22\n"
"v_accvgpr_write_b32 a163, v23\n"
"v_accvgpr_read_b32 v22, a184\n"
"v_accvgpr_read_b32 v23, a185\n"
"v_mul_f32 v22, v22, v26\n"
"v_mul_f32 v23, v23, v26\n"
"v_accvgpr_write_b32 a184, v22\n"
"v_accvgpr_write_b32 a185, v23\n"
"v_accvgpr_read_b32 v22, a186\n"
"v_accvgpr_read_b32 v23, a187\n"
"v_mul_f32 v22, v22, v26\n"
"v_mul_f32 v23, v23, v26\n"
"v_accvgpr_write_b32 a186, v22\n"
"v_accvgpr_write_b32 a187, v23\n"
"v_accvgpr_read_b32 v22, a208\n"
"v_accvgpr_read_b32 v23, a209\n"
"v_mul_f32 v22, v22, v26\n"
"v_mul_f32 v23, v23, v26\n"
"v_accvgpr_write_b32 a208, v22\n"
"v_accvgpr_write_b32 a209, v23\n"
"v_accvgpr_read_b32 v22, a210\n"
"v_accvgpr_read_b32 v23, a211\n"
"v_mul_f32 v22, v22, v26\n"
"v_mul_f32 v23, v23, v26\n"
"v_accvgpr_write_b32 a210, v22\n"
"v_accvgpr_write_b32 a211, v23\n"
"v_accvgpr_read_b32 v22, a232\n"
"v_accvgpr_read_b32 v23, a233\n"
"v_mul_f32 v22, v22, v26\n"
"v_mul_f32 v23, v23, v26\n"
"v_accvgpr_write_b32 a232, v22\n"
"v_accvgpr_write_b32 a233, v23\n"
"v_accvgpr_read_b32 v22, a234\n"
"v_accvgpr_read_b32 v23, a235\n"
"v_mul_f32 v22, v22, v26\n"
"v_mul_f32 v23, v23, v26\n"
"v_accvgpr_write_b32 a234, v22\n"
"v_accvgpr_write_b32 a235, v23\n"
"v_mul_f32 v128, v128, v26\n"
"v_mul_f32 v129, v129, v26\n"
"v_mul_f32 v130, v130, v26\n"
"v_mul_f32 v131, v131, v26\n"
"v_mul_f32 v152, v152, v26\n"
"v_mul_f32 v153, v153, v26\n"
"v_mul_f32 v154, v154, v26\n"
"v_mul_f32 v155, v155, v26\n"
"v_mul_f32 v176, v176, v26\n"
"v_mul_f32 v177, v177, v26\n"
"v_mul_f32 v178, v178, v26\n"
"v_mul_f32 v179, v179, v26\n"
"v_mul_f32 v200, v200, v26\n"
"v_mul_f32 v201, v201, v26\n"
"v_mul_f32 v202, v202, v26\n"
"v_mul_f32 v203, v203, v26\n"
"v_mul_f32 v224, v224, v26\n"
"v_mul_f32 v225, v225, v26\n"
"v_mul_f32 v226, v226, v26\n"
"v_mul_f32 v227, v227, v26\n"
"v_mul_f32 v248, v248, v26\n"
"v_mul_f32 v249, v249, v26\n"
"v_mul_f32 v250, v250, v26\n"
"v_mul_f32 v251, v251, v26\n"
"v_fma_f32 v22, v76, s62, v13\n"
"v_fma_f32 v23, v77, s62, v13\n"
"v_exp_f32 v22, v22\n"
"v_exp_f32 v23, v23\n"
"s_nop 0\n"
"v_add_f32 v21, v22, v23\n"
"v_cvt_pk_bf16_f32 v28, v22, v23\n"
"v_fma_f32 v22, v78, s62, v13\n"
"v_fma_f32 v23, v79, s62, v13\n"
"v_exp_f32 v22, v22\n"
"v_exp_f32 v23, v23\n"
"s_nop 0\n"
"v_add_f32 v21, v21, v22\n"
"v_add_f32 v21, v21, v23\n"
"v_cvt_pk_bf16_f32 v29, v22, v23\n"
"v_fma_f32 v22, v108, s62, v13\n"
"v_fma_f32 v23, v109, s62, v13\n"
"v_exp_f32 v22, v22\n"
"v_exp_f32 v23, v23\n"
"s_nop 0\n"
"v_add_f32 v21, v21, v22\n"
"v_add_f32 v21, v21, v23\n"
"v_cvt_pk_bf16_f32 v30, v22, v23\n"
"v_fma_f32 v22, v110, s62, v13\n"
"v_fma_f32 v23, v111, s62, v13\n"
"v_exp_f32 v22, v22\n"
"v_exp_f32 v23, v23\n"
"s_nop 0\n"
"v_add_f32 v21, v21, v22\n"
"v_add_f32 v21, v21, v23\n"
"v_cvt_pk_bf16_f32 v31, v22, v23\n"
"v_fma_f32 v22, v124, s62, v14\n"
"v_fma_f32 v23, v125, s62, v14\n"
"s_nop 1\n"
"s_branch .Lret_2_d4\n"
".Lrare_2_d5:\n"
"s_nop 7\n"
"v_max3_f32 v25, v124, v125, v126\n"
"v_max3_f32 v26, v127, v84, v85\n"
"v_max3_f32 v25, v25, v86, v87\n"
"v_max_f32 v25, v25, v26\n"
"v_mov_b32 v26, v25\n"
"s_nop 1\n"
"v_permlane32_swap_b32 v25, v26\n"
"s_nop 1\n"
"v_max_f32 v25, v25, v26\n"
"v_mov_b32 v26, v25\n"
"s_nop 1\n"
"v_permlane16_swap_b32 v25, v26\n"
"s_nop 1\n"
"v_max_f32 v25, v25, v26\n"
"v_mul_f32 v25, s62, v25\n"
"v_sub_f32 v25, 0, v25\n"
"v_min_f32 v25, v25, v14\n"
"v_sub_f32 v26, v25, v14\n"
"v_exp_f32 v26, v26\n"
"v_mov_b32 v14, v25\n"
"s_nop 0\n"
"v_mul_f32 v20, v20, v26\n"
"v_accvgpr_read_b32 v22, a116\n"
"v_accvgpr_read_b32 v23, a117\n"
"v_mul_f32 v22, v22, v26\n"
"v_mul_f32 v23, v23, v26\n"
"v_accvgpr_write_b32 a116, v22\n"
"v_accvgpr_write_b32 a117, v23\n"
"v_accvgpr_read_b32 v22, a118\n"
"v_accvgpr_read_b32 v23, a119\n"
"v_mul_f32 v22, v22, v26\n"
"v_mul_f32 v23, v23, v26\n"
"v_accvgpr_write_b32 a118, v22\n"
"v_accvgpr_write_b32 a119, v23\n"
"v_accvgpr_read_b32 v22, a140\n"
"v_accvgpr_read_b32 v23, a141\n"
"v_mul_f32 v22, v22, v26\n"
"v_mul_f32 v23, v23, v26\n"
"v_accvgpr_write_b32 a140, v22\n"
"v_accvgpr_write_b32 a141, v23\n"
"v_accvgpr_read_b32 v22, a142\n"
"v_accvgpr_read_b32 v23, a143\n"
"v_mul_f32 v22, v22, v26\n"
"v_mul_f32 v23, v23, v26\n"
"v_accvgpr_write_b32 a142, v22\n"
"v_accvgpr_write_b32 a143, v23\n"
"v_accvgpr_read_b32 v22, a164\n"
"v_accvgpr_read_b32 v23, a165\n"
"v_mul_f32 v22, v22, v26\n"
"v_mul_f32 v23, v23, v26\n"
"v_accvgpr_write_b32 a164, v22\n"
"v_accvgpr_write_b32 a165, v23\n"
"v_accvgpr_read_b32 v22, a166\n"
"v_accvgpr_read_b32 v23, a167\n"
"v_mul_f32 v22, v22, v26\n"
"v_mul_f32 v23, v23, v26\n"
"v_accvgpr_write_b32 a166, v22\n"
"v_accvgpr_write_b32 a167, v23\n"
"v_accvgpr_read_b32 v22, a188\n"
"v_accvgpr_read_b32 v23, a189\n"
"v_mul_f32 v22, v22, v26\n"
"v_mul_f32 v23, v23, v26\n"
"v_accvgpr_write_b32 a188, v22\n"
"v_accvgpr_write_b32 a189, v23\n"
"v_accvgpr_read_b32 v22, a190\n"
"v_accvgpr_read_b32 v23, a191\n"
"v_mul_f32 v22, v22, v26\n"
"v_mul_f32 v23, v23, v26\n"
"v_accvgpr_write_b32 a190, v22\n"
"v_accvgpr_write_b32 a191, v23\n"
"v_accvgpr_read_b32 v22, a212\n"
"v_accvgpr_read_b32 v23, a213\n"
"v_mul_f32 v22, v22, v26\n"
"v_mul_f32 v23, v23, v26\n"
"v_accvgpr_write_b32 a212, v22\n"
"v_accvgpr_write_b32 a213, v23\n"
"v_accvgpr_read_b32 v22, a214\n"
"v_accvgpr_read_b32 v23, a215\n"
"v_mul_f32 v22, v22, v26\n"
"v_mul_f32 v23, v23, v26\n"
"v_accvgpr_write_b32 a214, v22\n"
"v_accvgpr_write_b32 a215, v23\n"
"v_accvgpr_read_b32 v22, a236\n"
"v_accvgpr_read_b32 v23, a237\n"
"v_mul_f32 v22, v22, v26\n"
"v_mul_f32 v23, v23, v26\n"
"v_accvgpr_write_b32 a236, v22\n"
"v_accvgpr_write_b32 a237, v23\n"
"v_accvgpr_read_b32 v22, a238\n"
"v_accvgpr_read_b32 v23, a239\n"
"v_mul_f32 v22, v22, v26\n"
"v_mul_f32 v23, v23, v26\n"
"v_accvgpr_write_b32 a238, v22\n"
"v_accvgpr_write_b32 a239, v23\n"
"v_mul_f32 v132, v132, v26\n"
"v_mul_f32 v133, v133, v26\n"
"v_mul_f32 v134, v134, v26\n"
"v_mul_f32 v135, v135, v26\n"
"v_mul_f32 v156, v156, v26\n"
"v_mul_f32 v157, v157, v26\n"
"v_mul_f32 v158, v158, v26\n"
"v_mul_f32 v159, v159, v26\n"
"v_mul_f32 v180, v180, v26\n"
"v_mul_f32 v181, v181, v26\n"
"v_mul_f32 v182, v182, v26\n"
"v_mul_f32 v183, v183, v26\n"
"v_mul_f32 v204, v204, v26\n"
"v_mul_f32 v205, v205, v26\n"
"v_mul_f32 v206, v206, v26\n"
"v_mul_f32 v207, v207, v26\n"
"v_mul_f32 v228, v228, v26\n"
"v_mul_f32 v229, v229, v26\n"
"v_mul_f32 v230, v230, v26\n"
"v_mul_f32 v231, v231, v26\n"
"v_mul_f32 v252, v252, v26\n"
"v_mul_f32 v253, v253, v26\n"
"v_mul_f32 v254, v254, v26\n"
"v_mul_f32 v255, v255, v26\n"
"v_fma_f32 v22, v124, s62, v14\n"
"v_fma_f32 v23, v125, s62, v14\n"
"v_exp_f32 v22, v22\n"
"v_exp_f32 v23, v23\n"
"s_nop 0\n"
"v_add_f32 v21, v22, v23\n"
"v_cvt_pk_bf16_f32 v76, v22, v23\n"
"v_fma_f32 v22, v126, s62, v14\n"
"v_fma_f32 v23, v127, s62, v14\n"
"v_exp_f32 v22, v22\n"
"v_exp_f32 v23, v23\n"
"s_nop 0\n"
"v_add_f32 v21, v21, v22\n"
"v_add_f32 v21, v21, v23\n"
"v_cvt_pk_bf16_f32 v77, v22, v23\n"
"v_fma_f32 v22, v84, s62, v14\n"
"v_fma_f32 v23, v85, s62, v14\n"
"v_exp_f32 v22, v22\n"
"v_exp_f32 v23, v23\n"
"s_nop 0\n"
"v_add_f32 v21, v21, v22\n"
"v_add_f32 v21, v21, v23\n"
"v_cvt_pk_bf16_f32 v78, v22, v23\n"
"v_fma_f32 v22, v86, s62, v14\n"
"v_fma_f32 v23, v87, s62, v14\n"
"v_exp_f32 v22, v22\n"
"v_exp_f32 v23, v23\n"
"s_nop 0\n"
"v_add_f32 v21, v21, v22\n"
"v_add_f32 v21, v21, v23\n"
"v_cvt_pk_bf16_f32 v79, v22, v23\n"
"s_nop 1\n"
"s_branch .Lret_2_d5\n"
".Lrare_3_a:\n"
"s_nop 7\n"
"v_max3_f32 v25, v52, v53, v54\n"
"v_max3_f32 v26, v55, v104, v105\n"
"v_max3_f32 v25, v25, v106, v107\n"
"v_max_f32 v25, v25, v26\n"
"v_mov_b32 v26, v25\n"
"s_nop 1\n"
"v_permlane32_swap_b32 v25, v26\n"
"s_nop 1\n"
"v_max_f32 v25, v25, v26\n"
"v_mov_b32 v26, v25\n"
"s_nop 1\n"
"v_permlane16_swap_b32 v25, v26\n"
"s_nop 1\n"
"v_max_f32 v25, v25, v26\n"
"v_mul_f32 v25, s62, v25\n"
"v_sub_f32 v25, 0, v25\n"
"v_min_f32 v25, v25, v9\n"
"v_sub_f32 v26, v25, v9\n"
"v_exp_f32 v26, v26\n"
"v_mov_b32 v9, v25\n"
"s_nop 0\n"
"v_mul_f32 v15, v15, v26\n"
"v_accvgpr_read_b32 v22, a96\n"
"v_accvgpr_read_b32 v23, a97\n"
"v_mul_f32 v22, v22, v26\n"
"v_mul_f32 v23, v23, v26\n"
"v_accvgpr_write_b32 a96, v22\n"
"v_accvgpr_write_b32 a97, v23\n"
"v_accvgpr_read_b32 v22, a98\n"
"v_accvgpr_read_b32 v23, a99\n"
"v_mul_f32 v22, v22, v26\n"
"v_mul_f32 v23, v23, v26\n"
"v_accvgpr_write_b32 a98, v22\n"
"v_accvgpr_write_b32 a99, v23\n"
"v_accvgpr_read_b32 v22, a120\n"
"v_accvgpr_read_b32 v23, a121\n"
"v_mul_f32 v22, v22, v26\n"
"v_mul_f32 v23, v23, v26\n"
"v_accvgpr_write_b32 a120, v22\n"
"v_accvgpr_write_b32 a121, v23\n"
"v_accvgpr_read_b32 v22, a122\n"
"v_accvgpr_read_b32 v23, a123\n"
"v_mul_f32 v22, v22, v26\n"
"v_mul_f32 v23, v23, v26\n"
"v_accvgpr_write_b32 a122, v22\n"
"v_accvgpr_write_b32 a123, v23\n"
"v_accvgpr_read_b32 v22, a144\n"
"v_accvgpr_read_b32 v23, a145\n"
"v_mul_f32 v22, v22, v26\n"
"v_mul_f32 v23, v23, v26\n"
"v_accvgpr_write_b32 a144, v22\n"
"v_accvgpr_write_b32 a145, v23\n"
"v_accvgpr_read_b32 v22, a146\n"
"v_accvgpr_read_b32 v23, a147\n"
"v_mul_f32 v22, v22, v26\n"
"v_mul_f32 v23, v23, v26\n"
"v_accvgpr_write_b32 a146, v22\n"
"v_accvgpr_write_b32 a147, v23\n"
"v_accvgpr_read_b32 v22, a168\n"
"v_accvgpr_read_b32 v23, a169\n"
"v_mul_f32 v22, v22, v26\n"
"v_mul_f32 v23, v23, v26\n"
"v_accvgpr_write_b32 a168, v22\n"
"v_accvgpr_write_b32 a169, v23\n"
"v_accvgpr_read_b32 v22, a170\n"
"v_accvgpr_read_b32 v23, a171\n"
"v_mul_f32 v22, v22, v26\n"
"v_mul_f32 v23, v23, v26\n"
"v_accvgpr_write_b32 a170, v22\n"
"v_accvgpr_write_b32 a171, v23\n"
"v_accvgpr_read_b32 v22, a192\n"
"v_accvgpr_read_b32 v23, a193\n"
"v_mul_f32 v22, v22, v26\n"
"v_mul_f32 v23, v23, v26\n"
"v_accvgpr_write_b32 a192, v22\n"
"v_accvgpr_write_b32 a193, v23\n"
"v_accvgpr_read_b32 v22, a194\n"
"v_accvgpr_read_b32 v23, a195\n"
"v_mul_f32 v22, v22, v26\n"
"v_mul_f32 v23, v23, v26\n"
"v_accvgpr_write_b32 a194, v22\n"
"v_accvgpr_write_b32 a195, v23\n"
"v_accvgpr_read_b32 v22, a216\n"
"v_accvgpr_read_b32 v23, a217\n"
"v_mul_f32 v22, v22, v26\n"
"v_mul_f32 v23, v23, v26\n"
"v_accvgpr_write_b32 a216, v22\n"
"v_accvgpr_write_b32 a217, v23\n"
"v_accvgpr_read_b32 v22, a218\n"
"v_accvgpr_read_b32 v23, a219\n"
"v_mul_f32 v22, v22, v26\n"
"v_mul_f32 v23, v23, v26\n"
"v_accvgpr_write_b32 a218, v22\n"
"v_accvgpr_write_b32 a219, v23\n"
"v_accvgpr_read_b32 v22, a240\n"
"v_accvgpr_read_b32 v23, a241\n"
"v_mul_f32 v22, v22, v26\n"
"v_mul_f32 v23, v23, v26\n"
"v_accvgpr_write_b32 a240, v22\n"
"v_accvgpr_write_b32 a241, v23\n"
"v_accvgpr_read_b32 v22, a242\n"
"v_accvgpr_read_b32 v23, a243\n"
"v_mul_f32 v22, v22, v26\n"
"v_mul_f32 v23, v23, v26\n"
"v_accvgpr_write_b32 a242, v22\n"
"v_accvgpr_write_b32 a243, v23\n"
"v_mul_f32 v136, v136, v26\n"
"v_mul_f32 v137, v137, v26\n"
"v_mul_f32 v138, v138, v26\n"
"v_mul_f32 v139, v139, v26\n"
"v_mul_f32 v160, v160, v26\n"
"v_mul_f32 v161, v161, v26\n"
"v_mul_f32 v162, v162, v26\n"
"v_mul_f32 v163, v163, v26\n"
"v_mul_f32 v184, v184, v26\n"
"v_mul_f32 v185, v185, v26\n"
"v_mul_f32 v186, v186, v26\n"
"v_mul_f32 v187, v187, v26\n"
"v_mul_f32 v208, v208, v26\n"
"v_mul_f32 v209, v209, v26\n"
"v_mul_f32 v210, v210, v26\n"
"v_mul_f32 v211, v211, v26\n"
"v_mul_f32 v232, v232, v26\n"
"v_mul_f32 v233, v233, v26\n"
"v_mul_f32 v234, v234, v26\n"
"v_mul_f32 v235, v235, v26\n"
"v_lshlrev_b32 v22, 16, v80\n"
"v_and_b32 v23, 0xffff0000, v80\n"
"v_mul_f32 v22, v22, v26\n"
"v_mul_f32 v23, v23, v26\n"
"v_cvt_pk_bf16_f32 v80, v22, v23\n"
"v_lshlrev_b32 v22, 16, v81\n"
"v_and_b32 v23, 0xffff0000, v81\n"
"v_mul_f32 v22, v22, v26\n"
"v_mul_f32 v23, v23, v26\n"
"v_cvt_pk_bf16_f32 v81, v22, v23\n"
"v_lshlrev_b32 v22, 16, v82\n"
"v_and_b32 v23, 0xffff0000, v82\n"
"v_mul_f32 v22, v22, v26\n"
"v_mul_f32 v23, v23, v26\n"
"v_cvt_pk_bf16_f32 v82, v22, v23\n"
"v_lshlrev_b32 v22, 16, v83\n"
"v_and_b32 v23, 0xffff0000, v83\n"
"v_mul_f32 v22, v22, v26\n"
"v_mul_f32 v23, v23, v26\n"
"v_cvt_pk_bf16_f32 v83, v22, v23\n"
"v_fma_f32 v22, v52, s62, v9\n"
"v_fma_f32 v23, v53, s62, v9\n"
"v_exp_f32 v22, v22\n"
"v_exp_f32 v23, v23\n"
"s_nop 0\n"
"v_add_f32 v21, v22, v23\n"
"v_cvt_pk_bf16_f32 v108, v22, v23\n"
"v_fma_f32 v22, v54, s62, v9\n"
"v_fma_f32 v23, v55, s62, v9\n"
"v_exp_f32 v22, v22\n"
"v_exp_f32 v23, v23\n"
"s_nop 0\n"
"v_add_f32 v21, v21, v22\n"
"v_add_f32 v21, v21, v23\n"
"v_cvt_pk_bf16_f32 v109, v22, v23\n"
"v_fma_f32 v22, v104, s62, v9\n"
"v_fma_f32 v23, v105, s62, v9\n"
"v_exp_f32 v22, v22\n"
"v_exp_f32 v23, v23\n"
"s_nop 0\n"
"v_add_f32 v21, v21, v22\n"
"v_add_f32 v21, v21, v23\n"
"v_cvt_pk_bf16_f32 v110, v22, v23\n"
"v_fma_f32 v22, v106, s62, v9\n"
"v_fma_f32 v23, v107, s62, v9\n"
"v_exp_f32 v22, v22\n"
"v_exp_f32 v23, v23\n"
"s_nop 0\n"
"v_add_f32 v21, v21, v22\n"
"v_add_f32 v21, v21, v23\n"
"v_cvt_pk_bf16_f32 v111, v22, v23\n"
"v_max3_f32 v25, v96, v97, v98\n"
"v_max3_f32 v26, v99, v88, v89\n"
"v_max3_f32 v25, v25, v90, v91\n"
"v_max_f32 v25, v25, v26\n"
"v_mov_b32 v26, v25\n"
"s_nop 1\n"
"v_permlane32_swap_b32 v25, v26\n"
"s_nop 1\n"
"v_max_f32 v25, v25, v26\n"
"v_mov_b32 v26, v25\n"
"s_nop 1\n"
"v_permlane16_swap_b32 v25, v26\n"
"s_nop 1\n"
"v_max_f32 v25, v25, v26\n"
"v_mul_f32 v25, s62, v25\n"
"v_sub_f32 v25, 0, v25\n"
"v_min_f32 v25, v25, v10\n"
"v_sub_f32 v26, v25, v10\n"
"v_exp_f32 v26, v26\n"
"v_mov_b32 v10, v25\n"
"s_nop 0\n"
"v_mul_f32 v16, v16, v26\n"
"v_accvgpr_read_b32 v22, a100\n"
"v_accvgpr_read_b32 v23, a101\n"
"v_mul_f32 v22, v22, v26\n"
"v_mul_f32 v23, v23, v26\n"
"v_accvgpr_write_b32 a100, v22\n"
"v_accvgpr_write_b32 a101, v23\n"
"v_accvgpr_read_b32 v22, a102\n"
"v_accvgpr_read_b32 v23, a103\n"
"v_mul_f32 v22, v22, v26\n"
"v_mul_f32 v23, v23, v26\n"
"v_accvgpr_write_b32 a102, v22\n"
"v_accvgpr_write_b32 a103, v23\n"
"v_accvgpr_read_b32 v22, a124\n"
"v_accvgpr_read_b32 v23, a125\n"
"v_mul_f32 v22, v22, v26\n"
"v_mul_f32 v23, v23, v26\n"
"v_accvgpr_write_b32 a124, v22\n"
"v_accvgpr_write_b32 a125, v23\n"
"v_accvgpr_read_b32 v22, a126\n"
"v_accvgpr_read_b32 v23, a127\n"
"v_mul_f32 v22, v22, v26\n"
"v_mul_f32 v23, v23, v26\n"
"v_accvgpr_write_b32 a126, v22\n"
"v_accvgpr_write_b32 a127, v23\n"
"v_accvgpr_read_b32 v22, a148\n"
"v_accvgpr_read_b32 v23, a149\n"
"v_mul_f32 v22, v22, v26\n"
"v_mul_f32 v23, v23, v26\n"
"v_accvgpr_write_b32 a148, v22\n"
"v_accvgpr_write_b32 a149, v23\n"
"v_accvgpr_read_b32 v22, a150\n"
"v_accvgpr_read_b32 v23, a151\n"
"v_mul_f32 v22, v22, v26\n"
"v_mul_f32 v23, v23, v26\n"
"v_accvgpr_write_b32 a150, v22\n"
"v_accvgpr_write_b32 a151, v23\n"
"v_accvgpr_read_b32 v22, a172\n"
"v_accvgpr_read_b32 v23, a173\n"
"v_mul_f32 v22, v22, v26\n"
"v_mul_f32 v23, v23, v26\n"
"v_accvgpr_write_b32 a172, v22\n"
"v_accvgpr_write_b32 a173, v23\n"
"v_accvgpr_read_b32 v22, a174\n"
"v_accvgpr_read_b32 v23, a175\n"
"v_mul_f32 v22, v22, v26\n"
"v_mul_f32 v23, v23, v26\n"
"v_accvgpr_write_b32 a174, v22\n"
"v_accvgpr_write_b32 a175, v23\n"
"v_accvgpr_read_b32 v22, a196\n"
"v_accvgpr_read_b32 v23, a197\n"
"v_mul_f32 v22, v22, v26\n"
"v_mul_f32 v23, v23, v26\n"
"v_accvgpr_write_b32 a196, v22\n"
"v_accvgpr_write_b32 a197, v23\n"
"v_accvgpr_read_b32 v22, a198\n"
"v_accvgpr_read_b32 v23, a199\n"
"v_mul_f32 v22, v22, v26\n"
"v_mul_f32 v23, v23, v26\n"
"v_accvgpr_write_b32 a198, v22\n"
"v_accvgpr_write_b32 a199, v23\n"
"v_accvgpr_read_b32 v22, a220\n"
"v_accvgpr_read_b32 v23, a221\n"
"v_mul_f32 v22, v22, v26\n"
"v_mul_f32 v23, v23, v26\n"
"v_accvgpr_write_b32 a220, v22\n"
"v_accvgpr_write_b32 a221, v23\n"
"v_accvgpr_read_b32 v22, a222\n"
"v_accvgpr_read_b32 v23, a223\n"
"v_mul_f32 v22, v22, v26\n"
"v_mul_f32 v23, v23, v26\n"
"v_accvgpr_write_b32 a222, v22\n"
"v_accvgpr_write_b32 a223, v23\n"
"v_accvgpr_read_b32 v22, a244\n"
"v_accvgpr_read_b32 v23, a245\n"
"v_mul_f32 v22, v22, v26\n"
"v_mul_f32 v23, v23, v26\n"
"v_accvgpr_write_b32 a244, v22\n"
"v_accvgpr_write_b32 a245, v23\n"
"v_accvgpr_read_b32 v22, a246\n"
"v_accvgpr_read_b32 v23, a247\n"
"v_mul_f32 v22, v22, v26\n"
"v_mul_f32 v23, v23, v26\n"
"v_accvgpr_write_b32 a246, v22\n"
"v_accvgpr_write_b32 a247, v23\n"
"v_mul_f32 v140, v140, v26\n"
"v_mul_f32 v141, v141, v26\n"
"v_mul_f32 v142, v142, v26\n"
"v_mul_f32 v143, v143, v26\n"
"v_mul_f32 v164, v164, v26\n"
"v_mul_f32 v165, v165, v26\n"
"v_mul_f32 v166, v166, v26\n"
"v_mul_f32 v167, v167, v26\n"
"v_mul_f32 v188, v188, v26\n"
"v_mul_f32 v189, v189, v26\n"
"v_mul_f32 v190, v190, v26\n"
"v_mul_f32 v191, v191, v26\n"
"v_mul_f32 v212, v212, v26\n"
"v_mul_f32 v213, v213, v26\n"
"v_mul_f32 v214, v214, v26\n"
"v_mul_f32 v215, v215, v26\n"
"v_mul_f32 v236, v236, v26\n"
"v_mul_f32 v237, v237, v26\n"
"v_mul_f32 v238, v238, v26\n"
"v_mul_f32 v239, v239, v26\n"
"v_lshlrev_b32 v22, 16, v48\n"
"v_and_b32 v23, 0xffff0000, v48\n"
"v_mul_f32 v22, v22, v26\n"
"v_mul_f32 v23, v23, v26\n"
"v_cvt_pk_bf16_f32 v48, v22, v23\n"
"v_lshlrev_b32 v22, 16, v49\n"
"v_and_b32 v23, 0xffff0000, v49\n"
"v_mul_f32 v22, v22, v26\n"
"v_mul_f32 v23, v23, v26\n"
"v_cvt_pk_bf16_f32 v49, v22, v23\n"
"v_lshlrev_b32 v22, 16, v50\n"
"v_and_b32 v23, 0xffff0000, v50\n"
"v_mul_f32 v22, v22, v26\n"
"v_mul_f32 v23, v23, v26\n"
"v_cvt_pk_bf16_f32 v50, v22, v23\n"
"v_lshlrev_b32 v22, 16, v51\n"
"v_and_b32 v23, 0xffff0000, v51\n"
"v_mul_f32 v22, v22, v26\n"
"v_mul_f32 v23, v23, v26\n"
"v_cvt_pk_bf16_f32 v51, v22, v23\n"
"v_fma_f32 v22, v96, s62, v10\n"
"v_fma_f32 v23, v97, s62, v10\n"
"v_exp_f32 v22, v22\n"
"v_exp_f32 v23, v23\n"
"s_nop 0\n"
"v_add_f32 v3, v22, v23\n"
"v_cvt_pk_bf16_f32 v124, v22, v23\n"
"v_fma_f32 v22, v98, s62, v10\n"
"v_fma_f32 v23, v99, s62, v10\n"
"v_exp_f32 v22, v22\n"
"v_exp_f32 v23, v23\n"
"s_nop 0\n"
"v_add_f32 v3, v3, v22\n"
"v_add_f32 v3, v3, v23\n"
"v_cvt_pk_bf16_f32 v125, v22, v23\n"
"v_fma_f32 v22, v88, s62, v10\n"
"v_fma_f32 v23, v89, s62, v10\n"
"v_exp_f32 v22, v22\n"
"v_exp_f32 v23, v23\n"
"s_nop 0\n"
"v_add_f32 v3, v3, v22\n"
"v_add_f32 v3, v3, v23\n"
"v_cvt_pk_bf16_f32 v126, v22, v23\n"
"v_fma_f32 v22, v90, s62, v10\n"
"v_fma_f32 v23, v91, s62, v10\n"
"v_exp_f32 v22, v22\n"
"v_exp_f32 v23, v23\n"
"s_nop 0\n"
"v_add_f32 v3, v3, v22\n"
"v_add_f32 v3, v3, v23\n"
"v_cvt_pk_bf16_f32 v127, v22, v23\n"
"v_fma_f32 v22, v56, s62, v11\n"
"v_fma_f32 v23, v57, s62, v11\n"
"s_nop 1\n"
"s_branch .Lret_3_a\n"
".Lrare_3_b:\n"
"s_nop 7\n"
"v_max3_f32 v25, v56, v57, v58\n"
"v_max3_f32 v26, v59, v68, v69\n"
"v_max3_f32 v25, v25, v70, v71\n"
"v_max_f32 v25, v25, v26\n"
"v_mov_b32 v26, v25\n"
"s_nop 1\n"
"v_permlane32_swap_b32 v25, v26\n"
"s_nop 1\n"
"v_max_f32 v25, v25, v26\n"
"v_mov_b32 v26, v25\n"
"s_nop 1\n"
"v_permlane16_swap_b32 v25, v26\n"
"s_nop 1\n"
"v_max_f32 v25, v25, v26\n"
"v_mul_f32 v25, s62, v25\n"
"v_sub_f32 v25, 0, v25\n"
"v_min_f32 v25, v25, v11\n"
"v_sub_f32 v26, v25, v11\n"
"v_exp_f32 v26, v26\n"
"v_mov_b32 v11, v25\n"
"s_nop 0\n"
"v_mul_f32 v17, v17, v26\n"
"v_accvgpr_read_b32 v22, a104\n"
"v_accvgpr_read_b32 v23, a105\n"
"v_mul_f32 v22, v22, v26\n"
"v_mul_f32 v23, v23, v26\n"
"v_accvgpr_write_b32 a104, v22\n"
"v_accvgpr_write_b32 a105, v23\n"
"v_accvgpr_read_b32 v22, a106\n"
"v_accvgpr_read_b32 v23, a107\n"
"v_mul_f32 v22, v22, v26\n"
"v_mul_f32 v23, v23, v26\n"
"v_accvgpr_write_b32 a106, v22\n"
"v_accvgpr_write_b32 a107, v23\n"
"v_accvgpr_read_b32 v22, a128\n"
"v_accvgpr_read_b32 v23, a129\n"
"v_mul_f32 v22, v22, v26\n"
"v_mul_f32 v23, v23, v26\n"
"v_accvgpr_write_b32 a128, v22\n"
"v_accvgpr_write_b32 a129, v23\n"
"v_accvgpr_read_b32 v22, a130\n"
"v_accvgpr_read_b32 v23, a131\n"
"v_mul_f32 v22, v22, v26\n"
"v_mul_f32 v23, v23, v26\n"
"v_accvgpr_write_b32 a130, v22\n"
"v_accvgpr_write_b32 a131, v23\n"
"v_accvgpr_read_b32 v22, a152\n"
"v_accvgpr_read_b32 v23, a153\n"
"v_mul_f32 v22, v22, v26\n"
"v_mul_f32 v23, v23, v26\n"
"v_accvgpr_write_b32 a152, v22\n"
"v_accvgpr_write_b32 a153, v23\n"
"v_accvgpr_read_b32 v22, a154\n"
"v_accvgpr_read_b32 v23, a155\n"
"v_mul_f32 v22, v22, v26\n"
"v_mul_f32 v23, v23, v26\n"
"v_accvgpr_write_b32 a154, v22\n"
"v_accvgpr_write_b32 a155, v23\n"
"v_accvgpr_read_b32 v22, a176\n"
"v_accvgpr_read_b32 v23, a177\n"
"v_mul_f32 v22, v22, v26\n"
"v_mul_f32 v23, v23, v26\n"
"v_accvgpr_write_b32 a176, v22\n"
"v_accvgpr_write_b32 a177, v23\n"
"v_accvgpr_read_b32 v22, a178\n"
"v_accvgpr_read_b32 v23, a179\n"
"v_mul_f32 v22, v22, v26\n"
"v_mul_f32 v23, v23, v26\n"
"v_accvgpr_write_b32 a178, v22\n"
"v_accvgpr_write_b32 a179, v23\n"
"v_accvgpr_read_b32 v22, a200\n"
"v_accvgpr_read_b32 v23, a201\n"
"v_mul_f32 v22, v22, v26\n"
"v_mul_f32 v23, v23, v26\n"
"v_accvgpr_write_b32 a200, v22\n"
"v_accvgpr_write_b32 a201, v23\n"
"v_accvgpr_read_b32 v22, a202\n"
"v_accvgpr_read_b32 v23, a203\n"
"v_mul_f32 v22, v22, v26\n"
"v_mul_f32 v23, v23, v26\n"
"v_accvgpr_write_b32 a202, v22\n"
"v_accvgpr_write_b32 a203, v23\n"
"v_accvgpr_read_b32 v22, a224\n"
"v_accvgpr_read_b32 v23, a225\n"
"v_mul_f32 v22, v22, v26\n"
"v_mul_f32 v23, v23, v26\n"
"v_accvgpr_write_b32 a224, v22\n"
"v_accvgpr_write_b32 a225, v23\n"
"v_accvgpr_read_b32 v22, a226\n"
"v_accvgpr_read_b32 v23, a227\n"
"v_mul_f32 v22, v22, v26\n"
"v_mul_f32 v23, v23, v26\n"
"v_accvgpr_write_b32 a226, v22\n"
"v_accvgpr_write_b32 a227, v23\n"
"v_accvgpr_read_b32 v22, a248\n"
"v_accvgpr_read_b32 v23, a249\n"
"v_mul_f32 v22, v22, v26\n"
"v_mul_f32 v23, v23, v26\n"
"v_accvgpr_write_b32 a248, v22\n"
"v_accvgpr_write_b32 a249, v23\n"
"v_accvgpr_read_b32 v22, a250\n"
"v_accvgpr_read_b32 v23, a251\n"
"v_mul_f32 v22, v22, v26\n"
"v_mul_f32 v23, v23, v26\n"
"v_accvgpr_write_b32 a250, v22\n"
"v_accvgpr_write_b32 a251, v23\n"
"v_mul_f32 v144, v144, v26\n"
"v_mul_f32 v145, v145, v26\n"
"v_mul_f32 v146, v146, v26\n"
"v_mul_f32 v147, v147, v26\n"
"v_mul_f32 v168, v168, v26\n"
"v_mul_f32 v169, v169, v26\n"
"v_mul_f32 v170, v170, v26\n"
"v_mul_f32 v171, v171, v26\n"
"v_mul_f32 v192, v192, v26\n"
"v_mul_f32 v193, v193, v26\n"
"v_mul_f32 v194, v194, v26\n"
"v_mul_f32 v195, v195, v26\n"
"v_mul_f32 v216, v216, v26\n"
"v_mul_f32 v217, v217, v26\n"
"v_mul_f32 v218, v218, v26\n"
"v_mul_f32 v219, v219, v26\n"
"v_mul_f32 v240, v240, v26\n"
"v_mul_f32 v241, v241, v26\n"
"v_mul_f32 v242, v242, v26\n"
"v_mul_f32 v243, v243, v26\n"
"v_lshlrev_b32 v22, 16, v60\n"
"v_and_b32 v23, 0xffff0000, v60\n"
"v_mul_f32 v22, v22, v26\n"
"v_mul_f32 v23, v23, v26\n"
"v_cvt_pk_bf16_f32 v60, v22, v23\n"
"v_lshlrev_b32 v22, 16, v61\n"
"v_and_b32 v23, 0xffff0000, v61\n"
"v_mul_f32 v22, v22, v26\n"
"v_mul_f32 v23, v23, v26\n"
"v_cvt_pk_bf16_f32 v61, v22, v23\n"
"v_lshlrev_b32 v22, 16, v62\n"
"v_and_b32 v23, 0xffff0000, v62\n"
"v_mul_f32 v22, v22, v26\n"
"v_mul_f32 v23, v23, v26\n"
"v_cvt_pk_bf16_f32 v62, v22, v23\n"
"v_lshlrev_b32 v22, 16, v63\n"
"v_and_b32 v23, 0xffff0000, v63\n"
"v_mul_f32 v22, v22, v26\n"
"v_mul_f32 v23, v23, v26\n"
"v_cvt_pk_bf16_f32 v63, v22, v23\n"
"v_fma_f32 v22, v56, s62, v11\n"
"v_fma_f32 v23, v57, s62, v11\n"
"v_exp_f32 v22, v22\n"
"v_exp_f32 v23, v23\n"
"s_nop 0\n"
"v_add_f32 v104, v22, v23\n"
"v_cvt_pk_bf16_f32 v84, v22, v23\n"
"v_fma_f32 v22, v58, s62, v11\n"
"v_fma_f32 v23, v59, s62, v11\n"
"v_exp_f32 v22, v22\n"
"v_exp_f32 v23, v23\n"
"s_nop 0\n"
"v_add_f32 v104, v104, v22\n"
"v_add_f32 v104, v104, v23\n"
"v_cvt_pk_bf16_f32 v85, v22, v23\n"
"v_fma_f32 v22, v68, s62, v11\n"
"v_fma_f32 v23, v69, s62, v11\n"
"v_exp_f32 v22, v22\n"
"v_exp_f32 v23, v23\n"
"s_nop 0\n"
"v_add_f32 v104, v104, v22\n"
"v_add_f32 v104, v104, v23\n"
"v_cvt_pk_bf16_f32 v86, v22, v23\n"
"v_fma_f32 v22, v70, s62, v11\n"
"v_fma_f32 v23, v71, s62, v11\n"
"v_exp_f32 v22, v22\n"
"v_exp_f32 v23, v23\n"
"s_nop 0\n"
"v_add_f32 v104, v104, v22\n"
"v_add_f32 v104, v104, v23\n"
"v_cvt_pk_bf16_f32 v87, v22, v23\n"
"v_max3_f32 v25, v72, v73, v74\n"
"v_max3_f32 v26, v75, v92, v93\n"
"v_max3_f32 v25, v25, v94, v95\n"
"v_max_f32 v25, v25, v26\n"
"v_mov_b32 v26, v25\n"
"s_nop 1\n"
"v_permlane32_swap_b32 v25, v26\n"
"s_nop 1\n"
"v_max_f32 v25, v25, v26\n"
"v_mov_b32 v26, v25\n"
"s_nop 1\n"
"v_permlane16_swap_b32 v25, v26\n"
"s_nop 1\n"
"v_max_f32 v25, v25, v26\n"
"v_mul_f32 v25, s62, v25\n"
"v_sub_f32 v25, 0, v25\n"
"v_min_f32 v25, v25, v12\n"
"v_sub_f32 v26, v25, v12\n"
"v_exp_f32 v26, v26\n"
"v_mov_b32 v12, v25\n"
"s_nop 0\n"
"v_mul_f32 v18, v18, v26\n"
"v_accvgpr_read_b32 v22, a108\n"
"v_accvgpr_read_b32 v23, a109\n"
"v_mul_f32 v22, v22, v26\n"
"v_mul_f32 v23, v23, v26\n"
"v_accvgpr_write_b32 a108, v22\n"
"v_accvgpr_write_b32 a109, v23\n"
"v_accvgpr_read_b32 v22, a110\n"
"v_accvgpr_read_b32 v23, a111\n"
"v_mul_f32 v22, v22, v26\n"
"v_mul_f32 v23, v23, v26\n"
"v_accvgpr_write_b32 a110, v22\n"
"v_accvgpr_write_b32 a111, v23\n"
"v_accvgpr_read_b32 v22, a132\n"
"v_accvgpr_read_b32 v23, a133\n"
"v_mul_f32 v22, v22, v26\n"
"v_mul_f32 v23, v23, v26\n"
"v_accvgpr_write_b32 a132, v22\n"
"v_accvgpr_write_b32 a133, v23\n"
"v_accvgpr_read_b32 v22, a134\n"
"v_accvgpr_read_b32 v23, a135\n"
"v_mul_f32 v22, v22, v26\n"
"v_mul_f32 v23, v23, v26\n"
"v_accvgpr_write_b32 a134, v22\n"
"v_accvgpr_write_b32 a135, v23\n"
"v_accvgpr_read_b32 v22, a156\n"
"v_accvgpr_read_b32 v23, a157\n"
"v_mul_f32 v22, v22, v26\n"
"v_mul_f32 v23, v23, v26\n"
"v_accvgpr_write_b32 a156, v22\n"
"v_accvgpr_write_b32 a157, v23\n"
"v_accvgpr_read_b32 v22, a158\n"
"v_accvgpr_read_b32 v23, a159\n"
"v_mul_f32 v22, v22, v26\n"
"v_mul_f32 v23, v23, v26\n"
"v_accvgpr_write_b32 a158, v22\n"
"v_accvgpr_write_b32 a159, v23\n"
"v_accvgpr_read_b32 v22, a180\n"
"v_accvgpr_read_b32 v23, a181\n"
"v_mul_f32 v22, v22, v26\n"
"v_mul_f32 v23, v23, v26\n"
"v_accvgpr_write_b32 a180, v22\n"
"v_accvgpr_write_b32 a181, v23\n"
"v_accvgpr_read_b32 v22, a182\n"
"v_accvgpr_read_b32 v23, a183\n"
"v_mul_f32 v22, v22, v26\n"
"v_mul_f32 v23, v23, v26\n"
"v_accvgpr_write_b32 a182, v22\n"
"v_accvgpr_write_b32 a183, v23\n"
"v_accvgpr_read_b32 v22, a204\n"
"v_accvgpr_read_b32 v23, a205\n"
"v_mul_f32 v22, v22, v26\n"
"v_mul_f32 v23, v23, v26\n"
"v_accvgpr_write_b32 a204, v22\n"
"v_accvgpr_write_b32 a205, v23\n"
"v_accvgpr_read_b32 v22, a206\n"
"v_accvgpr_read_b32 v23, a207\n"
"v_mul_f32 v22, v22, v26\n"
"v_mul_f32 v23, v23, v26\n"
"v_accvgpr_write_b32 a206, v22\n"
"v_accvgpr_write_b32 a207, v23\n"
"v_accvgpr_read_b32 v22, a228\n"
"v_accvgpr_read_b32 v23, a229\n"
"v_mul_f32 v22, v22, v26\n"
"v_mul_f32 v23, v23, v26\n"
"v_accvgpr_write_b32 a228, v22\n"
"v_accvgpr_write_b32 a229, v23\n"
"v_accvgpr_read_b32 v22, a230\n"
"v_accvgpr_read_b32 v23, a231\n"
"v_mul_f32 v22, v22, v26\n"
"v_mul_f32 v23, v23, v26\n"
"v_accvgpr_write_b32 a230, v22\n"
"v_accvgpr_write_b32 a231, v23\n"
"v_accvgpr_read_b32 v22, a252\n"
"v_accvgpr_read_b32 v23, a253\n"
"v_mul_f32 v22, v22, v26\n"
"v_mul_f32 v23, v23, v26\n"
"v_accvgpr_write_b32 a252, v22\n"
"v_accvgpr_write_b32 a253, v23\n"
"v_accvgpr_read_b32 v22, a254\n"
"v_accvgpr_read_b32 v23, a255\n"
"v_mul_f32 v22, v22, v26\n"
"v_mul_f32 v23, v23, v26\n"
"v_accvgpr_write_b32 a254, v22\n"
"v_accvgpr_write_b32 a255, v23\n"
"v_mul_f32 v148, v148, v26\n"
"v_mul_f32 v149, v149, v26\n"
"v_mul_f32 v150, v150, v26\n"
"v_mul_f32 v151, v151, v26\n"
"v_mul_f32 v172, v172, v26\n"
"v_mul_f32 v173, v173, v26\n"
"v_mul_f32 v174, v174, v26\n"
"v_mul_f32 v175, v175, v26\n"
"v_mul_f32 v196, v196, v26\n"
"v_mul_f32 v197, v197, v26\n"
"v_mul_f32 v198, v198, v26\n"
"v_mul_f32 v199, v199, v26\n"
"v_mul_f32 v220, v220, v26\n"
"v_mul_f32 v221, v221, v26\n"
"v_mul_f32 v222, v222, v26\n"
"v_mul_f32 v223, v223, v26\n"
"v_mul_f32 v244, v244, v26\n"
"v_mul_f32 v245, v245, v26\n"
"v_mul_f32 v246, v246, v26\n"
"v_mul_f32 v247, v247, v26\n"
"v_lshlrev_b32 v22, 16, v116\n"
"v_and_b32 v23, 0xffff0000, v116\n"
"v_mul_f32 v22, v22, v26\n"
"v_mul_f32 v23, v23, v26\n"
"v_cvt_pk_bf16_f32 v116, v22, v23\n"
"v_lshlrev_b32 v22, 16, v117\n"
"v_and_b32 v23, 0xffff0000, v117\n"
"v_mul_f32 v22, v22, v26\n"
"v_mul_f32 v23, v23, v26\n"
"v_cvt_pk_bf16_f32 v117, v22, v23\n"
"v_lshlrev_b32 v22, 16, v118\n"
"v_and_b32 v23, 0xffff0000, v118\n"
"v_mul_f32 v22, v22, v26\n"
"v_mul_f32 v23, v23, v26\n"
"v_cvt_pk_bf16_f32 v118, v22, v23\n"
"v_lshlrev_b32 v22, 16, v119\n"
"v_and_b32 v23, 0xffff0000, v119\n"
"v_mul_f32 v22, v22, v26\n"
"v_mul_f32 v23, v23, v26\n"
"v_cvt_pk_bf16_f32 v119, v22, v23\n"
"v_fma_f32 v22, v72, s62, v12\n"
"v_fma_f32 v23, v73, s62, v12\n"
"v_exp_f32 v22, v22\n"
"v_exp_f32 v23, v23\n"
"s_nop 0\n"
"v_add_f32 v105, v22, v23\n"
"v_cvt_pk_bf16_f32 v52, v22, v23\n"
"v_fma_f32 v22, v74, s62, v12\n"
"v_fma_f32 v23, v75, s62, v12\n"
"v_exp_f32 v22, v22\n"
"v_exp_f32 v23, v23\n"
"s_nop 0\n"
"v_add_f32 v105, v105, v22\n"
"v_add_f32 v105, v105, v23\n"
"v_cvt_pk_bf16_f32 v53, v22, v23\n"
"v_fma_f32 v22, v92, s62, v12\n"
"v_fma_f32 v23, v93, s62, v12\n"
"v_exp_f32 v22, v22\n"
"v_exp_f32 v23, v23\n"
"s_nop 0\n"
"v_add_f32 v105, v105, v22\n"
"v_add_f32 v105, v105, v23\n"
"v_cvt_pk_bf16_f32 v54, v22, v23\n"
"v_fma_f32 v22, v94, s62, v12\n"
"v_fma_f32 v23, v95, s62, v12\n"
"v_exp_f32 v22, v22\n"
"v_exp_f32 v23, v23\n"
"s_nop 0\n"
"v_add_f32 v105, v105, v22\n"
"v_add_f32 v105, v105, v23\n"
"v_cvt_pk_bf16_f32 v55, v22, v23\n"
"v_fma_f32 v22, v100, s62, v13\n"
"v_fma_f32 v23, v101, s62, v13\n"
"s_nop 1\n"
"s_branch .Lret_3_b\n"
".Lrare_3_d4:\n"
"s_nop 7\n"
"v_max3_f32 v25, v100, v101, v102\n"
"v_max3_f32 v26, v103, v112, v113\n"
"v_max3_f32 v25, v25, v114, v115\n"
"v_max_f32 v25, v25, v26\n"
"v_mov_b32 v26, v25\n"
"s_nop 1\n"
"v_permlane32_swap_b32 v25, v26\n"
"s_nop 1\n"
"v_max_f32 v25, v25, v26\n"
"v_mov_b32 v26, v25\n"
"s_nop 1\n"
"v_permlane16_swap_b32 v25, v26\n"
"s_nop 1\n"
"v_max_f32 v25, v25, v26\n"
"v_mul_f32 v25, s62, v25\n"
"v_sub_f32 v25, 0, v25\n"
"v_min_f32 v25, v25, v13\n"
"v_sub_f32 v26, v25, v13\n"
"v_exp_f32 v26, v26\n"
"v_mov_b32 v13, v25\n"
"s_nop 0\n"
"v_mul_f32 v19, v19, v26\n"
"v_accvgpr_read_b32 v22, a112\n"
"v_accvgpr_read_b32 v23, a113\n"
"v_mul_f32 v22, v22, v26\n"
"v_mul_f32 v23, v23, v26\n"
"v_accvgpr_write_b32 a112, v22\n"
"v_accvgpr_write_b32 a113, v23\n"
"v_accvgpr_read_b32 v22, a114\n"
"v_accvgpr_read_b32 v23, a115\n"
"v_mul_f32 v22, v22, v26\n"
"v_mul_f32 v23, v23, v26\n"
"v_accvgpr_write_b32 a114, v22\n"
"v_accvgpr_write_b32 a115, v23\n"
"v_accvgpr_read_b32 v22, a136\n"
"v_accvgpr_read_b32 v23, a137\n"
"v_mul_f32 v22, v22, v26\n"
"v_mul_f32 v23, v23, v26\n"
"v_accvgpr_write_b32 a136, v22\n"
"v_accvgpr_write_b32 a137, v23\n"
"v_accvgpr_read_b32 v22, a138\n"
"v_accvgpr_read_b32 v23, a139\n"
"v_mul_f32 v22, v22, v26\n"
"v_mul_f32 v23, v23, v26\n"
"v_accvgpr_write_b32 a138, v22\n"
"v_accvgpr_write_b32 a139, v23\n"
"v_accvgpr_read_b32 v22, a160\n"
"v_accvgpr_read_b32 v23, a161\n"
"v_mul_f32 v22, v22, v26\n"
"v_mul_f32 v23, v23, v26\n"
"v_accvgpr_write_b32 a160, v22\n"
"v_accvgpr_write_b32 a161, v23\n"
"v_accvgpr_read_b32 v22, a162\n"
"v_accvgpr_read_b32 v23, a163\n"
"v_mul_f32 v22, v22, v26\n"
"v_mul_f32 v23, v23, v26\n"
"v_accvgpr_write_b32 a162, v22\n"
"v_accvgpr_write_b32 a163, v23\n"
"v_accvgpr_read_b32 v22, a184\n"
"v_accvgpr_read_b32 v23, a185\n"
"v_mul_f32 v22, v22, v26\n"
"v_mul_f32 v23, v23, v26\n"
"v_accvgpr_write_b32 a184, v22\n"
"v_accvgpr_write_b32 a185, v23\n"
"v_accvgpr_read_b32 v22, a186\n"
"v_accvgpr_read_b32 v23, a187\n"
"v_mul_f32 v22, v22, v26\n"
"v_mul_f32 v23, v23, v26\n"
"v_accvgpr_write_b32 a186, v22\n"
"v_accvgpr_write_b32 a187, v23\n"
"v_accvgpr_read_b32 v22, a208\n"
"v_accvgpr_read_b32 v23, a209\n"
"v_mul_f32 v22, v22, v26\n"
"v_mul_f32 v23, v23, v26\n"
"v_accvgpr_write_b32 a208, v22\n"
"v_accvgpr_write_b32 a209, v23\n"
"v_accvgpr_read_b32 v22, a210\n"
"v_accvgpr_read_b32 v23, a211\n"
"v_mul_f32 v22, v22, v26\n"
"v_mul_f32 v23, v23, v26\n"
"v_accvgpr_write_b32 a210, v22\n"
"v_accvgpr_write_b32 a211, v23\n"
"v_accvgpr_read_b32 v22, a232\n"
"v_accvgpr_read_b32 v23, a233\n"
"v_mul_f32 v22, v22, v26\n"
"v_mul_f32 v23, v23, v26\n"
"v_accvgpr_write_b32 a232, v22\n"
"v_accvgpr_write_b32 a233, v23\n"
"v_accvgpr_read_b32 v22, a234\n"
"v_accvgpr_read_b32 v23, a235\n"
"v_mul_f32 v22, v22, v26\n"
"v_mul_f32 v23, v23, v26\n"
"v_accvgpr_write_b32 a234, v22\n"
"v_accvgpr_write_b32 a235, v23\n"
"v_mul_f32 v128, v128, v26\n"
"v_mul_f32 v129, v129, v26\n"
"v_mul_f32 v130, v130, v26\n"
"v_mul_f32 v131, v131, v26\n"
"v_mul_f32 v152, v152, v26\n"
"v_mul_f32 v153, v153, v26\n"
"v_mul_f32 v154, v154, v26\n"
"v_mul_f32 v155, v155, v26\n"
"v_mul_f32 v176, v176, v26\n"
"v_mul_f32 v177, v177, v26\n"
"v_mul_f32 v178, v178, v26\n"
"v_mul_f32 v179, v179, v26\n"
"v_mul_f32 v200, v200, v26\n"
"v_mul_f32 v201, v201, v26\n"
"v_mul_f32 v202, v202, v26\n"
"v_mul_f32 v203, v203, v26\n"
"v_mul_f32 v224, v224, v26\n"
"v_mul_f32 v225, v225, v26\n"
"v_mul_f32 v226, v226, v26\n"
"v_mul_f32 v227, v227, v26\n"
"v_mul_f32 v248, v248, v26\n"
"v_mul_f32 v249, v249, v26\n"
"v_mul_f32 v250, v250, v26\n"
"v_mul_f32 v251, v251, v26\n"
"v_fma_f32 v22, v100, s62, v13\n"
"v_fma_f32 v23, v101, s62, v13\n"
"v_exp_f32 v22, v22\n"
"v_exp_f32 v23, v23\n"
"s_nop 0\n"
"v_add_f32 v21, v22, v23\n"
"v_cvt_pk_bf16_f32 v76, v22, v23\n"
"v_fma_f32 v22, v102, s62, v13\n"
"v_fma_f32 v23, v103, s62, v13\n"
"v_exp_f32 v22, v22\n"
"v_exp_f32 v23, v23\n"
"s_nop 0\n"
"v_add_f32 v21, v21, v22\n"
"v_add_f32 v21, v21, v23\n"
"v_cvt_pk_bf16_f32 v77, v22, v23\n"
"v_fma_f32 v22, v112, s62, v13\n"
"v_fma_f32 v23, v113, s62, v13\n"
"v_exp_f32 v22, v22\n"
"v_exp_f32 v23, v23\n"
"s_nop 0\n"
"v_add_f32 v21, v21, v22\n"
"v_add_f32 v21, v21, v23\n"
"v_cvt_pk_bf16_f32 v78, v22, v23\n"
"v_fma_f32 v22, v114, s62, v13\n"
"v_fma_f32 v23, v115, s62, v13\n"
"v_exp_f32 v22, v22\n"
"v_exp_f32 v23, v23\n"
"s_nop 0\n"
"v_add_f32 v21, v21, v22\n"
"v_add_f32 v21, v21, v23\n"
"v_cvt_pk_bf16_f32 v79, v22, v23\n"
"v_fma_f32 v22, v120, s62, v14\n"
"v_fma_f32 v23, v121, s62, v14\n"
"s_nop 1\n"
"s_branch .Lret_3_d4\n"
".Lrare_3_d5:\n"
"s_nop 7\n"
"v_max3_f32 v25, v120, v121, v122\n"
"v_max3_f32 v26, v123, v64, v65\n"
"v_max3_f32 v25, v25, v66, v67\n"
"v_max_f32 v25, v25, v26\n"
"v_mov_b32 v26, v25\n"
"s_nop 1\n"
"v_permlane32_swap_b32 v25, v26\n"
"s_nop 1\n"
"v_max_f32 v25, v25, v26\n"
"v_mov_b32 v26, v25\n"
"s_nop 1\n"
"v_permlane16_swap_b32 v25, v26\n"
"s_nop 1\n"
"v_max_f32 v25, v25, v26\n"
"v_mul_f32 v25, s62, v25\n"
"v_sub_f32 v25, 0, v25\n"
"v_min_f32 v25, v25, v14\n"
"v_sub_f32 v26, v25, v14\n"
"v_exp_f32 v26, v26\n"
"v_mov_b32 v14, v25\n"
"s_nop 0\n"
"v_mul_f32 v20, v20, v26\n"
"v_accvgpr_read_b32 v22, a116\n"
"v_accvgpr_read_b32 v23, a117\n"
"v_mul_f32 v22, v22, v26\n"
"v_mul_f32 v23, v23, v26\n"
"v_accvgpr_write_b32 a116, v22\n"
"v_accvgpr_write_b32 a117, v23\n"
"v_accvgpr_read_b32 v22, a118\n"
"v_accvgpr_read_b32 v23, a119\n"
"v_mul_f32 v22, v22, v26\n"
"v_mul_f32 v23, v23, v26\n"
"v_accvgpr_write_b32 a118, v22\n"
"v_accvgpr_write_b32 a119, v23\n"
"v_accvgpr_read_b32 v22, a140\n"
"v_accvgpr_read_b32 v23, a141\n"
"v_mul_f32 v22, v22, v26\n"
"v_mul_f32 v23, v23, v26\n"
"v_accvgpr_write_b32 a140, v22\n"
"v_accvgpr_write_b32 a141, v23\n"
"v_accvgpr_read_b32 v22, a142\n"
"v_accvgpr_read_b32 v23, a143\n"
"v_mul_f32 v22, v22, v26\n"
"v_mul_f32 v23, v23, v26\n"
"v_accvgpr_write_b32 a142, v22\n"
"v_accvgpr_write_b32 a143, v23\n"
"v_accvgpr_read_b32 v22, a164\n"
"v_accvgpr_read_b32 v23, a165\n"
"v_mul_f32 v22, v22, v26\n"
"v_mul_f32 v23, v23, v26\n"
"v_accvgpr_write_b32 a164, v22\n"
"v_accvgpr_write_b32 a165, v23\n"
"v_accvgpr_read_b32 v22, a166\n"
"v_accvgpr_read_b32 v23, a167\n"
"v_mul_f32 v22, v22, v26\n"
"v_mul_f32 v23, v23, v26\n"
"v_accvgpr_write_b32 a166, v22\n"
"v_accvgpr_write_b32 a167, v23\n"
"v_accvgpr_read_b32 v22, a188\n"
"v_accvgpr_read_b32 v23, a189\n"
"v_mul_f32 v22, v22, v26\n"
"v_mul_f32 v23, v23, v26\n"
"v_accvgpr_write_b32 a188, v22\n"
"v_accvgpr_write_b32 a189, v23\n"
"v_accvgpr_read_b32 v22, a190\n"
"v_accvgpr_read_b32 v23, a191\n"
"v_mul_f32 v22, v22, v26\n"
"v_mul_f32 v23, v23, v26\n"
"v_accvgpr_write_b32 a190, v22\n"
"v_accvgpr_write_b32 a191, v23\n"
"v_accvgpr_read_b32 v22, a212\n"
"v_accvgpr_read_b32 v23, a213\n"
"v_mul_f32 v22, v22, v26\n"
"v_mul_f32 v23, v23, v26\n"
"v_accvgpr_write_b32 a212, v22\n"
"v_accvgpr_write_b32 a213, v23\n"
"v_accvgpr_read_b32 v22, a214\n"
"v_accvgpr_read_b32 v23, a215\n"
"v_mul_f32 v22, v22, v26\n"
"v_mul_f32 v23, v23, v26\n"
"v_accvgpr_write_b32 a214, v22\n"
"v_accvgpr_write_b32 a215, v23\n"
"v_accvgpr_read_b32 v22, a236\n"
"v_accvgpr_read_b32 v23, a237\n"
"v_mul_f32 v22, v22, v26\n"
"v_mul_f32 v23, v23, v26\n"
"v_accvgpr_write_b32 a236, v22\n"
"v_accvgpr_write_b32 a237, v23\n"
"v_accvgpr_read_b32 v22, a238\n"
"v_accvgpr_read_b32 v23, a239\n"
"v_mul_f32 v22, v22, v26\n"
"v_mul_f32 v23, v23, v26\n"
"v_accvgpr_write_b32 a238, v22\n"
"v_accvgpr_write_b32 a239, v23\n"
"v_mul_f32 v132, v132, v26\n"
"v_mul_f32 v133, v133, v26\n"
"v_mul_f32 v134, v134, v26\n"
"v_mul_f32 v135, v135, v26\n"
"v_mul_f32 v156, v156, v26\n"
"v_mul_f32 v157, v157, v26\n"
"v_mul_f32 v158, v158, v26\n"
"v_mul_f32 v159, v159, v26\n"
"v_mul_f32 v180, v180, v26\n"
"v_mul_f32 v181, v181, v26\n"
"v_mul_f32 v182, v182, v26\n"
"v_mul_f32 v183, v183, v26\n"
"v_mul_f32 v204, v204, v26\n"
"v_mul_f32 v205, v205, v26\n"
"v_mul_f32 v206, v206, v26\n"
"v_mul_f32 v207, v207, v26\n"
"v_mul_f32 v228, v228, v26\n"
"v_mul_f32 v229, v229, v26\n"
"v_mul_f32 v230, v230, v26\n"
"v_mul_f32 v231, v231, v26\n"
"v_mul_f32 v252, v252, v26\n"
"v_mul_f32 v253, v253, v26\n"
"v_mul_f32 v254, v254, v26\n"
"v_mul_f32 v255, v255, v26\n"
"v_fma_f32 v22, v120, s62, v14\n"
"v_fma_f32 v23, v121, s62, v14\n"
"v_exp_f32 v22, v22\n"
"v_exp_f32 v23, v23\n"
"s_nop 0\n"
"v_add_f32 v21, v22, v23\n"
"v_cvt_pk_bf16_f32 v100, v22, v23\n"
"v_fma_f32 v22, v122, s62, v14\n"
"v_fma_f32 v23, v123, s62, v14\n"
"v_exp_f32 v22, v22\n"
"v_exp_f32 v23, v23\n"
"s_nop 0\n"
"v_add_f32 v21, v21, v22\n"
"v_add_f32 v21, v21, v23\n"
"v_cvt_pk_bf16_f32 v101, v22, v23\n"
"v_fma_f32 v22, v64, s62, v14\n"
"v_fma_f32 v23, v65, s62, v14\n"
"v_exp_f32 v22, v22\n"
"v_exp_f32 v23, v23\n"
"s_nop 0\n"
"v_add_f32 v21, v21, v22\n"
"v_add_f32 v21, v21, v23\n"
"v_cvt_pk_bf16_f32 v102, v22, v23\n"
"v_fma_f32 v22, v66, s62, v14\n"
"v_fma_f32 v23, v67, s62, v14\n"
"v_exp_f32 v22, v22\n"
"v_exp_f32 v23, v23\n"
"s_nop 0\n"
"v_add_f32 v21, v21, v22\n"
"v_add_f32 v21, v21, v23\n"
"v_cvt_pk_bf16_f32 v103, v22, v23\n"
"s_nop 1\n"
"s_branch .Lret_3_d5\n"
".Lrare_4_a:\n"
"s_nop 7\n"
"v_max3_f32 v25, v96, v97, v98\n"
"v_max3_f32 v26, v99, v92, v93\n"
"v_max3_f32 v25, v25, v94, v95\n"
"v_max_f32 v25, v25, v26\n"
"v_mov_b32 v26, v25\n"
"s_nop 1\n"
"v_permlane32_swap_b32 v25, v26\n"
"s_nop 1\n"
"v_max_f32 v25, v25, v26\n"
"v_mov_b32 v26, v25\n"
"s_nop 1\n"
"v_permlane16_swap_b32 v25, v26\n"
"s_nop 1\n"
"v_max_f32 v25, v25, v26\n"
"v_mul_f32 v25, s62, v25\n"
"v_sub_f32 v25, 0, v25\n"
"v_min_f32 v25, v25, v9\n"
"v_sub_f32 v26, v25, v9\n"
"v_exp_f32 v26, v26\n"
"v_mov_b32 v9, v25\n"
"s_nop 0\n"
"v_mul_f32 v15, v15, v26\n"
"v_accvgpr_read_b32 v22, a96\n"
"v_accvgpr_read_b32 v23, a97\n"
"v_mul_f32 v22, v22, v26\n"
"v_mul_f32 v23, v23, v26\n"
"v_accvgpr_write_b32 a96, v22\n"
"v_accvgpr_write_b32 a97, v23\n"
"v_accvgpr_read_b32 v22, a98\n"
"v_accvgpr_read_b32 v23, a99\n"
"v_mul_f32 v22, v22, v26\n"
"v_mul_f32 v23, v23, v26\n"
"v_accvgpr_write_b32 a98, v22\n"
"v_accvgpr_write_b32 a99, v23\n"
"v_accvgpr_read_b32 v22, a120\n"
"v_accvgpr_read_b32 v23, a121\n"
"v_mul_f32 v22, v22, v26\n"
"v_mul_f32 v23, v23, v26\n"
"v_accvgpr_write_b32 a120, v22\n"
"v_accvgpr_write_b32 a121, v23\n"
"v_accvgpr_read_b32 v22, a122\n"
"v_accvgpr_read_b32 v23, a123\n"
"v_mul_f32 v22, v22, v26\n"
"v_mul_f32 v23, v23, v26\n"
"v_accvgpr_write_b32 a122, v22\n"
"v_accvgpr_write_b32 a123, v23\n"
"v_accvgpr_read_b32 v22, a144\n"
"v_accvgpr_read_b32 v23, a145\n"
"v_mul_f32 v22, v22, v26\n"
"v_mul_f32 v23, v23, v26\n"
"v_accvgpr_write_b32 a144, v22\n"
"v_accvgpr_write_b32 a145, v23\n"
"v_accvgpr_read_b32 v22, a146\n"
"v_accvgpr_read_b32 v23, a147\n"
"v_mul_f32 v22, v22, v26\n"
"v_mul_f32 v23, v23, v26\n"
"v_accvgpr_write_b32 a146, v22\n"
"v_accvgpr_write_b32 a147, v23\n"
"v_accvgpr_read_b32 v22, a168\n"
"v_accvgpr_read_b32 v23, a169\n"
"v_mul_f32 v22, v22, v26\n"
"v_mul_f32 v23, v23, v26\n"
"v_accvgpr_write_b32 a168, v22\n"
"v_accvgpr_write_b32 a169, v23\n"
"v_accvgpr_read_b32 v22, a170\n"
"v_accvgpr_read_b32 v23, a171\n"
"v_mul_f32 v22, v22, v26\n"
"v_mul_f32 v23, v23, v26\n"
"v_accvgpr_write_b32 a170, v22\n"
"v_accvgpr_write_b32 a171, v23\n"
"v_accvgpr_read_b32 v22, a192\n"
"v_accvgpr_read_b32 v23, a193\n"
"v_mul_f32 v22, v22, v26\n"
"v_mul_f32 v23, v23, v26\n"
"v_accvgpr_write_b32 a192, v22\n"
"v_accvgpr_write_b32 a193, v23\n"
"v_accvgpr_read_b32 v22, a194\n"
"v_accvgpr_read_b32 v23, a195\n"
"v_mul_f32 v22, v22, v26\n"
"v_mul_f32 v23, v23, v26\n"
"v_accvgpr_write_b32 a194, v22\n"
"v_accvgpr_write_b32 a195, v23\n"
"v_accvgpr_read_b32 v22, a216\n"
"v_accvgpr_read_b32 v23, a217\n"
"v_mul_f32 v22, v22, v26\n"
"v_mul_f32 v23, v23, v26\n"
"v_accvgpr_write_b32 a216, v22\n"
"v_accvgpr_write_b32 a217, v23\n"
"v_accvgpr_read_b32 v22, a218\n"
"v_accvgpr_read_b32 v23, a219\n"
"v_mul_f32 v22, v22, v26\n"
"v_mul_f32 v23, v23, v26\n"
"v_accvgpr_write_b32 a218, v22\n"
"v_accvgpr_write_b32 a219, v23\n"
"v_accvgpr_read_b32 v22, a240\n"
"v_accvgpr_read_b32 v23, a241\n"
"v_mul_f32 v22, v22, v26\n"
"v_mul_f32 v23, v23, v26\n"
"v_accvgpr_write_b32 a240, v22\n"
"v_accvgpr_write_b32 a241, v23\n"
"v_accvgpr_read_b32 v22, a242\n"
"v_accvgpr_read_b32 v23, a243\n"
"v_mul_f32 v22, v22, v26\n"
"v_mul_f32 v23, v23, v26\n"
"v_accvgpr_write_b32 a242, v22\n"
"v_accvgpr_write_b32 a243, v23\n"
"v_mul_f32 v136, v136, v26\n"
"v_mul_f32 v137, v137, v26\n"
"v_mul_f32 v138, v138, v26\n"
"v_mul_f32 v139, v139, v26\n"
"v_mul_f32 v160, v160, v26\n"
"v_mul_f32 v161, v161, v26\n"
"v_mul_f32 v162, v162, v26\n"
"v_mul_f32 v163, v163, v26\n"
"v_mul_f32 v184, v184, v26\n"
"v_mul_f32 v185, v185, v26\n"
"v_mul_f32 v186, v186, v26\n"
"v_mul_f32 v187, v187, v26\n"
"v_mul_f32 v208, v208, v26\n"
"v_mul_f32 v209, v209, v26\n"
"v_mul_f32 v210, v210, v26\n"
"v_mul_f32 v211, v211, v26\n"
"v_mul_f32 v232, v232, v26\n"
"v_mul_f32 v233, v233, v26\n"
"v_mul_f32 v234, v234, v26\n"
"v_mul_f32 v235, v235, v26\n"
"v_lshlrev_b32 v22, 16, v108\n"
"v_and_b32 v23, 0xffff0000, v108\n"
"v_mul_f32 v22, v22, v26\n"
"v_mul_f32 v23, v23, v26\n"
"v_cvt_pk_bf16_f32 v108, v22, v23\n"
"v_lshlrev_b32 v22, 16, v109\n"
"v_and_b32 v23, 0xffff0000, v109\n"
"v_mul_f32 v22, v22, v26\n"
"v_mul_f32 v23, v23, v26\n"
"v_cvt_pk_bf16_f32 v109, v22, v23\n"
"v_lshlrev_b32 v22, 16, v110\n"
"v_and_b32 v23, 0xffff0000, v110\n"
"v_mul_f32 v22, v22, v26\n"
"v_mul_f32 v23, v23, v26\n"
"v_cvt_pk_bf16_f32 v110, v22, v23\n"
"v_lshlrev_b32 v22, 16, v111\n"
"v_and_b32 v23, 0xffff0000, v111\n"
"v_mul_f32 v22, v22, v26\n"
"v_mul_f32 v23, v23, v26\n"
"v_cvt_pk_bf16_f32 v111, v22, v23\n"
"v_fma_f32 v22, v96, s62, v9\n"
"v_fma_f32 v23, v97, s62, v9\n"
"v_exp_f32 v22, v22\n"
"v_exp_f32 v23, v23\n"
"s_nop 0\n"
"v_add_f32 v21, v22, v23\n"
"v_cvt_pk_bf16_f32 v112, v22, v23\n"
"v_fma_f32 v22, v98, s62, v9\n"
"v_fma_f32 v23, v99, s62, v9\n"
"v_exp_f32 v22, v22\n"
"v_exp_f32 v23, v23\n"
"s_nop 0\n"
"v_add_f32 v21, v21, v22\n"
"v_add_f32 v21, v21, v23\n"
"v_cvt_pk_bf16_f32 v113, v22, v23\n"
"v_fma_f32 v22, v92, s62, v9\n"
"v_fma_f32 v23, v93, s62, v9\n"
"v_exp_f32 v22, v22\n"
"v_exp_f32 v23, v23\n"
"s_nop 0\n"
"v_add_f32 v21, v21, v22\n"
"v_add_f32 v21, v21, v23\n"
"v_cvt_pk_bf16_f32 v114, v22, v23\n"
"v_fma_f32 v22, v94, s62, v9\n"
"v_fma_f32 v23, v95, s62, v9\n"
"v_exp_f32 v22, v22\n"
"v_exp_f32 v23, v23\n"
"s_nop 0\n"
"v_add_f32 v21, v21, v22\n"
"v_add_f32 v21, v21, v23\n"
"v_cvt_pk_bf16_f32 v115, v22, v23\n"
"v_max3_f32 v25, v88, v89, v90\n"
"v_max3_f32 v26, v91, v80, v81\n"
"v_max3_f32 v25, v25, v82, v83\n"
"v_max_f32 v25, v25, v26\n"
"v_mov_b32 v26, v25\n"
"s_nop 1\n"
"v_permlane32_swap_b32 v25, v26\n"
"s_nop 1\n"
"v_max_f32 v25, v25, v26\n"
"v_mov_b32 v26, v25\n"
"s_nop 1\n"
"v_permlane16_swap_b32 v25, v26\n"
"s_nop 1\n"
"v_max_f32 v25, v25, v26\n"
"v_mul_f32 v25, s62, v25\n"
"v_sub_f32 v25, 0, v25\n"
"v_min_f32 v25, v25, v10\n"
"v_sub_f32 v26, v25, v10\n"
"v_exp_f32 v26, v26\n"
"v_mov_b32 v10, v25\n"
"s_nop 0\n"
"v_mul_f32 v16, v16, v26\n"
"v_accvgpr_read_b32 v22, a100\n"
"v_accvgpr_read_b32 v23, a101\n"
"v_mul_f32 v22, v22, v26\n"
"v_mul_f32 v23, v23, v26\n"
"v_accvgpr_write_b32 a100, v22\n"
"v_accvgpr_write_b32 a101, v23\n"
"v_accvgpr_read_b32 v22, a102\n"
"v_accvgpr_read_b32 v23, a103\n"
"v_mul_f32 v22, v22, v26\n"
"v_mul_f32 v23, v23, v26\n"
"v_accvgpr_write_b32 a102, v22\n"
"v_accvgpr_write_b32 a103, v23\n"
"v_accvgpr_read_b32 v22, a124\n"
"v_accvgpr_read_b32 v23, a125\n"
"v_mul_f32 v22, v22, v26\n"
"v_mul_f32 v23, v23, v26\n"
"v_accvgpr_write_b32 a124, v22\n"
"v_accvgpr_write_b32 a125, v23\n"
"v_accvgpr_read_b32 v22, a126\n"
"v_accvgpr_read_b32 v23, a127\n"
"v_mul_f32 v22, v22, v26\n"
"v_mul_f32 v23, v23, v26\n"
"v_accvgpr_write_b32 a126, v22\n"
"v_accvgpr_write_b32 a127, v23\n"
"v_accvgpr_read_b32 v22, a148\n"
"v_accvgpr_read_b32 v23, a149\n"
"v_mul_f32 v22, v22, v26\n"
"v_mul_f32 v23, v23, v26\n"
"v_accvgpr_write_b32 a148, v22\n"
"v_accvgpr_write_b32 a149, v23\n"
"v_accvgpr_read_b32 v22, a150\n"
"v_accvgpr_read_b32 v23, a151\n"
"v_mul_f32 v22, v22, v26\n"
"v_mul_f32 v23, v23, v26\n"
"v_accvgpr_write_b32 a150, v22\n"
"v_accvgpr_write_b32 a151, v23\n"
"v_accvgpr_read_b32 v22, a172\n"
"v_accvgpr_read_b32 v23, a173\n"
"v_mul_f32 v22, v22, v26\n"
"v_mul_f32 v23, v23, v26\n"
"v_accvgpr_write_b32 a172, v22\n"
"v_accvgpr_write_b32 a173, v23\n"
"v_accvgpr_read_b32 v22, a174\n"
"v_accvgpr_read_b32 v23, a175\n"
"v_mul_f32 v22, v22, v26\n"
"v_mul_f32 v23, v23, v26\n"
"v_accvgpr_write_b32 a174, v22\n"
"v_accvgpr_write_b32 a175, v23\n"
"v_accvgpr_read_b32 v22, a196\n"
"v_accvgpr_read_b32 v23, a197\n"
"v_mul_f32 v22, v22, v26\n"
"v_mul_f32 v23, v23, v26\n"
"v_accvgpr_write_b32 a196, v22\n"
"v_accvgpr_write_b32 a197, v23\n"
"v_accvgpr_read_b32 v22, a198\n"
"v_accvgpr_read_b32 v23, a199\n"
"v_mul_f32 v22, v22, v26\n"
"v_mul_f32 v23, v23, v26\n"
"v_accvgpr_write_b32 a198, v22\n"
"v_accvgpr_write_b32 a199, v23\n"
"v_accvgpr_read_b32 v22, a220\n"
"v_accvgpr_read_b32 v23, a221\n"
"v_mul_f32 v22, v22, v26\n"
"v_mul_f32 v23, v23, v26\n"
"v_accvgpr_write_b32 a220, v22\n"
"v_accvgpr_write_b32 a221, v23\n"
"v_accvgpr_read_b32 v22, a222\n"
"v_accvgpr_read_b32 v23, a223\n"
"v_mul_f32 v22, v22, v26\n"
"v_mul_f32 v23, v23, v26\n"
"v_accvgpr_write_b32 a222, v22\n"
"v_accvgpr_write_b32 a223, v23\n"
"v_accvgpr_read_b32 v22, a244\n"
"v_accvgpr_read_b32 v23, a245\n"
"v_mul_f32 v22, v22, v26\n"
"v_mul_f32 v23, v23, v26\n"
"v_accvgpr_write_b32 a244, v22\n"
"v_accvgpr_write_b32 a245, v23\n"
"v_accvgpr_read_b32 v22, a246\n"
"v_accvgpr_read_b32 v23, a247\n"
"v_mul_f32 v22, v22, v26\n"
"v_mul_f32 v23, v23, v26\n"
"v_accvgpr_write_b32 a246, v22\n"
"v_accvgpr_write_b32 a247, v23\n"
"v_mul_f32 v140, v140, v26\n"
"v_mul_f32 v141, v141, v26\n"
"v_mul_f32 v142, v142, v26\n"
"v_mul_f32 v143, v143, v26\n"
"v_mul_f32 v164, v164, v26\n"
"v_mul_f32 v165, v165, v26\n"
"v_mul_f32 v166, v166, v26\n"
"v_mul_f32 v167, v167, v26\n"
"v_mul_f32 v188, v188, v26\n"
"v_mul_f32 v189, v189, v26\n"
"v_mul_f32 v190, v190, v26\n"
"v_mul_f32 v191, v191, v26\n"
"v_mul_f32 v212, v212, v26\n"
"v_mul_f32 v213, v213, v26\n"
"v_mul_f32 v214, v214, v26\n"
"v_mul_f32 v215, v215, v26\n"
"v_mul_f32 v236, v236, v26\n"
"v_mul_f32 v237, v237, v26\n"
"v_mul_f32 v238, v238, v26\n"
"v_mul_f32 v239, v239, v26\n"
"v_lshlrev_b32 v22, 16, v124\n"
"v_and_b32 v23, 0xffff0000, v124\n"
"v_mul_f32 v22, v22, v26\n"
"v_mul_f32 v23, v23, v26\n"
"v_cvt_pk_bf16_f32 v124, v22, v23\n"
"v_lshlrev_b32 v22, 16, v125\n"
"v_and_b32 v23, 0xffff0000, v125\n"
"v_mul_f32 v22, v22, v26\n"
"v_mul_f32 v23, v23, v26\n"
"v_cvt_pk_bf16_f32 v125, v22, v23\n"
"v_lshlrev_b32 v22, 16, v126\n"
"v_and_b32 v23, 0xffff0000, v126\n"
"v_mul_f32 v22, v22, v26\n"
"v_mul_f32 v23, v23, v26\n"
"v_cvt_pk_bf16_f32 v126, v22, v23\n"
"v_lshlrev_b32 v22, 16, v127\n"
"v_and_b32 v23, 0xffff0000, v127\n"
"v_mul_f32 v22, v22, v26\n"
"v_mul_f32 v23, v23, v26\n"
"v_cvt_pk_bf16_f32 v127, v22, v23\n"
"v_fma_f32 v22, v88, s62, v10\n"
"v_fma_f32 v23, v89, s62, v10\n"
"v_exp_f32 v22, v22\n"
"v_exp_f32 v23, v23\n"
"s_nop 0\n"
"v_add_f32 v3, v22, v23\n"
"v_cvt_pk_bf16_f32 v120, v22, v23\n"
"v_fma_f32 v22, v90, s62, v10\n"
"v_fma_f32 v23, v91, s62, v10\n"
"v_exp_f32 v22, v22\n"
"v_exp_f32 v23, v23\n"
"s_nop 0\n"
"v_add_f32 v3, v3, v22\n"
"v_add_f32 v3, v3, v23\n"
"v_cvt_pk_bf16_f32 v121, v22, v23\n"
"v_fma_f32 v22, v80, s62, v10\n"
"v_fma_f32 v23, v81, s62, v10\n"
"v_exp_f32 v22, v22\n"
"v_exp_f32 v23, v23\n"
"s_nop 0\n"
"v_add_f32 v3, v3, v22\n"
"v_add_f32 v3, v3, v23\n"
"v_cvt_pk_bf16_f32 v122, v22, v23\n"
"v_fma_f32 v22, v82, s62, v10\n"
"v_fma_f32 v23, v83, s62, v10\n"
"v_exp_f32 v22, v22\n"
"v_exp_f32 v23, v23\n"
"s_nop 0\n"
"v_add_f32 v3, v3, v22\n"
"v_add_f32 v3, v3, v23\n"
"v_cvt_pk_bf16_f32 v123, v22, v23\n"
"v_fma_f32 v22, v104, s62, v11\n"
"v_fma_f32 v23, v105, s62, v11\n"
"s_nop 1\n"
"s_branch .Lret_4_a\n"
".Lrare_4_b:\n"
"s_nop 7\n"
"v_max3_f32 v25, v104, v105, v106\n"
"v_max3_f32 v26, v107, v48, v49\n"
"v_max3_f32 v25, v25, v50, v51\n"
"v_max_f32 v25, v25, v26\n"
"v_mov_b32 v26, v25\n"
"s_nop 1\n"
"v_permlane32_swap_b32 v25, v26\n"
"s_nop 1\n"
"v_max_f32 v25, v25, v26\n"
"v_mov_b32 v26, v25\n"
"s_nop 1\n"
"v_permlane16_swap_b32 v25, v26\n"
"s_nop 1\n"
"v_max_f32 v25, v25, v26\n"
"v_mul_f32 v25, s62, v25\n"
"v_sub_f32 v25, 0, v25\n"
"v_min_f32 v25, v25, v11\n"
"v_sub_f32 v26, v25, v11\n"
"v_exp_f32 v26, v26\n"
"v_mov_b32 v11, v25\n"
"s_nop 0\n"
"v_mul_f32 v17, v17, v26\n"
"v_accvgpr_read_b32 v22, a104\n"
"v_accvgpr_read_b32 v23, a105\n"
"v_mul_f32 v22, v22, v26\n"
"v_mul_f32 v23, v23, v26\n"
"v_accvgpr_write_b32 a104, v22\n"
"v_accvgpr_write_b32 a105, v23\n"
"v_accvgpr_read_b32 v22, a106\n"
"v_accvgpr_read_b32 v23, a107\n"
"v_mul_f32 v22, v22, v26\n"
"v_mul_f32 v23, v23, v26\n"
"v_accvgpr_write_b32 a106, v22\n"
"v_accvgpr_write_b32 a107, v23\n"
"v_accvgpr_read_b32 v22, a128\n"
"v_accvgpr_read_b32 v23, a129\n"
"v_mul_f32 v22, v22, v26\n"
"v_mul_f32 v23, v23, v26\n"
"v_accvgpr_write_b32 a128, v22\n"
"v_accvgpr_write_b32 a129, v23\n"
"v_accvgpr_read_b32 v22, a130\n"
"v_accvgpr_read_b32 v23, a131\n"
"v_mul_f32 v22, v22, v26\n"
"v_mul_f32 v23, v23, v26\n"
"v_accvgpr_write_b32 a130, v22\n"
"v_accvgpr_write_b32 a131, v23\n"
"v_accvgpr_read_b32 v22, a152\n"
"v_accvgpr_read_b32 v23, a153\n"
"v_mul_f32 v22, v22, v26\n"
"v_mul_f32 v23, v23, v26\n"
"v_accvgpr_write_b32 a152, v22\n"
"v_accvgpr_write_b32 a153, v23\n"
"v_accvgpr_read_b32 v22, a154\n"
"v_accvgpr_read_b32 v23, a155\n"
"v_mul_f32 v22, v22, v26\n"
"v_mul_f32 v23, v23, v26\n"
"v_accvgpr_write_b32 a154, v22\n"
"v_accvgpr_write_b32 a155, v23\n"
"v_accvgpr_read_b32 v22, a176\n"
"v_accvgpr_read_b32 v23, a177\n"
"v_mul_f32 v22, v22, v26\n"
"v_mul_f32 v23, v23, v26\n"
"v_accvgpr_write_b32 a176, v22\n"
"v_accvgpr_write_b32 a177, v23\n"
"v_accvgpr_read_b32 v22, a178\n"
"v_accvgpr_read_b32 v23, a179\n"
"v_mul_f32 v22, v22, v26\n"
"v_mul_f32 v23, v23, v26\n"
"v_accvgpr_write_b32 a178, v22\n"
"v_accvgpr_write_b32 a179, v23\n"
"v_accvgpr_read_b32 v22, a200\n"
"v_accvgpr_read_b32 v23, a201\n"
"v_mul_f32 v22, v22, v26\n"
"v_mul_f32 v23, v23, v26\n"
"v_accvgpr_write_b32 a200, v22\n"
"v_accvgpr_write_b32 a201, v23\n"
"v_accvgpr_read_b32 v22, a202\n"
"v_accvgpr_read_b32 v23, a203\n"
"v_mul_f32 v22, v22, v26\n"
"v_mul_f32 v23, v23, v26\n"
"v_accvgpr_write_b32 a202, v22\n"
"v_accvgpr_write_b32 a203, v23\n"
"v_accvgpr_read_b32 v22, a224\n"
"v_accvgpr_read_b32 v23, a225\n"
"v_mul_f32 v22, v22, v26\n"
"v_mul_f32 v23, v23, v26\n"
"v_accvgpr_write_b32 a224, v22\n"
"v_accvgpr_write_b32 a225, v23\n"
"v_accvgpr_read_b32 v22, a226\n"
"v_accvgpr_read_b32 v23, a227\n"
"v_mul_f32 v22, v22, v26\n"
"v_mul_f32 v23, v23, v26\n"
"v_accvgpr_write_b32 a226, v22\n"
"v_accvgpr_write_b32 a227, v23\n"
"v_accvgpr_read_b32 v22, a248\n"
"v_accvgpr_read_b32 v23, a249\n"
"v_mul_f32 v22, v22, v26\n"
"v_mul_f32 v23, v23, v26\n"
"v_accvgpr_write_b32 a248, v22\n"
"v_accvgpr_write_b32 a249, v23\n"
"v_accvgpr_read_b32 v22, a250\n"
"v_accvgpr_read_b32 v23, a251\n"
"v_mul_f32 v22, v22, v26\n"
"v_mul_f32 v23, v23, v26\n"
"v_accvgpr_write_b32 a250, v22\n"
"v_accvgpr_write_b32 a251, v23\n"
"v_mul_f32 v144, v144, v26\n"
"v_mul_f32 v145, v145, v26\n"
"v_mul_f32 v146, v146, v26\n"
"v_mul_f32 v147, v147, v26\n"
"v_mul_f32 v168, v168, v26\n"
"v_mul_f32 v169, v169, v26\n"
"v_mul_f32 v170, v170, v26\n"
"v_mul_f32 v171, v171, v26\n"
"v_mul_f32 v192, v192, v26\n"
"v_mul_f32 v193, v193, v26\n"
"v_mul_f32 v194, v194, v26\n"
"v_mul_f32 v195, v195, v26\n"
"v_mul_f32 v216, v216, v26\n"
"v_mul_f32 v217, v217, v26\n"
"v_mul_f32 v218, v218, v26\n"
"v_mul_f32 v219, v219, v26\n"
"v_mul_f32 v240, v240, v26\n"
"v_mul_f32 v241, v241, v26\n"
"v_mul_f32 v242, v242, v26\n"
"v_mul_f32 v243, v243, v26\n"
"v_lshlrev_b32 v22, 16, v84\n"
"v_and_b32 v23, 0xffff0000, v84\n"
"v_mul_f32 v22, v22, v26\n"
"v_mul_f32 v23, v23, v26\n"
"v_cvt_pk_bf16_f32 v84, v22, v23\n"
"v_lshlrev_b32 v22, 16, v85\n"
"v_and_b32 v23, 0xffff0000, v85\n"
"v_mul_f32 v22, v22, v26\n"
"v_mul_f32 v23, v23, v26\n"
"v_cvt_pk_bf16_f32 v85, v22, v23\n"
"v_lshlrev_b32 v22, 16, v86\n"
"v_and_b32 v23, 0xffff0000, v86\n"
"v_mul_f32 v22, v22, v26\n"
"v_mul_f32 v23, v23, v26\n"
"v_cvt_pk_bf16_f32 v86, v22, v23\n"
"v_lshlrev_b32 v22, 16, v87\n"
"v_and_b32 v23, 0xffff0000, v87\n"
"v_mul_f32 v22, v22, v26\n"
"v_mul_f32 v23, v23, v26\n"
"v_cvt_pk_bf16_f32 v87, v22, v23\n"
"v_fma_f32 v22, v104, s62, v11\n"
"v_fma_f32 v23, v105, s62, v11\n"
"v_exp_f32 v22, v22\n"
"v_exp_f32 v23, v23\n"
"s_nop 0\n"
"v_add_f32 v92, v22, v23\n"
"v_cvt_pk_bf16_f32 v64, v22, v23\n"
"v_fma_f32 v22, v106, s62, v11\n"
"v_fma_f32 v23, v107, s62, v11\n"
"v_exp_f32 v22, v22\n"
"v_exp_f32 v23, v23\n"
"s_nop 0\n"
"v_add_f32 v92, v92, v22\n"
"v_add_f32 v92, v92, v23\n"
"v_cvt_pk_bf16_f32 v65, v22, v23\n"
"v_fma_f32 v22, v48, s62, v11\n"
"v_fma_f32 v23, v49, s62, v11\n"
"v_exp_f32 v22, v22\n"
"v_exp_f32 v23, v23\n"
"s_nop 0\n"
"v_add_f32 v92, v92, v22\n"
"v_add_f32 v92, v92, v23\n"
"v_cvt_pk_bf16_f32 v66, v22, v23\n"
"v_fma_f32 v22, v50, s62, v11\n"
"v_fma_f32 v23, v51, s62, v11\n"
"v_exp_f32 v22, v22\n"
"v_exp_f32 v23, v23\n"
"s_nop 0\n"
"v_add_f32 v92, v92, v22\n"
"v_add_f32 v92, v92, v23\n"
"v_cvt_pk_bf16_f32 v67, v22, v23\n"
"v_max3_f32 v25, v56, v57, v58\n"
"v_max3_f32 v26, v59, v60, v61\n"
"v_max3_f32 v25, v25, v62, v63\n"
"v_max_f32 v25, v25, v26\n"
"v_mov_b32 v26, v25\n"
"s_nop 1\n"
"v_permlane32_swap_b32 v25, v26\n"
"s_nop 1\n"
"v_max_f32 v25, v25, v26\n"
"v_mov_b32 v26, v25\n"
"s_nop 1\n"
"v_permlane16_swap_b32 v25, v26\n"
"s_nop 1\n"
"v_max_f32 v25, v25, v26\n"
"v_mul_f32 v25, s62, v25\n"
"v_sub_f32 v25, 0, v25\n"
"v_min_f32 v25, v25, v12\n"
"v_sub_f32 v26, v25, v12\n"
"v_exp_f32 v26, v26\n"
"v_mov_b32 v12, v25\n"
"s_nop 0\n"
"v_mul_f32 v18, v18, v26\n"
"v_accvgpr_read_b32 v22, a108\n"
"v_accvgpr_read_b32 v23, a109\n"
"v_mul_f32 v22, v22, v26\n"
"v_mul_f32 v23, v23, v26\n"
"v_accvgpr_write_b32 a108, v22\n"
"v_accvgpr_write_b32 a109, v23\n"
"v_accvgpr_read_b32 v22, a110\n"
"v_accvgpr_read_b32 v23, a111\n"
"v_mul_f32 v22, v22, v26\n"
"v_mul_f32 v23, v23, v26\n"
"v_accvgpr_write_b32 a110, v22\n"
"v_accvgpr_write_b32 a111, v23\n"
"v_accvgpr_read_b32 v22, a132\n"
"v_accvgpr_read_b32 v23, a133\n"
"v_mul_f32 v22, v22, v26\n"
"v_mul_f32 v23, v23, v26\n"
"v_accvgpr_write_b32 a132, v22\n"
"v_accvgpr_write_b32 a133, v23\n"
"v_accvgpr_read_b32 v22, a134\n"
"v_accvgpr_read_b32 v23, a135\n"
"v_mul_f32 v22, v22, v26\n"
"v_mul_f32 v23, v23, v26\n"
"v_accvgpr_write_b32 a134, v22\n"
"v_accvgpr_write_b32 a135, v23\n"
"v_accvgpr_read_b32 v22, a156\n"
"v_accvgpr_read_b32 v23, a157\n"
"v_mul_f32 v22, v22, v26\n"
"v_mul_f32 v23, v23, v26\n"
"v_accvgpr_write_b32 a156, v22\n"
"v_accvgpr_write_b32 a157, v23\n"
"v_accvgpr_read_b32 v22, a158\n"
"v_accvgpr_read_b32 v23, a159\n"
"v_mul_f32 v22, v22, v26\n"
"v_mul_f32 v23, v23, v26\n"
"v_accvgpr_write_b32 a158, v22\n"
"v_accvgpr_write_b32 a159, v23\n"
"v_accvgpr_read_b32 v22, a180\n"
"v_accvgpr_read_b32 v23, a181\n"
"v_mul_f32 v22, v22, v26\n"
"v_mul_f32 v23, v23, v26\n"
"v_accvgpr_write_b32 a180, v22\n"
"v_accvgpr_write_b32 a181, v23\n"
"v_accvgpr_read_b32 v22, a182\n"
"v_accvgpr_read_b32 v23, a183\n"
"v_mul_f32 v22, v22, v26\n"
"v_mul_f32 v23, v23, v26\n"
"v_accvgpr_write_b32 a182, v22\n"
"v_accvgpr_write_b32 a183, v23\n"
"v_accvgpr_read_b32 v22, a204\n"
"v_accvgpr_read_b32 v23, a205\n"
"v_mul_f32 v22, v22, v26\n"
"v_mul_f32 v23, v23, v26\n"
"v_accvgpr_write_b32 a204, v22\n"
"v_accvgpr_write_b32 a205, v23\n"
"v_accvgpr_read_b32 v22, a206\n"
"v_accvgpr_read_b32 v23, a207\n"
"v_mul_f32 v22, v22, v26\n"
"v_mul_f32 v23, v23, v26\n"
"v_accvgpr_write_b32 a206, v22\n"
"v_accvgpr_write_b32 a207, v23\n"
"v_accvgpr_read_b32 v22, a228\n"
"v_accvgpr_read_b32 v23, a229\n"
"v_mul_f32 v22, v22, v26\n"
"v_mul_f32 v23, v23, v26\n"
"v_accvgpr_write_b32 a228, v22\n"
"v_accvgpr_write_b32 a229, v23\n"
"v_accvgpr_read_b32 v22, a230\n"
"v_accvgpr_read_b32 v23, a231\n"
"v_mul_f32 v22, v22, v26\n"
"v_mul_f32 v23, v23, v26\n"
"v_accvgpr_write_b32 a230, v22\n"
"v_accvgpr_write_b32 a231, v23\n"
"v_accvgpr_read_b32 v22, a252\n"
"v_accvgpr_read_b32 v23, a253\n"
"v_mul_f32 v22, v22, v26\n"
"v_mul_f32 v23, v23, v26\n"
"v_accvgpr_write_b32 a252, v22\n"
"v_accvgpr_write_b32 a253, v23\n"
"v_accvgpr_read_b32 v22, a254\n"
"v_accvgpr_read_b32 v23, a255\n"
"v_mul_f32 v22, v22, v26\n"
"v_mul_f32 v23, v23, v26\n"
"v_accvgpr_write_b32 a254, v22\n"
"v_accvgpr_write_b32 a255, v23\n"
"v_mul_f32 v148, v148, v26\n"
"v_mul_f32 v149, v149, v26\n"
"v_mul_f32 v150, v150, v26\n"
"v_mul_f32 v151, v151, v26\n"
"v_mul_f32 v172, v172, v26\n"
"v_mul_f32 v173, v173, v26\n"
"v_mul_f32 v174, v174, v26\n"
"v_mul_f32 v175, v175, v26\n"
"v_mul_f32 v196, v196, v26\n"
"v_mul_f32 v197, v197, v26\n"
"v_mul_f32 v198, v198, v26\n"
"v_mul_f32 v199, v199, v26\n"
"v_mul_f32 v220, v220, v26\n"
"v_mul_f32 v221, v221, v26\n"
"v_mul_f32 v222, v222, v26\n"
"v_mul_f32 v223, v223, v26\n"
"v_mul_f32 v244, v244, v26\n"
"v_mul_f32 v245, v245, v26\n"
"v_mul_f32 v246, v246, v26\n"
"v_mul_f32 v247, v247, v26\n"
"v_lshlrev_b32 v22, 16, v52\n"
"v_and_b32 v23, 0xffff0000, v52\n"
"v_mul_f32 v22, v22, v26\n"
"v_mul_f32 v23, v23, v26\n"
"v_cvt_pk_bf16_f32 v52, v22, v23\n"
"v_lshlrev_b32 v22, 16, v53\n"
"v_and_b32 v23, 0xffff0000, v53\n"
"v_mul_f32 v22, v22, v26\n"
"v_mul_f32 v23, v23, v26\n"
"v_cvt_pk_bf16_f32 v53, v22, v23\n"
"v_lshlrev_b32 v22, 16, v54\n"
"v_and_b32 v23, 0xffff0000, v54\n"
"v_mul_f32 v22, v22, v26\n"
"v_mul_f32 v23, v23, v26\n"
"v_cvt_pk_bf16_f32 v54, v22, v23\n"
"v_lshlrev_b32 v22, 16, v55\n"
"v_and_b32 v23, 0xffff0000, v55\n"
"v_mul_f32 v22, v22, v26\n"
"v_mul_f32 v23, v23, v26\n"
"v_cvt_pk_bf16_f32 v55, v22, v23\n"
"v_fma_f32 v22, v56, s62, v12\n"
"v_fma_f32 v23, v57, s62, v12\n"
"v_exp_f32 v22, v22\n"
"v_exp_f32 v23, v23\n"
"s_nop 0\n"
"v_add_f32 v93, v22, v23\n"
"v_cvt_pk_bf16_f32 v96, v22, v23\n"
"v_fma_f32 v22, v58, s62, v12\n"
"v_fma_f32 v23, v59, s62, v12\n"
"v_exp_f32 v22, v22\n"
"v_exp_f32 v23, v23\n"
"s_nop 0\n"
"v_add_f32 v93, v93, v22\n"
"v_add_f32 v93, v93, v23\n"
"v_cvt_pk_bf16_f32 v97, v22, v23\n"
"v_fma_f32 v22, v60, s62, v12\n"
"v_fma_f32 v23, v61, s62, v12\n"
"v_exp_f32 v22, v22\n"
"v_exp_f32 v23, v23\n"
"s_nop 0\n"
"v_add_f32 v93, v93, v22\n"
"v_add_f32 v93, v93, v23\n"
"v_cvt_pk_bf16_f32 v98, v22, v23\n"
"v_fma_f32 v22, v62, s62, v12\n"
"v_fma_f32 v23, v63, s62, v12\n"
"v_exp_f32 v22, v22\n"
"v_exp_f32 v23, v23\n"
"s_nop 0\n"
"v_add_f32 v93, v93, v22\n"
"v_add_f32 v93, v93, v23\n"
"v_cvt_pk_bf16_f32 v99, v22, v23\n"
"v_fma_f32 v22, v68, s62, v13\n"
"v_fma_f32 v23, v69, s62, v13\n"
"s_nop 1\n"
"s_branch .Lret_4_b\n"
".Lrare_4_d4:\n"
"s_nop 7\n"
"v_max3_f32 v25, v68, v69, v70\n"
"v_max3_f32 v26, v71, v116, v117\n"
"v_max3_f32 v25, v25, v118, v119\n"
"v_max_f32 v25, v25, v26\n"
"v_mov_b32 v26, v25\n"
"s_nop 1\n"
"v_permlane32_swap_b32 v25, v26\n"
"s_nop 1\n"
"v_max_f32 v25, v25, v26\n"
"v_mov_b32 v26, v25\n"
"s_nop 1\n"
"v_permlane16_swap_b32 v25, v26\n"
"s_nop 1\n"
"v_max_f32 v25, v25, v26\n"
"v_mul_f32 v25, s62, v25\n"
"v_sub_f32 v25, 0, v25\n"
"v_min_f32 v25, v25, v13\n"
"v_sub_f32 v26, v25, v13\n"
"v_exp_f32 v26, v26\n"
"v_mov_b32 v13, v25\n"
"s_nop 0\n"
"v_mul_f32 v19, v19, v26\n"
"v_accvgpr_read_b32 v22, a112\n"
"v_accvgpr_read_b32 v23, a113\n"
"v_mul_f32 v22, v22, v26\n"
"v_mul_f32 v23, v23, v26\n"
"v_accvgpr_write_b32 a112, v22\n"
"v_accvgpr_write_b32 a113, v23\n"
"v_accvgpr_read_b32 v22, a114\n"
"v_accvgpr_read_b32 v23, a115\n"
"v_mul_f32 v22, v22, v26\n"
"v_mul_f32 v23, v23, v26\n"
"v_accvgpr_write_b32 a114, v22\n"
"v_accvgpr_write_b32 a115, v23\n"
"v_accvgpr_read_b32 v22, a136\n"
"v_accvgpr_read_b32 v23, a137\n"
"v_mul_f32 v22, v22, v26\n"
"v_mul_f32 v23, v23, v26\n"
"v_accvgpr_write_b32 a136, v22\n"
"v_accvgpr_write_b32 a137, v23\n"
"v_accvgpr_read_b32 v22, a138\n"
"v_accvgpr_read_b32 v23, a139\n"
"v_mul_f32 v22, v22, v26\n"
"v_mul_f32 v23, v23, v26\n"
"v_accvgpr_write_b32 a138, v22\n"
"v_accvgpr_write_b32 a139, v23\n"
"v_accvgpr_read_b32 v22, a160\n"
"v_accvgpr_read_b32 v23, a161\n"
"v_mul_f32 v22, v22, v26\n"
"v_mul_f32 v23, v23, v26\n"
"v_accvgpr_write_b32 a160, v22\n"
"v_accvgpr_write_b32 a161, v23\n"
"v_accvgpr_read_b32 v22, a162\n"
"v_accvgpr_read_b32 v23, a163\n"
"v_mul_f32 v22, v22, v26\n"
"v_mul_f32 v23, v23, v26\n"
"v_accvgpr_write_b32 a162, v22\n"
"v_accvgpr_write_b32 a163, v23\n"
"v_accvgpr_read_b32 v22, a184\n"
"v_accvgpr_read_b32 v23, a185\n"
"v_mul_f32 v22, v22, v26\n"
"v_mul_f32 v23, v23, v26\n"
"v_accvgpr_write_b32 a184, v22\n"
"v_accvgpr_write_b32 a185, v23\n"
"v_accvgpr_read_b32 v22, a186\n"
"v_accvgpr_read_b32 v23, a187\n"
"v_mul_f32 v22, v22, v26\n"
"v_mul_f32 v23, v23, v26\n"
"v_accvgpr_write_b32 a186, v22\n"
"v_accvgpr_write_b32 a187, v23\n"
"v_accvgpr_read_b32 v22, a208\n"
"v_accvgpr_read_b32 v23, a209\n"
"v_mul_f32 v22, v22, v26\n"
"v_mul_f32 v23, v23, v26\n"
"v_accvgpr_write_b32 a208, v22\n"
"v_accvgpr_write_b32 a209, v23\n"
"v_accvgpr_read_b32 v22, a210\n"
"v_accvgpr_read_b32 v23, a211\n"
"v_mul_f32 v22, v22, v26\n"
"v_mul_f32 v23, v23, v26\n"
"v_accvgpr_write_b32 a210, v22\n"
"v_accvgpr_write_b32 a211, v23\n"
"v_accvgpr_read_b32 v22, a232\n"
"v_accvgpr_read_b32 v23, a233\n"
"v_mul_f32 v22, v22, v26\n"
"v_mul_f32 v23, v23, v26\n"
"v_accvgpr_write_b32 a232, v22\n"
"v_accvgpr_write_b32 a233, v23\n"
"v_accvgpr_read_b32 v22, a234\n"
"v_accvgpr_read_b32 v23, a235\n"
"v_mul_f32 v22, v22, v26\n"
"v_mul_f32 v23, v23, v26\n"
"v_accvgpr_write_b32 a234, v22\n"
"v_accvgpr_write_b32 a235, v23\n"
"v_mul_f32 v128, v128, v26\n"
"v_mul_f32 v129, v129, v26\n"
"v_mul_f32 v130, v130, v26\n"
"v_mul_f32 v131, v131, v26\n"
"v_mul_f32 v152, v152, v26\n"
"v_mul_f32 v153, v153, v26\n"
"v_mul_f32 v154, v154, v26\n"
"v_mul_f32 v155, v155, v26\n"
"v_mul_f32 v176, v176, v26\n"
"v_mul_f32 v177, v177, v26\n"
"v_mul_f32 v178, v178, v26\n"
"v_mul_f32 v179, v179, v26\n"
"v_mul_f32 v200, v200, v26\n"
"v_mul_f32 v201, v201, v26\n"
"v_mul_f32 v202, v202, v26\n"
"v_mul_f32 v203, v203, v26\n"
"v_mul_f32 v224, v224, v26\n"
"v_mul_f32 v225, v225, v26\n"
"v_mul_f32 v226, v226, v26\n"
"v_mul_f32 v227, v227, v26\n"
"v_mul_f32 v248, v248, v26\n"
"v_mul_f32 v249, v249, v26\n"
"v_mul_f32 v250, v250, v26\n"
"v_mul_f32 v251, v251, v26\n"
"v_fma_f32 v22, v68, s62, v13\n"
"v_fma_f32 v23, v69, s62, v13\n"
"v_exp_f32 v22, v22\n"
"v_exp_f32 v23, v23\n"
"s_nop 0\n"
"v_add_f32 v21, v22, v23\n"
"v_cvt_pk_bf16_f32 v100, v22, v23\n"
"v_fma_f32 v22, v70, s62, v13\n"
"v_fma_f32 v23, v71, s62, v13\n"
"v_exp_f32 v22, v22\n"
"v_exp_f32 v23, v23\n"
"s_nop 0\n"
"v_add_f32 v21, v21, v22\n"
"v_add_f32 v21, v21, v23\n"
"v_cvt_pk_bf16_f32 v101, v22, v23\n"
"v_fma_f32 v22, v116, s62, v13\n"
"v_fma_f32 v23, v117, s62, v13\n"
"v_exp_f32 v22, v22\n"
"v_exp_f32 v23, v23\n"
"s_nop 0\n"
"v_add_f32 v21, v21, v22\n"
"v_add_f32 v21, v21, v23\n"
"v_cvt_pk_bf16_f32 v102, v22, v23\n"
"v_fma_f32 v22, v118, s62, v13\n"
"v_fma_f32 v23, v119, s62, v13\n"
"v_exp_f32 v22, v22\n"
"v_exp_f32 v23, v23\n"
"s_nop 0\n"
"v_add_f32 v21, v21, v22\n"
"v_add_f32 v21, v21, v23\n"
"v_cvt_pk_bf16_f32 v103, v22, v23\n"
"v_fma_f32 v22, v72, s62, v14\n"
"v_fma_f32 v23, v73, s62, v14\n"
"s_nop 1\n"
"s_branch .Lret_4_d4\n"
".Lrare_4_d5:\n"
"s_nop 7\n"
"v_max3_f32 v25, v72, v73, v74\n"
"v_max3_f32 v26, v75, v28, v29\n"
"v_max3_f32 v25, v25, v30, v31\n"
"v_max_f32 v25, v25, v26\n"
"v_mov_b32 v26, v25\n"
"s_nop 1\n"
"v_permlane32_swap_b32 v25, v26\n"
"s_nop 1\n"
"v_max_f32 v25, v25, v26\n"
"v_mov_b32 v26, v25\n"
"s_nop 1\n"
"v_permlane16_swap_b32 v25, v26\n"
"s_nop 1\n"
"v_max_f32 v25, v25, v26\n"
"v_mul_f32 v25, s62, v25\n"
"v_sub_f32 v25, 0, v25\n"
"v_min_f32 v25, v25, v14\n"
"v_sub_f32 v26, v25, v14\n"
"v_exp_f32 v26, v26\n"
"v_mov_b32 v14, v25\n"
"s_nop 0\n"
"v_mul_f32 v20, v20, v26\n"
"v_accvgpr_read_b32 v22, a116\n"
"v_accvgpr_read_b32 v23, a117\n"
"v_mul_f32 v22, v22, v26\n"
"v_mul_f32 v23, v23, v26\n"
"v_accvgpr_write_b32 a116, v22\n"
"v_accvgpr_write_b32 a117, v23\n"
"v_accvgpr_read_b32 v22, a118\n"
"v_accvgpr_read_b32 v23, a119\n"
"v_mul_f32 v22, v22, v26\n"
"v_mul_f32 v23, v23, v26\n"
"v_accvgpr_write_b32 a118, v22\n"
"v_accvgpr_write_b32 a119, v23\n"
"v_accvgpr_read_b32 v22, a140\n"
"v_accvgpr_read_b32 v23, a141\n"
"v_mul_f32 v22, v22, v26\n"
"v_mul_f32 v23, v23, v26\n"
"v_accvgpr_write_b32 a140, v22\n"
"v_accvgpr_write_b32 a141, v23\n"
"v_accvgpr_read_b32 v22, a142\n"
"v_accvgpr_read_b32 v23, a143\n"
"v_mul_f32 v22, v22, v26\n"
"v_mul_f32 v23, v23, v26\n"
"v_accvgpr_write_b32 a142, v22\n"
"v_accvgpr_write_b32 a143, v23\n"
"v_accvgpr_read_b32 v22, a164\n"
"v_accvgpr_read_b32 v23, a165\n"
"v_mul_f32 v22, v22, v26\n"
"v_mul_f32 v23, v23, v26\n"
"v_accvgpr_write_b32 a164, v22\n"
"v_accvgpr_write_b32 a165, v23\n"
"v_accvgpr_read_b32 v22, a166\n"
"v_accvgpr_read_b32 v23, a167\n"
"v_mul_f32 v22, v22, v26\n"
"v_mul_f32 v23, v23, v26\n"
"v_accvgpr_write_b32 a166, v22\n"
"v_accvgpr_write_b32 a167, v23\n"
"v_accvgpr_read_b32 v22, a188\n"
"v_accvgpr_read_b32 v23, a189\n"
"v_mul_f32 v22, v22, v26\n"
"v_mul_f32 v23, v23, v26\n"
"v_accvgpr_write_b32 a188, v22\n"
"v_accvgpr_write_b32 a189, v23\n"
"v_accvgpr_read_b32 v22, a190\n"
"v_accvgpr_read_b32 v23, a191\n"
"v_mul_f32 v22, v22, v26\n"
"v_mul_f32 v23, v23, v26\n"
"v_accvgpr_write_b32 a190, v22\n"
"v_accvgpr_write_b32 a191, v23\n"
"v_accvgpr_read_b32 v22, a212\n"
"v_accvgpr_read_b32 v23, a213\n"
"v_mul_f32 v22, v22, v26\n"
"v_mul_f32 v23, v23, v26\n"
"v_accvgpr_write_b32 a212, v22\n"
"v_accvgpr_write_b32 a213, v23\n"
"v_accvgpr_read_b32 v22, a214\n"
"v_accvgpr_read_b32 v23, a215\n"
"v_mul_f32 v22, v22, v26\n"
"v_mul_f32 v23, v23, v26\n"
"v_accvgpr_write_b32 a214, v22\n"
"v_accvgpr_write_b32 a215, v23\n"
"v_accvgpr_read_b32 v22, a236\n"
"v_accvgpr_read_b32 v23, a237\n"
"v_mul_f32 v22, v22, v26\n"
"v_mul_f32 v23, v23, v26\n"
"v_accvgpr_write_b32 a236, v22\n"
"v_accvgpr_write_b32 a237, v23\n"
"v_accvgpr_read_b32 v22, a238\n"
"v_accvgpr_read_b32 v23, a239\n"
"v_mul_f32 v22, v22, v26\n"
"v_mul_f32 v23, v23, v26\n"
"v_accvgpr_write_b32 a238, v22\n"
"v_accvgpr_write_b32 a239, v23\n"
"v_mul_f32 v132, v132, v26\n"
"v_mul_f32 v133, v133, v26\n"
"v_mul_f32 v134, v134, v26\n"
"v_mul_f32 v135, v135, v26\n"
"v_mul_f32 v156, v156, v26\n"
"v_mul_f32 v157, v157, v26\n"
"v_mul_f32 v158, v158, v26\n"
"v_mul_f32 v159, v159, v26\n"
"v_mul_f32 v180, v180, v26\n"
"v_mul_f32 v181, v181, v26\n"
"v_mul_f32 v182, v182, v26\n"
"v_mul_f32 v183, v183, v26\n"
"v_mul_f32 v204, v204, v26\n"
"v_mul_f32 v205, v205, v26\n"
"v_mul_f32 v206, v206, v26\n"
"v_mul_f32 v207, v207, v26\n"
"v_mul_f32 v228, v228, v26\n"
"v_mul_f32 v229, v229, v26\n"
"v_mul_f32 v230, v230, v26\n"
"v_mul_f32 v231, v231, v26\n"
"v_mul_f32 v252, v252, v26\n"
"v_mul_f32 v253, v253, v26\n"
"v_mul_f32 v254, v254, v26\n"
"v_mul_f32 v255, v255, v26\n"
"v_fma_f32 v22, v72, s62, v14\n"
"v_fma_f32 v23, v73, s62, v14\n"
"v_exp_f32 v22, v22\n"
"v_exp_f32 v23, v23\n"
"s_nop 0\n"
"v_add_f32 v21, v22, v23\n"
"v_cvt_pk_bf16_f32 v68, v22, v23\n"
"v_fma_f32 v22, v74, s62, v14\n"
"v_fma_f32 v23, v75, s62, v14\n"
"v_exp_f32 v22, v22\n"
"v_exp_f32 v23, v23\n"
"s_nop 0\n"
"v_add_f32 v21, v21, v22\n"
"v_add_f32 v21, v21, v23\n"
"v_cvt_pk_bf16_f32 v69, v22, v23\n"
"v_fma_f32 v22, v28, s62, v14\n"
"v_fma_f32 v23, v29, s62, v14\n"
"v_exp_f32 v22, v22\n"
"v_exp_f32 v23, v23\n"
"s_nop 0\n"
"v_add_f32 v21, v21, v22\n"
"v_add_f32 v21, v21, v23\n"
"v_cvt_pk_bf16_f32 v70, v22, v23\n"
"v_fma_f32 v22, v30, s62, v14\n"
"v_fma_f32 v23, v31, s62, v14\n"
"v_exp_f32 v22, v22\n"
"v_exp_f32 v23, v23\n"
"s_nop 0\n"
"v_add_f32 v21, v21, v22\n"
"v_add_f32 v21, v21, v23\n"
"v_cvt_pk_bf16_f32 v71, v22, v23\n"
"s_nop 1\n"
"s_branch .Lret_4_d5\n"
".Lrare_5_a:\n"
"s_nop 7\n"
"v_max3_f32 v25, v88, v89, v90\n"
"v_max3_f32 v26, v91, v60, v61\n"
"v_max3_f32 v25, v25, v62, v63\n"
"v_max_f32 v25, v25, v26\n"
"v_mov_b32 v26, v25\n"
"s_nop 1\n"
"v_permlane32_swap_b32 v25, v26\n"
"s_nop 1\n"
"v_max_f32 v25, v25, v26\n"
"v_mov_b32 v26, v25\n"
"s_nop 1\n"
"v_permlane16_swap_b32 v25, v26\n"
"s_nop 1\n"
"v_max_f32 v25, v25, v26\n"
"v_mul_f32 v25, s62, v25\n"
"v_sub_f32 v25, 0, v25\n"
"v_min_f32 v25, v25, v9\n"
"v_sub_f32 v26, v25, v9\n"
"v_exp_f32 v26, v26\n"
"v_mov_b32 v9, v25\n"
"s_nop 0\n"
"v_mul_f32 v15, v15, v26\n"
"v_accvgpr_read_b32 v22, a96\n"
"v_accvgpr_read_b32 v23, a97\n"
"v_mul_f32 v22, v22, v26\n"
"v_mul_f32 v23, v23, v26\n"
"v_accvgpr_write_b32 a96, v22\n"
"v_accvgpr_write_b32 a97, v23\n"
"v_accvgpr_read_b32 v22, a98\n"
"v_accvgpr_read_b32 v23, a99\n"
"v_mul_f32 v22, v22, v26\n"
"v_mul_f32 v23, v23, v26\n"
"v_accvgpr_write_b32 a98, v22\n"
"v_accvgpr_write_b32 a99, v23\n"
"v_accvgpr_read_b32 v22, a120\n"
"v_accvgpr_read_b32 v23, a121\n"
"v_mul_f32 v22, v22, v26\n"
"v_mul_f32 v23, v23, v26\n"
"v_accvgpr_write_b32 a120, v22\n"
"v_accvgpr_write_b32 a121, v23\n"
"v_accvgpr_read_b32 v22, a122\n"
"v_accvgpr_read_b32 v23, a123\n"
"v_mul_f32 v22, v22, v26\n"
"v_mul_f32 v23, v23, v26\n"
"v_accvgpr_write_b32 a122, v22\n"
"v_accvgpr_write_b32 a123, v23\n"
"v_accvgpr_read_b32 v22, a144\n"
"v_accvgpr_read_b32 v23, a145\n"
"v_mul_f32 v22, v22, v26\n"
"v_mul_f32 v23, v23, v26\n"
"v_accvgpr_write_b32 a144, v22\n"
"v_accvgpr_write_b32 a145, v23\n"
"v_accvgpr_read_b32 v22, a146\n"
"v_accvgpr_read_b32 v23, a147\n"
"v_mul_f32 v22, v22, v26\n"
"v_mul_f32 v23, v23, v26\n"
"v_accvgpr_write_b32 a146, v22\n"
"v_accvgpr_write_b32 a147, v23\n"
"v_accvgpr_read_b32 v22, a168\n"
"v_accvgpr_read_b32 v23, a169\n"
"v_mul_f32 v22, v22, v26\n"
"v_mul_f32 v23, v23, v26\n"
"v_accvgpr_write_b32 a168, v22\n"
"v_accvgpr_write_b32 a169, v23\n"
"v_accvgpr_read_b32 v22, a170\n"
"v_accvgpr_read_b32 v23, a171\n"
"v_mul_f32 v22, v22, v26\n"
"v_mul_f32 v23, v23, v26\n"
"v_accvgpr_write_b32 a170, v22\n"
"v_accvgpr_write_b32 a171, v23\n"
"v_accvgpr_read_b32 v22, a192\n"
"v_accvgpr_read_b32 v23, a193\n"
"v_mul_f32 v22, v22, v26\n"
"v_mul_f32 v23, v23, v26\n"
"v_accvgpr_write_b32 a192, v22\n"
"v_accvgpr_write_b32 a193, v23\n"
"v_accvgpr_read_b32 v22, a194\n"
"v_accvgpr_read_b32 v23, a195\n"
"v_mul_f32 v22, v22, v26\n"
"v_mul_f32 v23, v23, v26\n"
"v_accvgpr_write_b32 a194, v22\n"
"v_accvgpr_write_b32 a195, v23\n"
"v_accvgpr_read_b32 v22, a216\n"
"v_accvgpr_read_b32 v23, a217\n"
"v_mul_f32 v22, v22, v26\n"
"v_mul_f32 v23, v23, v26\n"
"v_accvgpr_write_b32 a216, v22\n"
"v_accvgpr_write_b32 a217, v23\n"
"v_accvgpr_read_b32 v22, a218\n"
"v_accvgpr_read_b32 v23, a219\n"
"v_mul_f32 v22, v22, v26\n"
"v_mul_f32 v23, v23, v26\n"
"v_accvgpr_write_b32 a218, v22\n"
"v_accvgpr_write_b32 a219, v23\n"
"v_accvgpr_read_b32 v22, a240\n"
"v_accvgpr_read_b32 v23, a241\n"
"v_mul_f32 v22, v22, v26\n"
"v_mul_f32 v23, v23, v26\n"
"v_accvgpr_write_b32 a240, v22\n"
"v_accvgpr_write_b32 a241, v23\n"
"v_accvgpr_read_b32 v22, a242\n"
"v_accvgpr_read_b32 v23, a243\n"
"v_mul_f32 v22, v22, v26\n"
"v_mul_f32 v23, v23, v26\n"
"v_accvgpr_write_b32 a242, v22\n"
"v_accvgpr_write_b32 a243, v23\n"
"v_mul_f32 v136, v136, v26\n"
"v_mul_f32 v137, v137, v26\n"
"v_mul_f32 v138, v138, v26\n"
"v_mul_f32 v139, v139, v26\n"
"v_mul_f32 v160, v160, v26\n"
"v_mul_f32 v161, v161, v26\n"
"v_mul_f32 v162, v162, v26\n"
"v_mul_f32 v163, v163, v26\n"
"v_mul_f32 v184, v184, v26\n"
"v_mul_f32 v185, v185, v26\n"
"v_mul_f32 v186, v186, v26\n"
"v_mul_f32 v187, v187, v26\n"
"v_mul_f32 v208, v208, v26\n"
"v_mul_f32 v209, v209, v26\n"
"v_mul_f32 v210, v210, v26\n"
"v_mul_f32 v211, v211, v26\n"
"v_mul_f32 v232, v232, v26\n"
"v_mul_f32 v233, v233, v26\n"
"v_mul_f32 v234, v234, v26\n"
"v_mul_f32 v235, v235, v26\n"
"v_lshlrev_b32 v22, 16, v112\n"
"v_and_b32 v23, 0xffff0000, v112\n"
"v_mul_f32 v22, v22, v26\n"
"v_mul_f32 v23, v23, v26\n"
"v_cvt_pk_bf16_f32 v112, v22, v23\n"
"v_lshlrev_b32 v22, 16, v113\n"
"v_and_b32 v23, 0xffff0000, v113\n"
"v_mul_f32 v22, v22, v26\n"
"v_mul_f32 v23, v23, v26\n"
"v_cvt_pk_bf16_f32 v113, v22, v23\n"
"v_lshlrev_b32 v22, 16, v114\n"
"v_and_b32 v23, 0xffff0000, v114\n"
"v_mul_f32 v22, v22, v26\n"
"v_mul_f32 v23, v23, v26\n"
"v_cvt_pk_bf16_f32 v114, v22, v23\n"
"v_lshlrev_b32 v22, 16, v115\n"
"v_and_b32 v23, 0xffff0000, v115\n"
"v_mul_f32 v22, v22, v26\n"
"v_mul_f32 v23, v23, v26\n"
"v_cvt_pk_bf16_f32 v115, v22, v23\n"
"v_fma_f32 v22, v88, s62, v9\n"
"v_fma_f32 v23, v89, s62, v9\n"
"v_exp_f32 v22, v22\n"
"v_exp_f32 v23, v23\n"
"s_nop 0\n"
"v_add_f32 v21, v22, v23\n"
"v_cvt_pk_bf16_f32 v116, v22, v23\n"
"v_fma_f32 v22, v90, s62, v9\n"
"v_fma_f32 v23, v91, s62, v9\n"
"v_exp_f32 v22, v22\n"
"v_exp_f32 v23, v23\n"
"s_nop 0\n"
"v_add_f32 v21, v21, v22\n"
"v_add_f32 v21, v21, v23\n"
"v_cvt_pk_bf16_f32 v117, v22, v23\n"
"v_fma_f32 v22, v60, s62, v9\n"
"v_fma_f32 v23, v61, s62, v9\n"
"v_exp_f32 v22, v22\n"
"v_exp_f32 v23, v23\n"
"s_nop 0\n"
"v_add_f32 v21, v21, v22\n"
"v_add_f32 v21, v21, v23\n"
"v_cvt_pk_bf16_f32 v118, v22, v23\n"
"v_fma_f32 v22, v62, s62, v9\n"
"v_fma_f32 v23, v63, s62, v9\n"
"v_exp_f32 v22, v22\n"
"v_exp_f32 v23, v23\n"
"s_nop 0\n"
"v_add_f32 v21, v21, v22\n"
"v_add_f32 v21, v21, v23\n"
"v_cvt_pk_bf16_f32 v119, v22, v23\n"
"v_max3_f32 v25, v80, v81, v82\n"
"v_max3_f32 v26, v83, v108, v109\n"
"v_max3_f32 v25, v25, v110, v111\n"
"v_max_f32 v25, v25, v26\n"
"v_mov_b32 v26, v25\n"
"s_nop 1\n"
"v_permlane32_swap_b32 v25, v26\n"
"s_nop 1\n"
"v_max_f32 v25, v25, v26\n"
"v_mov_b32 v26, v25\n"
"s_nop 1\n"
"v_permlane16_swap_b32 v25, v26\n"
"s_nop 1\n"
"v_max_f32 v25, v25, v26\n"
"v_mul_f32 v25, s62, v25\n"
"v_sub_f32 v25, 0, v25\n"
"v_min_f32 v25, v25, v10\n"
"v_sub_f32 v26, v25, v10\n"
"v_exp_f32 v26, v26\n"
"v_mov_b32 v10, v25\n"
"s_nop 0\n"
"v_mul_f32 v16, v16, v26\n"
"v_accvgpr_read_b32 v22, a100\n"
"v_accvgpr_read_b32 v23, a101\n"
"v_mul_f32 v22, v22, v26\n"
"v_mul_f32 v23, v23, v26\n"
"v_accvgpr_write_b32 a100, v22\n"
"v_accvgpr_write_b32 a101, v23\n"
"v_accvgpr_read_b32 v22, a102\n"
"v_accvgpr_read_b32 v23, a103\n"
"v_mul_f32 v22, v22, v26\n"
"v_mul_f32 v23, v23, v26\n"
"v_accvgpr_write_b32 a102, v22\n"
"v_accvgpr_write_b32 a103, v23\n"
"v_accvgpr_read_b32 v22, a124\n"
"v_accvgpr_read_b32 v23, a125\n"
"v_mul_f32 v22, v22, v26\n"
"v_mul_f32 v23, v23, v26\n"
"v_accvgpr_write_b32 a124, v22\n"
"v_accvgpr_write_b32 a125, v23\n"
"v_accvgpr_read_b32 v22, a126\n"
"v_accvgpr_read_b32 v23, a127\n"
"v_mul_f32 v22, v22, v26\n"
"v_mul_f32 v23, v23, v26\n"
"v_accvgpr_write_b32 a126, v22\n"
"v_accvgpr_write_b32 a127, v23\n"
"v_accvgpr_read_b32 v22, a148\n"
"v_accvgpr_read_b32 v23, a149\n"
"v_mul_f32 v22, v22, v26\n"
"v_mul_f32 v23, v23, v26\n"
"v_accvgpr_write_b32 a148, v22\n"
"v_accvgpr_write_b32 a149, v23\n"
"v_accvgpr_read_b32 v22, a150\n"
"v_accvgpr_read_b32 v23, a151\n"
"v_mul_f32 v22, v22, v26\n"
"v_mul_f32 v23, v23, v26\n"
"v_accvgpr_write_b32 a150, v22\n"
"v_accvgpr_write_b32 a151, v23\n"
"v_accvgpr_read_b32 v22, a172\n"
"v_accvgpr_read_b32 v23, a173\n"
"v_mul_f32 v22, v22, v26\n"
"v_mul_f32 v23, v23, v26\n"
"v_accvgpr_write_b32 a172, v22\n"
"v_accvgpr_write_b32 a173, v23\n"
"v_accvgpr_read_b32 v22, a174\n"
"v_accvgpr_read_b32 v23, a175\n"
"v_mul_f32 v22, v22, v26\n"
"v_mul_f32 v23, v23, v26\n"
"v_accvgpr_write_b32 a174, v22\n"
"v_accvgpr_write_b32 a175, v23\n"
"v_accvgpr_read_b32 v22, a196\n"
"v_accvgpr_read_b32 v23, a197\n"
"v_mul_f32 v22, v22, v26\n"
"v_mul_f32 v23, v23, v26\n"
"v_accvgpr_write_b32 a196, v22\n"
"v_accvgpr_write_b32 a197, v23\n"
"v_accvgpr_read_b32 v22, a198\n"
"v_accvgpr_read_b32 v23, a199\n"
"v_mul_f32 v22, v22, v26\n"
"v_mul_f32 v23, v23, v26\n"
"v_accvgpr_write_b32 a198, v22\n"
"v_accvgpr_write_b32 a199, v23\n"
"v_accvgpr_read_b32 v22, a220\n"
"v_accvgpr_read_b32 v23, a221\n"
"v_mul_f32 v22, v22, v26\n"
"v_mul_f32 v23, v23, v26\n"
"v_accvgpr_write_b32 a220, v22\n"
"v_accvgpr_write_b32 a221, v23\n"
"v_accvgpr_read_b32 v22, a222\n"
"v_accvgpr_read_b32 v23, a223\n"
"v_mul_f32 v22, v22, v26\n"
"v_mul_f32 v23, v23, v26\n"
"v_accvgpr_write_b32 a222, v22\n"
"v_accvgpr_write_b32 a223, v23\n"
"v_accvgpr_read_b32 v22, a244\n"
"v_accvgpr_read_b32 v23, a245\n"
"v_mul_f32 v22, v22, v26\n"
"v_mul_f32 v23, v23, v26\n"
"v_accvgpr_write_b32 a244, v22\n"
"v_accvgpr_write_b32 a245, v23\n"
"v_accvgpr_read_b32 v22, a246\n"
"v_accvgpr_read_b32 v23, a247\n"
"v_mul_f32 v22, v22, v26\n"
"v_mul_f32 v23, v23, v26\n"
"v_accvgpr_write_b32 a246, v22\n"
"v_accvgpr_write_b32 a247, v23\n"
"v_mul_f32 v140, v140, v26\n"
"v_mul_f32 v141, v141, v26\n"
"v_mul_f32 v142, v142, v26\n"
"v_mul_f32 v143, v143, v26\n"
"v_mul_f32 v164, v164, v26\n"
"v_mul_f32 v165, v165, v26\n"
"v_mul_f32 v166, v166, v26\n"
"v_mul_f32 v167, v167, v26\n"
"v_mul_f32 v188, v188, v26\n"
"v_mul_f32 v189, v189, v26\n"
"v_mul_f32 v190, v190, v26\n"
"v_mul_f32 v191, v191, v26\n"
"v_mul_f32 v212, v212, v26\n"
"v_mul_f32 v213, v213, v26\n"
"v_mul_f32 v214, v214, v26\n"
"v_mul_f32 v215, v215, v26\n"
"v_mul_f32 v236, v236, v26\n"
"v_mul_f32 v237, v237, v26\n"
"v_mul_f32 v238, v238, v26\n"
"v_mul_f32 v239, v239, v26\n"
"v_lshlrev_b32 v22, 16, v120\n"
"v_and_b32 v23, 0xffff0000, v120\n"
"v_mul_f32 v22, v22, v26\n"
"v_mul_f32 v23, v23, v26\n"
"v_cvt_pk_bf16_f32 v120, v22, v23\n"
"v_lshlrev_b32 v22, 16, v121\n"
"v_and_b32 v23, 0xffff0000, v121\n"
"v_mul_f32 v22, v22, v26\n"
"v_mul_f32 v23, v23, v26\n"
"v_cvt_pk_bf16_f32 v121, v22, v23\n"
"v_lshlrev_b32 v22, 16, v122\n"
"v_and_b32 v23, 0xffff0000, v122\n"
"v_mul_f32 v22, v22, v26\n"
"v_mul_f32 v23, v23, v26\n"
"v_cvt_pk_bf16_f32 v122, v22, v23\n"
"v_lshlrev_b32 v22, 16, v123\n"
"v_and_b32 v23, 0xffff0000, v123\n"
"v_mul_f32 v22, v22, v26\n"
"v_mul_f32 v23, v23, v26\n"
"v_cvt_pk_bf16_f32 v123, v22, v23\n"
"v_fma_f32 v22, v80, s62, v10\n"
"v_fma_f32 v23, v81, s62, v10\n"
"v_exp_f32 v22, v22\n"
"v_exp_f32 v23, v23\n"
"s_nop 0\n"
"v_add_f32 v3, v22, v23\n"
"v_cvt_pk_bf16_f32 v72, v22, v23\n"
"v_fma_f32 v22, v82, s62, v10\n"
"v_fma_f32 v23, v83, s62, v10\n"
"v_exp_f32 v22, v22\n"
"v_exp_f32 v23, v23\n"
"s_nop 0\n"
"v_add_f32 v3, v3, v22\n"
"v_add_f32 v3, v3, v23\n"
"v_cvt_pk_bf16_f32 v73, v22, v23\n"
"v_fma_f32 v22, v108, s62, v10\n"
"v_fma_f32 v23, v109, s62, v10\n"
"v_exp_f32 v22, v22\n"
"v_exp_f32 v23, v23\n"
"s_nop 0\n"
"v_add_f32 v3, v3, v22\n"
"v_add_f32 v3, v3, v23\n"
"v_cvt_pk_bf16_f32 v74, v22, v23\n"
"v_fma_f32 v22, v110, s62, v10\n"
"v_fma_f32 v23, v111, s62, v10\n"
"v_exp_f32 v22, v22\n"
"v_exp_f32 v23, v23\n"
"s_nop 0\n"
"v_add_f32 v3, v3, v22\n"
"v_add_f32 v3, v3, v23\n"
"v_cvt_pk_bf16_f32 v75, v22, v23\n"
"v_fma_f32 v22, v92, s62, v11\n"
"v_fma_f32 v23, v93, s62, v11\n"
"s_nop 1\n"
"s_branch .Lret_5_a\n"
".Lrare_5_b:\n"
"s_nop 7\n"
"v_max3_f32 v25, v92, v93, v94\n"
"v_max3_f32 v26, v95, v124, v125\n"
"v_max3_f32 v25, v25, v126, v127\n"
"v_max_f32 v25, v25, v26\n"
"v_mov_b32 v26, v25\n"
"s_nop 1\n"
"v_permlane32_swap_b32 v25, v26\n"
"s_nop 1\n"
"v_max_f32 v25, v25, v26\n"
"v_mov_b32 v26, v25\n"
"s_nop 1\n"
"v_permlane16_swap_b32 v25, v26\n"
"s_nop 1\n"
"v_max_f32 v25, v25, v26\n"
"v_mul_f32 v25, s62, v25\n"
"v_sub_f32 v25, 0, v25\n"
"v_min_f32 v25, v25, v11\n"
"v_sub_f32 v26, v25, v11\n"
"v_exp_f32 v26, v26\n"
"v_mov_b32 v11, v25\n"
"s_nop 0\n"
"v_mul_f32 v17, v17, v26\n"
"v_accvgpr_read_b32 v22, a104\n"
"v_accvgpr_read_b32 v23, a105\n"
"v_mul_f32 v22, v22, v26\n"
"v_mul_f32 v23, v23, v26\n"
"v_accvgpr_write_b32 a104, v22\n"
"v_accvgpr_write_b32 a105, v23\n"
"v_accvgpr_read_b32 v22, a106\n"
"v_accvgpr_read_b32 v23, a107\n"
"v_mul_f32 v22, v22, v26\n"
"v_mul_f32 v23, v23, v26\n"
"v_accvgpr_write_b32 a106, v22\n"
"v_accvgpr_write_b32 a107, v23\n"
"v_accvgpr_read_b32 v22, a128\n"
"v_accvgpr_read_b32 v23, a129\n"
"v_mul_f32 v22, v22, v26\n"
"v_mul_f32 v23, v23, v26\n"
"v_accvgpr_write_b32 a128, v22\n"
"v_accvgpr_write_b32 a129, v23\n"
"v_accvgpr_read_b32 v22, a130\n"
"v_accvgpr_read_b32 v23, a131\n"
"v_mul_f32 v22, v22, v26\n"
"v_mul_f32 v23, v23, v26\n"
"v_accvgpr_write_b32 a130, v22\n"
"v_accvgpr_write_b32 a131, v23\n"
"v_accvgpr_read_b32 v22, a152\n"
"v_accvgpr_read_b32 v23, a153\n"
"v_mul_f32 v22, v22, v26\n"
"v_mul_f32 v23, v23, v26\n"
"v_accvgpr_write_b32 a152, v22\n"
"v_accvgpr_write_b32 a153, v23\n"
"v_accvgpr_read_b32 v22, a154\n"
"v_accvgpr_read_b32 v23, a155\n"
"v_mul_f32 v22, v22, v26\n"
"v_mul_f32 v23, v23, v26\n"
"v_accvgpr_write_b32 a154, v22\n"
"v_accvgpr_write_b32 a155, v23\n"
"v_accvgpr_read_b32 v22, a176\n"
"v_accvgpr_read_b32 v23, a177\n"
"v_mul_f32 v22, v22, v26\n"
"v_mul_f32 v23, v23, v26\n"
"v_accvgpr_write_b32 a176, v22\n"
"v_accvgpr_write_b32 a177, v23\n"
"v_accvgpr_read_b32 v22, a178\n"
"v_accvgpr_read_b32 v23, a179\n"
"v_mul_f32 v22, v22, v26\n"
"v_mul_f32 v23, v23, v26\n"
"v_accvgpr_write_b32 a178, v22\n"
"v_accvgpr_write_b32 a179, v23\n"
"v_accvgpr_read_b32 v22, a200\n"
"v_accvgpr_read_b32 v23, a201\n"
"v_mul_f32 v22, v22, v26\n"
"v_mul_f32 v23, v23, v26\n"
"v_accvgpr_write_b32 a200, v22\n"
"v_accvgpr_write_b32 a201, v23\n"
"v_accvgpr_read_b32 v22, a202\n"
"v_accvgpr_read_b32 v23, a203\n"
"v_mul_f32 v22, v22, v26\n"
"v_mul_f32 v23, v23, v26\n"
"v_accvgpr_write_b32 a202, v22\n"
"v_accvgpr_write_b32 a203, v23\n"
"v_accvgpr_read_b32 v22, a224\n"
"v_accvgpr_read_b32 v23, a225\n"
"v_mul_f32 v22, v22, v26\n"
"v_mul_f32 v23, v23, v26\n"
"v_accvgpr_write_b32 a224, v22\n"
"v_accvgpr_write_b32 a225, v23\n"
"v_accvgpr_read_b32 v22, a226\n"
"v_accvgpr_read_b32 v23, a227\n"
"v_mul_f32 v22, v22, v26\n"
"v_mul_f32 v23, v23, v26\n"
"v_accvgpr_write_b32 a226, v22\n"
"v_accvgpr_write_b32 a227, v23\n"
"v_accvgpr_read_b32 v22, a248\n"
"v_accvgpr_read_b32 v23, a249\n"
"v_mul_f32 v22, v22, v26\n"
"v_mul_f32 v23, v23, v26\n"
"v_accvgpr_write_b32 a248, v22\n"
"v_accvgpr_write_b32 a249, v23\n"
"v_accvgpr_read_b32 v22, a250\n"
"v_accvgpr_read_b32 v23, a251\n"
"v_mul_f32 v22, v22, v26\n"
"v_mul_f32 v23, v23, v26\n"
"v_accvgpr_write_b32 a250, v22\n"
"v_accvgpr_write_b32 a251, v23\n"
"v_mul_f32 v144, v144, v26\n"
"v_mul_f32 v145, v145, v26\n"
"v_mul_f32 v146, v146, v26\n"
"v_mul_f32 v147, v147, v26\n"
"v_mul_f32 v168, v168, v26\n"
"v_mul_f32 v169, v169, v26\n"
"v_mul_f32 v170, v170, v26\n"
"v_mul_f32 v171, v171, v26\n"
"v_mul_f32 v192, v192, v26\n"
"v_mul_f32 v193, v193, v26\n"
"v_mul_f32 v194, v194, v26\n"
"v_mul_f32 v195, v195, v26\n"
"v_mul_f32 v216, v216, v26\n"
"v_mul_f32 v217, v217, v26\n"
"v_mul_f32 v218, v218, v26\n"
"v_mul_f32 v219, v219, v26\n"
"v_mul_f32 v240, v240, v26\n"
"v_mul_f32 v241, v241, v26\n"
"v_mul_f32 v242, v242, v26\n"
"v_mul_f32 v243, v243, v26\n"
"v_lshlrev_b32 v22, 16, v64\n"
"v_and_b32 v23, 0xffff0000, v64\n"
"v_mul_f32 v22, v22, v26\n"
"v_mul_f32 v23, v23, v26\n"
"v_cvt_pk_bf16_f32 v64, v22, v23\n"
"v_lshlrev_b32 v22, 16, v65\n"
"v_and_b32 v23, 0xffff0000, v65\n"
"v_mul_f32 v22, v22, v26\n"
"v_mul_f32 v23, v23, v26\n"
"v_cvt_pk_bf16_f32 v65, v22, v23\n"
"v_lshlrev_b32 v22, 16, v66\n"
"v_and_b32 v23, 0xffff0000, v66\n"
"v_mul_f32 v22, v22, v26\n"
"v_mul_f32 v23, v23, v26\n"
"v_cvt_pk_bf16_f32 v66, v22, v23\n"
"v_lshlrev_b32 v22, 16, v67\n"
"v_and_b32 v23, 0xffff0000, v67\n"
"v_mul_f32 v22, v22, v26\n"
"v_mul_f32 v23, v23, v26\n"
"v_cvt_pk_bf16_f32 v67, v22, v23\n"
"v_fma_f32 v22, v92, s62, v11\n"
"v_fma_f32 v23, v93, s62, v11\n"
"v_exp_f32 v22, v22\n"
"v_exp_f32 v23, v23\n"
"s_nop 0\n"
"v_add_f32 v60, v22, v23\n"
"v_cvt_pk_bf16_f32 v28, v22, v23\n"
"v_fma_f32 v22, v94, s62, v11\n"
"v_fma_f32 v23, v95, s62, v11\n"
"v_exp_f32 v22, v22\n"
"v_exp_f32 v23, v23\n"
"s_nop 0\n"
"v_add_f32 v60, v60, v22\n"
"v_add_f32 v60, v60, v23\n"
"v_cvt_pk_bf16_f32 v29, v22, v23\n"
"v_fma_f32 v22, v124, s62, v11\n"
"v_fma_f32 v23, v125, s62, v11\n"
"v_exp_f32 v22, v22\n"
"v_exp_f32 v23, v23\n"
"s_nop 0\n"
"v_add_f32 v60, v60, v22\n"
"v_add_f32 v60, v60, v23\n"
"v_cvt_pk_bf16_f32 v30, v22, v23\n"
"v_fma_f32 v22, v126, s62, v11\n"
"v_fma_f32 v23, v127, s62, v11\n"
"v_exp_f32 v22, v22\n"
"v_exp_f32 v23, v23\n"
"s_nop 0\n"
"v_add_f32 v60, v60, v22\n"
"v_add_f32 v60, v60, v23\n"
"v_cvt_pk_bf16_f32 v31, v22, v23\n"
"v_max3_f32 v25, v104, v105, v106\n"
"v_max3_f32 v26, v107, v84, v85\n"
"v_max3_f32 v25, v25, v86, v87\n"
"v_max_f32 v25, v25, v26\n"
"v_mov_b32 v26, v25\n"
"s_nop 1\n"
"v_permlane32_swap_b32 v25, v26\n"
"s_nop 1\n"
"v_max_f32 v25, v25, v26\n"
"v_mov_b32 v26, v25\n"
"s_nop 1\n"
"v_permlane16_swap_b32 v25, v26\n"
"s_nop 1\n"
"v_max_f32 v25, v25, v26\n"
"v_mul_f32 v25, s62, v25\n"
"v_sub_f32 v25, 0, v25\n"
"v_min_f32 v25, v25, v12\n"
"v_sub_f32 v26, v25, v12\n"
"v_exp_f32 v26, v26\n"
"v_mov_b32 v12, v25\n"
"s_nop 0\n"
"v_mul_f32 v18, v18, v26\n"
"v_accvgpr_read_b32 v22, a108\n"
"v_accvgpr_read_b32 v23, a109\n"
"v_mul_f32 v22, v22, v26\n"
"v_mul_f32 v23, v23, v26\n"
"v_accvgpr_write_b32 a108, v22\n"
"v_accvgpr_write_b32 a109, v23\n"
"v_accvgpr_read_b32 v22, a110\n"
"v_accvgpr_read_b32 v23, a111\n"
"v_mul_f32 v22, v22, v26\n"
"v_mul_f32 v23, v23, v26\n"
"v_accvgpr_write_b32 a110, v22\n"
"v_accvgpr_write_b32 a111, v23\n"
"v_accvgpr_read_b32 v22, a132\n"
"v_accvgpr_read_b32 v23, a133\n"
"v_mul_f32 v22, v22, v26\n"
"v_mul_f32 v23, v23, v26\n"
"v_accvgpr_write_b32 a132, v22\n"
"v_accvgpr_write_b32 a133, v23\n"
"v_accvgpr_read_b32 v22, a134\n"
"v_accvgpr_read_b32 v23, a135\n"
"v_mul_f32 v22, v22, v26\n"
"v_mul_f32 v23, v23, v26\n"
"v_accvgpr_write_b32 a134, v22\n"
"v_accvgpr_write_b32 a135, v23\n"
"v_accvgpr_read_b32 v22, a156\n"
"v_accvgpr_read_b32 v23, a157\n"
"v_mul_f32 v22, v22, v26\n"
"v_mul_f32 v23, v23, v26\n"
"v_accvgpr_write_b32 a156, v22\n"
"v_accvgpr_write_b32 a157, v23\n"
"v_accvgpr_read_b32 v22, a158\n"
"v_accvgpr_read_b32 v23, a159\n"
"v_mul_f32 v22, v22, v26\n"
"v_mul_f32 v23, v23, v26\n"
"v_accvgpr_write_b32 a158, v22\n"
"v_accvgpr_write_b32 a159, v23\n"
"v_accvgpr_read_b32 v22, a180\n"
"v_accvgpr_read_b32 v23, a181\n"
"v_mul_f32 v22, v22, v26\n"
"v_mul_f32 v23, v23, v26\n"
"v_accvgpr_write_b32 a180, v22\n"
"v_accvgpr_write_b32 a181, v23\n"
"v_accvgpr_read_b32 v22, a182\n"
"v_accvgpr_read_b32 v23, a183\n"
"v_mul_f32 v22, v22, v26\n"
"v_mul_f32 v23, v23, v26\n"
"v_accvgpr_write_b32 a182, v22\n"
"v_accvgpr_write_b32 a183, v23\n"
"v_accvgpr_read_b32 v22, a204\n"
"v_accvgpr_read_b32 v23, a205\n"
"v_mul_f32 v22, v22, v26\n"
"v_mul_f32 v23, v23, v26\n"
"v_accvgpr_write_b32 a204, v22\n"
"v_accvgpr_write_b32 a205, v23\n"
"v_accvgpr_read_b32 v22, a206\n"
"v_accvgpr_read_b32 v23, a207\n"
"v_mul_f32 v22, v22, v26\n"
"v_mul_f32 v23, v23, v26\n"
"v_accvgpr_write_b32 a206, v22\n"
"v_accvgpr_write_b32 a207, v23\n"
"v_accvgpr_read_b32 v22, a228\n"
"v_accvgpr_read_b32 v23, a229\n"
"v_mul_f32 v22, v22, v26\n"
"v_mul_f32 v23, v23, v26\n"
"v_accvgpr_write_b32 a228, v22\n"
"v_accvgpr_write_b32 a229, v23\n"
"v_accvgpr_read_b32 v22, a230\n"
"v_accvgpr_read_b32 v23, a231\n"
"v_mul_f32 v22, v22, v26\n"
"v_mul_f32 v23, v23, v26\n"
"v_accvgpr_write_b32 a230, v22\n"
"v_accvgpr_write_b32 a231, v23\n"
"v_accvgpr_read_b32 v22, a252\n"
"v_accvgpr_read_b32 v23, a253\n"
"v_mul_f32 v22, v22, v26\n"
"v_mul_f32 v23, v23, v26\n"
"v_accvgpr_write_b32 a252, v22\n"
"v_accvgpr_write_b32 a253, v23\n"
"v_accvgpr_read_b32 v22, a254\n"
"v_accvgpr_read_b32 v23, a255\n"
"v_mul_f32 v22, v22, v26\n"
"v_mul_f32 v23, v23, v26\n"
"v_accvgpr_write_b32 a254, v22\n"
"v_accvgpr_write_b32 a255, v23\n"
"v_mul_f32 v148, v148, v26\n"
"v_mul_f32 v149, v149, v26\n"
"v_mul_f32 v150, v150, v26\n"
"v_mul_f32 v151, v151, v26\n"
"v_mul_f32 v172, v172, v26\n"
"v_mul_f32 v173, v173, v26\n"
"v_mul_f32 v174, v174, v26\n"
"v_mul_f32 v175, v175, v26\n"
"v_mul_f32 v196, v196, v26\n"
"v_mul_f32 v197, v197, v26\n"
"v_mul_f32 v198, v198, v26\n"
"v_mul_f32 v199, v199, v26\n"
"v_mul_f32 v220, v220, v26\n"
"v_mul_f32 v221, v221, v26\n"
"v_mul_f32 v222, v222, v26\n"
"v_mul_f32 v223, v223, v26\n"
"v_mul_f32 v244, v244, v26\n"
"v_mul_f32 v245, v245, v26\n"
"v_mul_f32 v246, v246, v26\n"
"v_mul_f32 v247, v247, v26\n"
"v_lshlrev_b32 v22, 16, v96\n"
"v_and_b32 v23, 0xffff0000, v96\n"
"v_mul_f32 v22, v22, v26\n"
"v_mul_f32 v23, v23, v26\n"
"v_cvt_pk_bf16_f32 v96, v22, v23\n"
"v_lshlrev_b32 v22, 16, v97\n"
"v_and_b32 v23, 0xffff0000, v97\n"
"v_mul_f32 v22, v22, v26\n"
"v_mul_f32 v23, v23, v26\n"
"v_cvt_pk_bf16_f32 v97, v22, v23\n"
"v_lshlrev_b32 v22, 16, v98\n"
"v_and_b32 v23, 0xffff0000, v98\n"
"v_mul_f32 v22, v22, v26\n"
"v_mul_f32 v23, v23, v26\n"
"v_cvt_pk_bf16_f32 v98, v22, v23\n"
"v_lshlrev_b32 v22, 16, v99\n"
"v_and_b32 v23, 0xffff0000, v99\n"
"v_mul_f32 v22, v22, v26\n"
"v_mul_f32 v23, v23, v26\n"
"v_cvt_pk_bf16_f32 v99, v22, v23\n"
"v_fma_f32 v22, v104, s62, v12\n"
"v_fma_f32 v23, v105, s62, v12\n"
"v_exp_f32 v22, v22\n"
"v_exp_f32 v23, v23\n"
"s_nop 0\n"
"v_add_f32 v61, v22, v23\n"
"v_cvt_pk_bf16_f32 v88, v22, v23\n"
"v_fma_f32 v22, v106, s62, v12\n"
"v_fma_f32 v23, v107, s62, v12\n"
"v_exp_f32 v22, v22\n"
"v_exp_f32 v23, v23\n"
"s_nop 0\n"
"v_add_f32 v61, v61, v22\n"
"v_add_f32 v61, v61, v23\n"
"v_cvt_pk_bf16_f32 v89, v22, v23\n"
"v_fma_f32 v22, v84, s62, v12\n"
"v_fma_f32 v23, v85, s62, v12\n"
"v_exp_f32 v22, v22\n"
"v_exp_f32 v23, v23\n"
"s_nop 0\n"
"v_add_f32 v61, v61, v22\n"
"v_add_f32 v61, v61, v23\n"
"v_cvt_pk_bf16_f32 v90, v22, v23\n"
"v_fma_f32 v22, v86, s62, v12\n"
"v_fma_f32 v23, v87, s62, v12\n"
"v_exp_f32 v22, v22\n"
"v_exp_f32 v23, v23\n"
"s_nop 0\n"
"v_add_f32 v61, v61, v22\n"
"v_add_f32 v61, v61, v23\n"
"v_cvt_pk_bf16_f32 v91, v22, v23\n"
"v_fma_f32 v22, v48, s62, v13\n"
"v_fma_f32 v23, v49, s62, v13\n"
"s_nop 1\n"
"s_branch .Lret_5_b\n"
".Lrare_5_d4:\n"
"s_nop 7\n"
"v_max3_f32 v25, v48, v49, v50\n"
"v_max3_f32 v26, v51, v52, v53\n"
"v_max3_f32 v25, v25, v54, v55\n"
"v_max_f32 v25, v25, v26\n"
"v_mov_b32 v26, v25\n"
"s_nop 1\n"
"v_permlane32_swap_b32 v25, v26\n"
"s_nop 1\n"
"v_max_f32 v25, v25, v26\n"
"v_mov_b32 v26, v25\n"
"s_nop 1\n"
"v_permlane16_swap_b32 v25, v26\n"
"s_nop 1\n"
"v_max_f32 v25, v25, v26\n"
"v_mul_f32 v25, s62, v25\n"
"v_sub_f32 v25, 0, v25\n"
"v_min_f32 v25, v25, v13\n"
"v_sub_f32 v26, v25, v13\n"
"v_exp_f32 v26, v26\n"
"v_mov_b32 v13, v25\n"
"s_nop 0\n"
"v_mul_f32 v19, v19, v26\n"
"v_accvgpr_read_b32 v22, a112\n"
"v_accvgpr_read_b32 v23, a113\n"
"v_mul_f32 v22, v22, v26\n"
"v_mul_f32 v23, v23, v26\n"
"v_accvgpr_write_b32 a112, v22\n"
"v_accvgpr_write_b32 a113, v23\n"
"v_accvgpr_read_b32 v22, a114\n"
"v_accvgpr_read_b32 v23, a115\n"
"v_mul_f32 v22, v22, v26\n"
"v_mul_f32 v23, v23, v26\n"
"v_accvgpr_write_b32 a114, v22\n"
"v_accvgpr_write_b32 a115, v23\n"
"v_accvgpr_read_b32 v22, a136\n"
"v_accvgpr_read_b32 v23, a137\n"
"v_mul_f32 v22, v22, v26\n"
"v_mul_f32 v23, v23, v26\n"
"v_accvgpr_write_b32 a136, v22\n"
"v_accvgpr_write_b32 a137, v23\n"
"v_accvgpr_read_b32 v22, a138\n"
"v_accvgpr_read_b32 v23, a139\n"
"v_mul_f32 v22, v22, v26\n"
"v_mul_f32 v23, v23, v26\n"
"v_accvgpr_write_b32 a138, v22\n"
"v_accvgpr_write_b32 a139, v23\n"
"v_accvgpr_read_b32 v22, a160\n"
"v_accvgpr_read_b32 v23, a161\n"
"v_mul_f32 v22, v22, v26\n"
"v_mul_f32 v23, v23, v26\n"
"v_accvgpr_write_b32 a160, v22\n"
"v_accvgpr_write_b32 a161, v23\n"
"v_accvgpr_read_b32 v22, a162\n"
"v_accvgpr_read_b32 v23, a163\n"
"v_mul_f32 v22, v22, v26\n"
"v_mul_f32 v23, v23, v26\n"
"v_accvgpr_write_b32 a162, v22\n"
"v_accvgpr_write_b32 a163, v23\n"
"v_accvgpr_read_b32 v22, a184\n"
"v_accvgpr_read_b32 v23, a185\n"
"v_mul_f32 v22, v22, v26\n"
"v_mul_f32 v23, v23, v26\n"
"v_accvgpr_write_b32 a184, v22\n"
"v_accvgpr_write_b32 a185, v23\n"
"v_accvgpr_read_b32 v22, a186\n"
"v_accvgpr_read_b32 v23, a187\n"
"v_mul_f32 v22, v22, v26\n"
"v_mul_f32 v23, v23, v26\n"
"v_accvgpr_write_b32 a186, v22\n"
"v_accvgpr_write_b32 a187, v23\n"
"v_accvgpr_read_b32 v22, a208\n"
"v_accvgpr_read_b32 v23, a209\n"
"v_mul_f32 v22, v22, v26\n"
"v_mul_f32 v23, v23, v26\n"
"v_accvgpr_write_b32 a208, v22\n"
"v_accvgpr_write_b32 a209, v23\n"
"v_accvgpr_read_b32 v22, a210\n"
"v_accvgpr_read_b32 v23, a211\n"
"v_mul_f32 v22, v22, v26\n"
"v_mul_f32 v23, v23, v26\n"
"v_accvgpr_write_b32 a210, v22\n"
"v_accvgpr_write_b32 a211, v23\n"
"v_accvgpr_read_b32 v22, a232\n"
"v_accvgpr_read_b32 v23, a233\n"
"v_mul_f32 v22, v22, v26\n"
"v_mul_f32 v23, v23, v26\n"
"v_accvgpr_write_b32 a232, v22\n"
"v_accvgpr_write_b32 a233, v23\n"
"v_accvgpr_read_b32 v22, a234\n"
"v_accvgpr_read_b32 v23, a235\n"
"v_mul_f32 v22, v22, v26\n"
"v_mul_f32 v23, v23, v26\n"
"v_accvgpr_write_b32 a234, v22\n"
"v_accvgpr_write_b32 a235, v23\n"
"v_mul_f32 v128, v128, v26\n"
"v_mul_f32 v129, v129, v26\n"
"v_mul_f32 v130, v130, v26\n"
"v_mul_f32 v131, v131, v26\n"
"v_mul_f32 v152, v152, v26\n"
"v_mul_f32 v153, v153, v26\n"
"v_mul_f32 v154, v154, v26\n"
"v_mul_f32 v155, v155, v26\n"
"v_mul_f32 v176, v176, v26\n"
"v_mul_f32 v177, v177, v26\n"
"v_mul_f32 v178, v178, v26\n"
"v_mul_f32 v179, v179, v26\n"
"v_mul_f32 v200, v200, v26\n"
"v_mul_f32 v201, v201, v26\n"
"v_mul_f32 v202, v202, v26\n"
"v_mul_f32 v203, v203, v26\n"
"v_mul_f32 v224, v224, v26\n"
"v_mul_f32 v225, v225, v26\n"
"v_mul_f32 v226, v226, v26\n"
"v_mul_f32 v227, v227, v26\n"
"v_mul_f32 v248, v248, v26\n"
"v_mul_f32 v249, v249, v26\n"
"v_mul_f32 v250, v250, v26\n"
"v_mul_f32 v251, v251, v26\n"
"v_fma_f32 v22, v48, s62, v13\n"
"v_fma_f32 v23, v49, s62, v13\n"
"v_exp_f32 v22, v22\n"
"v_exp_f32 v23, v23\n"
"s_nop 0\n"
"v_add_f32 v21, v22, v23\n"
"v_cvt_pk_bf16_f32 v68, v22, v23\n"
"v_fma_f32 v22, v50, s62, v13\n"
"v_fma_f32 v23, v51, s62, v13\n"
"v_exp_f32 v22, v22\n"
"v_exp_f32 v23, v23\n"
"s_nop 0\n"
"v_add_f32 v21, v21, v22\n"
"v_add_f32 v21, v21, v23\n"
"v_cvt_pk_bf16_f32 v69, v22, v23\n"
"v_fma_f32 v22, v52, s62, v13\n"
"v_fma_f32 v23, v53, s62, v13\n"
"v_exp_f32 v22, v22\n"
"v_exp_f32 v23, v23\n"
"s_nop 0\n"
"v_add_f32 v21, v21, v22\n"
"v_add_f32 v21, v21, v23\n"
"v_cvt_pk_bf16_f32 v70, v22, v23\n"
"v_fma_f32 v22, v54, s62, v13\n"
"v_fma_f32 v23, v55, s62, v13\n"
"v_exp_f32 v22, v22\n"
"v_exp_f32 v23, v23\n"
"s_nop 0\n"
"v_add_f32 v21, v21, v22\n"
"v_add_f32 v21, v21, v23\n"
"v_cvt_pk_bf16_f32 v71, v22, v23\n"
"v_fma_f32 v22, v56, s62, v14\n"
"v_fma_f32 v23, v57, s62, v14\n"
"s_nop 1\n"
"s_branch .Lret_5_d4\n"
".Lrare_5_d5:\n"
"s_nop 7\n"
"v_max3_f32 v25, v56, v57, v58\n"
"v_max3_f32 v26, v59, v76, v77\n"
"v_max3_f32 v25, v25, v78, v79\n"
"v_max_f32 v25, v25, v26\n"
"v_mov_b32 v26, v25\n"
"s_nop 1\n"
"v_permlane32_swap_b32 v25, v26\n"
"s_nop 1\n"
"v_max_f32 v25, v25, v26\n"
"v_mov_b32 v26, v25\n"
"s_nop 1\n"
"v_permlane16_swap_b32 v25, v26\n"
"s_nop 1\n"
"v_max_f32 v25, v25, v26\n"
"v_mul_f32 v25, s62, v25\n"
"v_sub_f32 v25, 0, v25\n"
"v_min_f32 v25, v25, v14\n"
"v_sub_f32 v26, v25, v14\n"
"v_exp_f32 v26, v26\n"
"v_mov_b32 v14, v25\n"
"s_nop 0\n"
"v_mul_f32 v20, v20, v26\n"
"v_accvgpr_read_b32 v22, a116\n"
"v_accvgpr_read_b32 v23, a117\n"
"v_mul_f32 v22, v22, v26\n"
"v_mul_f32 v23, v23, v26\n"
"v_accvgpr_write_b32 a116, v22\n"
"v_accvgpr_write_b32 a117, v23\n"
"v_accvgpr_read_b32 v22, a118\n"
"v_accvgpr_read_b32 v23, a119\n"
"v_mul_f32 v22, v22, v26\n"
"v_mul_f32 v23, v23, v26\n"
"v_accvgpr_write_b32 a118, v22\n"
"v_accvgpr_write_b32 a119, v23\n"
"v_accvgpr_read_b32 v22, a140\n"
"v_accvgpr_read_b32 v23, a141\n"
"v_mul_f32 v22, v22, v26\n"
"v_mul_f32 v23, v23, v26\n"
"v_accvgpr_write_b32 a140, v22\n"
"v_accvgpr_write_b32 a141, v23\n"
"v_accvgpr_read_b32 v22, a142\n"
"v_accvgpr_read_b32 v23, a143\n"
"v_mul_f32 v22, v22, v26\n"
"v_mul_f32 v23, v23, v26\n"
"v_accvgpr_write_b32 a142, v22\n"
"v_accvgpr_write_b32 a143, v23\n"
"v_accvgpr_read_b32 v22, a164\n"
"v_accvgpr_read_b32 v23, a165\n"
"v_mul_f32 v22, v22, v26\n"
"v_mul_f32 v23, v23, v26\n"
"v_accvgpr_write_b32 a164, v22\n"
"v_accvgpr_write_b32 a165, v23\n"
"v_accvgpr_read_b32 v22, a166\n"
"v_accvgpr_read_b32 v23, a167\n"
"v_mul_f32 v22, v22, v26\n"
"v_mul_f32 v23, v23, v26\n"
"v_accvgpr_write_b32 a166, v22\n"
"v_accvgpr_write_b32 a167, v23\n"
"v_accvgpr_read_b32 v22, a188\n"
"v_accvgpr_read_b32 v23, a189\n"
"v_mul_f32 v22, v22, v26\n"
"v_mul_f32 v23, v23, v26\n"
"v_accvgpr_write_b32 a188, v22\n"
"v_accvgpr_write_b32 a189, v23\n"
"v_accvgpr_read_b32 v22, a190\n"
"v_accvgpr_read_b32 v23, a191\n"
"v_mul_f32 v22, v22, v26\n"
"v_mul_f32 v23, v23, v26\n"
"v_accvgpr_write_b32 a190, v22\n"
"v_accvgpr_write_b32 a191, v23\n"
"v_accvgpr_read_b32 v22, a212\n"
"v_accvgpr_read_b32 v23, a213\n"
"v_mul_f32 v22, v22, v26\n"
"v_mul_f32 v23, v23, v26\n"
"v_accvgpr_write_b32 a212, v22\n"
"v_accvgpr_write_b32 a213, v23\n"
"v_accvgpr_read_b32 v22, a214\n"
"v_accvgpr_read_b32 v23, a215\n"
"v_mul_f32 v22, v22, v26\n"
"v_mul_f32 v23, v23, v26\n"
"v_accvgpr_write_b32 a214, v22\n"
"v_accvgpr_write_b32 a215, v23\n"
"v_accvgpr_read_b32 v22, a236\n"
"v_accvgpr_read_b32 v23, a237\n"
"v_mul_f32 v22, v22, v26\n"
"v_mul_f32 v23, v23, v26\n"
"v_accvgpr_write_b32 a236, v22\n"
"v_accvgpr_write_b32 a237, v23\n"
"v_accvgpr_read_b32 v22, a238\n"
"v_accvgpr_read_b32 v23, a239\n"
"v_mul_f32 v22, v22, v26\n"
"v_mul_f32 v23, v23, v26\n"
"v_accvgpr_write_b32 a238, v22\n"
"v_accvgpr_write_b32 a239, v23\n"
"v_mul_f32 v132, v132, v26\n"
"v_mul_f32 v133, v133, v26\n"
"v_mul_f32 v134, v134, v26\n"
"v_mul_f32 v135, v135, v26\n"
"v_mul_f32 v156, v156, v26\n"
"v_mul_f32 v157, v157, v26\n"
"v_mul_f32 v158, v158, v26\n"
"v_mul_f32 v159, v159, v26\n"
"v_mul_f32 v180, v180, v26\n"
"v_mul_f32 v181, v181, v26\n"
"v_mul_f32 v182, v182, v26\n"
"v_mul_f32 v183, v183, v26\n"
"v_mul_f32 v204, v204, v26\n"
"v_mul_f32 v205, v205, v26\n"
"v_mul_f32 v206, v206, v26\n"
"v_mul_f32 v207, v207, v26\n"
"v_mul_f32 v228, v228, v26\n"
"v_mul_f32 v229, v229, v26\n"
"v_mul_f32 v230, v230, v26\n"
"v_mul_f32 v231, v231, v26\n"
"v_mul_f32 v252, v252, v26\n"
"v_mul_f32 v253, v253, v26\n"
"v_mul_f32 v254, v254, v26\n"
"v_mul_f32 v255, v255, v26\n"
"v_fma_f32 v22, v56, s62, v14\n"
"v_fma_f32 v23, v57, s62, v14\n"
"v_exp_f32 v22, v22\n"
"v_exp_f32 v23, v23\n"
"s_nop 0\n"
"v_add_f32 v21, v22, v23\n"
"v_cvt_pk_bf16_f32 v48, v22, v23\n"
"v_fma_f32 v22, v58, s62, v14\n"
"v_fma_f32 v23, v59, s62, v14\n"
"v_exp_f32 v22, v22\n"
"v_exp_f32 v23, v23\n"
"s_nop 0\n"
"v_add_f32 v21, v21, v22\n"
"v_add_f32 v21, v21, v23\n"
"v_cvt_pk_bf16_f32 v49, v22, v23\n"
"v_fma_f32 v22, v76, s62, v14\n"
"v_fma_f32 v23, v77, s62, v14\n"
"v_exp_f32 v22, v22\n"
"v_exp_f32 v23, v23\n"
"s_nop 0\n"
"v_add_f32 v21, v21, v22\n"
"v_add_f32 v21, v21, v23\n"
"v_cvt_pk_bf16_f32 v50, v22, v23\n"
"v_fma_f32 v22, v78, s62, v14\n"
"v_fma_f32 v23, v79, s62, v14\n"
"v_exp_f32 v22, v22\n"
"v_exp_f32 v23, v23\n"
"s_nop 0\n"
"v_add_f32 v21, v21, v22\n"
"v_add_f32 v21, v21, v23\n"
"v_cvt_pk_bf16_f32 v51, v22, v23\n"
"s_nop 1\n"
"s_branch .Lret_5_d5\n"
".Lrare_6_a:\n"
"s_nop 7\n"
"v_max3_f32 v25, v80, v81, v82\n"
"v_max3_f32 v26, v83, v84, v85\n"
"v_max3_f32 v25, v25, v86, v87\n"
"v_max_f32 v25, v25, v26\n"
"v_mov_b32 v26, v25\n"
"s_nop 1\n"
"v_permlane32_swap_b32 v25, v26\n"
"s_nop 1\n"
"v_max_f32 v25, v25, v26\n"
"v_mov_b32 v26, v25\n"
"s_nop 1\n"
"v_permlane16_swap_b32 v25, v26\n"
"s_nop 1\n"
"v_max_f32 v25, v25, v26\n"
"v_mul_f32 v25, s62, v25\n"
"v_sub_f32 v25, 0, v25\n"
"v_min_f32 v25, v25, v9\n"
"v_sub_f32 v26, v25, v9\n"
"v_exp_f32 v26, v26\n"
"v_mov_b32 v9, v25\n"
"s_nop 0\n"
"v_mul_f32 v15, v15, v26\n"
"v_accvgpr_read_b32 v22, a96\n"
"v_accvgpr_read_b32 v23, a97\n"
"v_mul_f32 v22, v22, v26\n"
"v_mul_f32 v23, v23, v26\n"
"v_accvgpr_write_b32 a96, v22\n"
"v_accvgpr_write_b32 a97, v23\n"
"v_accvgpr_read_b32 v22, a98\n"
"v_accvgpr_read_b32 v23, a99\n"
"v_mul_f32 v22, v22, v26\n"
"v_mul_f32 v23, v23, v26\n"
"v_accvgpr_write_b32 a98, v22\n"
"v_accvgpr_write_b32 a99, v23\n"
"v_accvgpr_read_b32 v22, a120\n"
"v_accvgpr_read_b32 v23, a121\n"
"v_mul_f32 v22, v22, v26\n"
"v_mul_f32 v23, v23, v26\n"
"v_accvgpr_write_b32 a120, v22\n"
"v_accvgpr_write_b32 a121, v23\n"
"v_accvgpr_read_b32 v22, a122\n"
"v_accvgpr_read_b32 v23, a123\n"
"v_mul_f32 v22, v22, v26\n"
"v_mul_f32 v23, v23, v26\n"
"v_accvgpr_write_b32 a122, v22\n"
"v_accvgpr_write_b32 a123, v23\n"
"v_accvgpr_read_b32 v22, a144\n"
"v_accvgpr_read_b32 v23, a145\n"
"v_mul_f32 v22, v22, v26\n"
"v_mul_f32 v23, v23, v26\n"
"v_accvgpr_write_b32 a144, v22\n"
"v_accvgpr_write_b32 a145, v23\n"
"v_accvgpr_read_b32 v22, a146\n"
"v_accvgpr_read_b32 v23, a147\n"
"v_mul_f32 v22, v22, v26\n"
"v_mul_f32 v23, v23, v26\n"
"v_accvgpr_write_b32 a146, v22\n"
"v_accvgpr_write_b32 a147, v23\n"
"v_accvgpr_read_b32 v22, a168\n"
"v_accvgpr_read_b32 v23, a169\n"
"v_mul_f32 v22, v22, v26\n"
"v_mul_f32 v23, v23, v26\n"
"v_accvgpr_write_b32 a168, v22\n"
"v_accvgpr_write_b32 a169, v23\n"
"v_accvgpr_read_b32 v22, a170\n"
"v_accvgpr_read_b32 v23, a171\n"
"v_mul_f32 v22, v22, v26\n"
"v_mul_f32 v23, v23, v26\n"
"v_accvgpr_write_b32 a170, v22\n"
"v_accvgpr_write_b32 a171, v23\n"
"v_accvgpr_read_b32 v22, a192\n"
"v_accvgpr_read_b32 v23, a193\n"
"v_mul_f32 v22, v22, v26\n"
"v_mul_f32 v23, v23, v26\n"
"v_accvgpr_write_b32 a192, v22\n"
"v_accvgpr_write_b32 a193, v23\n"
"v_accvgpr_read_b32 v22, a194\n"
"v_accvgpr_read_b32 v23, a195\n"
"v_mul_f32 v22, v22, v26\n"
"v_mul_f32 v23, v23, v26\n"
"v_accvgpr_write_b32 a194, v22\n"
"v_accvgpr_write_b32 a195, v23\n"
"v_accvgpr_read_b32 v22, a216\n"
"v_accvgpr_read_b32 v23, a217\n"
"v_mul_f32 v22, v22, v26\n"
"v_mul_f32 v23, v23, v26\n"
"v_accvgpr_write_b32 a216, v22\n"
"v_accvgpr_write_b32 a217, v23\n"
"v_accvgpr_read_b32 v22, a218\n"
"v_accvgpr_read_b32 v23, a219\n"
"v_mul_f32 v22, v22, v26\n"
"v_mul_f32 v23, v23, v26\n"
"v_accvgpr_write_b32 a218, v22\n"
"v_accvgpr_write_b32 a219, v23\n"
"v_accvgpr_read_b32 v22, a240\n"
"v_accvgpr_read_b32 v23, a241\n"
"v_mul_f32 v22, v22, v26\n"
"v_mul_f32 v23, v23, v26\n"
"v_accvgpr_write_b32 a240, v22\n"
"v_accvgpr_write_b32 a241, v23\n"
"v_accvgpr_read_b32 v22, a242\n"
"v_accvgpr_read_b32 v23, a243\n"
"v_mul_f32 v22, v22, v26\n"
"v_mul_f32 v23, v23, v26\n"
"v_accvgpr_write_b32 a242, v22\n"
"v_accvgpr_write_b32 a243, v23\n"
"v_mul_f32 v136, v136, v26\n"
"v_mul_f32 v137, v137, v26\n"
"v_mul_f32 v138, v138, v26\n"
"v_mul_f32 v139, v139, v26\n"
"v_mul_f32 v160, v160, v26\n"
"v_mul_f32 v161, v161, v26\n"
"v_mul_f32 v162, v162, v26\n"
"v_mul_f32 v163, v163, v26\n"
"v_mul_f32 v184, v184, v26\n"
"v_mul_f32 v185, v185, v26\n"
"v_mul_f32 v186, v186, v26\n"
"v_mul_f32 v187, v187, v26\n"
"v_mul_f32 v208, v208, v26\n"
"v_mul_f32 v209, v209, v26\n"
"v_mul_f32 v210, v210, v26\n"
"v_mul_f32 v211, v211, v26\n"
"v_mul_f32 v232, v232, v26\n"
"v_mul_f32 v233, v233, v26\n"
"v_mul_f32 v234, v234, v26\n"
"v_mul_f32 v235, v235, v26\n"
"v_lshlrev_b32 v22, 16, v116\n"
"v_and_b32 v23, 0xffff0000, v116\n"
"v_mul_f32 v22, v22, v26\n"
"v_mul_f32 v23, v23, v26\n"
"v_cvt_pk_bf16_f32 v116, v22, v23\n"
"v_lshlrev_b32 v22, 16, v117\n"
"v_and_b32 v23, 0xffff0000, v117\n"
"v_mul_f32 v22, v22, v26\n"
"v_mul_f32 v23, v23, v26\n"
"v_cvt_pk_bf16_f32 v117, v22, v23\n"
"v_lshlrev_b32 v22, 16, v118\n"
"v_and_b32 v23, 0xffff0000, v118\n"
"v_mul_f32 v22, v22, v26\n"
"v_mul_f32 v23, v23, v26\n"
"v_cvt_pk_bf16_f32 v118, v22, v23\n"
"v_lshlrev_b32 v22, 16, v119\n"
"v_and_b32 v23, 0xffff0000, v119\n"
"v_mul_f32 v22, v22, v26\n"
"v_mul_f32 v23, v23, v26\n"
"v_cvt_pk_bf16_f32 v119, v22, v23\n"
"v_fma_f32 v22, v80, s62, v9\n"
"v_fma_f32 v23, v81, s62, v9\n"
"v_exp_f32 v22, v22\n"
"v_exp_f32 v23, v23\n"
"s_nop 0\n"
"v_add_f32 v21, v22, v23\n"
"v_cvt_pk_bf16_f32 v52, v22, v23\n"
"v_fma_f32 v22, v82, s62, v9\n"
"v_fma_f32 v23, v83, s62, v9\n"
"v_exp_f32 v22, v22\n"
"v_exp_f32 v23, v23\n"
"s_nop 0\n"
"v_add_f32 v21, v21, v22\n"
"v_add_f32 v21, v21, v23\n"
"v_cvt_pk_bf16_f32 v53, v22, v23\n"
"v_fma_f32 v22, v84, s62, v9\n"
"v_fma_f32 v23, v85, s62, v9\n"
"v_exp_f32 v22, v22\n"
"v_exp_f32 v23, v23\n"
"s_nop 0\n"
"v_add_f32 v21, v21, v22\n"
"v_add_f32 v21, v21, v23\n"
"v_cvt_pk_bf16_f32 v54, v22, v23\n"
"v_fma_f32 v22, v86, s62, v9\n"
"v_fma_f32 v23, v87, s62, v9\n"
"v_exp_f32 v22, v22\n"
"v_exp_f32 v23, v23\n"
"s_nop 0\n"
"v_add_f32 v21, v21, v22\n"
"v_add_f32 v21, v21, v23\n"
"v_cvt_pk_bf16_f32 v55, v22, v23\n"
"v_max3_f32 v25, v108, v109, v110\n"
"v_max3_f32 v26, v111, v112, v113\n"
"v_max3_f32 v25, v25, v114, v115\n"
"v_max_f32 v25, v25, v26\n"
"v_mov_b32 v26, v25\n"
"s_nop 1\n"
"v_permlane32_swap_b32 v25, v26\n"
"s_nop 1\n"
"v_max_f32 v25, v25, v26\n"
"v_mov_b32 v26, v25\n"
"s_nop 1\n"
"v_permlane16_swap_b32 v25, v26\n"
"s_nop 1\n"
"v_max_f32 v25, v25, v26\n"
"v_mul_f32 v25, s62, v25\n"
"v_sub_f32 v25, 0, v25\n"
"v_min_f32 v25, v25, v10\n"
"v_sub_f32 v26, v25, v10\n"
"v_exp_f32 v26, v26\n"
"v_mov_b32 v10, v25\n"
"s_nop 0\n"
"v_mul_f32 v16, v16, v26\n"
"v_accvgpr_read_b32 v22, a100\n"
"v_accvgpr_read_b32 v23, a101\n"
"v_mul_f32 v22, v22, v26\n"
"v_mul_f32 v23, v23, v26\n"
"v_accvgpr_write_b32 a100, v22\n"
"v_accvgpr_write_b32 a101, v23\n"
"v_accvgpr_read_b32 v22, a102\n"
"v_accvgpr_read_b32 v23, a103\n"
"v_mul_f32 v22, v22, v26\n"
"v_mul_f32 v23, v23, v26\n"
"v_accvgpr_write_b32 a102, v22\n"
"v_accvgpr_write_b32 a103, v23\n"
"v_accvgpr_read_b32 v22, a124\n"
"v_accvgpr_read_b32 v23, a125\n"
"v_mul_f32 v22, v22, v26\n"
"v_mul_f32 v23, v23, v26\n"
"v_accvgpr_write_b32 a124, v22\n"
"v_accvgpr_write_b32 a125, v23\n"
"v_accvgpr_read_b32 v22, a126\n"
"v_accvgpr_read_b32 v23, a127\n"
"v_mul_f32 v22, v22, v26\n"
"v_mul_f32 v23, v23, v26\n"
"v_accvgpr_write_b32 a126, v22\n"
"v_accvgpr_write_b32 a127, v23\n"
"v_accvgpr_read_b32 v22, a148\n"
"v_accvgpr_read_b32 v23, a149\n"
"v_mul_f32 v22, v22, v26\n"
"v_mul_f32 v23, v23, v26\n"
"v_accvgpr_write_b32 a148, v22\n"
"v_accvgpr_write_b32 a149, v23\n"
"v_accvgpr_read_b32 v22, a150\n"
"v_accvgpr_read_b32 v23, a151\n"
"v_mul_f32 v22, v22, v26\n"
"v_mul_f32 v23, v23, v26\n"
"v_accvgpr_write_b32 a150, v22\n"
"v_accvgpr_write_b32 a151, v23\n"
"v_accvgpr_read_b32 v22, a172\n"
"v_accvgpr_read_b32 v23, a173\n"
"v_mul_f32 v22, v22, v26\n"
"v_mul_f32 v23, v23, v26\n"
"v_accvgpr_write_b32 a172, v22\n"
"v_accvgpr_write_b32 a173, v23\n"
"v_accvgpr_read_b32 v22, a174\n"
"v_accvgpr_read_b32 v23, a175\n"
"v_mul_f32 v22, v22, v26\n"
"v_mul_f32 v23, v23, v26\n"
"v_accvgpr_write_b32 a174, v22\n"
"v_accvgpr_write_b32 a175, v23\n"
"v_accvgpr_read_b32 v22, a196\n"
"v_accvgpr_read_b32 v23, a197\n"
"v_mul_f32 v22, v22, v26\n"
"v_mul_f32 v23, v23, v26\n"
"v_accvgpr_write_b32 a196, v22\n"
"v_accvgpr_write_b32 a197, v23\n"
"v_accvgpr_read_b32 v22, a198\n"
"v_accvgpr_read_b32 v23, a199\n"
"v_mul_f32 v22, v22, v26\n"
"v_mul_f32 v23, v23, v26\n"
"v_accvgpr_write_b32 a198, v22\n"
"v_accvgpr_write_b32 a199, v23\n"
"v_accvgpr_read_b32 v22, a220\n"
"v_accvgpr_read_b32 v23, a221\n"
"v_mul_f32 v22, v22, v26\n"
"v_mul_f32 v23, v23, v26\n"
"v_accvgpr_write_b32 a220, v22\n"
"v_accvgpr_write_b32 a221, v23\n"
"v_accvgpr_read_b32 v22, a222\n"
"v_accvgpr_read_b32 v23, a223\n"
"v_mul_f32 v22, v22, v26\n"
"v_mul_f32 v23, v23, v26\n"
"v_accvgpr_write_b32 a222, v22\n"
"v_accvgpr_write_b32 a223, v23\n"
"v_accvgpr_read_b32 v22, a244\n"
"v_accvgpr_read_b32 v23, a245\n"
"v_mul_f32 v22, v22, v26\n"
"v_mul_f32 v23, v23, v26\n"
"v_accvgpr_write_b32 a244, v22\n"
"v_accvgpr_write_b32 a245, v23\n"
"v_accvgpr_read_b32 v22, a246\n"
"v_accvgpr_read_b32 v23, a247\n"
"v_mul_f32 v22, v22, v26\n"
"v_mul_f32 v23, v23, v26\n"
"v_accvgpr_write_b32 a246, v22\n"
"v_accvgpr_write_b32 a247, v23\n"
"v_mul_f32 v140, v140, v26\n"
"v_mul_f32 v141, v141, v26\n"
"v_mul_f32 v142, v142, v26\n"
"v_mul_f32 v143, v143, v26\n"
"v_mul_f32 v164, v164, v26\n"
"v_mul_f32 v165, v165, v26\n"
"v_mul_f32 v166, v166, v26\n"
"v_mul_f32 v167, v167, v26\n"
"v_mul_f32 v188, v188, v26\n"
"v_mul_f32 v189, v189, v26\n"
"v_mul_f32 v190, v190, v26\n"
"v_mul_f32 v191, v191, v26\n"
"v_mul_f32 v212, v212, v26\n"
"v_mul_f32 v213, v213, v26\n"
"v_mul_f32 v214, v214, v26\n"
"v_mul_f32 v215, v215, v26\n"
"v_mul_f32 v236, v236, v26\n"
"v_mul_f32 v237, v237, v26\n"
"v_mul_f32 v238, v238, v26\n"
"v_mul_f32 v239, v239, v26\n"
"v_lshlrev_b32 v22, 16, v72\n"
"v_and_b32 v23, 0xffff0000, v72\n"
"v_mul_f32 v22, v22, v26\n"
"v_mul_f32 v23, v23, v26\n"
"v_cvt_pk_bf16_f32 v72, v22, v23\n"
"v_lshlrev_b32 v22, 16, v73\n"
"v_and_b32 v23, 0xffff0000, v73\n"
"v_mul_f32 v22, v22, v26\n"
"v_mul_f32 v23, v23, v26\n"
"v_cvt_pk_bf16_f32 v73, v22, v23\n"
"v_lshlrev_b32 v22, 16, v74\n"
"v_and_b32 v23, 0xffff0000, v74\n"
"v_mul_f32 v22, v22, v26\n"
"v_mul_f32 v23, v23, v26\n"
"v_cvt_pk_bf16_f32 v74, v22, v23\n"
"v_lshlrev_b32 v22, 16, v75\n"
"v_and_b32 v23, 0xffff0000, v75\n"
"v_mul_f32 v22, v22, v26\n"
"v_mul_f32 v23, v23, v26\n"
"v_cvt_pk_bf16_f32 v75, v22, v23\n"
"v_fma_f32 v22, v108, s62, v10\n"
"v_fma_f32 v23, v109, s62, v10\n"
"v_exp_f32 v22, v22\n"
"v_exp_f32 v23, v23\n"
"s_nop 0\n"
"v_add_f32 v3, v22, v23\n"
"v_cvt_pk_bf16_f32 v56, v22, v23\n"
"v_fma_f32 v22, v110, s62, v10\n"
"v_fma_f32 v23, v111, s62, v10\n"
"v_exp_f32 v22, v22\n"
"v_exp_f32 v23, v23\n"
"s_nop 0\n"
"v_add_f32 v3, v3, v22\n"
"v_add_f32 v3, v3, v23\n"
"v_cvt_pk_bf16_f32 v57, v22, v23\n"
"v_fma_f32 v22, v112, s62, v10\n"
"v_fma_f32 v23, v113, s62, v10\n"
"v_exp_f32 v22, v22\n"
"v_exp_f32 v23, v23\n"
"s_nop 0\n"
"v_add_f32 v3, v3, v22\n"
"v_add_f32 v3, v3, v23\n"
"v_cvt_pk_bf16_f32 v58, v22, v23\n"
"v_fma_f32 v22, v114, s62, v10\n"
"v_fma_f32 v23, v115, s62, v10\n"
"v_exp_f32 v22, v22\n"
"v_exp_f32 v23, v23\n"
"s_nop 0\n"
"v_add_f32 v3, v3, v22\n"
"v_add_f32 v3, v3, v23\n"
"v_cvt_pk_bf16_f32 v59, v22, v23\n"
"v_fma_f32 v22, v60, s62, v11\n"
"v_fma_f32 v23, v61, s62, v11\n"
"s_nop 1\n"
"s_branch .Lret_6_a\n"
".Lrare_6_b:\n"
"s_nop 7\n"
"v_max3_f32 v25, v60, v61, v62\n"
"v_max3_f32 v26, v63, v120, v121\n"
"v_max3_f32 v25, v25, v122, v123\n"
"v_max_f32 v25, v25, v26\n"
"v_mov_b32 v26, v25\n"
"s_nop 1\n"
"v_permlane32_swap_b32 v25, v26\n"
"s_nop 1\n"
"v_max_f32 v25, v25, v26\n"
"v_mov_b32 v26, v25\n"
"s_nop 1\n"
"v_permlane16_swap_b32 v25, v26\n"
"s_nop 1\n"
"v_max_f32 v25, v25, v26\n"
"v_mul_f32 v25, s62, v25\n"
"v_sub_f32 v25, 0, v25\n"
"v_min_f32 v25, v25, v11\n"
"v_sub_f32 v26, v25, v11\n"
"v_exp_f32 v26, v26\n"
"v_mov_b32 v11, v25\n"
"s_nop 0\n"
"v_mul_f32 v17, v17, v26\n"
"v_accvgpr_read_b32 v22, a104\n"
"v_accvgpr_read_b32 v23, a105\n"
"v_mul_f32 v22, v22, v26\n"
"v_mul_f32 v23, v23, v26\n"
"v_accvgpr_write_b32 a104, v22\n"
"v_accvgpr_write_b32 a105, v23\n"
"v_accvgpr_read_b32 v22, a106\n"
"v_accvgpr_read_b32 v23, a107\n"
"v_mul_f32 v22, v22, v26\n"
"v_mul_f32 v23, v23, v26\n"
"v_accvgpr_write_b32 a106, v22\n"
"v_accvgpr_write_b32 a107, v23\n"
"v_accvgpr_read_b32 v22, a128\n"
"v_accvgpr_read_b32 v23, a129\n"
"v_mul_f32 v22, v22, v26\n"
"v_mul_f32 v23, v23, v26\n"
"v_accvgpr_write_b32 a128, v22\n"
"v_accvgpr_write_b32 a129, v23\n"
"v_accvgpr_read_b32 v22, a130\n"
"v_accvgpr_read_b32 v23, a131\n"
"v_mul_f32 v22, v22, v26\n"
"v_mul_f32 v23, v23, v26\n"
"v_accvgpr_write_b32 a130, v22\n"
"v_accvgpr_write_b32 a131, v23\n"
"v_accvgpr_read_b32 v22, a152\n"
"v_accvgpr_read_b32 v23, a153\n"
"v_mul_f32 v22, v22, v26\n"
"v_mul_f32 v23, v23, v26\n"
"v_accvgpr_write_b32 a152, v22\n"
"v_accvgpr_write_b32 a153, v23\n"
"v_accvgpr_read_b32 v22, a154\n"
"v_accvgpr_read_b32 v23, a155\n"
"v_mul_f32 v22, v22, v26\n"
"v_mul_f32 v23, v23, v26\n"
"v_accvgpr_write_b32 a154, v22\n"
"v_accvgpr_write_b32 a155, v23\n"
"v_accvgpr_read_b32 v22, a176\n"
"v_accvgpr_read_b32 v23, a177\n"
"v_mul_f32 v22, v22, v26\n"
"v_mul_f32 v23, v23, v26\n"
"v_accvgpr_write_b32 a176, v22\n"
"v_accvgpr_write_b32 a177, v23\n"
"v_accvgpr_read_b32 v22, a178\n"
"v_accvgpr_read_b32 v23, a179\n"
"v_mul_f32 v22, v22, v26\n"
"v_mul_f32 v23, v23, v26\n"
"v_accvgpr_write_b32 a178, v22\n"
"v_accvgpr_write_b32 a179, v23\n"
"v_accvgpr_read_b32 v22, a200\n"
"v_accvgpr_read_b32 v23, a201\n"
"v_mul_f32 v22, v22, v26\n"
"v_mul_f32 v23, v23, v26\n"
"v_accvgpr_write_b32 a200, v22\n"
"v_accvgpr_write_b32 a201, v23\n"
"v_accvgpr_read_b32 v22, a202\n"
"v_accvgpr_read_b32 v23, a203\n"
"v_mul_f32 v22, v22, v26\n"
"v_mul_f32 v23, v23, v26\n"
"v_accvgpr_write_b32 a202, v22\n"
"v_accvgpr_write_b32 a203, v23\n"
"v_accvgpr_read_b32 v22, a224\n"
"v_accvgpr_read_b32 v23, a225\n"
"v_mul_f32 v22, v22, v26\n"
"v_mul_f32 v23, v23, v26\n"
"v_accvgpr_write_b32 a224, v22\n"
"v_accvgpr_write_b32 a225, v23\n"
"v_accvgpr_read_b32 v22, a226\n"
"v_accvgpr_read_b32 v23, a227\n"
"v_mul_f32 v22, v22, v26\n"
"v_mul_f32 v23, v23, v26\n"
"v_accvgpr_write_b32 a226, v22\n"
"v_accvgpr_write_b32 a227, v23\n"
"v_accvgpr_read_b32 v22, a248\n"
"v_accvgpr_read_b32 v23, a249\n"
"v_mul_f32 v22, v22, v26\n"
"v_mul_f32 v23, v23, v26\n"
"v_accvgpr_write_b32 a248, v22\n"
"v_accvgpr_write_b32 a249, v23\n"
"v_accvgpr_read_b32 v22, a250\n"
"v_accvgpr_read_b32 v23, a251\n"
"v_mul_f32 v22, v22, v26\n"
"v_mul_f32 v23, v23, v26\n"
"v_accvgpr_write_b32 a250, v22\n"
"v_accvgpr_write_b32 a251, v23\n"
"v_mul_f32 v144, v144, v26\n"
"v_mul_f32 v145, v145, v26\n"
"v_mul_f32 v146, v146, v26\n"
"v_mul_f32 v147, v147, v26\n"
"v_mul_f32 v168, v168, v26\n"
"v_mul_f32 v169, v169, v26\n"
"v_mul_f32 v170, v170, v26\n"
"v_mul_f32 v171, v171, v26\n"
"v_mul_f32 v192, v192, v26\n"
"v_mul_f32 v193, v193, v26\n"
"v_mul_f32 v194, v194, v26\n"
"v_mul_f32 v195, v195, v26\n"
"v_mul_f32 v216, v216, v26\n"
"v_mul_f32 v217, v217, v26\n"
"v_mul_f32 v218, v218, v26\n"
"v_mul_f32 v219, v219, v26\n"
"v_mul_f32 v240, v240, v26\n"
"v_mul_f32 v241, v241, v26\n"
"v_mul_f32 v242, v242, v26\n"
"v_mul_f32 v243, v243, v26\n"
"v_lshlrev_b32 v22, 16, v28\n"
"v_and_b32 v23, 0xffff0000, v28\n"
"v_mul_f32 v22, v22, v26\n"
"v_mul_f32 v23, v23, v26\n"
"v_cvt_pk_bf16_f32 v28, v22, v23\n"
"v_lshlrev_b32 v22, 16, v29\n"
"v_and_b32 v23, 0xffff0000, v29\n"
"v_mul_f32 v22, v22, v26\n"
"v_mul_f32 v23, v23, v26\n"
"v_cvt_pk_bf16_f32 v29, v22, v23\n"
"v_lshlrev_b32 v22, 16, v30\n"
"v_and_b32 v23, 0xffff0000, v30\n"
"v_mul_f32 v22, v22, v26\n"
"v_mul_f32 v23, v23, v26\n"
"v_cvt_pk_bf16_f32 v30, v22, v23\n"
"v_lshlrev_b32 v22, 16, v31\n"
"v_and_b32 v23, 0xffff0000, v31\n"
"v_mul_f32 v22, v22, v26\n"
"v_mul_f32 v23, v23, v26\n"
"v_cvt_pk_bf16_f32 v31, v22, v23\n"
"v_fma_f32 v22, v60, s62, v11\n"
"v_fma_f32 v23, v61, s62, v11\n"
"v_exp_f32 v22, v22\n"
"v_exp_f32 v23, v23\n"
"s_nop 0\n"
"v_add_f32 v84, v22, v23\n"
"v_cvt_pk_bf16_f32 v76, v22, v23\n"
"v_fma_f32 v22, v62, s62, v11\n"
"v_fma_f32 v23, v63, s62, v11\n"
"v_exp_f32 v22, v22\n"
"v_exp_f32 v23, v23\n"
"s_nop 0\n"
"v_add_f32 v84, v84, v22\n"
"v_add_f32 v84, v84, v23\n"
"v_cvt_pk_bf16_f32 v77, v22, v23\n"
"v_fma_f32 v22, v120, s62, v11\n"
"v_fma_f32 v23, v121, s62, v11\n"
"v_exp_f32 v22, v22\n"
"v_exp_f32 v23, v23\n"
"s_nop 0\n"
"v_add_f32 v84, v84, v22\n"
"v_add_f32 v84, v84, v23\n"
"v_cvt_pk_bf16_f32 v78, v22, v23\n"
"v_fma_f32 v22, v122, s62, v11\n"
"v_fma_f32 v23, v123, s62, v11\n"
"v_exp_f32 v22, v22\n"
"v_exp_f32 v23, v23\n"
"s_nop 0\n"
"v_add_f32 v84, v84, v22\n"
"v_add_f32 v84, v84, v23\n"
"v_cvt_pk_bf16_f32 v79, v22, v23\n"
"v_max3_f32 v25, v92, v93, v94\n"
"v_max3_f32 v26, v95, v64, v65\n"
"v_max3_f32 v25, v25, v66, v67\n"
"v_max_f32 v25, v25, v26\n"
"v_mov_b32 v26, v25\n"
"s_nop 1\n"
"v_permlane32_swap_b32 v25, v26\n"
"s_nop 1\n"
"v_max_f32 v25, v25, v26\n"
"v_mov_b32 v26, v25\n"
"s_nop 1\n"
"v_permlane16_swap_b32 v25, v26\n"
"s_nop 1\n"
"v_max_f32 v25, v25, v26\n"
"v_mul_f32 v25, s62, v25\n"
"v_sub_f32 v25, 0, v25\n"
"v_min_f32 v25, v25, v12\n"
"v_sub_f32 v26, v25, v12\n"
"v_exp_f32 v26, v26\n"
"v_mov_b32 v12, v25\n"
"s_nop 0\n"
"v_mul_f32 v18, v18, v26\n"
"v_accvgpr_read_b32 v22, a108\n"
"v_accvgpr_read_b32 v23, a109\n"
"v_mul_f32 v22, v22, v26\n"
"v_mul_f32 v23, v23, v26\n"
"v_accvgpr_write_b32 a108, v22\n"
"v_accvgpr_write_b32 a109, v23\n"
"v_accvgpr_read_b32 v22, a110\n"
"v_accvgpr_read_b32 v23, a111\n"
"v_mul_f32 v22, v22, v26\n"
"v_mul_f32 v23, v23, v26\n"
"v_accvgpr_write_b32 a110, v22\n"
"v_accvgpr_write_b32 a111, v23\n"
"v_accvgpr_read_b32 v22, a132\n"
"v_accvgpr_read_b32 v23, a133\n"
"v_mul_f32 v22, v22, v26\n"
"v_mul_f32 v23, v23, v26\n"
"v_accvgpr_write_b32 a132, v22\n"
"v_accvgpr_write_b32 a133, v23\n"
"v_accvgpr_read_b32 v22, a134\n"
"v_accvgpr_read_b32 v23, a135\n"
"v_mul_f32 v22, v22, v26\n"
"v_mul_f32 v23, v23, v26\n"
"v_accvgpr_write_b32 a134, v22\n"
"v_accvgpr_write_b32 a135, v23\n"
"v_accvgpr_read_b32 v22, a156\n"
"v_accvgpr_read_b32 v23, a157\n"
"v_mul_f32 v22, v22, v26\n"
"v_mul_f32 v23, v23, v26\n"
"v_accvgpr_write_b32 a156, v22\n"
"v_accvgpr_write_b32 a157, v23\n"
"v_accvgpr_read_b32 v22, a158\n"
"v_accvgpr_read_b32 v23, a159\n"
"v_mul_f32 v22, v22, v26\n"
"v_mul_f32 v23, v23, v26\n"
"v_accvgpr_write_b32 a158, v22\n"
"v_accvgpr_write_b32 a159, v23\n"
"v_accvgpr_read_b32 v22, a180\n"
"v_accvgpr_read_b32 v23, a181\n"
"v_mul_f32 v22, v22, v26\n"
"v_mul_f32 v23, v23, v26\n"
"v_accvgpr_write_b32 a180, v22\n"
"v_accvgpr_write_b32 a181, v23\n"
"v_accvgpr_read_b32 v22, a182\n"
"v_accvgpr_read_b32 v23, a183\n"
"v_mul_f32 v22, v22, v26\n"
"v_mul_f32 v23, v23, v26\n"
"v_accvgpr_write_b32 a182, v22\n"
"v_accvgpr_write_b32 a183, v23\n"
"v_accvgpr_read_b32 v22, a204\n"
"v_accvgpr_read_b32 v23, a205\n"
"v_mul_f32 v22, v22, v26\n"
"v_mul_f32 v23, v23, v26\n"
"v_accvgpr_write_b32 a204, v22\n"
"v_accvgpr_write_b32 a205, v23\n"
"v_accvgpr_read_b32 v22, a206\n"
"v_accvgpr_read_b32 v23, a207\n"
"v_mul_f32 v22, v22, v26\n"
"v_mul_f32 v23, v23, v26\n"
"v_accvgpr_write_b32 a206, v22\n"
"v_accvgpr_write_b32 a207, v23\n"
"v_accvgpr_read_b32 v22, a228\n"
"v_accvgpr_read_b32 v23, a229\n"
"v_mul_f32 v22, v22, v26\n"
"v_mul_f32 v23, v23, v26\n"
"v_accvgpr_write_b32 a228, v22\n"
"v_accvgpr_write_b32 a229, v23\n"
"v_accvgpr_read_b32 v22, a230\n"
"v_accvgpr_read_b32 v23, a231\n"
"v_mul_f32 v22, v22, v26\n"
"v_mul_f32 v23, v23, v26\n"
"v_accvgpr_write_b32 a230, v22\n"
"v_accvgpr_write_b32 a231, v23\n"
"v_accvgpr_read_b32 v22, a252\n"
"v_accvgpr_read_b32 v23, a253\n"
"v_mul_f32 v22, v22, v26\n"
"v_mul_f32 v23, v23, v26\n"
"v_accvgpr_write_b32 a252, v22\n"
"v_accvgpr_write_b32 a253, v23\n"
"v_accvgpr_read_b32 v22, a254\n"
"v_accvgpr_read_b32 v23, a255\n"
"v_mul_f32 v22, v22, v26\n"
"v_mul_f32 v23, v23, v26\n"
"v_accvgpr_write_b32 a254, v22\n"
"v_accvgpr_write_b32 a255, v23\n"
"v_mul_f32 v148, v148, v26\n"
"v_mul_f32 v149, v149, v26\n"
"v_mul_f32 v150, v150, v26\n"
"v_mul_f32 v151, v151, v26\n"
"v_mul_f32 v172, v172, v26\n"
"v_mul_f32 v173, v173, v26\n"
"v_mul_f32 v174, v174, v26\n"
"v_mul_f32 v175, v175, v26\n"
"v_mul_f32 v196, v196, v26\n"
"v_mul_f32 v197, v197, v26\n"
"v_mul_f32 v198, v198, v26\n"
"v_mul_f32 v199, v199, v26\n"
"v_mul_f32 v220, v220, v26\n"
"v_mul_f32 v221, v221, v26\n"
"v_mul_f32 v222, v222, v26\n"
"v_mul_f32 v223, v223, v26\n"
"v_mul_f32 v244, v244, v26\n"
"v_mul_f32 v245, v245, v26\n"
"v_mul_f32 v246, v246, v26\n"
"v_mul_f32 v247, v247, v26\n"
"v_lshlrev_b32 v22, 16, v88\n"
"v_and_b32 v23, 0xffff0000, v88\n"
"v_mul_f32 v22, v22, v26\n"
"v_mul_f32 v23, v23, v26\n"
"v_cvt_pk_bf16_f32 v88, v22, v23\n"
"v_lshlrev_b32 v22, 16, v89\n"
"v_and_b32 v23, 0xffff0000, v89\n"
"v_mul_f32 v22, v22, v26\n"
"v_mul_f32 v23, v23, v26\n"
"v_cvt_pk_bf16_f32 v89, v22, v23\n"
"v_lshlrev_b32 v22, 16, v90\n"
"v_and_b32 v23, 0xffff0000, v90\n"
"v_mul_f32 v22, v22, v26\n"
"v_mul_f32 v23, v23, v26\n"
"v_cvt_pk_bf16_f32 v90, v22, v23\n"
"v_lshlrev_b32 v22, 16, v91\n"
"v_and_b32 v23, 0xffff0000, v91\n"
"v_mul_f32 v22, v22, v26\n"
"v_mul_f32 v23, v23, v26\n"
"v_cvt_pk_bf16_f32 v91, v22, v23\n"
"v_fma_f32 v22, v92, s62, v12\n"
"v_fma_f32 v23, v93, s62, v12\n"
"v_exp_f32 v22, v22\n"
"v_exp_f32 v23, v23\n"
"s_nop 0\n"
"v_add_f32 v85, v22, v23\n"
"v_cvt_pk_bf16_f32 v80, v22, v23\n"
"v_fma_f32 v22, v94, s62, v12\n"
"v_fma_f32 v23, v95, s62, v12\n"
"v_exp_f32 v22, v22\n"
"v_exp_f32 v23, v23\n"
"s_nop 0\n"
"v_add_f32 v85, v85, v22\n"
"v_add_f32 v85, v85, v23\n"
"v_cvt_pk_bf16_f32 v81, v22, v23\n"
"v_fma_f32 v22, v64, s62, v12\n"
"v_fma_f32 v23, v65, s62, v12\n"
"v_exp_f32 v22, v22\n"
"v_exp_f32 v23, v23\n"
"s_nop 0\n"
"v_add_f32 v85, v85, v22\n"
"v_add_f32 v85, v85, v23\n"
"v_cvt_pk_bf16_f32 v82, v22, v23\n"
"v_fma_f32 v22, v66, s62, v12\n"
"v_fma_f32 v23, v67, s62, v12\n"
"v_exp_f32 v22, v22\n"
"v_exp_f32 v23, v23\n"
"s_nop 0\n"
"v_add_f32 v85, v85, v22\n"
"v_add_f32 v85, v85, v23\n"
"v_cvt_pk_bf16_f32 v83, v22, v23\n"
"v_fma_f32 v22, v124, s62, v13\n"
"v_fma_f32 v23, v125, s62, v13\n"
"s_nop 1\n"
"s_branch .Lret_6_b\n"
".Lrare_6_d4:\n"
"s_nop 7\n"
"v_max3_f32 v25, v124, v125, v126\n"
"v_max3_f32 v26, v127, v96, v97\n"
"v_max3_f32 v25, v25, v98, v99\n"
"v_max_f32 v25, v25, v26\n"
"v_mov_b32 v26, v25\n"
"s_nop 1\n"
"v_permlane32_swap_b32 v25, v26\n"
"s_nop 1\n"
"v_max_f32 v25, v25, v26\n"
"v_mov_b32 v26, v25\n"
"s_nop 1\n"
"v_permlane16_swap_b32 v25, v26\n"
"s_nop 1\n"
"v_max_f32 v25, v25, v26\n"
"v_mul_f32 v25, s62, v25\n"
"v_sub_f32 v25, 0, v25\n"
"v_min_f32 v25, v25, v13\n"
"v_sub_f32 v26, v25, v13\n"
"v_exp_f32 v26, v26\n"
"v_mov_b32 v13, v25\n"
"s_nop 0\n"
"v_mul_f32 v19, v19, v26\n"
"v_accvgpr_read_b32 v22, a112\n"
"v_accvgpr_read_b32 v23, a113\n"
"v_mul_f32 v22, v22, v26\n"
"v_mul_f32 v23, v23, v26\n"
"v_accvgpr_write_b32 a112, v22\n"
"v_accvgpr_write_b32 a113, v23\n"
"v_accvgpr_read_b32 v22, a114\n"
"v_accvgpr_read_b32 v23, a115\n"
"v_mul_f32 v22, v22, v26\n"
"v_mul_f32 v23, v23, v26\n"
"v_accvgpr_write_b32 a114, v22\n"
"v_accvgpr_write_b32 a115, v23\n"
"v_accvgpr_read_b32 v22, a136\n"
"v_accvgpr_read_b32 v23, a137\n"
"v_mul_f32 v22, v22, v26\n"
"v_mul_f32 v23, v23, v26\n"
"v_accvgpr_write_b32 a136, v22\n"
"v_accvgpr_write_b32 a137, v23\n"
"v_accvgpr_read_b32 v22, a138\n"
"v_accvgpr_read_b32 v23, a139\n"
"v_mul_f32 v22, v22, v26\n"
"v_mul_f32 v23, v23, v26\n"
"v_accvgpr_write_b32 a138, v22\n"
"v_accvgpr_write_b32 a139, v23\n"
"v_accvgpr_read_b32 v22, a160\n"
"v_accvgpr_read_b32 v23, a161\n"
"v_mul_f32 v22, v22, v26\n"
"v_mul_f32 v23, v23, v26\n"
"v_accvgpr_write_b32 a160, v22\n"
"v_accvgpr_write_b32 a161, v23\n"
"v_accvgpr_read_b32 v22, a162\n"
"v_accvgpr_read_b32 v23, a163\n"
"v_mul_f32 v22, v22, v26\n"
"v_mul_f32 v23, v23, v26\n"
"v_accvgpr_write_b32 a162, v22\n"
"v_accvgpr_write_b32 a163, v23\n"
"v_accvgpr_read_b32 v22, a184\n"
"v_accvgpr_read_b32 v23, a185\n"
"v_mul_f32 v22, v22, v26\n"
"v_mul_f32 v23, v23, v26\n"
"v_accvgpr_write_b32 a184, v22\n"
"v_accvgpr_write_b32 a185, v23\n"
"v_accvgpr_read_b32 v22, a186\n"
"v_accvgpr_read_b32 v23, a187\n"
"v_mul_f32 v22, v22, v26\n"
"v_mul_f32 v23, v23, v26\n"
"v_accvgpr_write_b32 a186, v22\n"
"v_accvgpr_write_b32 a187, v23\n"
"v_accvgpr_read_b32 v22, a208\n"
"v_accvgpr_read_b32 v23, a209\n"
"v_mul_f32 v22, v22, v26\n"
"v_mul_f32 v23, v23, v26\n"
"v_accvgpr_write_b32 a208, v22\n"
"v_accvgpr_write_b32 a209, v23\n"
"v_accvgpr_read_b32 v22, a210\n"
"v_accvgpr_read_b32 v23, a211\n"
"v_mul_f32 v22, v22, v26\n"
"v_mul_f32 v23, v23, v26\n"
"v_accvgpr_write_b32 a210, v22\n"
"v_accvgpr_write_b32 a211, v23\n"
"v_accvgpr_read_b32 v22, a232\n"
"v_accvgpr_read_b32 v23, a233\n"
"v_mul_f32 v22, v22, v26\n"
"v_mul_f32 v23, v23, v26\n"
"v_accvgpr_write_b32 a232, v22\n"
"v_accvgpr_write_b32 a233, v23\n"
"v_accvgpr_read_b32 v22, a234\n"
"v_accvgpr_read_b32 v23, a235\n"
"v_mul_f32 v22, v22, v26\n"
"v_mul_f32 v23, v23, v26\n"
"v_accvgpr_write_b32 a234, v22\n"
"v_accvgpr_write_b32 a235, v23\n"
"v_mul_f32 v128, v128, v26\n"
"v_mul_f32 v129, v129, v26\n"
"v_mul_f32 v130, v130, v26\n"
"v_mul_f32 v131, v131, v26\n"
"v_mul_f32 v152, v152, v26\n"
"v_mul_f32 v153, v153, v26\n"
"v_mul_f32 v154, v154, v26\n"
"v_mul_f32 v155, v155, v26\n"
"v_mul_f32 v176, v176, v26\n"
"v_mul_f32 v177, v177, v26\n"
"v_mul_f32 v178, v178, v26\n"
"v_mul_f32 v179, v179, v26\n"
"v_mul_f32 v200, v200, v26\n"
"v_mul_f32 v201, v201, v26\n"
"v_mul_f32 v202, v202, v26\n"
"v_mul_f32 v203, v203, v26\n"
"v_mul_f32 v224, v224, v26\n"
"v_mul_f32 v225, v225, v26\n"
"v_mul_f32 v226, v226, v26\n"
"v_mul_f32 v227, v227, v26\n"
"v_mul_f32 v248, v248, v26\n"
"v_mul_f32 v249, v249, v26\n"
"v_mul_f32 v250, v250, v26\n"
"v_mul_f32 v251, v251, v26\n"
"v_fma_f32 v22, v124, s62, v13\n"
"v_fma_f32 v23, v125, s62, v13\n"
"v_exp_f32 v22, v22\n"
"v_exp_f32 v23, v23\n"
"s_nop 0\n"
"v_add_f32 v21, v22, v23\n"
"v_cvt_pk_bf16_f32 v48, v22, v23\n"
"v_fma_f32 v22, v126, s62, v13\n"
"v_fma_f32 v23, v127, s62, v13\n"
"v_exp_f32 v22, v22\n"
"v_exp_f32 v23, v23\n"
"s_nop 0\n"
"v_add_f32 v21, v21, v22\n"
"v_add_f32 v21, v21, v23\n"
"v_cvt_pk_bf16_f32 v49, v22, v23\n"
"v_fma_f32 v22, v96, s62, v13\n"
"v_fma_f32 v23, v97, s62, v13\n"
"v_exp_f32 v22, v22\n"
"v_exp_f32 v23, v23\n"
"s_nop 0\n"
"v_add_f32 v21, v21, v22\n"
"v_add_f32 v21, v21, v23\n"
"v_cvt_pk_bf16_f32 v50, v22, v23\n"
"v_fma_f32 v22, v98, s62, v13\n"
"v_fma_f32 v23, v99, s62, v13\n"
"v_exp_f32 v22, v22\n"
"v_exp_f32 v23, v23\n"
"s_nop 0\n"
"v_add_f32 v21, v21, v22\n"
"v_add_f32 v21, v21, v23\n"
"v_cvt_pk_bf16_f32 v51, v22, v23\n"
"v_fma_f32 v22, v104, s62, v14\n"
"v_fma_f32 v23, v105, s62, v14\n"
"s_nop 1\n"
"s_branch .Lret_6_d4\n"
".Lrare_6_d5:\n"
"s_nop 7\n"
"v_max3_f32 v25, v104, v105, v106\n"
"v_max3_f32 v26, v107, v100, v101\n"
"v_max3_f32 v25, v25, v102, v103\n"
"v_max_f32 v25, v25, v26\n"
"v_mov_b32 v26, v25\n"
"s_nop 1\n"
"v_permlane32_swap_b32 v25, v26\n"
"s_nop 1\n"
"v_max_f32 v25, v25, v26\n"
"v_mov_b32 v26, v25\n"
"s_nop 1\n"
"v_permlane16_swap_b32 v25, v26\n"
"s_nop 1\n"
"v_max_f32 v25, v25, v26\n"
"v_mul_f32 v25, s62, v25\n"
"v_sub_f32 v25, 0, v25\n"
"v_min_f32 v25, v25, v14\n"
"v_sub_f32 v26, v25, v14\n"
"v_exp_f32 v26, v26\n"
"v_mov_b32 v14, v25\n"
"s_nop 0\n"
"v_mul_f32 v20, v20, v26\n"
"v_accvgpr_read_b32 v22, a116\n"
"v_accvgpr_read_b32 v23, a117\n"
"v_mul_f32 v22, v22, v26\n"
"v_mul_f32 v23, v23, v26\n"
"v_accvgpr_write_b32 a116, v22\n"
"v_accvgpr_write_b32 a117, v23\n"
"v_accvgpr_read_b32 v22, a118\n"
"v_accvgpr_read_b32 v23, a119\n"
"v_mul_f32 v22, v22, v26\n"
"v_mul_f32 v23, v23, v26\n"
"v_accvgpr_write_b32 a118, v22\n"
"v_accvgpr_write_b32 a119, v23\n"
"v_accvgpr_read_b32 v22, a140\n"
"v_accvgpr_read_b32 v23, a141\n"
"v_mul_f32 v22, v22, v26\n"
"v_mul_f32 v23, v23, v26\n"
"v_accvgpr_write_b32 a140, v22\n"
"v_accvgpr_write_b32 a141, v23\n"
"v_accvgpr_read_b32 v22, a142\n"
"v_accvgpr_read_b32 v23, a143\n"
"v_mul_f32 v22, v22, v26\n"
"v_mul_f32 v23, v23, v26\n"
"v_accvgpr_write_b32 a142, v22\n"
"v_accvgpr_write_b32 a143, v23\n"
"v_accvgpr_read_b32 v22, a164\n"
"v_accvgpr_read_b32 v23, a165\n"
"v_mul_f32 v22, v22, v26\n"
"v_mul_f32 v23, v23, v26\n"
"v_accvgpr_write_b32 a164, v22\n"
"v_accvgpr_write_b32 a165, v23\n"
"v_accvgpr_read_b32 v22, a166\n"
"v_accvgpr_read_b32 v23, a167\n"
"v_mul_f32 v22, v22, v26\n"
"v_mul_f32 v23, v23, v26\n"
"v_accvgpr_write_b32 a166, v22\n"
"v_accvgpr_write_b32 a167, v23\n"
"v_accvgpr_read_b32 v22, a188\n"
"v_accvgpr_read_b32 v23, a189\n"
"v_mul_f32 v22, v22, v26\n"
"v_mul_f32 v23, v23, v26\n"
"v_accvgpr_write_b32 a188, v22\n"
"v_accvgpr_write_b32 a189, v23\n"
"v_accvgpr_read_b32 v22, a190\n"
"v_accvgpr_read_b32 v23, a191\n"
"v_mul_f32 v22, v22, v26\n"
"v_mul_f32 v23, v23, v26\n"
"v_accvgpr_write_b32 a190, v22\n"
"v_accvgpr_write_b32 a191, v23\n"
"v_accvgpr_read_b32 v22, a212\n"
"v_accvgpr_read_b32 v23, a213\n"
"v_mul_f32 v22, v22, v26\n"
"v_mul_f32 v23, v23, v26\n"
"v_accvgpr_write_b32 a212, v22\n"
"v_accvgpr_write_b32 a213, v23\n"
"v_accvgpr_read_b32 v22, a214\n"
"v_accvgpr_read_b32 v23, a215\n"
"v_mul_f32 v22, v22, v26\n"
"v_mul_f32 v23, v23, v26\n"
"v_accvgpr_write_b32 a214, v22\n"
"v_accvgpr_write_b32 a215, v23\n"
"v_accvgpr_read_b32 v22, a236\n"
"v_accvgpr_read_b32 v23, a237\n"
"v_mul_f32 v22, v22, v26\n"
"v_mul_f32 v23, v23, v26\n"
"v_accvgpr_write_b32 a236, v22\n"
"v_accvgpr_write_b32 a237, v23\n"
"v_accvgpr_read_b32 v22, a238\n"
"v_accvgpr_read_b32 v23, a239\n"
"v_mul_f32 v22, v22, v26\n"
"v_mul_f32 v23, v23, v26\n"
"v_accvgpr_write_b32 a238, v22\n"
"v_accvgpr_write_b32 a239, v23\n"
"v_mul_f32 v132, v132, v26\n"
"v_mul_f32 v133, v133, v26\n"
"v_mul_f32 v134, v134, v26\n"
"v_mul_f32 v135, v135, v26\n"
"v_mul_f32 v156, v156, v26\n"
"v_mul_f32 v157, v157, v26\n"
"v_mul_f32 v158, v158, v26\n"
"v_mul_f32 v159, v159, v26\n"
"v_mul_f32 v180, v180, v26\n"
"v_mul_f32 v181, v181, v26\n"
"v_mul_f32 v182, v182, v26\n"
"v_mul_f32 v183, v183, v26\n"
"v_mul_f32 v204, v204, v26\n"
"v_mul_f32 v205, v205, v26\n"
"v_mul_f32 v206, v206, v26\n"
"v_mul_f32 v207, v207, v26\n"
"v_mul_f32 v228, v228, v26\n"
"v_mul_f32 v229, v229, v26\n"
"v_mul_f32 v230, v230, v26\n"
"v_mul_f32 v231, v231, v26\n"
"v_mul_f32 v252, v252, v26\n"
"v_mul_f32 v253, v253, v26\n"
"v_mul_f32 v254, v254, v26\n"
"v_mul_f32 v255, v255, v26\n"
"v_fma_f32 v22, v104, s62, v14\n"
"v_fma_f32 v23, v105, s62, v14\n"
"v_exp_f32 v22, v22\n"
"v_exp_f32 v23, v23\n"
"s_nop 0\n"
"v_add_f32 v21, v22, v23\n"
"v_cvt_pk_bf16_f32 v124, v22, v23\n"
"v_fma_f32 v22, v106, s62, v14\n"
"v_fma_f32 v23, v107, s62, v14\n"
"v_exp_f32 v22, v22\n"
"v_exp_f32 v23, v23\n"
"s_nop 0\n"
"v_add_f32 v21, v21, v22\n"
"v_add_f32 v21, v21, v23\n"
"v_cvt_pk_bf16_f32 v125, v22, v23\n"
"v_fma_f32 v22, v100, s62, v14\n"
"v_fma_f32 v23, v101, s62, v14\n"
"v_exp_f32 v22, v22\n"
"v_exp_f32 v23, v23\n"
"s_nop 0\n"
"v_add_f32 v21, v21, v22\n"
"v_add_f32 v21, v21, v23\n"
"v_cvt_pk_bf16_f32 v126, v22, v23\n"
"v_fma_f32 v22, v102, s62, v14\n"
"v_fma_f32 v23, v103, s62, v14\n"
"v_exp_f32 v22, v22\n"
"v_exp_f32 v23, v23\n"
"s_nop 0\n"
"v_add_f32 v21, v21, v22\n"
"v_add_f32 v21, v21, v23\n"
"v_cvt_pk_bf16_f32 v127, v22, v23\n"
"s_nop 1\n"
"s_branch .Lret_6_d5\n"
".Lrare_7_a:\n"
"s_nop 7\n"
"v_max3_f32 v25, v108, v109, v110\n"
"v_max3_f32 v26, v111, v64, v65\n"
"v_max3_f32 v25, v25, v66, v67\n"
"v_max_f32 v25, v25, v26\n"
"v_mov_b32 v26, v25\n"
"s_nop 1\n"
"v_permlane32_swap_b32 v25, v26\n"
"s_nop 1\n"
"v_max_f32 v25, v25, v26\n"
"v_mov_b32 v26, v25\n"
"s_nop 1\n"
"v_permlane16_swap_b32 v25, v26\n"
"s_nop 1\n"
"v_max_f32 v25, v25, v26\n"
"v_mul_f32 v25, s62, v25\n"
"v_sub_f32 v25, 0, v25\n"
"v_min_f32 v25, v25, v9\n"
"v_sub_f32 v26, v25, v9\n"
"v_exp_f32 v26, v26\n"
"v_mov_b32 v9, v25\n"
"s_nop 0\n"
"v_mul_f32 v15, v15, v26\n"
"v_accvgpr_read_b32 v22, a96\n"
"v_accvgpr_read_b32 v23, a97\n"
"v_mul_f32 v22, v22, v26\n"
"v_mul_f32 v23, v23, v26\n"
"v_accvgpr_write_b32 a96, v22\n"
"v_accvgpr_write_b32 a97, v23\n"
"v_accvgpr_read_b32 v22, a98\n"
"v_accvgpr_read_b32 v23, a99\n"
"v_mul_f32 v22, v22, v26\n"
"v_mul_f32 v23, v23, v26\n"
"v_accvgpr_write_b32 a98, v22\n"
"v_accvgpr_write_b32 a99, v23\n"
"v_accvgpr_read_b32 v22, a120\n"
"v_accvgpr_read_b32 v23, a121\n"
"v_mul_f32 v22, v22, v26\n"
"v_mul_f32 v23, v23, v26\n"
"v_accvgpr_write_b32 a120, v22\n"
"v_accvgpr_write_b32 a121, v23\n"
"v_accvgpr_read_b32 v22, a122\n"
"v_accvgpr_read_b32 v23, a123\n"
"v_mul_f32 v22, v22, v26\n"
"v_mul_f32 v23, v23, v26\n"
"v_accvgpr_write_b32 a122, v22\n"
"v_accvgpr_write_b32 a123, v23\n"
"v_accvgpr_read_b32 v22, a144\n"
"v_accvgpr_read_b32 v23, a145\n"
"v_mul_f32 v22, v22, v26\n"
"v_mul_f32 v23, v23, v26\n"
"v_accvgpr_write_b32 a144, v22\n"
"v_accvgpr_write_b32 a145, v23\n"
"v_accvgpr_read_b32 v22, a146\n"
"v_accvgpr_read_b32 v23, a147\n"
"v_mul_f32 v22, v22, v26\n"
"v_mul_f32 v23, v23, v26\n"
"v_accvgpr_write_b32 a146, v22\n"
"v_accvgpr_write_b32 a147, v23\n"
"v_accvgpr_read_b32 v22, a168\n"
"v_accvgpr_read_b32 v23, a169\n"
"v_mul_f32 v22, v22, v26\n"
"v_mul_f32 v23, v23, v26\n"
"v_accvgpr_write_b32 a168, v22\n"
"v_accvgpr_write_b32 a169, v23\n"
"v_accvgpr_read_b32 v22, a170\n"
"v_accvgpr_read_b32 v23, a171\n"
"v_mul_f32 v22, v22, v26\n"
"v_mul_f32 v23, v23, v26\n"
"v_accvgpr_write_b32 a170, v22\n"
"v_accvgpr_write_b32 a171, v23\n"
"v_accvgpr_read_b32 v22, a192\n"
"v_accvgpr_read_b32 v23, a193\n"
"v_mul_f32 v22, v22, v26\n"
"v_mul_f32 v23, v23, v26\n"
"v_accvgpr_write_b32 a192, v22\n"
"v_accvgpr_write_b32 a193, v23\n"
"v_accvgpr_read_b32 v22, a194\n"
"v_accvgpr_read_b32 v23, a195\n"
"v_mul_f32 v22, v22, v26\n"
"v_mul_f32 v23, v23, v26\n"
"v_accvgpr_write_b32 a194, v22\n"
"v_accvgpr_write_b32 a195, v23\n"
"v_accvgpr_read_b32 v22, a216\n"
"v_accvgpr_read_b32 v23, a217\n"
"v_mul_f32 v22, v22, v26\n"
"v_mul_f32 v23, v23, v26\n"
"v_accvgpr_write_b32 a216, v22\n"
"v_accvgpr_write_b32 a217, v23\n"
"v_accvgpr_read_b32 v22, a218\n"
"v_accvgpr_read_b32 v23, a219\n"
"v_mul_f32 v22, v22, v26\n"
"v_mul_f32 v23, v23, v26\n"
"v_accvgpr_write_b32 a218, v22\n"
"v_accvgpr_write_b32 a219, v23\n"
"v_accvgpr_read_b32 v22, a240\n"
"v_accvgpr_read_b32 v23, a241\n"
"v_mul_f32 v22, v22, v26\n"
"v_mul_f32 v23, v23, v26\n"
"v_accvgpr_write_b32 a240, v22\n"
"v_accvgpr_write_b32 a241, v23\n"
"v_accvgpr_read_b32 v22, a242\n"
"v_accvgpr_read_b32 v23, a243\n"
"v_mul_f32 v22, v22, v26\n"
"v_mul_f32 v23, v23, v26\n"
"v_accvgpr_write_b32 a242, v22\n"
"v_accvgpr_write_b32 a243, v23\n"
"v_mul_f32 v136, v136, v26\n"
"v_mul_f32 v137, v137, v26\n"
"v_mul_f32 v138, v138, v26\n"
"v_mul_f32 v139, v139, v26\n"
"v_mul_f32 v160, v160, v26\n"
"v_mul_f32 v161, v161, v26\n"
"v_mul_f32 v162, v162, v26\n"
"v_mul_f32 v163, v163, v26\n"
"v_mul_f32 v184, v184, v26\n"
"v_mul_f32 v185, v185, v26\n"
"v_mul_f32 v186, v186, v26\n"
"v_mul_f32 v187, v187, v26\n"
"v_mul_f32 v208, v208, v26\n"
"v_mul_f32 v209, v209, v26\n"
"v_mul_f32 v210, v210, v26\n"
"v_mul_f32 v211, v211, v26\n"
"v_mul_f32 v232, v232, v26\n"
"v_mul_f32 v233, v233, v26\n"
"v_mul_f32 v234, v234, v26\n"
"v_mul_f32 v235, v235, v26\n"
"v_lshlrev_b32 v22, 16, v52\n"
"v_and_b32 v23, 0xffff0000, v52\n"
"v_mul_f32 v22, v22, v26\n"
"v_mul_f32 v23, v23, v26\n"
"v_cvt_pk_bf16_f32 v52, v22, v23\n"
"v_lshlrev_b32 v22, 16, v53\n"
"v_and_b32 v23, 0xffff0000, v53\n"
"v_mul_f32 v22, v22, v26\n"
"v_mul_f32 v23, v23, v26\n"
"v_cvt_pk_bf16_f32 v53, v22, v23\n"
"v_lshlrev_b32 v22, 16, v54\n"
"v_and_b32 v23, 0xffff0000, v54\n"
"v_mul_f32 v22, v22, v26\n"
"v_mul_f32 v23, v23, v26\n"
"v_cvt_pk_bf16_f32 v54, v22, v23\n"
"v_lshlrev_b32 v22, 16, v55\n"
"v_and_b32 v23, 0xffff0000, v55\n"
"v_mul_f32 v22, v22, v26\n"
"v_mul_f32 v23, v23, v26\n"
"v_cvt_pk_bf16_f32 v55, v22, v23\n"
"v_fma_f32 v22, v108, s62, v9\n"
"v_fma_f32 v23, v109, s62, v9\n"
"v_exp_f32 v22, v22\n"
"v_exp_f32 v23, v23\n"
"s_nop 0\n"
"v_add_f32 v21, v22, v23\n"
"v_cvt_pk_bf16_f32 v96, v22, v23\n"
"v_fma_f32 v22, v110, s62, v9\n"
"v_fma_f32 v23, v111, s62, v9\n"
"v_exp_f32 v22, v22\n"
"v_exp_f32 v23, v23\n"
"s_nop 0\n"
"v_add_f32 v21, v21, v22\n"
"v_add_f32 v21, v21, v23\n"
"v_cvt_pk_bf16_f32 v97, v22, v23\n"
"v_fma_f32 v22, v64, s62, v9\n"
"v_fma_f32 v23, v65, s62, v9\n"
"v_exp_f32 v22, v22\n"
"v_exp_f32 v23, v23\n"
"s_nop 0\n"
"v_add_f32 v21, v21, v22\n"
"v_add_f32 v21, v21, v23\n"
"v_cvt_pk_bf16_f32 v98, v22, v23\n"
"v_fma_f32 v22, v66, s62, v9\n"
"v_fma_f32 v23, v67, s62, v9\n"
"v_exp_f32 v22, v22\n"
"v_exp_f32 v23, v23\n"
"s_nop 0\n"
"v_add_f32 v21, v21, v22\n"
"v_add_f32 v21, v21, v23\n"
"v_cvt_pk_bf16_f32 v99, v22, v23\n"
"v_max3_f32 v25, v112, v113, v114\n"
"v_max3_f32 v26, v115, v116, v117\n"
"v_max3_f32 v25, v25, v118, v119\n"
"v_max_f32 v25, v25, v26\n"
"v_mov_b32 v26, v25\n"
"s_nop 1\n"
"v_permlane32_swap_b32 v25, v26\n"
"s_nop 1\n"
"v_max_f32 v25, v25, v26\n"
"v_mov_b32 v26, v25\n"
"s_nop 1\n"
"v_permlane16_swap_b32 v25, v26\n"
"s_nop 1\n"
"v_max_f32 v25, v25, v26\n"
"v_mul_f32 v25, s62, v25\n"
"v_sub_f32 v25, 0, v25\n"
"v_min_f32 v25, v25, v10\n"
"v_sub_f32 v26, v25, v10\n"
"v_exp_f32 v26, v26\n"
"v_mov_b32 v10, v25\n"
"s_nop 0\n"
"v_mul_f32 v16, v16, v26\n"
"v_accvgpr_read_b32 v22, a100\n"
"v_accvgpr_read_b32 v23, a101\n"
"v_mul_f32 v22, v22, v26\n"
"v_mul_f32 v23, v23, v26\n"
"v_accvgpr_write_b32 a100, v22\n"
"v_accvgpr_write_b32 a101, v23\n"
"v_accvgpr_read_b32 v22, a102\n"
"v_accvgpr_read_b32 v23, a103\n"
"v_mul_f32 v22, v22, v26\n"
"v_mul_f32 v23, v23, v26\n"
"v_accvgpr_write_b32 a102, v22\n"
"v_accvgpr_write_b32 a103, v23\n"
"v_accvgpr_read_b32 v22, a124\n"
"v_accvgpr_read_b32 v23, a125\n"
"v_mul_f32 v22, v22, v26\n"
"v_mul_f32 v23, v23, v26\n"
"v_accvgpr_write_b32 a124, v22\n"
"v_accvgpr_write_b32 a125, v23\n"
"v_accvgpr_read_b32 v22, a126\n"
"v_accvgpr_read_b32 v23, a127\n"
"v_mul_f32 v22, v22, v26\n"
"v_mul_f32 v23, v23, v26\n"
"v_accvgpr_write_b32 a126, v22\n"
"v_accvgpr_write_b32 a127, v23\n"
"v_accvgpr_read_b32 v22, a148\n"
"v_accvgpr_read_b32 v23, a149\n"
"v_mul_f32 v22, v22, v26\n"
"v_mul_f32 v23, v23, v26\n"
"v_accvgpr_write_b32 a148, v22\n"
"v_accvgpr_write_b32 a149, v23\n"
"v_accvgpr_read_b32 v22, a150\n"
"v_accvgpr_read_b32 v23, a151\n"
"v_mul_f32 v22, v22, v26\n"
"v_mul_f32 v23, v23, v26\n"
"v_accvgpr_write_b32 a150, v22\n"
"v_accvgpr_write_b32 a151, v23\n"
"v_accvgpr_read_b32 v22, a172\n"
"v_accvgpr_read_b32 v23, a173\n"
"v_mul_f32 v22, v22, v26\n"
"v_mul_f32 v23, v23, v26\n"
"v_accvgpr_write_b32 a172, v22\n"
"v_accvgpr_write_b32 a173, v23\n"
"v_accvgpr_read_b32 v22, a174\n"
"v_accvgpr_read_b32 v23, a175\n"
"v_mul_f32 v22, v22, v26\n"
"v_mul_f32 v23, v23, v26\n"
"v_accvgpr_write_b32 a174, v22\n"
"v_accvgpr_write_b32 a175, v23\n"
"v_accvgpr_read_b32 v22, a196\n"
"v_accvgpr_read_b32 v23, a197\n"
"v_mul_f32 v22, v22, v26\n"
"v_mul_f32 v23, v23, v26\n"
"v_accvgpr_write_b32 a196, v22\n"
"v_accvgpr_write_b32 a197, v23\n"
"v_accvgpr_read_b32 v22, a198\n"
"v_accvgpr_read_b32 v23, a199\n"
"v_mul_f32 v22, v22, v26\n"
"v_mul_f32 v23, v23, v26\n"
"v_accvgpr_write_b32 a198, v22\n"
"v_accvgpr_write_b32 a199, v23\n"
"v_accvgpr_read_b32 v22, a220\n"
"v_accvgpr_read_b32 v23, a221\n"
"v_mul_f32 v22, v22, v26\n"
"v_mul_f32 v23, v23, v26\n"
"v_accvgpr_write_b32 a220, v22\n"
"v_accvgpr_write_b32 a221, v23\n"
"v_accvgpr_read_b32 v22, a222\n"
"v_accvgpr_read_b32 v23, a223\n"
"v_mul_f32 v22, v22, v26\n"
"v_mul_f32 v23, v23, v26\n"
"v_accvgpr_write_b32 a222, v22\n"
"v_accvgpr_write_b32 a223, v23\n"
"v_accvgpr_read_b32 v22, a244\n"
"v_accvgpr_read_b32 v23, a245\n"
"v_mul_f32 v22, v22, v26\n"
"v_mul_f32 v23, v23, v26\n"
"v_accvgpr_write_b32 a244, v22\n"
"v_accvgpr_write_b32 a245, v23\n"
"v_accvgpr_read_b32 v22, a246\n"
"v_accvgpr_read_b32 v23, a247\n"
"v_mul_f32 v22, v22, v26\n"
"v_mul_f32 v23, v23, v26\n"
"v_accvgpr_write_b32 a246, v22\n"
"v_accvgpr_write_b32 a247, v23\n"
"v_mul_f32 v140, v140, v26\n"
"v_mul_f32 v141, v141, v26\n"
"v_mul_f32 v142, v142, v26\n"
"v_mul_f32 v143, v143, v26\n"
"v_mul_f32 v164, v164, v26\n"
"v_mul_f32 v165, v165, v26\n"
"v_mul_f32 v166, v166, v26\n"
"v_mul_f32 v167, v167, v26\n"
"v_mul_f32 v188, v188, v26\n"
"v_mul_f32 v189, v189, v26\n"
"v_mul_f32 v190, v190, v26\n"
"v_mul_f32 v191, v191, v26\n"
"v_mul_f32 v212, v212, v26\n"
"v_mul_f32 v213, v213, v26\n"
"v_mul_f32 v214, v214, v26\n"
"v_mul_f32 v215, v215, v26\n"
"v_mul_f32 v236, v236, v26\n"
"v_mul_f32 v237, v237, v26\n"
"v_mul_f32 v238, v238, v26\n"
"v_mul_f32 v239, v239, v26\n"
"v_lshlrev_b32 v22, 16, v56\n"
"v_and_b32 v23, 0xffff0000, v56\n"
"v_mul_f32 v22, v22, v26\n"
"v_mul_f32 v23, v23, v26\n"
"v_cvt_pk_bf16_f32 v56, v22, v23\n"
"v_lshlrev_b32 v22, 16, v57\n"
"v_and_b32 v23, 0xffff0000, v57\n"
"v_mul_f32 v22, v22, v26\n"
"v_mul_f32 v23, v23, v26\n"
"v_cvt_pk_bf16_f32 v57, v22, v23\n"
"v_lshlrev_b32 v22, 16, v58\n"
"v_and_b32 v23, 0xffff0000, v58\n"
"v_mul_f32 v22, v22, v26\n"
"v_mul_f32 v23, v23, v26\n"
"v_cvt_pk_bf16_f32 v58, v22, v23\n"
"v_lshlrev_b32 v22, 16, v59\n"
"v_and_b32 v23, 0xffff0000, v59\n"
"v_mul_f32 v22, v22, v26\n"
"v_mul_f32 v23, v23, v26\n"
"v_cvt_pk_bf16_f32 v59, v22, v23\n"
"v_fma_f32 v22, v112, s62, v10\n"
"v_fma_f32 v23, v113, s62, v10\n"
"v_exp_f32 v22, v22\n"
"v_exp_f32 v23, v23\n"
"s_nop 0\n"
"v_add_f32 v3, v22, v23\n"
"v_cvt_pk_bf16_f32 v104, v22, v23\n"
"v_fma_f32 v22, v114, s62, v10\n"
"v_fma_f32 v23, v115, s62, v10\n"
"v_exp_f32 v22, v22\n"
"v_exp_f32 v23, v23\n"
"s_nop 0\n"
"v_add_f32 v3, v3, v22\n"
"v_add_f32 v3, v3, v23\n"
"v_cvt_pk_bf16_f32 v105, v22, v23\n"
"v_fma_f32 v22, v116, s62, v10\n"
"v_fma_f32 v23, v117, s62, v10\n"
"v_exp_f32 v22, v22\n"
"v_exp_f32 v23, v23\n"
"s_nop 0\n"
"v_add_f32 v3, v3, v22\n"
"v_add_f32 v3, v3, v23\n"
"v_cvt_pk_bf16_f32 v106, v22, v23\n"
"v_fma_f32 v22, v118, s62, v10\n"
"v_fma_f32 v23, v119, s62, v10\n"
"v_exp_f32 v22, v22\n"
"v_exp_f32 v23, v23\n"
"s_nop 0\n"
"v_add_f32 v3, v3, v22\n"
"v_add_f32 v3, v3, v23\n"
"v_cvt_pk_bf16_f32 v107, v22, v23\n"
"v_fma_f32 v22, v84, s62, v11\n"
"v_fma_f32 v23, v85, s62, v11\n"
"s_nop 1\n"
"s_branch .Lret_7_a\n"
".Lrare_7_b:\n"
"s_nop 7\n"
"v_max3_f32 v25, v84, v85, v86\n"
"v_max3_f32 v26, v87, v72, v73\n"
"v_max3_f32 v25, v25, v74, v75\n"
"v_max_f32 v25, v25, v26\n"
"v_mov_b32 v26, v25\n"
"s_nop 1\n"
"v_permlane32_swap_b32 v25, v26\n"
"s_nop 1\n"
"v_max_f32 v25, v25, v26\n"
"v_mov_b32 v26, v25\n"
"s_nop 1\n"
"v_permlane16_swap_b32 v25, v26\n"
"s_nop 1\n"
"v_max_f32 v25, v25, v26\n"
"v_mul_f32 v25, s62, v25\n"
"v_sub_f32 v25, 0, v25\n"
"v_min_f32 v25, v25, v11\n"
"v_sub_f32 v26, v25, v11\n"
"v_exp_f32 v26, v26\n"
"v_mov_b32 v11, v25\n"
"s_nop 0\n"
"v_mul_f32 v17, v17, v26\n"
"v_accvgpr_read_b32 v22, a104\n"
"v_accvgpr_read_b32 v23, a105\n"
"v_mul_f32 v22, v22, v26\n"
"v_mul_f32 v23, v23, v26\n"
"v_accvgpr_write_b32 a104, v22\n"
"v_accvgpr_write_b32 a105, v23\n"
"v_accvgpr_read_b32 v22, a106\n"
"v_accvgpr_read_b32 v23, a107\n"
"v_mul_f32 v22, v22, v26\n"
"v_mul_f32 v23, v23, v26\n"
"v_accvgpr_write_b32 a106, v22\n"
"v_accvgpr_write_b32 a107, v23\n"
"v_accvgpr_read_b32 v22, a128\n"
"v_accvgpr_read_b32 v23, a129\n"
"v_mul_f32 v22, v22, v26\n"
"v_mul_f32 v23, v23, v26\n"
"v_accvgpr_write_b32 a128, v22\n"
"v_accvgpr_write_b32 a129, v23\n"
"v_accvgpr_read_b32 v22, a130\n"
"v_accvgpr_read_b32 v23, a131\n"
"v_mul_f32 v22, v22, v26\n"
"v_mul_f32 v23, v23, v26\n"
"v_accvgpr_write_b32 a130, v22\n"
"v_accvgpr_write_b32 a131, v23\n"
"v_accvgpr_read_b32 v22, a152\n"
"v_accvgpr_read_b32 v23, a153\n"
"v_mul_f32 v22, v22, v26\n"
"v_mul_f32 v23, v23, v26\n"
"v_accvgpr_write_b32 a152, v22\n"
"v_accvgpr_write_b32 a153, v23\n"
"v_accvgpr_read_b32 v22, a154\n"
"v_accvgpr_read_b32 v23, a155\n"
"v_mul_f32 v22, v22, v26\n"
"v_mul_f32 v23, v23, v26\n"
"v_accvgpr_write_b32 a154, v22\n"
"v_accvgpr_write_b32 a155, v23\n"
"v_accvgpr_read_b32 v22, a176\n"
"v_accvgpr_read_b32 v23, a177\n"
"v_mul_f32 v22, v22, v26\n"
"v_mul_f32 v23, v23, v26\n"
"v_accvgpr_write_b32 a176, v22\n"
"v_accvgpr_write_b32 a177, v23\n"
"v_accvgpr_read_b32 v22, a178\n"
"v_accvgpr_read_b32 v23, a179\n"
"v_mul_f32 v22, v22, v26\n"
"v_mul_f32 v23, v23, v26\n"
"v_accvgpr_write_b32 a178, v22\n"
"v_accvgpr_write_b32 a179, v23\n"
"v_accvgpr_read_b32 v22, a200\n"
"v_accvgpr_read_b32 v23, a201\n"
"v_mul_f32 v22, v22, v26\n"
"v_mul_f32 v23, v23, v26\n"
"v_accvgpr_write_b32 a200, v22\n"
"v_accvgpr_write_b32 a201, v23\n"
"v_accvgpr_read_b32 v22, a202\n"
"v_accvgpr_read_b32 v23, a203\n"
"v_mul_f32 v22, v22, v26\n"
"v_mul_f32 v23, v23, v26\n"
"v_accvgpr_write_b32 a202, v22\n"
"v_accvgpr_write_b32 a203, v23\n"
"v_accvgpr_read_b32 v22, a224\n"
"v_accvgpr_read_b32 v23, a225\n"
"v_mul_f32 v22, v22, v26\n"
"v_mul_f32 v23, v23, v26\n"
"v_accvgpr_write_b32 a224, v22\n"
"v_accvgpr_write_b32 a225, v23\n"
"v_accvgpr_read_b32 v22, a226\n"
"v_accvgpr_read_b32 v23, a227\n"
"v_mul_f32 v22, v22, v26\n"
"v_mul_f32 v23, v23, v26\n"
"v_accvgpr_write_b32 a226, v22\n"
"v_accvgpr_write_b32 a227, v23\n"
"v_accvgpr_read_b32 v22, a248\n"
"v_accvgpr_read_b32 v23, a249\n"
"v_mul_f32 v22, v22, v26\n"
"v_mul_f32 v23, v23, v26\n"
"v_accvgpr_write_b32 a248, v22\n"
"v_accvgpr_write_b32 a249, v23\n"
"v_accvgpr_read_b32 v22, a250\n"
"v_accvgpr_read_b32 v23, a251\n"
"v_mul_f32 v22, v22, v26\n"
"v_mul_f32 v23, v23, v26\n"
"v_accvgpr_write_b32 a250, v22\n"
"v_accvgpr_write_b32 a251, v23\n"
"v_mul_f32 v144, v144, v26\n"
"v_mul_f32 v145, v145, v26\n"
"v_mul_f32 v146, v146, v26\n"
"v_mul_f32 v147, v147, v26\n"
"v_mul_f32 v168, v168, v26\n"
"v_mul_f32 v169, v169, v26\n"
"v_mul_f32 v170, v170, v26\n"
"v_mul_f32 v171, v171, v26\n"
"v_mul_f32 v192, v192, v26\n"
"v_mul_f32 v193, v193, v26\n"
"v_mul_f32 v194, v194, v26\n"
"v_mul_f32 v195, v195, v26\n"
"v_mul_f32 v216, v216, v26\n"
"v_mul_f32 v217, v217, v26\n"
"v_mul_f32 v218, v218, v26\n"
"v_mul_f32 v219, v219, v26\n"
"v_mul_f32 v240, v240, v26\n"
"v_mul_f32 v241, v241, v26\n"
"v_mul_f32 v242, v242, v26\n"
"v_mul_f32 v243, v243, v26\n"
"v_lshlrev_b32 v22, 16, v76\n"
"v_and_b32 v23, 0xffff0000, v76\n"
"v_mul_f32 v22, v22, v26\n"
"v_mul_f32 v23, v23, v26\n"
"v_cvt_pk_bf16_f32 v76, v22, v23\n"
"v_lshlrev_b32 v22, 16, v77\n"
"v_and_b32 v23, 0xffff0000, v77\n"
"v_mul_f32 v22, v22, v26\n"
"v_mul_f32 v23, v23, v26\n"
"v_cvt_pk_bf16_f32 v77, v22, v23\n"
"v_lshlrev_b32 v22, 16, v78\n"
"v_and_b32 v23, 0xffff0000, v78\n"
"v_mul_f32 v22, v22, v26\n"
"v_mul_f32 v23, v23, v26\n"
"v_cvt_pk_bf16_f32 v78, v22, v23\n"
"v_lshlrev_b32 v22, 16, v79\n"
"v_and_b32 v23, 0xffff0000, v79\n"
"v_mul_f32 v22, v22, v26\n"
"v_mul_f32 v23, v23, v26\n"
"v_cvt_pk_bf16_f32 v79, v22, v23\n"
"v_fma_f32 v22, v84, s62, v11\n"
"v_fma_f32 v23, v85, s62, v11\n"
"v_exp_f32 v22, v22\n"
"v_exp_f32 v23, v23\n"
"s_nop 0\n"
"v_add_f32 v64, v22, v23\n"
"v_cvt_pk_bf16_f32 v100, v22, v23\n"
"v_fma_f32 v22, v86, s62, v11\n"
"v_fma_f32 v23, v87, s62, v11\n"
"v_exp_f32 v22, v22\n"
"v_exp_f32 v23, v23\n"
"s_nop 0\n"
"v_add_f32 v64, v64, v22\n"
"v_add_f32 v64, v64, v23\n"
"v_cvt_pk_bf16_f32 v101, v22, v23\n"
"v_fma_f32 v22, v72, s62, v11\n"
"v_fma_f32 v23, v73, s62, v11\n"
"v_exp_f32 v22, v22\n"
"v_exp_f32 v23, v23\n"
"s_nop 0\n"
"v_add_f32 v64, v64, v22\n"
"v_add_f32 v64, v64, v23\n"
"v_cvt_pk_bf16_f32 v102, v22, v23\n"
"v_fma_f32 v22, v74, s62, v11\n"
"v_fma_f32 v23, v75, s62, v11\n"
"v_exp_f32 v22, v22\n"
"v_exp_f32 v23, v23\n"
"s_nop 0\n"
"v_add_f32 v64, v64, v22\n"
"v_add_f32 v64, v64, v23\n"
"v_cvt_pk_bf16_f32 v103, v22, v23\n"
"v_max3_f32 v25, v60, v61, v62\n"
"v_max3_f32 v26, v63, v28, v29\n"
"v_max3_f32 v25, v25, v30, v31\n"
"v_max_f32 v25, v25, v26\n"
"v_mov_b32 v26, v25\n"
"s_nop 1\n"
"v_permlane32_swap_b32 v25, v26\n"
"s_nop 1\n"
"v_max_f32 v25, v25, v26\n"
"v_mov_b32 v26, v25\n"
"s_nop 1\n"
"v_permlane16_swap_b32 v25, v26\n"
"s_nop 1\n"
"v_max_f32 v25, v25, v26\n"
"v_mul_f32 v25, s62, v25\n"
"v_sub_f32 v25, 0, v25\n"
"v_min_f32 v25, v25, v12\n"
"v_sub_f32 v26, v25, v12\n"
"v_exp_f32 v26, v26\n"
"v_mov_b32 v12, v25\n"
"s_nop 0\n"
"v_mul_f32 v18, v18, v26\n"
"v_accvgpr_read_b32 v22, a108\n"
"v_accvgpr_read_b32 v23, a109\n"
"v_mul_f32 v22, v22, v26\n"
"v_mul_f32 v23, v23, v26\n"
"v_accvgpr_write_b32 a108, v22\n"
"v_accvgpr_write_b32 a109, v23\n"
"v_accvgpr_read_b32 v22, a110\n"
"v_accvgpr_read_b32 v23, a111\n"
"v_mul_f32 v22, v22, v26\n"
"v_mul_f32 v23, v23, v26\n"
"v_accvgpr_write_b32 a110, v22\n"
"v_accvgpr_write_b32 a111, v23\n"
"v_accvgpr_read_b32 v22, a132\n"
"v_accvgpr_read_b32 v23, a133\n"
"v_mul_f32 v22, v22, v26\n"
"v_mul_f32 v23, v23, v26\n"
"v_accvgpr_write_b32 a132, v22\n"
"v_accvgpr_write_b32 a133, v23\n"
"v_accvgpr_read_b32 v22, a134\n"
"v_accvgpr_read_b32 v23, a135\n"
"v_mul_f32 v22, v22, v26\n"
"v_mul_f32 v23, v23, v26\n"
"v_accvgpr_write_b32 a134, v22\n"
"v_accvgpr_write_b32 a135, v23\n"
"v_accvgpr_read_b32 v22, a156\n"
"v_accvgpr_read_b32 v23, a157\n"
"v_mul_f32 v22, v22, v26\n"
"v_mul_f32 v23, v23, v26\n"
"v_accvgpr_write_b32 a156, v22\n"
"v_accvgpr_write_b32 a157, v23\n"
"v_accvgpr_read_b32 v22, a158\n"
"v_accvgpr_read_b32 v23, a159\n"
"v_mul_f32 v22, v22, v26\n"
"v_mul_f32 v23, v23, v26\n"
"v_accvgpr_write_b32 a158, v22\n"
"v_accvgpr_write_b32 a159, v23\n"
"v_accvgpr_read_b32 v22, a180\n"
"v_accvgpr_read_b32 v23, a181\n"
"v_mul_f32 v22, v22, v26\n"
"v_mul_f32 v23, v23, v26\n"
"v_accvgpr_write_b32 a180, v22\n"
"v_accvgpr_write_b32 a181, v23\n"
"v_accvgpr_read_b32 v22, a182\n"
"v_accvgpr_read_b32 v23, a183\n"
"v_mul_f32 v22, v22, v26\n"
"v_mul_f32 v23, v23, v26\n"
"v_accvgpr_write_b32 a182, v22\n"
"v_accvgpr_write_b32 a183, v23\n"
"v_accvgpr_read_b32 v22, a204\n"
"v_accvgpr_read_b32 v23, a205\n"
"v_mul_f32 v22, v22, v26\n"
"v_mul_f32 v23, v23, v26\n"
"v_accvgpr_write_b32 a204, v22\n"
"v_accvgpr_write_b32 a205, v23\n"
"v_accvgpr_read_b32 v22, a206\n"
"v_accvgpr_read_b32 v23, a207\n"
"v_mul_f32 v22, v22, v26\n"
"v_mul_f32 v23, v23, v26\n"
"v_accvgpr_write_b32 a206, v22\n"
"v_accvgpr_write_b32 a207, v23\n"
"v_accvgpr_read_b32 v22, a228\n"
"v_accvgpr_read_b32 v23, a229\n"
"v_mul_f32 v22, v22, v26\n"
"v_mul_f32 v23, v23, v26\n"
"v_accvgpr_write_b32 a228, v22\n"
"v_accvgpr_write_b32 a229, v23\n"
"v_accvgpr_read_b32 v22, a230\n"
"v_accvgpr_read_b32 v23, a231\n"
"v_mul_f32 v22, v22, v26\n"
"v_mul_f32 v23, v23, v26\n"
"v_accvgpr_write_b32 a230, v22\n"
"v_accvgpr_write_b32 a231, v23\n"
"v_accvgpr_read_b32 v22, a252\n"
"v_accvgpr_read_b32 v23, a253\n"
"v_mul_f32 v22, v22, v26\n"
"v_mul_f32 v23, v23, v26\n"
"v_accvgpr_write_b32 a252, v22\n"
"v_accvgpr_write_b32 a253, v23\n"
"v_accvgpr_read_b32 v22, a254\n"
"v_accvgpr_read_b32 v23, a255\n"
"v_mul_f32 v22, v22, v26\n"
"v_mul_f32 v23, v23, v26\n"
"v_accvgpr_write_b32 a254, v22\n"
"v_accvgpr_write_b32 a255, v23\n"
"v_mul_f32 v148, v148, v26\n"
"v_mul_f32 v149, v149, v26\n"
"v_mul_f32 v150, v150, v26\n"
"v_mul_f32 v151, v151, v26\n"
"v_mul_f32 v172, v172, v26\n"
"v_mul_f32 v173, v173, v26\n"
"v_mul_f32 v174, v174, v26\n"
"v_mul_f32 v175, v175, v26\n"
"v_mul_f32 v196, v196, v26\n"
"v_mul_f32 v197, v197, v26\n"
"v_mul_f32 v198, v198, v26\n"
"v_mul_f32 v199, v199, v26\n"
"v_mul_f32 v220, v220, v26\n"
"v_mul_f32 v221, v221, v26\n"
"v_mul_f32 v222, v222, v26\n"
"v_mul_f32 v223, v223, v26\n"
"v_mul_f32 v244, v244, v26\n"
"v_mul_f32 v245, v245, v26\n"
"v_mul_f32 v246, v246, v26\n"
"v_mul_f32 v247, v247, v26\n"
"v_lshlrev_b32 v22, 16, v80\n"
"v_and_b32 v23, 0xffff0000, v80\n"
"v_mul_f32 v22, v22, v26\n"
"v_mul_f32 v23, v23, v26\n"
"v_cvt_pk_bf16_f32 v80, v22, v23\n"
"v_lshlrev_b32 v22, 16, v81\n"
"v_and_b32 v23, 0xffff0000, v81\n"
"v_mul_f32 v22, v22, v26\n"
"v_mul_f32 v23, v23, v26\n"
"v_cvt_pk_bf16_f32 v81, v22, v23\n"
"v_lshlrev_b32 v22, 16, v82\n"
"v_and_b32 v23, 0xffff0000, v82\n"
"v_mul_f32 v22, v22, v26\n"
"v_mul_f32 v23, v23, v26\n"
"v_cvt_pk_bf16_f32 v82, v22, v23\n"
"v_lshlrev_b32 v22, 16, v83\n"
"v_and_b32 v23, 0xffff0000, v83\n"
"v_mul_f32 v22, v22, v26\n"
"v_mul_f32 v23, v23, v26\n"
"v_cvt_pk_bf16_f32 v83, v22, v23\n"
"v_fma_f32 v22, v60, s62, v12\n"
"v_fma_f32 v23, v61, s62, v12\n"
"v_exp_f32 v22, v22\n"
"v_exp_f32 v23, v23\n"
"s_nop 0\n"
"v_add_f32 v65, v22, v23\n"
"v_cvt_pk_bf16_f32 v108, v22, v23\n"
"v_fma_f32 v22, v62, s62, v12\n"
"v_fma_f32 v23, v63, s62, v12\n"
"v_exp_f32 v22, v22\n"
"v_exp_f32 v23, v23\n"
"s_nop 0\n"
"v_add_f32 v65, v65, v22\n"
"v_add_f32 v65, v65, v23\n"
"v_cvt_pk_bf16_f32 v109, v22, v23\n"
"v_fma_f32 v22, v28, s62, v12\n"
"v_fma_f32 v23, v29, s62, v12\n"
"v_exp_f32 v22, v22\n"
"v_exp_f32 v23, v23\n"
"s_nop 0\n"
"v_add_f32 v65, v65, v22\n"
"v_add_f32 v65, v65, v23\n"
"v_cvt_pk_bf16_f32 v110, v22, v23\n"
"v_fma_f32 v22, v30, s62, v12\n"
"v_fma_f32 v23, v31, s62, v12\n"
"v_exp_f32 v22, v22\n"
"v_exp_f32 v23, v23\n"
"s_nop 0\n"
"v_add_f32 v65, v65, v22\n"
"v_add_f32 v65, v65, v23\n"
"v_cvt_pk_bf16_f32 v111, v22, v23\n"
"v_fma_f32 v22, v120, s62, v13\n"
"v_fma_f32 v23, v121, s62, v13\n"
"s_nop 1\n"
"s_branch .Lret_7_b\n"
".Lrare_7_d4:\n"
"s_nop 7\n"
"v_max3_f32 v25, v120, v121, v122\n"
"v_max3_f32 v26, v123, v88, v89\n"
"v_max3_f32 v25, v25, v90, v91\n"
"v_max_f32 v25, v25, v26\n"
"v_mov_b32 v26, v25\n"
"s_nop 1\n"
"v_permlane32_swap_b32 v25, v26\n"
"s_nop 1\n"
"v_max_f32 v25, v25, v26\n"
"v_mov_b32 v26, v25\n"
"s_nop 1\n"
"v_permlane16_swap_b32 v25, v26\n"
"s_nop 1\n"
"v_max_f32 v25, v25, v26\n"
"v_mul_f32 v25, s62, v25\n"
"v_sub_f32 v25, 0, v25\n"
"v_min_f32 v25, v25, v13\n"
"v_sub_f32 v26, v25, v13\n"
"v_exp_f32 v26, v26\n"
"v_mov_b32 v13, v25\n"
"s_nop 0\n"
"v_mul_f32 v19, v19, v26\n"
"v_accvgpr_read_b32 v22, a112\n"
"v_accvgpr_read_b32 v23, a113\n"
"v_mul_f32 v22, v22, v26\n"
"v_mul_f32 v23, v23, v26\n"
"v_accvgpr_write_b32 a112, v22\n"
"v_accvgpr_write_b32 a113, v23\n"
"v_accvgpr_read_b32 v22, a114\n"
"v_accvgpr_read_b32 v23, a115\n"
"v_mul_f32 v22, v22, v26\n"
"v_mul_f32 v23, v23, v26\n"
"v_accvgpr_write_b32 a114, v22\n"
"v_accvgpr_write_b32 a115, v23\n"
"v_accvgpr_read_b32 v22, a136\n"
"v_accvgpr_read_b32 v23, a137\n"
"v_mul_f32 v22, v22, v26\n"
"v_mul_f32 v23, v23, v26\n"
"v_accvgpr_write_b32 a136, v22\n"
"v_accvgpr_write_b32 a137, v23\n"
"v_accvgpr_read_b32 v22, a138\n"
"v_accvgpr_read_b32 v23, a139\n"
"v_mul_f32 v22, v22, v26\n"
"v_mul_f32 v23, v23, v26\n"
"v_accvgpr_write_b32 a138, v22\n"
"v_accvgpr_write_b32 a139, v23\n"
"v_accvgpr_read_b32 v22, a160\n"
"v_accvgpr_read_b32 v23, a161\n"
"v_mul_f32 v22, v22, v26\n"
"v_mul_f32 v23, v23, v26\n"
"v_accvgpr_write_b32 a160, v22\n"
"v_accvgpr_write_b32 a161, v23\n"
"v_accvgpr_read_b32 v22, a162\n"
"v_accvgpr_read_b32 v23, a163\n"
"v_mul_f32 v22, v22, v26\n"
"v_mul_f32 v23, v23, v26\n"
"v_accvgpr_write_b32 a162, v22\n"
"v_accvgpr_write_b32 a163, v23\n"
"v_accvgpr_read_b32 v22, a184\n"
"v_accvgpr_read_b32 v23, a185\n"
"v_mul_f32 v22, v22, v26\n"
"v_mul_f32 v23, v23, v26\n"
"v_accvgpr_write_b32 a184, v22\n"
"v_accvgpr_write_b32 a185, v23\n"
"v_accvgpr_read_b32 v22, a186\n"
"v_accvgpr_read_b32 v23, a187\n"
"v_mul_f32 v22, v22, v26\n"
"v_mul_f32 v23, v23, v26\n"
"v_accvgpr_write_b32 a186, v22\n"
"v_accvgpr_write_b32 a187, v23\n"
"v_accvgpr_read_b32 v22, a208\n"
"v_accvgpr_read_b32 v23, a209\n"
"v_mul_f32 v22, v22, v26\n"
"v_mul_f32 v23, v23, v26\n"
"v_accvgpr_write_b32 a208, v22\n"
"v_accvgpr_write_b32 a209, v23\n"
"v_accvgpr_read_b32 v22, a210\n"
"v_accvgpr_read_b32 v23, a211\n"
"v_mul_f32 v22, v22, v26\n"
"v_mul_f32 v23, v23, v26\n"
"v_accvgpr_write_b32 a210, v22\n"
"v_accvgpr_write_b32 a211, v23\n"
"v_accvgpr_read_b32 v22, a232\n"
"v_accvgpr_read_b32 v23, a233\n"
"v_mul_f32 v22, v22, v26\n"
"v_mul_f32 v23, v23, v26\n"
"v_accvgpr_write_b32 a232, v22\n"
"v_accvgpr_write_b32 a233, v23\n"
"v_accvgpr_read_b32 v22, a234\n"
"v_accvgpr_read_b32 v23, a235\n"
"v_mul_f32 v22, v22, v26\n"
"v_mul_f32 v23, v23, v26\n"
"v_accvgpr_write_b32 a234, v22\n"
"v_accvgpr_write_b32 a235, v23\n"
"v_mul_f32 v128, v128, v26\n"
"v_mul_f32 v129, v129, v26\n"
"v_mul_f32 v130, v130, v26\n"
"v_mul_f32 v131, v131, v26\n"
"v_mul_f32 v152, v152, v26\n"
"v_mul_f32 v153, v153, v26\n"
"v_mul_f32 v154, v154, v26\n"
"v_mul_f32 v155, v155, v26\n"
"v_mul_f32 v176, v176, v26\n"
"v_mul_f32 v177, v177, v26\n"
"v_mul_f32 v178, v178, v26\n"
"v_mul_f32 v179, v179, v26\n"
"v_mul_f32 v200, v200, v26\n"
"v_mul_f32 v201, v201, v26\n"
"v_mul_f32 v202, v202, v26\n"
"v_mul_f32 v203, v203, v26\n"
"v_mul_f32 v224, v224, v26\n"
"v_mul_f32 v225, v225, v26\n"
"v_mul_f32 v226, v226, v26\n"
"v_mul_f32 v227, v227, v26\n"
"v_mul_f32 v248, v248, v26\n"
"v_mul_f32 v249, v249, v26\n"
"v_mul_f32 v250, v250, v26\n"
"v_mul_f32 v251, v251, v26\n"
"v_fma_f32 v22, v120, s62, v13\n"
"v_fma_f32 v23, v121, s62, v13\n"
"v_exp_f32 v22, v22\n"
"v_exp_f32 v23, v23\n"
"s_nop 0\n"
"v_add_f32 v21, v22, v23\n"
"v_cvt_pk_bf16_f32 v124, v22, v23\n"
"v_fma_f32 v22, v122, s62, v13\n"
"v_fma_f32 v23, v123, s62, v13\n"
"v_exp_f32 v22, v22\n"
"v_exp_f32 v23, v23\n"
"s_nop 0\n"
"v_add_f32 v21, v21, v22\n"
"v_add_f32 v21, v21, v23\n"
"v_cvt_pk_bf16_f32 v125, v22, v23\n"
"v_fma_f32 v22, v88, s62, v13\n"
"v_fma_f32 v23, v89, s62, v13\n"
"v_exp_f32 v22, v22\n"
"v_exp_f32 v23, v23\n"
"s_nop 0\n"
"v_add_f32 v21, v21, v22\n"
"v_add_f32 v21, v21, v23\n"
"v_cvt_pk_bf16_f32 v126, v22, v23\n"
"v_fma_f32 v22, v90, s62, v13\n"
"v_fma_f32 v23, v91, s62, v13\n"
"v_exp_f32 v22, v22\n"
"v_exp_f32 v23, v23\n"
"s_nop 0\n"
"v_add_f32 v21, v21, v22\n"
"v_add_f32 v21, v21, v23\n"
"v_cvt_pk_bf16_f32 v127, v22, v23\n"
"v_fma_f32 v22, v92, s62, v14\n"
"v_fma_f32 v23, v93, s62, v14\n"
"s_nop 1\n"
"s_branch .Lret_7_d4\n"
".Lrare_7_d5:\n"
"s_nop 7\n"
"v_max3_f32 v25, v92, v93, v94\n"
"v_max3_f32 v26, v95, v68, v69\n"
"v_max3_f32 v25, v25, v70, v71\n"
"v_max_f32 v25, v25, v26\n"
"v_mov_b32 v26, v25\n"
"s_nop 1\n"
"v_permlane32_swap_b32 v25, v26\n"
"s_nop 1\n"
"v_max_f32 v25, v25, v26\n"
"v_mov_b32 v26, v25\n"
"s_nop 1\n"
"v_permlane16_swap_b32 v25, v26\n"
"s_nop 1\n"
"v_max_f32 v25, v25, v26\n"
"v_mul_f32 v25, s62, v25\n"
"v_sub_f32 v25, 0, v25\n"
"v_min_f32 v25, v25, v14\n"
"v_sub_f32 v26, v25, v14\n"
"v_exp_f32 v26, v26\n"
"v_mov_b32 v14, v25\n"
"s_nop 0\n"
"v_mul_f32 v20, v20, v26\n"
"v_accvgpr_read_b32 v22, a116\n"
"v_accvgpr_read_b32 v23, a117\n"
"v_mul_f32 v22, v22, v26\n"
"v_mul_f32 v23, v23, v26\n"
"v_accvgpr_write_b32 a116, v22\n"
"v_accvgpr_write_b32 a117, v23\n"
"v_accvgpr_read_b32 v22, a118\n"
"v_accvgpr_read_b32 v23, a119\n"
"v_mul_f32 v22, v22, v26\n"
"v_mul_f32 v23, v23, v26\n"
"v_accvgpr_write_b32 a118, v22\n"
"v_accvgpr_write_b32 a119, v23\n"
"v_accvgpr_read_b32 v22, a140\n"
"v_accvgpr_read_b32 v23, a141\n"
"v_mul_f32 v22, v22, v26\n"
"v_mul_f32 v23, v23, v26\n"
"v_accvgpr_write_b32 a140, v22\n"
"v_accvgpr_write_b32 a141, v23\n"
"v_accvgpr_read_b32 v22, a142\n"
"v_accvgpr_read_b32 v23, a143\n"
"v_mul_f32 v22, v22, v26\n"
"v_mul_f32 v23, v23, v26\n"
"v_accvgpr_write_b32 a142, v22\n"
"v_accvgpr_write_b32 a143, v23\n"
"v_accvgpr_read_b32 v22, a164\n"
"v_accvgpr_read_b32 v23, a165\n"
"v_mul_f32 v22, v22, v26\n"
"v_mul_f32 v23, v23, v26\n"
"v_accvgpr_write_b32 a164, v22\n"
"v_accvgpr_write_b32 a165, v23\n"
"v_accvgpr_read_b32 v22, a166\n"
"v_accvgpr_read_b32 v23, a167\n"
"v_mul_f32 v22, v22, v26\n"
"v_mul_f32 v23, v23, v26\n"
"v_accvgpr_write_b32 a166, v22\n"
"v_accvgpr_write_b32 a167, v23\n"
"v_accvgpr_read_b32 v22, a188\n"
"v_accvgpr_read_b32 v23, a189\n"
"v_mul_f32 v22, v22, v26\n"
"v_mul_f32 v23, v23, v26\n"
"v_accvgpr_write_b32 a188, v22\n"
"v_accvgpr_write_b32 a189, v23\n"
"v_accvgpr_read_b32 v22, a190\n"
"v_accvgpr_read_b32 v23, a191\n"
"v_mul_f32 v22, v22, v26\n"
"v_mul_f32 v23, v23, v26\n"
"v_accvgpr_write_b32 a190, v22\n"
"v_accvgpr_write_b32 a191, v23\n"
"v_accvgpr_read_b32 v22, a212\n"
"v_accvgpr_read_b32 v23, a213\n"
"v_mul_f32 v22, v22, v26\n"
"v_mul_f32 v23, v23, v26\n"
"v_accvgpr_write_b32 a212, v22\n"
"v_accvgpr_write_b32 a213, v23\n"
"v_accvgpr_read_b32 v22, a214\n"
"v_accvgpr_read_b32 v23, a215\n"
"v_mul_f32 v22, v22, v26\n"
"v_mul_f32 v23, v23, v26\n"
"v_accvgpr_write_b32 a214, v22\n"
"v_accvgpr_write_b32 a215, v23\n"
"v_accvgpr_read_b32 v22, a236\n"
"v_accvgpr_read_b32 v23, a237\n"
"v_mul_f32 v22, v22, v26\n"
"v_mul_f32 v23, v23, v26\n"
"v_accvgpr_write_b32 a236, v22\n"
"v_accvgpr_write_b32 a237, v23\n"
"v_accvgpr_read_b32 v22, a238\n"
"v_accvgpr_read_b32 v23, a239\n"
"v_mul_f32 v22, v22, v26\n"
"v_mul_f32 v23, v23, v26\n"
"v_accvgpr_write_b32 a238, v22\n"
"v_accvgpr_write_b32 a239, v23\n"
"v_mul_f32 v132, v132, v26\n"
"v_mul_f32 v133, v133, v26\n"
"v_mul_f32 v134, v134, v26\n"
"v_mul_f32 v135, v135, v26\n"
"v_mul_f32 v156, v156, v26\n"
"v_mul_f32 v157, v157, v26\n"
"v_mul_f32 v158, v158, v26\n"
"v_mul_f32 v159, v159, v26\n"
"v_mul_f32 v180, v180, v26\n"
"v_mul_f32 v181, v181, v26\n"
"v_mul_f32 v182, v182, v26\n"
"v_mul_f32 v183, v183, v26\n"
"v_mul_f32 v204, v204, v26\n"
"v_mul_f32 v205, v205, v26\n"
"v_mul_f32 v206, v206, v26\n"
"v_mul_f32 v207, v207, v26\n"
"v_mul_f32 v228, v228, v26\n"
"v_mul_f32 v229, v229, v26\n"
"v_mul_f32 v230, v230, v26\n"
"v_mul_f32 v231, v231, v26\n"
"v_mul_f32 v252, v252, v26\n"
"v_mul_f32 v253, v253, v26\n"
"v_mul_f32 v254, v254, v26\n"
"v_mul_f32 v255, v255, v26\n"
"v_fma_f32 v22, v92, s62, v14\n"
"v_fma_f32 v23, v93, s62, v14\n"
"v_exp_f32 v22, v22\n"
"v_exp_f32 v23, v23\n"
"s_nop 0\n"
"v_add_f32 v21, v22, v23\n"
"v_cvt_pk_bf16_f32 v120, v22, v23\n"
"v_fma_f32 v22, v94, s62, v14\n"
"v_fma_f32 v23, v95, s62, v14\n"
"v_exp_f32 v22, v22\n"
"v_exp_f32 v23, v23\n"
"s_nop 0\n"
"v_add_f32 v21, v21, v22\n"
"v_add_f32 v21, v21, v23\n"
"v_cvt_pk_bf16_f32 v121, v22, v23\n"
"v_fma_f32 v22, v68, s62, v14\n"
"v_fma_f32 v23, v69, s62, v14\n"
"v_exp_f32 v22, v22\n"
"v_exp_f32 v23, v23\n"
"s_nop 0\n"
"v_add_f32 v21, v21, v22\n"
"v_add_f32 v21, v21, v23\n"
"v_cvt_pk_bf16_f32 v122, v22, v23\n"
"v_fma_f32 v22, v70, s62, v14\n"
"v_fma_f32 v23, v71, s62, v14\n"
"v_exp_f32 v22, v22\n"
"v_exp_f32 v23, v23\n"
"s_nop 0\n"
"v_add_f32 v21, v21, v22\n"
"v_add_f32 v21, v21, v23\n"
"v_cvt_pk_bf16_f32 v123, v22, v23\n"
"s_nop 1\n"
"s_branch .Lret_7_d5\n"
".Lrare_8_a:\n"
"s_nop 7\n"
"v_max3_f32 v25, v112, v113, v114\n"
"v_max3_f32 v26, v115, v28, v29\n"
"v_max3_f32 v25, v25, v30, v31\n"
"v_max_f32 v25, v25, v26\n"
"v_mov_b32 v26, v25\n"
"s_nop 1\n"
"v_permlane32_swap_b32 v25, v26\n"
"s_nop 1\n"
"v_max_f32 v25, v25, v26\n"
"v_mov_b32 v26, v25\n"
"s_nop 1\n"
"v_permlane16_swap_b32 v25, v26\n"
"s_nop 1\n"
"v_max_f32 v25, v25, v26\n"
"v_mul_f32 v25, s62, v25\n"
"v_sub_f32 v25, 0, v25\n"
"v_min_f32 v25, v25, v9\n"
"v_sub_f32 v26, v25, v9\n"
"v_exp_f32 v26, v26\n"
"v_mov_b32 v9, v25\n"
"s_nop 0\n"
"v_mul_f32 v15, v15, v26\n"
"v_accvgpr_read_b32 v22, a96\n"
"v_accvgpr_read_b32 v23, a97\n"
"v_mul_f32 v22, v22, v26\n"
"v_mul_f32 v23, v23, v26\n"
"v_accvgpr_write_b32 a96, v22\n"
"v_accvgpr_write_b32 a97, v23\n"
"v_accvgpr_read_b32 v22, a98\n"
"v_accvgpr_read_b32 v23, a99\n"
"v_mul_f32 v22, v22, v26\n"
"v_mul_f32 v23, v23, v26\n"
"v_accvgpr_write_b32 a98, v22\n"
"v_accvgpr_write_b32 a99, v23\n"
"v_accvgpr_read_b32 v22, a120\n"
"v_accvgpr_read_b32 v23, a121\n"
"v_mul_f32 v22, v22, v26\n"
"v_mul_f32 v23, v23, v26\n"
"v_accvgpr_write_b32 a120, v22\n"
"v_accvgpr_write_b32 a121, v23\n"
"v_accvgpr_read_b32 v22, a122\n"
"v_accvgpr_read_b32 v23, a123\n"
"v_mul_f32 v22, v22, v26\n"
"v_mul_f32 v23, v23, v26\n"
"v_accvgpr_write_b32 a122, v22\n"
"v_accvgpr_write_b32 a123, v23\n"
"v_accvgpr_read_b32 v22, a144\n"
"v_accvgpr_read_b32 v23, a145\n"
"v_mul_f32 v22, v22, v26\n"
"v_mul_f32 v23, v23, v26\n"
"v_accvgpr_write_b32 a144, v22\n"
"v_accvgpr_write_b32 a145, v23\n"
"v_accvgpr_read_b32 v22, a146\n"
"v_accvgpr_read_b32 v23, a147\n"
"v_mul_f32 v22, v22, v26\n"
"v_mul_f32 v23, v23, v26\n"
"v_accvgpr_write_b32 a146, v22\n"
"v_accvgpr_write_b32 a147, v23\n"
"v_accvgpr_read_b32 v22, a168\n"
"v_accvgpr_read_b32 v23, a169\n"
"v_mul_f32 v22, v22, v26\n"
"v_mul_f32 v23, v23, v26\n"
"v_accvgpr_write_b32 a168, v22\n"
"v_accvgpr_write_b32 a169, v23\n"
"v_accvgpr_read_b32 v22, a170\n"
"v_accvgpr_read_b32 v23, a171\n"
"v_mul_f32 v22, v22, v26\n"
"v_mul_f32 v23, v23, v26\n"
"v_accvgpr_write_b32 a170, v22\n"
"v_accvgpr_write_b32 a171, v23\n"
"v_accvgpr_read_b32 v22, a192\n"
"v_accvgpr_read_b32 v23, a193\n"
"v_mul_f32 v22, v22, v26\n"
"v_mul_f32 v23, v23, v26\n"
"v_accvgpr_write_b32 a192, v22\n"
"v_accvgpr_write_b32 a193, v23\n"
"v_accvgpr_read_b32 v22, a194\n"
"v_accvgpr_read_b32 v23, a195\n"
"v_mul_f32 v22, v22, v26\n"
"v_mul_f32 v23, v23, v26\n"
"v_accvgpr_write_b32 a194, v22\n"
"v_accvgpr_write_b32 a195, v23\n"
"v_accvgpr_read_b32 v22, a216\n"
"v_accvgpr_read_b32 v23, a217\n"
"v_mul_f32 v22, v22, v26\n"
"v_mul_f32 v23, v23, v26\n"
"v_accvgpr_write_b32 a216, v22\n"
"v_accvgpr_write_b32 a217, v23\n"
"v_accvgpr_read_b32 v22, a218\n"
"v_accvgpr_read_b32 v23, a219\n"
"v_mul_f32 v22, v22, v26\n"
"v_mul_f32 v23, v23, v26\n"
"v_accvgpr_write_b32 a218, v22\n"
"v_accvgpr_write_b32 a219, v23\n"
"v_accvgpr_read_b32 v22, a240\n"
"v_accvgpr_read_b32 v23, a241\n"
"v_mul_f32 v22, v22, v26\n"
"v_mul_f32 v23, v23, v26\n"
"v_accvgpr_write_b32 a240, v22\n"
"v_accvgpr_write_b32 a241, v23\n"
"v_accvgpr_read_b32 v22, a242\n"
"v_accvgpr_read_b32 v23, a243\n"
"v_mul_f32 v22, v22, v26\n"
"v_mul_f32 v23, v23, v26\n"
"v_accvgpr_write_b32 a242, v22\n"
"v_accvgpr_write_b32 a243, v23\n"
"v_mul_f32 v136, v136, v26\n"
"v_mul_f32 v137, v137, v26\n"
"v_mul_f32 v138, v138, v26\n"
"v_mul_f32 v139, v139, v26\n"
"v_mul_f32 v160, v160, v26\n"
"v_mul_f32 v161, v161, v26\n"
"v_mul_f32 v162, v162, v26\n"
"v_mul_f32 v163, v163, v26\n"
"v_mul_f32 v184, v184, v26\n"
"v_mul_f32 v185, v185, v26\n"
"v_mul_f32 v186, v186, v26\n"
"v_mul_f32 v187, v187, v26\n"
"v_mul_f32 v208, v208, v26\n"
"v_mul_f32 v209, v209, v26\n"
"v_mul_f32 v210, v210, v26\n"
"v_mul_f32 v211, v211, v26\n"
"v_mul_f32 v232, v232, v26\n"
"v_mul_f32 v233, v233, v26\n"
"v_mul_f32 v234, v234, v26\n"
"v_mul_f32 v235, v235, v26\n"
"v_lshlrev_b32 v22, 16, v96\n"
"v_and_b32 v23, 0xffff0000, v96\n"
"v_mul_f32 v22, v22, v26\n"
"v_mul_f32 v23, v23, v26\n"
"v_cvt_pk_bf16_f32 v96, v22, v23\n"
"v_lshlrev_b32 v22, 16, v97\n"
"v_and_b32 v23, 0xffff0000, v97\n"
"v_mul_f32 v22, v22, v26\n"
"v_mul_f32 v23, v23, v26\n"
"v_cvt_pk_bf16_f32 v97, v22, v23\n"
"v_lshlrev_b32 v22, 16, v98\n"
"v_and_b32 v23, 0xffff0000, v98\n"
"v_mul_f32 v22, v22, v26\n"
"v_mul_f32 v23, v23, v26\n"
"v_cvt_pk_bf16_f32 v98, v22, v23\n"
"v_lshlrev_b32 v22, 16, v99\n"
"v_and_b32 v23, 0xffff0000, v99\n"
"v_mul_f32 v22, v22, v26\n"
"v_mul_f32 v23, v23, v26\n"
"v_cvt_pk_bf16_f32 v99, v22, v23\n"
"v_fma_f32 v22, v112, s62, v9\n"
"v_fma_f32 v23, v113, s62, v9\n"
"v_exp_f32 v22, v22\n"
"v_exp_f32 v23, v23\n"
"s_nop 0\n"
"v_add_f32 v21, v22, v23\n"
"v_cvt_pk_bf16_f32 v88, v22, v23\n"
"v_fma_f32 v22, v114, s62, v9\n"
"v_fma_f32 v23, v115, s62, v9\n"
"v_exp_f32 v22, v22\n"
"v_exp_f32 v23, v23\n"
"s_nop 0\n"
"v_add_f32 v21, v21, v22\n"
"v_add_f32 v21, v21, v23\n"
"v_cvt_pk_bf16_f32 v89, v22, v23\n"
"v_fma_f32 v22, v28, s62, v9\n"
"v_fma_f32 v23, v29, s62, v9\n"
"v_exp_f32 v22, v22\n"
"v_exp_f32 v23, v23\n"
"s_nop 0\n"
"v_add_f32 v21, v21, v22\n"
"v_add_f32 v21, v21, v23\n"
"v_cvt_pk_bf16_f32 v90, v22, v23\n"
"v_fma_f32 v22, v30, s62, v9\n"
"v_fma_f32 v23, v31, s62, v9\n"
"v_exp_f32 v22, v22\n"
"v_exp_f32 v23, v23\n"
"s_nop 0\n"
"v_add_f32 v21, v21, v22\n"
"v_add_f32 v21, v21, v23\n"
"v_cvt_pk_bf16_f32 v91, v22, v23\n"
"v_max3_f32 v25, v116, v117, v118\n"
"v_max3_f32 v26, v119, v52, v53\n"
"v_max3_f32 v25, v25, v54, v55\n"
"v_max_f32 v25, v25, v26\n"
"v_mov_b32 v26, v25\n"
"s_nop 1\n"
"v_permlane32_swap_b32 v25, v26\n"
"s_nop 1\n"
"v_max_f32 v25, v25, v26\n"
"v_mov_b32 v26, v25\n"
"s_nop 1\n"
"v_permlane16_swap_b32 v25, v26\n"
"s_nop 1\n"
"v_max_f32 v25, v25, v26\n"
"v_mul_f32 v25, s62, v25\n"
"v_sub_f32 v25, 0, v25\n"
"v_min_f32 v25, v25, v10\n"
"v_sub_f32 v26, v25, v10\n"
"v_exp_f32 v26, v26\n"
"v_mov_b32 v10, v25\n"
"s_nop 0\n"
"v_mul_f32 v16, v16, v26\n"
"v_accvgpr_read_b32 v22, a100\n"
"v_accvgpr_read_b32 v23, a101\n"
"v_mul_f32 v22, v22, v26\n"
"v_mul_f32 v23, v23, v26\n"
"v_accvgpr_write_b32 a100, v22\n"
"v_accvgpr_write_b32 a101, v23\n"
"v_accvgpr_read_b32 v22, a102\n"
"v_accvgpr_read_b32 v23, a103\n"
"v_mul_f32 v22, v22, v26\n"
"v_mul_f32 v23, v23, v26\n"
"v_accvgpr_write_b32 a102, v22\n"
"v_accvgpr_write_b32 a103, v23\n"
"v_accvgpr_read_b32 v22, a124\n"
"v_accvgpr_read_b32 v23, a125\n"
"v_mul_f32 v22, v22, v26\n"
"v_mul_f32 v23, v23, v26\n"
"v_accvgpr_write_b32 a124, v22\n"
"v_accvgpr_write_b32 a125, v23\n"
"v_accvgpr_read_b32 v22, a126\n"
"v_accvgpr_read_b32 v23, a127\n"
"v_mul_f32 v22, v22, v26\n"
"v_mul_f32 v23, v23, v26\n"
"v_accvgpr_write_b32 a126, v22\n"
"v_accvgpr_write_b32 a127, v23\n"
"v_accvgpr_read_b32 v22, a148\n"
"v_accvgpr_read_b32 v23, a149\n"
"v_mul_f32 v22, v22, v26\n"
"v_mul_f32 v23, v23, v26\n"
"v_accvgpr_write_b32 a148, v22\n"
"v_accvgpr_write_b32 a149, v23\n"
"v_accvgpr_read_b32 v22, a150\n"
"v_accvgpr_read_b32 v23, a151\n"
"v_mul_f32 v22, v22, v26\n"
"v_mul_f32 v23, v23, v26\n"
"v_accvgpr_write_b32 a150, v22\n"
"v_accvgpr_write_b32 a151, v23\n"
"v_accvgpr_read_b32 v22, a172\n"
"v_accvgpr_read_b32 v23, a173\n"
"v_mul_f32 v22, v22, v26\n"
"v_mul_f32 v23, v23, v26\n"
"v_accvgpr_write_b32 a172, v22\n"
"v_accvgpr_write_b32 a173, v23\n"
"v_accvgpr_read_b32 v22, a174\n"
"v_accvgpr_read_b32 v23, a175\n"
"v_mul_f32 v22, v22, v26\n"
"v_mul_f32 v23, v23, v26\n"
"v_accvgpr_write_b32 a174, v22\n"
"v_accvgpr_write_b32 a175, v23\n"
"v_accvgpr_read_b32 v22, a196\n"
"v_accvgpr_read_b32 v23, a197\n"
"v_mul_f32 v22, v22, v26\n"
"v_mul_f32 v23, v23, v26\n"
"v_accvgpr_write_b32 a196, v22\n"
"v_accvgpr_write_b32 a197, v23\n"
"v_accvgpr_read_b32 v22, a198\n"
"v_accvgpr_read_b32 v23, a199\n"
"v_mul_f32 v22, v22, v26\n"
"v_mul_f32 v23, v23, v26\n"
"v_accvgpr_write_b32 a198, v22\n"
"v_accvgpr_write_b32 a199, v23\n"
"v_accvgpr_read_b32 v22, a220\n"
"v_accvgpr_read_b32 v23, a221\n"
"v_mul_f32 v22, v22, v26\n"
"v_mul_f32 v23, v23, v26\n"
"v_accvgpr_write_b32 a220, v22\n"
"v_accvgpr_write_b32 a221, v23\n"
"v_accvgpr_read_b32 v22, a222\n"
"v_accvgpr_read_b32 v23, a223\n"
"v_mul_f32 v22, v22, v26\n"
"v_mul_f32 v23, v23, v26\n"
"v_accvgpr_write_b32 a222, v22\n"
"v_accvgpr_write_b32 a223, v23\n"
"v_accvgpr_read_b32 v22, a244\n"
"v_accvgpr_read_b32 v23, a245\n"
"v_mul_f32 v22, v22, v26\n"
"v_mul_f32 v23, v23, v26\n"
"v_accvgpr_write_b32 a244, v22\n"
"v_accvgpr_write_b32 a245, v23\n"
"v_accvgpr_read_b32 v22, a246\n"
"v_accvgpr_read_b32 v23, a247\n"
"v_mul_f32 v22, v22, v26\n"
"v_mul_f32 v23, v23, v26\n"
"v_accvgpr_write_b32 a246, v22\n"
"v_accvgpr_write_b32 a247, v23\n"
"v_mul_f32 v140, v140, v26\n"
"v_mul_f32 v141, v141, v26\n"
"v_mul_f32 v142, v142, v26\n"
"v_mul_f32 v143, v143, v26\n"
"v_mul_f32 v164, v164, v26\n"
"v_mul_f32 v165, v165, v26\n"
"v_mul_f32 v166, v166, v26\n"
"v_mul_f32 v167, v167, v26\n"
"v_mul_f32 v188, v188, v26\n"
"v_mul_f32 v189, v189, v26\n"
"v_mul_f32 v190, v190, v26\n"
"v_mul_f32 v191, v191, v26\n"
"v_mul_f32 v212, v212, v26\n"
"v_mul_f32 v213, v213, v26\n"
"v_mul_f32 v214, v214, v26\n"
"v_mul_f32 v215, v215, v26\n"
"v_mul_f32 v236, v236, v26\n"
"v_mul_f32 v237, v237, v26\n"
"v_mul_f32 v238, v238, v26\n"
"v_mul_f32 v239, v239, v26\n"
"v_lshlrev_b32 v22, 16, v104\n"
"v_and_b32 v23, 0xffff0000, v104\n"
"v_mul_f32 v22, v22, v26\n"
"v_mul_f32 v23, v23, v26\n"
"v_cvt_pk_bf16_f32 v104, v22, v23\n"
"v_lshlrev_b32 v22, 16, v105\n"
"v_and_b32 v23, 0xffff0000, v105\n"
"v_mul_f32 v22, v22, v26\n"
"v_mul_f32 v23, v23, v26\n"
"v_cvt_pk_bf16_f32 v105, v22, v23\n"
"v_lshlrev_b32 v22, 16, v106\n"
"v_and_b32 v23, 0xffff0000, v106\n"
"v_mul_f32 v22, v22, v26\n"
"v_mul_f32 v23, v23, v26\n"
"v_cvt_pk_bf16_f32 v106, v22, v23\n"
"v_lshlrev_b32 v22, 16, v107\n"
"v_and_b32 v23, 0xffff0000, v107\n"
"v_mul_f32 v22, v22, v26\n"
"v_mul_f32 v23, v23, v26\n"
"v_cvt_pk_bf16_f32 v107, v22, v23\n"
"v_fma_f32 v22, v116, s62, v10\n"
"v_fma_f32 v23, v117, s62, v10\n"
"v_exp_f32 v22, v22\n"
"v_exp_f32 v23, v23\n"
"s_nop 0\n"
"v_add_f32 v3, v22, v23\n"
"v_cvt_pk_bf16_f32 v92, v22, v23\n"
"v_fma_f32 v22, v118, s62, v10\n"
"v_fma_f32 v23, v119, s62, v10\n"
"v_exp_f32 v22, v22\n"
"v_exp_f32 v23, v23\n"
"s_nop 0\n"
"v_add_f32 v3, v3, v22\n"
"v_add_f32 v3, v3, v23\n"
"v_cvt_pk_bf16_f32 v93, v22, v23\n"
"v_fma_f32 v22, v52, s62, v10\n"
"v_fma_f32 v23, v53, s62, v10\n"
"v_exp_f32 v22, v22\n"
"v_exp_f32 v23, v23\n"
"s_nop 0\n"
"v_add_f32 v3, v3, v22\n"
"v_add_f32 v3, v3, v23\n"
"v_cvt_pk_bf16_f32 v94, v22, v23\n"
"v_fma_f32 v22, v54, s62, v10\n"
"v_fma_f32 v23, v55, s62, v10\n"
"v_exp_f32 v22, v22\n"
"v_exp_f32 v23, v23\n"
"s_nop 0\n"
"v_add_f32 v3, v3, v22\n"
"v_add_f32 v3, v3, v23\n"
"v_cvt_pk_bf16_f32 v95, v22, v23\n"
"v_fma_f32 v22, v64, s62, v11\n"
"v_fma_f32 v23, v65, s62, v11\n"
"s_nop 1\n"
"s_branch .Lret_8_a\n"
".Lrare_8_b:\n"
"s_nop 7\n"
"v_max3_f32 v25, v64, v65, v66\n"
"v_max3_f32 v26, v67, v56, v57\n"
"v_max3_f32 v25, v25, v58, v59\n"
"v_max_f32 v25, v25, v26\n"
"v_mov_b32 v26, v25\n"
"s_nop 1\n"
"v_permlane32_swap_b32 v25, v26\n"
"s_nop 1\n"
"v_max_f32 v25, v25, v26\n"
"v_mov_b32 v26, v25\n"
"s_nop 1\n"
"v_permlane16_swap_b32 v25, v26\n"
"s_nop 1\n"
"v_max_f32 v25, v25, v26\n"
"v_mul_f32 v25, s62, v25\n"
"v_sub_f32 v25, 0, v25\n"
"v_min_f32 v25, v25, v11\n"
"v_sub_f32 v26, v25, v11\n"
"v_exp_f32 v26, v26\n"
"v_mov_b32 v11, v25\n"
"s_nop 0\n"
"v_mul_f32 v17, v17, v26\n"
"v_accvgpr_read_b32 v22, a104\n"
"v_accvgpr_read_b32 v23, a105\n"
"v_mul_f32 v22, v22, v26\n"
"v_mul_f32 v23, v23, v26\n"
"v_accvgpr_write_b32 a104, v22\n"
"v_accvgpr_write_b32 a105, v23\n"
"v_accvgpr_read_b32 v22, a106\n"
"v_accvgpr_read_b32 v23, a107\n"
"v_mul_f32 v22, v22, v26\n"
"v_mul_f32 v23, v23, v26\n"
"v_accvgpr_write_b32 a106, v22\n"
"v_accvgpr_write_b32 a107, v23\n"
"v_accvgpr_read_b32 v22, a128\n"
"v_accvgpr_read_b32 v23, a129\n"
"v_mul_f32 v22, v22, v26\n"
"v_mul_f32 v23, v23, v26\n"
"v_accvgpr_write_b32 a128, v22\n"
"v_accvgpr_write_b32 a129, v23\n"
"v_accvgpr_read_b32 v22, a130\n"
"v_accvgpr_read_b32 v23, a131\n"
"v_mul_f32 v22, v22, v26\n"
"v_mul_f32 v23, v23, v26\n"
"v_accvgpr_write_b32 a130, v22\n"
"v_accvgpr_write_b32 a131, v23\n"
"v_accvgpr_read_b32 v22, a152\n"
"v_accvgpr_read_b32 v23, a153\n"
"v_mul_f32 v22, v22, v26\n"
"v_mul_f32 v23, v23, v26\n"
"v_accvgpr_write_b32 a152, v22\n"
"v_accvgpr_write_b32 a153, v23\n"
"v_accvgpr_read_b32 v22, a154\n"
"v_accvgpr_read_b32 v23, a155\n"
"v_mul_f32 v22, v22, v26\n"
"v_mul_f32 v23, v23, v26\n"
"v_accvgpr_write_b32 a154, v22\n"
"v_accvgpr_write_b32 a155, v23\n"
"v_accvgpr_read_b32 v22, a176\n"
"v_accvgpr_read_b32 v23, a177\n"
"v_mul_f32 v22, v22, v26\n"
"v_mul_f32 v23, v23, v26\n"
"v_accvgpr_write_b32 a176, v22\n"
"v_accvgpr_write_b32 a177, v23\n"
"v_accvgpr_read_b32 v22, a178\n"
"v_accvgpr_read_b32 v23, a179\n"
"v_mul_f32 v22, v22, v26\n"
"v_mul_f32 v23, v23, v26\n"
"v_accvgpr_write_b32 a178, v22\n"
"v_accvgpr_write_b32 a179, v23\n"
"v_accvgpr_read_b32 v22, a200\n"
"v_accvgpr_read_b32 v23, a201\n"
"v_mul_f32 v22, v22, v26\n"
"v_mul_f32 v23, v23, v26\n"
"v_accvgpr_write_b32 a200, v22\n"
"v_accvgpr_write_b32 a201, v23\n"
"v_accvgpr_read_b32 v22, a202\n"
"v_accvgpr_read_b32 v23, a203\n"
"v_mul_f32 v22, v22, v26\n"
"v_mul_f32 v23, v23, v26\n"
"v_accvgpr_write_b32 a202, v22\n"
"v_accvgpr_write_b32 a203, v23\n"
"v_accvgpr_read_b32 v22, a224\n"
"v_accvgpr_read_b32 v23, a225\n"
"v_mul_f32 v22, v22, v26\n"
"v_mul_f32 v23, v23, v26\n"
"v_accvgpr_write_b32 a224, v22\n"
"v_accvgpr_write_b32 a225, v23\n"
"v_accvgpr_read_b32 v22, a226\n"
"v_accvgpr_read_b32 v23, a227\n"
"v_mul_f32 v22, v22, v26\n"
"v_mul_f32 v23, v23, v26\n"
"v_accvgpr_write_b32 a226, v22\n"
"v_accvgpr_write_b32 a227, v23\n"
"v_accvgpr_read_b32 v22, a248\n"
"v_accvgpr_read_b32 v23, a249\n"
"v_mul_f32 v22, v22, v26\n"
"v_mul_f32 v23, v23, v26\n"
"v_accvgpr_write_b32 a248, v22\n"
"v_accvgpr_write_b32 a249, v23\n"
"v_accvgpr_read_b32 v22, a250\n"
"v_accvgpr_read_b32 v23, a251\n"
"v_mul_f32 v22, v22, v26\n"
"v_mul_f32 v23, v23, v26\n"
"v_accvgpr_write_b32 a250, v22\n"
"v_accvgpr_write_b32 a251, v23\n"
"v_mul_f32 v144, v144, v26\n"
"v_mul_f32 v145, v145, v26\n"
"v_mul_f32 v146, v146, v26\n"
"v_mul_f32 v147, v147, v26\n"
"v_mul_f32 v168, v168, v26\n"
"v_mul_f32 v169, v169, v26\n"
"v_mul_f32 v170, v170, v26\n"
"v_mul_f32 v171, v171, v26\n"
"v_mul_f32 v192, v192, v26\n"
"v_mul_f32 v193, v193, v26\n"
"v_mul_f32 v194, v194, v26\n"
"v_mul_f32 v195, v195, v26\n"
"v_mul_f32 v216, v216, v26\n"
"v_mul_f32 v217, v217, v26\n"
"v_mul_f32 v218, v218, v26\n"
"v_mul_f32 v219, v219, v26\n"
"v_mul_f32 v240, v240, v26\n"
"v_mul_f32 v241, v241, v26\n"
"v_mul_f32 v242, v242, v26\n"
"v_mul_f32 v243, v243, v26\n"
"v_lshlrev_b32 v22, 16, v100\n"
"v_and_b32 v23, 0xffff0000, v100\n"
"v_mul_f32 v22, v22, v26\n"
"v_mul_f32 v23, v23, v26\n"
"v_cvt_pk_bf16_f32 v100, v22, v23\n"
"v_lshlrev_b32 v22, 16, v101\n"
"v_and_b32 v23, 0xffff0000, v101\n"
"v_mul_f32 v22, v22, v26\n"
"v_mul_f32 v23, v23, v26\n"
"v_cvt_pk_bf16_f32 v101, v22, v23\n"
"v_lshlrev_b32 v22, 16, v102\n"
"v_and_b32 v23, 0xffff0000, v102\n"
"v_mul_f32 v22, v22, v26\n"
"v_mul_f32 v23, v23, v26\n"
"v_cvt_pk_bf16_f32 v102, v22, v23\n"
"v_lshlrev_b32 v22, 16, v103\n"
"v_and_b32 v23, 0xffff0000, v103\n"
"v_mul_f32 v22, v22, v26\n"
"v_mul_f32 v23, v23, v26\n"
"v_cvt_pk_bf16_f32 v103, v22, v23\n"
"v_fma_f32 v22, v64, s62, v11\n"
"v_fma_f32 v23, v65, s62, v11\n"
"v_exp_f32 v22, v22\n"
"v_exp_f32 v23, v23\n"
"s_nop 0\n"
"v_add_f32 v28, v22, v23\n"
"v_cvt_pk_bf16_f32 v68, v22, v23\n"
"v_fma_f32 v22, v66, s62, v11\n"
"v_fma_f32 v23, v67, s62, v11\n"
"v_exp_f32 v22, v22\n"
"v_exp_f32 v23, v23\n"
"s_nop 0\n"
"v_add_f32 v28, v28, v22\n"
"v_add_f32 v28, v28, v23\n"
"v_cvt_pk_bf16_f32 v69, v22, v23\n"
"v_fma_f32 v22, v56, s62, v11\n"
"v_fma_f32 v23, v57, s62, v11\n"
"v_exp_f32 v22, v22\n"
"v_exp_f32 v23, v23\n"
"s_nop 0\n"
"v_add_f32 v28, v28, v22\n"
"v_add_f32 v28, v28, v23\n"
"v_cvt_pk_bf16_f32 v70, v22, v23\n"
"v_fma_f32 v22, v58, s62, v11\n"
"v_fma_f32 v23, v59, s62, v11\n"
"v_exp_f32 v22, v22\n"
"v_exp_f32 v23, v23\n"
"s_nop 0\n"
"v_add_f32 v28, v28, v22\n"
"v_add_f32 v28, v28, v23\n"
"v_cvt_pk_bf16_f32 v71, v22, v23\n"
"v_max3_f32 v25, v84, v85, v86\n"
"v_max3_f32 v26, v87, v76, v77\n"
"v_max3_f32 v25, v25, v78, v79\n"
"v_max_f32 v25, v25, v26\n"
"v_mov_b32 v26, v25\n"
"s_nop 1\n"
"v_permlane32_swap_b32 v25, v26\n"
"s_nop 1\n"
"v_max_f32 v25, v25, v26\n"
"v_mov_b32 v26, v25\n"
"s_nop 1\n"
"v_permlane16_swap_b32 v25, v26\n"
"s_nop 1\n"
"v_max_f32 v25, v25, v26\n"
"v_mul_f32 v25, s62, v25\n"
"v_sub_f32 v25, 0, v25\n"
"v_min_f32 v25, v25, v12\n"
"v_sub_f32 v26, v25, v12\n"
"v_exp_f32 v26, v26\n"
"v_mov_b32 v12, v25\n"
"s_nop 0\n"
"v_mul_f32 v18, v18, v26\n"
"v_accvgpr_read_b32 v22, a108\n"
"v_accvgpr_read_b32 v23, a109\n"
"v_mul_f32 v22, v22, v26\n"
"v_mul_f32 v23, v23, v26\n"
"v_accvgpr_write_b32 a108, v22\n"
"v_accvgpr_write_b32 a109, v23\n"
"v_accvgpr_read_b32 v22, a110\n"
"v_accvgpr_read_b32 v23, a111\n"
"v_mul_f32 v22, v22, v26\n"
"v_mul_f32 v23, v23, v26\n"
"v_accvgpr_write_b32 a110, v22\n"
"v_accvgpr_write_b32 a111, v23\n"
"v_accvgpr_read_b32 v22, a132\n"
"v_accvgpr_read_b32 v23, a133\n"
"v_mul_f32 v22, v22, v26\n"
"v_mul_f32 v23, v23, v26\n"
"v_accvgpr_write_b32 a132, v22\n"
"v_accvgpr_write_b32 a133, v23\n"
"v_accvgpr_read_b32 v22, a134\n"
"v_accvgpr_read_b32 v23, a135\n"
"v_mul_f32 v22, v22, v26\n"
"v_mul_f32 v23, v23, v26\n"
"v_accvgpr_write_b32 a134, v22\n"
"v_accvgpr_write_b32 a135, v23\n"
"v_accvgpr_read_b32 v22, a156\n"
"v_accvgpr_read_b32 v23, a157\n"
"v_mul_f32 v22, v22, v26\n"
"v_mul_f32 v23, v23, v26\n"
"v_accvgpr_write_b32 a156, v22\n"
"v_accvgpr_write_b32 a157, v23\n"
"v_accvgpr_read_b32 v22, a158\n"
"v_accvgpr_read_b32 v23, a159\n"
"v_mul_f32 v22, v22, v26\n"
"v_mul_f32 v23, v23, v26\n"
"v_accvgpr_write_b32 a158, v22\n"
"v_accvgpr_write_b32 a159, v23\n"
"v_accvgpr_read_b32 v22, a180\n"
"v_accvgpr_read_b32 v23, a181\n"
"v_mul_f32 v22, v22, v26\n"
"v_mul_f32 v23, v23, v26\n"
"v_accvgpr_write_b32 a180, v22\n"
"v_accvgpr_write_b32 a181, v23\n"
"v_accvgpr_read_b32 v22, a182\n"
"v_accvgpr_read_b32 v23, a183\n"
"v_mul_f32 v22, v22, v26\n"
"v_mul_f32 v23, v23, v26\n"
"v_accvgpr_write_b32 a182, v22\n"
"v_accvgpr_write_b32 a183, v23\n"
"v_accvgpr_read_b32 v22, a204\n"
"v_accvgpr_read_b32 v23, a205\n"
"v_mul_f32 v22, v22, v26\n"
"v_mul_f32 v23, v23, v26\n"
"v_accvgpr_write_b32 a204, v22\n"
"v_accvgpr_write_b32 a205, v23\n"
"v_accvgpr_read_b32 v22, a206\n"
"v_accvgpr_read_b32 v23, a207\n"
"v_mul_f32 v22, v22, v26\n"
"v_mul_f32 v23, v23, v26\n"
"v_accvgpr_write_b32 a206, v22\n"
"v_accvgpr_write_b32 a207, v23\n"
"v_accvgpr_read_b32 v22, a228\n"
"v_accvgpr_read_b32 v23, a229\n"
"v_mul_f32 v22, v22, v26\n"
"v_mul_f32 v23, v23, v26\n"
"v_accvgpr_write_b32 a228, v22\n"
"v_accvgpr_write_b32 a229, v23\n"
"v_accvgpr_read_b32 v22, a230\n"
"v_accvgpr_read_b32 v23, a231\n"
"v_mul_f32 v22, v22, v26\n"
"v_mul_f32 v23, v23, v26\n"
"v_accvgpr_write_b32 a230, v22\n"
"v_accvgpr_write_b32 a231, v23\n"
"v_accvgpr_read_b32 v22, a252\n"
"v_accvgpr_read_b32 v23, a253\n"
"v_mul_f32 v22, v22, v26\n"
"v_mul_f32 v23, v23, v26\n"
"v_accvgpr_write_b32 a252, v22\n"
"v_accvgpr_write_b32 a253, v23\n"
"v_accvgpr_read_b32 v22, a254\n"
"v_accvgpr_read_b32 v23, a255\n"
"v_mul_f32 v22, v22, v26\n"
"v_mul_f32 v23, v23, v26\n"
"v_accvgpr_write_b32 a254, v22\n"
"v_accvgpr_write_b32 a255, v23\n"
"v_mul_f32 v148, v148, v26\n"
"v_mul_f32 v149, v149, v26\n"
"v_mul_f32 v150, v150, v26\n"
"v_mul_f32 v151, v151, v26\n"
"v_mul_f32 v172, v172, v26\n"
"v_mul_f32 v173, v173, v26\n"
"v_mul_f32 v174, v174, v26\n"
"v_mul_f32 v175, v175, v26\n"
"v_mul_f32 v196, v196, v26\n"
"v_mul_f32 v197, v197, v26\n"
"v_mul_f32 v198, v198, v26\n"
"v_mul_f32 v199, v199, v26\n"
"v_mul_f32 v220, v220, v26\n"
"v_mul_f32 v221, v221, v26\n"
"v_mul_f32 v222, v222, v26\n"
"v_mul_f32 v223, v223, v26\n"
"v_mul_f32 v244, v244, v26\n"
"v_mul_f32 v245, v245, v26\n"
"v_mul_f32 v246, v246, v26\n"
"v_mul_f32 v247, v247, v26\n"
"v_lshlrev_b32 v22, 16, v108\n"
"v_and_b32 v23, 0xffff0000, v108\n"
"v_mul_f32 v22, v22, v26\n"
"v_mul_f32 v23, v23, v26\n"
"v_cvt_pk_bf16_f32 v108, v22, v23\n"
"v_lshlrev_b32 v22, 16, v109\n"
"v_and_b32 v23, 0xffff0000, v109\n"
"v_mul_f32 v22, v22, v26\n"
"v_mul_f32 v23, v23, v26\n"
"v_cvt_pk_bf16_f32 v109, v22, v23\n"
"v_lshlrev_b32 v22, 16, v110\n"
"v_and_b32 v23, 0xffff0000, v110\n"
"v_mul_f32 v22, v22, v26\n"
"v_mul_f32 v23, v23, v26\n"
"v_cvt_pk_bf16_f32 v110, v22, v23\n"
"v_lshlrev_b32 v22, 16, v111\n"
"v_and_b32 v23, 0xffff0000, v111\n"
"v_mul_f32 v22, v22, v26\n"
"v_mul_f32 v23, v23, v26\n"
"v_cvt_pk_bf16_f32 v111, v22, v23\n"
"v_fma_f32 v22, v84, s62, v12\n"
"v_fma_f32 v23, v85, s62, v12\n"
"v_exp_f32 v22, v22\n"
"v_exp_f32 v23, v23\n"
"s_nop 0\n"
"v_add_f32 v29, v22, v23\n"
"v_cvt_pk_bf16_f32 v112, v22, v23\n"
"v_fma_f32 v22, v86, s62, v12\n"
"v_fma_f32 v23, v87, s62, v12\n"
"v_exp_f32 v22, v22\n"
"v_exp_f32 v23, v23\n"
"s_nop 0\n"
"v_add_f32 v29, v29, v22\n"
"v_add_f32 v29, v29, v23\n"
"v_cvt_pk_bf16_f32 v113, v22, v23\n"
"v_fma_f32 v22, v76, s62, v12\n"
"v_fma_f32 v23, v77, s62, v12\n"
"v_exp_f32 v22, v22\n"
"v_exp_f32 v23, v23\n"
"s_nop 0\n"
"v_add_f32 v29, v29, v22\n"
"v_add_f32 v29, v29, v23\n"
"v_cvt_pk_bf16_f32 v114, v22, v23\n"
"v_fma_f32 v22, v78, s62, v12\n"
"v_fma_f32 v23, v79, s62, v12\n"
"v_exp_f32 v22, v22\n"
"v_exp_f32 v23, v23\n"
"s_nop 0\n"
"v_add_f32 v29, v29, v22\n"
"v_add_f32 v29, v29, v23\n"
"v_cvt_pk_bf16_f32 v115, v22, v23\n"
"v_fma_f32 v22, v72, s62, v13\n"
"v_fma_f32 v23, v73, s62, v13\n"
"s_nop 1\n"
"s_branch .Lret_8_b\n"
".Lrare_8_d4:\n"
"s_nop 7\n"
"v_max3_f32 v25, v72, v73, v74\n"
"v_max3_f32 v26, v75, v80, v81\n"
"v_max3_f32 v25, v25, v82, v83\n"
"v_max_f32 v25, v25, v26\n"
"v_mov_b32 v26, v25\n"
"s_nop 1\n"
"v_permlane32_swap_b32 v25, v26\n"
"s_nop 1\n"
"v_max_f32 v25, v25, v26\n"
"v_mov_b32 v26, v25\n"
"s_nop 1\n"
"v_permlane16_swap_b32 v25, v26\n"
"s_nop 1\n"
"v_max_f32 v25, v25, v26\n"
"v_mul_f32 v25, s62, v25\n"
"v_sub_f32 v25, 0, v25\n"
"v_min_f32 v25, v25, v13\n"
"v_sub_f32 v26, v25, v13\n"
"v_exp_f32 v26, v26\n"
"v_mov_b32 v13, v25\n"
"s_nop 0\n"
"v_mul_f32 v19, v19, v26\n"
"v_accvgpr_read_b32 v22, a112\n"
"v_accvgpr_read_b32 v23, a113\n"
"v_mul_f32 v22, v22, v26\n"
"v_mul_f32 v23, v23, v26\n"
"v_accvgpr_write_b32 a112, v22\n"
"v_accvgpr_write_b32 a113, v23\n"
"v_accvgpr_read_b32 v22, a114\n"
"v_accvgpr_read_b32 v23, a115\n"
"v_mul_f32 v22, v22, v26\n"
"v_mul_f32 v23, v23, v26\n"
"v_accvgpr_write_b32 a114, v22\n"
"v_accvgpr_write_b32 a115, v23\n"
"v_accvgpr_read_b32 v22, a136\n"
"v_accvgpr_read_b32 v23, a137\n"
"v_mul_f32 v22, v22, v26\n"
"v_mul_f32 v23, v23, v26\n"
"v_accvgpr_write_b32 a136, v22\n"
"v_accvgpr_write_b32 a137, v23\n"
"v_accvgpr_read_b32 v22, a138\n"
"v_accvgpr_read_b32 v23, a139\n"
"v_mul_f32 v22, v22, v26\n"
"v_mul_f32 v23, v23, v26\n"
"v_accvgpr_write_b32 a138, v22\n"
"v_accvgpr_write_b32 a139, v23\n"
"v_accvgpr_read_b32 v22, a160\n"
"v_accvgpr_read_b32 v23, a161\n"
"v_mul_f32 v22, v22, v26\n"
"v_mul_f32 v23, v23, v26\n"
"v_accvgpr_write_b32 a160, v22\n"
"v_accvgpr_write_b32 a161, v23\n"
"v_accvgpr_read_b32 v22, a162\n"
"v_accvgpr_read_b32 v23, a163\n"
"v_mul_f32 v22, v22, v26\n"
"v_mul_f32 v23, v23, v26\n"
"v_accvgpr_write_b32 a162, v22\n"
"v_accvgpr_write_b32 a163, v23\n"
"v_accvgpr_read_b32 v22, a184\n"
"v_accvgpr_read_b32 v23, a185\n"
"v_mul_f32 v22, v22, v26\n"
"v_mul_f32 v23, v23, v26\n"
"v_accvgpr_write_b32 a184, v22\n"
"v_accvgpr_write_b32 a185, v23\n"
"v_accvgpr_read_b32 v22, a186\n"
"v_accvgpr_read_b32 v23, a187\n"
"v_mul_f32 v22, v22, v26\n"
"v_mul_f32 v23, v23, v26\n"
"v_accvgpr_write_b32 a186, v22\n"
"v_accvgpr_write_b32 a187, v23\n"
"v_accvgpr_read_b32 v22, a208\n"
"v_accvgpr_read_b32 v23, a209\n"
"v_mul_f32 v22, v22, v26\n"
"v_mul_f32 v23, v23, v26\n"
"v_accvgpr_write_b32 a208, v22\n"
"v_accvgpr_write_b32 a209, v23\n"
"v_accvgpr_read_b32 v22, a210\n"
"v_accvgpr_read_b32 v23, a211\n"
"v_mul_f32 v22, v22, v26\n"
"v_mul_f32 v23, v23, v26\n"
"v_accvgpr_write_b32 a210, v22\n"
"v_accvgpr_write_b32 a211, v23\n"
"v_accvgpr_read_b32 v22, a232\n"
"v_accvgpr_read_b32 v23, a233\n"
"v_mul_f32 v22, v22, v26\n"
"v_mul_f32 v23, v23, v26\n"
"v_accvgpr_write_b32 a232, v22\n"
"v_accvgpr_write_b32 a233, v23\n"
"v_accvgpr_read_b32 v22, a234\n"
"v_accvgpr_read_b32 v23, a235\n"
"v_mul_f32 v22, v22, v26\n"
"v_mul_f32 v23, v23, v26\n"
"v_accvgpr_write_b32 a234, v22\n"
"v_accvgpr_write_b32 a235, v23\n"
"v_mul_f32 v128, v128, v26\n"
"v_mul_f32 v129, v129, v26\n"
"v_mul_f32 v130, v130, v26\n"
"v_mul_f32 v131, v131, v26\n"
"v_mul_f32 v152, v152, v26\n"
"v_mul_f32 v153, v153, v26\n"
"v_mul_f32 v154, v154, v26\n"
"v_mul_f32 v155, v155, v26\n"
"v_mul_f32 v176, v176, v26\n"
"v_mul_f32 v177, v177, v26\n"
"v_mul_f32 v178, v178, v26\n"
"v_mul_f32 v179, v179, v26\n"
"v_mul_f32 v200, v200, v26\n"
"v_mul_f32 v201, v201, v26\n"
"v_mul_f32 v202, v202, v26\n"
"v_mul_f32 v203, v203, v26\n"
"v_mul_f32 v224, v224, v26\n"
"v_mul_f32 v225, v225, v26\n"
"v_mul_f32 v226, v226, v26\n"
"v_mul_f32 v227, v227, v26\n"
"v_mul_f32 v248, v248, v26\n"
"v_mul_f32 v249, v249, v26\n"
"v_mul_f32 v250, v250, v26\n"
"v_mul_f32 v251, v251, v26\n"
"v_fma_f32 v22, v72, s62, v13\n"
"v_fma_f32 v23, v73, s62, v13\n"
"v_exp_f32 v22, v22\n"
"v_exp_f32 v23, v23\n"
"s_nop 0\n"
"v_add_f32 v21, v22, v23\n"
"v_cvt_pk_bf16_f32 v120, v22, v23\n"
"v_fma_f32 v22, v74, s62, v13\n"
"v_fma_f32 v23, v75, s62, v13\n"
"v_exp_f32 v22, v22\n"
"v_exp_f32 v23, v23\n"
"s_nop 0\n"
"v_add_f32 v21, v21, v22\n"
"v_add_f32 v21, v21, v23\n"
"v_cvt_pk_bf16_f32 v121, v22, v23\n"
"v_fma_f32 v22, v80, s62, v13\n"
"v_fma_f32 v23, v81, s62, v13\n"
"v_exp_f32 v22, v22\n"
"v_exp_f32 v23, v23\n"
"s_nop 0\n"
"v_add_f32 v21, v21, v22\n"
"v_add_f32 v21, v21, v23\n"
"v_cvt_pk_bf16_f32 v122, v22, v23\n"
"v_fma_f32 v22, v82, s62, v13\n"
"v_fma_f32 v23, v83, s62, v13\n"
"v_exp_f32 v22, v22\n"
"v_exp_f32 v23, v23\n"
"s_nop 0\n"
"v_add_f32 v21, v21, v22\n"
"v_add_f32 v21, v21, v23\n"
"v_cvt_pk_bf16_f32 v123, v22, v23\n"
"v_fma_f32 v22, v60, s62, v14\n"
"v_fma_f32 v23, v61, s62, v14\n"
"s_nop 1\n"
"s_branch .Lret_8_d4\n"
".Lrare_8_d5:\n"
"s_nop 7\n"
"v_max3_f32 v25, v60, v61, v62\n"
"v_max3_f32 v26, v63, v48, v49\n"
"v_max3_f32 v25, v25, v50, v51\n"
"v_max_f32 v25, v25, v26\n"
"v_mov_b32 v26, v25\n"
"s_nop 1\n"
"v_permlane32_swap_b32 v25, v26\n"
"s_nop 1\n"
"v_max_f32 v25, v25, v26\n"
"v_mov_b32 v26, v25\n"
"s_nop 1\n"
"v_permlane16_swap_b32 v25, v26\n"
"s_nop 1\n"
"v_max_f32 v25, v25, v26\n"
"v_mul_f32 v25, s62, v25\n"
"v_sub_f32 v25, 0, v25\n"
"v_min_f32 v25, v25, v14\n"
"v_sub_f32 v26, v25, v14\n"
"v_exp_f32 v26, v26\n"
"v_mov_b32 v14, v25\n"
"s_nop 0\n"
"v_mul_f32 v20, v20, v26\n"
"v_accvgpr_read_b32 v22, a116\n"
"v_accvgpr_read_b32 v23, a117\n"
"v_mul_f32 v22, v22, v26\n"
"v_mul_f32 v23, v23, v26\n"
"v_accvgpr_write_b32 a116, v22\n"
"v_accvgpr_write_b32 a117, v23\n"
"v_accvgpr_read_b32 v22, a118\n"
"v_accvgpr_read_b32 v23, a119\n"
"v_mul_f32 v22, v22, v26\n"
"v_mul_f32 v23, v23, v26\n"
"v_accvgpr_write_b32 a118, v22\n"
"v_accvgpr_write_b32 a119, v23\n"
"v_accvgpr_read_b32 v22, a140\n"
"v_accvgpr_read_b32 v23, a141\n"
"v_mul_f32 v22, v22, v26\n"
"v_mul_f32 v23, v23, v26\n"
"v_accvgpr_write_b32 a140, v22\n"
"v_accvgpr_write_b32 a141, v23\n"
"v_accvgpr_read_b32 v22, a142\n"
"v_accvgpr_read_b32 v23, a143\n"
"v_mul_f32 v22, v22, v26\n"
"v_mul_f32 v23, v23, v26\n"
"v_accvgpr_write_b32 a142, v22\n"
"v_accvgpr_write_b32 a143, v23\n"
"v_accvgpr_read_b32 v22, a164\n"
"v_accvgpr_read_b32 v23, a165\n"
"v_mul_f32 v22, v22, v26\n"
"v_mul_f32 v23, v23, v26\n"
"v_accvgpr_write_b32 a164, v22\n"
"v_accvgpr_write_b32 a165, v23\n"
"v_accvgpr_read_b32 v22, a166\n"
"v_accvgpr_read_b32 v23, a167\n"
"v_mul_f32 v22, v22, v26\n"
"v_mul_f32 v23, v23, v26\n"
"v_accvgpr_write_b32 a166, v22\n"
"v_accvgpr_write_b32 a167, v23\n"
"v_accvgpr_read_b32 v22, a188\n"
"v_accvgpr_read_b32 v23, a189\n"
"v_mul_f32 v22, v22, v26\n"
"v_mul_f32 v23, v23, v26\n"
"v_accvgpr_write_b32 a188, v22\n"
"v_accvgpr_write_b32 a189, v23\n"
"v_accvgpr_read_b32 v22, a190\n"
"v_accvgpr_read_b32 v23, a191\n"
"v_mul_f32 v22, v22, v26\n"
"v_mul_f32 v23, v23, v26\n"
"v_accvgpr_write_b32 a190, v22\n"
"v_accvgpr_write_b32 a191, v23\n"
"v_accvgpr_read_b32 v22, a212\n"
"v_accvgpr_read_b32 v23, a213\n"
"v_mul_f32 v22, v22, v26\n"
"v_mul_f32 v23, v23, v26\n"
"v_accvgpr_write_b32 a212, v22\n"
"v_accvgpr_write_b32 a213, v23\n"
"v_accvgpr_read_b32 v22, a214\n"
"v_accvgpr_read_b32 v23, a215\n"
"v_mul_f32 v22, v22, v26\n"
"v_mul_f32 v23, v23, v26\n"
"v_accvgpr_write_b32 a214, v22\n"
"v_accvgpr_write_b32 a215, v23\n"
"v_accvgpr_read_b32 v22, a236\n"
"v_accvgpr_read_b32 v23, a237\n"
"v_mul_f32 v22, v22, v26\n"
"v_mul_f32 v23, v23, v26\n"
"v_accvgpr_write_b32 a236, v22\n"
"v_accvgpr_write_b32 a237, v23\n"
"v_accvgpr_read_b32 v22, a238\n"
"v_accvgpr_read_b32 v23, a239\n"
"v_mul_f32 v22, v22, v26\n"
"v_mul_f32 v23, v23, v26\n"
"v_accvgpr_write_b32 a238, v22\n"
"v_accvgpr_write_b32 a239, v23\n"
"v_mul_f32 v132, v132, v26\n"
"v_mul_f32 v133, v133, v26\n"
"v_mul_f32 v134, v134, v26\n"
"v_mul_f32 v135, v135, v26\n"
"v_mul_f32 v156, v156, v26\n"
"v_mul_f32 v157, v157, v26\n"
"v_mul_f32 v158, v158, v26\n"
"v_mul_f32 v159, v159, v26\n"
"v_mul_f32 v180, v180, v26\n"
"v_mul_f32 v181, v181, v26\n"
"v_mul_f32 v182, v182, v26\n"
"v_mul_f32 v183, v183, v26\n"
"v_mul_f32 v204, v204, v26\n"
"v_mul_f32 v205, v205, v26\n"
"v_mul_f32 v206, v206, v26\n"
"v_mul_f32 v207, v207, v26\n"
"v_mul_f32 v228, v228, v26\n"
"v_mul_f32 v229, v229, v26\n"
"v_mul_f32 v230, v230, v26\n"
"v_mul_f32 v231, v231, v26\n"
"v_mul_f32 v252, v252, v26\n"
"v_mul_f32 v253, v253, v26\n"
"v_mul_f32 v254, v254, v26\n"
"v_mul_f32 v255, v255, v26\n"
"v_fma_f32 v22, v60, s62, v14\n"
"v_fma_f32 v23, v61, s62, v14\n"
"v_exp_f32 v22, v22\n"
"v_exp_f32 v23, v23\n"
"s_nop 0\n"
"v_add_f32 v21, v22, v23\n"
"v_cvt_pk_bf16_f32 v72, v22, v23\n"
"v_fma_f32 v22, v62, s62, v14\n"
"v_fma_f32 v23, v63, s62, v14\n"
"v_exp_f32 v22, v22\n"
"v_exp_f32 v23, v23\n"
"s_nop 0\n"
"v_add_f32 v21, v21, v22\n"
"v_add_f32 v21, v21, v23\n"
"v_cvt_pk_bf16_f32 v73, v22, v23\n"
"v_fma_f32 v22, v48, s62, v14\n"
"v_fma_f32 v23, v49, s62, v14\n"
"v_exp_f32 v22, v22\n"
"v_exp_f32 v23, v23\n"
"s_nop 0\n"
"v_add_f32 v21, v21, v22\n"
"v_add_f32 v21, v21, v23\n"
"v_cvt_pk_bf16_f32 v74, v22, v23\n"
"v_fma_f32 v22, v50, s62, v14\n"
"v_fma_f32 v23, v51, s62, v14\n"
"v_exp_f32 v22, v22\n"
"v_exp_f32 v23, v23\n"
"s_nop 0\n"
"v_add_f32 v21, v21, v22\n"
"v_add_f32 v21, v21, v23\n"
"v_cvt_pk_bf16_f32 v75, v22, v23\n"
"s_nop 1\n"
"s_branch .Lret_8_d5\n"
".Lrare_9_a:\n"
"s_nop 7\n"
"v_max3_f32 v25, v116, v117, v118\n"
"v_max3_f32 v26, v119, v76, v77\n"
"v_max3_f32 v25, v25, v78, v79\n"
"v_max_f32 v25, v25, v26\n"
"v_mov_b32 v26, v25\n"
"s_nop 1\n"
"v_permlane32_swap_b32 v25, v26\n"
"s_nop 1\n"
"v_max_f32 v25, v25, v26\n"
"v_mov_b32 v26, v25\n"
"s_nop 1\n"
"v_permlane16_swap_b32 v25, v26\n"
"s_nop 1\n"
"v_max_f32 v25, v25, v26\n"
"v_mul_f32 v25, s62, v25\n"
"v_sub_f32 v25, 0, v25\n"
"v_min_f32 v25, v25, v9\n"
"v_sub_f32 v26, v25, v9\n"
"v_exp_f32 v26, v26\n"
"v_mov_b32 v9, v25\n"
"s_nop 0\n"
"v_mul_f32 v15, v15, v26\n"
"v_accvgpr_read_b32 v22, a96\n"
"v_accvgpr_read_b32 v23, a97\n"
"v_mul_f32 v22, v22, v26\n"
"v_mul_f32 v23, v23, v26\n"
"v_accvgpr_write_b32 a96, v22\n"
"v_accvgpr_write_b32 a97, v23\n"
"v_accvgpr_read_b32 v22, a98\n"
"v_accvgpr_read_b32 v23, a99\n"
"v_mul_f32 v22, v22, v26\n"
"v_mul_f32 v23, v23, v26\n"
"v_accvgpr_write_b32 a98, v22\n"
"v_accvgpr_write_b32 a99, v23\n"
"v_accvgpr_read_b32 v22, a120\n"
"v_accvgpr_read_b32 v23, a121\n"
"v_mul_f32 v22, v22, v26\n"
"v_mul_f32 v23, v23, v26\n"
"v_accvgpr_write_b32 a120, v22\n"
"v_accvgpr_write_b32 a121, v23\n"
"v_accvgpr_read_b32 v22, a122\n"
"v_accvgpr_read_b32 v23, a123\n"
"v_mul_f32 v22, v22, v26\n"
"v_mul_f32 v23, v23, v26\n"
"v_accvgpr_write_b32 a122, v22\n"
"v_accvgpr_write_b32 a123, v23\n"
"v_accvgpr_read_b32 v22, a144\n"
"v_accvgpr_read_b32 v23, a145\n"
"v_mul_f32 v22, v22, v26\n"
"v_mul_f32 v23, v23, v26\n"
"v_accvgpr_write_b32 a144, v22\n"
"v_accvgpr_write_b32 a145, v23\n"
"v_accvgpr_read_b32 v22, a146\n"
"v_accvgpr_read_b32 v23, a147\n"
"v_mul_f32 v22, v22, v26\n"
"v_mul_f32 v23, v23, v26\n"
"v_accvgpr_write_b32 a146, v22\n"
"v_accvgpr_write_b32 a147, v23\n"
"v_accvgpr_read_b32 v22, a168\n"
"v_accvgpr_read_b32 v23, a169\n"
"v_mul_f32 v22, v22, v26\n"
"v_mul_f32 v23, v23, v26\n"
"v_accvgpr_write_b32 a168, v22\n"
"v_accvgpr_write_b32 a169, v23\n"
"v_accvgpr_read_b32 v22, a170\n"
"v_accvgpr_read_b32 v23, a171\n"
"v_mul_f32 v22, v22, v26\n"
"v_mul_f32 v23, v23, v26\n"
"v_accvgpr_write_b32 a170, v22\n"
"v_accvgpr_write_b32 a171, v23\n"
"v_accvgpr_read_b32 v22, a192\n"
"v_accvgpr_read_b32 v23, a193\n"
"v_mul_f32 v22, v22, v26\n"
"v_mul_f32 v23, v23, v26\n"
"v_accvgpr_write_b32 a192, v22\n"
"v_accvgpr_write_b32 a193, v23\n"
"v_accvgpr_read_b32 v22, a194\n"
"v_accvgpr_read_b32 v23, a195\n"
"v_mul_f32 v22, v22, v26\n"
"v_mul_f32 v23, v23, v26\n"
"v_accvgpr_write_b32 a194, v22\n"
"v_accvgpr_write_b32 a195, v23\n"
"v_accvgpr_read_b32 v22, a216\n"
"v_accvgpr_read_b32 v23, a217\n"
"v_mul_f32 v22, v22, v26\n"
"v_mul_f32 v23, v23, v26\n"
"v_accvgpr_write_b32 a216, v22\n"
"v_accvgpr_write_b32 a217, v23\n"
"v_accvgpr_read_b32 v22, a218\n"
"v_accvgpr_read_b32 v23, a219\n"
"v_mul_f32 v22, v22, v26\n"
"v_mul_f32 v23, v23, v26\n"
"v_accvgpr_write_b32 a218, v22\n"
"v_accvgpr_write_b32 a219, v23\n"
"v_accvgpr_read_b32 v22, a240\n"
"v_accvgpr_read_b32 v23, a241\n"
"v_mul_f32 v22, v22, v26\n"
"v_mul_f32 v23, v23, v26\n"
"v_accvgpr_write_b32 a240, v22\n"
"v_accvgpr_write_b32 a241, v23\n"
"v_accvgpr_read_b32 v22, a242\n"
"v_accvgpr_read_b32 v23, a243\n"
"v_mul_f32 v22, v22, v26\n"
"v_mul_f32 v23, v23, v26\n"
"v_accvgpr_write_b32 a242, v22\n"
"v_accvgpr_write_b32 a243, v23\n"
"v_mul_f32 v136, v136, v26\n"
"v_mul_f32 v137, v137, v26\n"
"v_mul_f32 v138, v138, v26\n"
"v_mul_f32 v139, v139, v26\n"
"v_mul_f32 v160, v160, v26\n"
"v_mul_f32 v161, v161, v26\n"
"v_mul_f32 v162, v162, v26\n"
"v_mul_f32 v163, v163, v26\n"
"v_mul_f32 v184, v184, v26\n"
"v_mul_f32 v185, v185, v26\n"
"v_mul_f32 v186, v186, v26\n"
"v_mul_f32 v187, v187, v26\n"
"v_mul_f32 v208, v208, v26\n"
"v_mul_f32 v209, v209, v26\n"
"v_mul_f32 v210, v210, v26\n"
"v_mul_f32 v211, v211, v26\n"
"v_mul_f32 v232, v232, v26\n"
"v_mul_f32 v233, v233, v26\n"
"v_mul_f32 v234, v234, v26\n"
"v_mul_f32 v235, v235, v26\n"
"v_lshlrev_b32 v22, 16, v88\n"
"v_and_b32 v23, 0xffff0000, v88\n"
"v_mul_f32 v22, v22, v26\n"
"v_mul_f32 v23, v23, v26\n"
"v_cvt_pk_bf16_f32 v88, v22, v23\n"
"v_lshlrev_b32 v22, 16, v89\n"
"v_and_b32 v23, 0xffff0000, v89\n"
"v_mul_f32 v22, v22, v26\n"
"v_mul_f32 v23, v23, v26\n"
"v_cvt_pk_bf16_f32 v89, v22, v23\n"
"v_lshlrev_b32 v22, 16, v90\n"
"v_and_b32 v23, 0xffff0000, v90\n"
"v_mul_f32 v22, v22, v26\n"
"v_mul_f32 v23, v23, v26\n"
"v_cvt_pk_bf16_f32 v90, v22, v23\n"
"v_lshlrev_b32 v22, 16, v91\n"
"v_and_b32 v23, 0xffff0000, v91\n"
"v_mul_f32 v22, v22, v26\n"
"v_mul_f32 v23, v23, v26\n"
"v_cvt_pk_bf16_f32 v91, v22, v23\n"
"v_fma_f32 v22, v116, s62, v9\n"
"v_fma_f32 v23, v117, s62, v9\n"
"v_exp_f32 v22, v22\n"
"v_exp_f32 v23, v23\n"
"s_nop 0\n"
"v_add_f32 v21, v22, v23\n"
"v_cvt_pk_bf16_f32 v80, v22, v23\n"
"v_fma_f32 v22, v118, s62, v9\n"
"v_fma_f32 v23, v119, s62, v9\n"
"v_exp_f32 v22, v22\n"
"v_exp_f32 v23, v23\n"
"s_nop 0\n"
"v_add_f32 v21, v21, v22\n"
"v_add_f32 v21, v21, v23\n"
"v_cvt_pk_bf16_f32 v81, v22, v23\n"
"v_fma_f32 v22, v76, s62, v9\n"
"v_fma_f32 v23, v77, s62, v9\n"
"v_exp_f32 v22, v22\n"
"v_exp_f32 v23, v23\n"
"s_nop 0\n"
"v_add_f32 v21, v21, v22\n"
"v_add_f32 v21, v21, v23\n"
"v_cvt_pk_bf16_f32 v82, v22, v23\n"
"v_fma_f32 v22, v78, s62, v9\n"
"v_fma_f32 v23, v79, s62, v9\n"
"v_exp_f32 v22, v22\n"
"v_exp_f32 v23, v23\n"
"s_nop 0\n"
"v_add_f32 v21, v21, v22\n"
"v_add_f32 v21, v21, v23\n"
"v_cvt_pk_bf16_f32 v83, v22, v23\n"
"v_max3_f32 v25, v52, v53, v54\n"
"v_max3_f32 v26, v55, v96, v97\n"
"v_max3_f32 v25, v25, v98, v99\n"
"v_max_f32 v25, v25, v26\n"
"v_mov_b32 v26, v25\n"
"s_nop 1\n"
"v_permlane32_swap_b32 v25, v26\n"
"s_nop 1\n"
"v_max_f32 v25, v25, v26\n"
"v_mov_b32 v26, v25\n"
"s_nop 1\n"
"v_permlane16_swap_b32 v25, v26\n"
"s_nop 1\n"
"v_max_f32 v25, v25, v26\n"
"v_mul_f32 v25, s62, v25\n"
"v_sub_f32 v25, 0, v25\n"
"v_min_f32 v25, v25, v10\n"
"v_sub_f32 v26, v25, v10\n"
"v_exp_f32 v26, v26\n"
"v_mov_b32 v10, v25\n"
"s_nop 0\n"
"v_mul_f32 v16, v16, v26\n"
"v_accvgpr_read_b32 v22, a100\n"
"v_accvgpr_read_b32 v23, a101\n"
"v_mul_f32 v22, v22, v26\n"
"v_mul_f32 v23, v23, v26\n"
"v_accvgpr_write_b32 a100, v22\n"
"v_accvgpr_write_b32 a101, v23\n"
"v_accvgpr_read_b32 v22, a102\n"
"v_accvgpr_read_b32 v23, a103\n"
"v_mul_f32 v22, v22, v26\n"
"v_mul_f32 v23, v23, v26\n"
"v_accvgpr_write_b32 a102, v22\n"
"v_accvgpr_write_b32 a103, v23\n"
"v_accvgpr_read_b32 v22, a124\n"
"v_accvgpr_read_b32 v23, a125\n"
"v_mul_f32 v22, v22, v26\n"
"v_mul_f32 v23, v23, v26\n"
"v_accvgpr_write_b32 a124, v22\n"
"v_accvgpr_write_b32 a125, v23\n"
"v_accvgpr_read_b32 v22, a126\n"
"v_accvgpr_read_b32 v23, a127\n"
"v_mul_f32 v22, v22, v26\n"
"v_mul_f32 v23, v23, v26\n"
"v_accvgpr_write_b32 a126, v22\n"
"v_accvgpr_write_b32 a127, v23\n"
"v_accvgpr_read_b32 v22, a148\n"
"v_accvgpr_read_b32 v23, a149\n"
"v_mul_f32 v22, v22, v26\n"
"v_mul_f32 v23, v23, v26\n"
"v_accvgpr_write_b32 a148, v22\n"
"v_accvgpr_write_b32 a149, v23\n"
"v_accvgpr_read_b32 v22, a150\n"
"v_accvgpr_read_b32 v23, a151\n"
"v_mul_f32 v22, v22, v26\n"
"v_mul_f32 v23, v23, v26\n"
"v_accvgpr_write_b32 a150, v22\n"
"v_accvgpr_write_b32 a151, v23\n"
"v_accvgpr_read_b32 v22, a172\n"
"v_accvgpr_read_b32 v23, a173\n"
"v_mul_f32 v22, v22, v26\n"
"v_mul_f32 v23, v23, v26\n"
"v_accvgpr_write_b32 a172, v22\n"
"v_accvgpr_write_b32 a173, v23\n"
"v_accvgpr_read_b32 v22, a174\n"
"v_accvgpr_read_b32 v23, a175\n"
"v_mul_f32 v22, v22, v26\n"
"v_mul_f32 v23, v23, v26\n"
"v_accvgpr_write_b32 a174, v22\n"
"v_accvgpr_write_b32 a175, v23\n"
"v_accvgpr_read_b32 v22, a196\n"
"v_accvgpr_read_b32 v23, a197\n"
"v_mul_f32 v22, v22, v26\n"
"v_mul_f32 v23, v23, v26\n"
"v_accvgpr_write_b32 a196, v22\n"
"v_accvgpr_write_b32 a197, v23\n"
"v_accvgpr_read_b32 v22, a198\n"
"v_accvgpr_read_b32 v23, a199\n"
"v_mul_f32 v22, v22, v26\n"
"v_mul_f32 v23, v23, v26\n"
"v_accvgpr_write_b32 a198, v22\n"
"v_accvgpr_write_b32 a199, v23\n"
"v_accvgpr_read_b32 v22, a220\n"
"v_accvgpr_read_b32 v23, a221\n"
"v_mul_f32 v22, v22, v26\n"
"v_mul_f32 v23, v23, v26\n"
"v_accvgpr_write_b32 a220, v22\n"
"v_accvgpr_write_b32 a221, v23\n"
"v_accvgpr_read_b32 v22, a222\n"
"v_accvgpr_read_b32 v23, a223\n"
"v_mul_f32 v22, v22, v26\n"
"v_mul_f32 v23, v23, v26\n"
"v_accvgpr_write_b32 a222, v22\n"
"v_accvgpr_write_b32 a223, v23\n"
"v_accvgpr_read_b32 v22, a244\n"
"v_accvgpr_read_b32 v23, a245\n"
"v_mul_f32 v22, v22, v26\n"
"v_mul_f32 v23, v23, v26\n"
"v_accvgpr_write_b32 a244, v22\n"
"v_accvgpr_write_b32 a245, v23\n"
"v_accvgpr_read_b32 v22, a246\n"
"v_accvgpr_read_b32 v23, a247\n"
"v_mul_f32 v22, v22, v26\n"
"v_mul_f32 v23, v23, v26\n"
"v_accvgpr_write_b32 a246, v22\n"
"v_accvgpr_write_b32 a247, v23\n"
"v_mul_f32 v140, v140, v26\n"
"v_mul_f32 v141, v141, v26\n"
"v_mul_f32 v142, v142, v26\n"
"v_mul_f32 v143, v143, v26\n"
"v_mul_f32 v164, v164, v26\n"
"v_mul_f32 v165, v165, v26\n"
"v_mul_f32 v166, v166, v26\n"
"v_mul_f32 v167, v167, v26\n"
"v_mul_f32 v188, v188, v26\n"
"v_mul_f32 v189, v189, v26\n"
"v_mul_f32 v190, v190, v26\n"
"v_mul_f32 v191, v191, v26\n"
"v_mul_f32 v212, v212, v26\n"
"v_mul_f32 v213, v213, v26\n"
"v_mul_f32 v214, v214, v26\n"
"v_mul_f32 v215, v215, v26\n"
"v_mul_f32 v236, v236, v26\n"
"v_mul_f32 v237, v237, v26\n"
"v_mul_f32 v238, v238, v26\n"
"v_mul_f32 v239, v239, v26\n"
"v_lshlrev_b32 v22, 16, v92\n"
"v_and_b32 v23, 0xffff0000, v92\n"
"v_mul_f32 v22, v22, v26\n"
"v_mul_f32 v23, v23, v26\n"
"v_cvt_pk_bf16_f32 v92, v22, v23\n"
"v_lshlrev_b32 v22, 16, v93\n"
"v_and_b32 v23, 0xffff0000, v93\n"
"v_mul_f32 v22, v22, v26\n"
"v_mul_f32 v23, v23, v26\n"
"v_cvt_pk_bf16_f32 v93, v22, v23\n"
"v_lshlrev_b32 v22, 16, v94\n"
"v_and_b32 v23, 0xffff0000, v94\n"
"v_mul_f32 v22, v22, v26\n"
"v_mul_f32 v23, v23, v26\n"
"v_cvt_pk_bf16_f32 v94, v22, v23\n"
"v_lshlrev_b32 v22, 16, v95\n"
"v_and_b32 v23, 0xffff0000, v95\n"
"v_mul_f32 v22, v22, v26\n"
"v_mul_f32 v23, v23, v26\n"
"v_cvt_pk_bf16_f32 v95, v22, v23\n"
"v_fma_f32 v22, v52, s62, v10\n"
"v_fma_f32 v23, v53, s62, v10\n"
"v_exp_f32 v22, v22\n"
"v_exp_f32 v23, v23\n"
"s_nop 0\n"
"v_add_f32 v3, v22, v23\n"
"v_cvt_pk_bf16_f32 v60, v22, v23\n"
"v_fma_f32 v22, v54, s62, v10\n"
"v_fma_f32 v23, v55, s62, v10\n"
"v_exp_f32 v22, v22\n"
"v_exp_f32 v23, v23\n"
"s_nop 0\n"
"v_add_f32 v3, v3, v22\n"
"v_add_f32 v3, v3, v23\n"
"v_cvt_pk_bf16_f32 v61, v22, v23\n"
"v_fma_f32 v22, v96, s62, v10\n"
"v_fma_f32 v23, v97, s62, v10\n"
"v_exp_f32 v22, v22\n"
"v_exp_f32 v23, v23\n"
"s_nop 0\n"
"v_add_f32 v3, v3, v22\n"
"v_add_f32 v3, v3, v23\n"
"v_cvt_pk_bf16_f32 v62, v22, v23\n"
"v_fma_f32 v22, v98, s62, v10\n"
"v_fma_f32 v23, v99, s62, v10\n"
"v_exp_f32 v22, v22\n"
"v_exp_f32 v23, v23\n"
"s_nop 0\n"
"v_add_f32 v3, v3, v22\n"
"v_add_f32 v3, v3, v23\n"
"v_cvt_pk_bf16_f32 v63, v22, v23\n"
"v_fma_f32 v22, v28, s62, v11\n"
"v_fma_f32 v23, v29, s62, v11\n"
"s_nop 1\n"
"s_branch .Lret_9_a\n"
".Lrare_9_b:\n"
"s_nop 7\n"
"v_max3_f32 v25, v28, v29, v30\n"
"v_max3_f32 v26, v31, v104, v105\n"
"v_max3_f32 v25, v25, v106, v107\n"
"v_max_f32 v25, v25, v26\n"
"v_mov_b32 v26, v25\n"
"s_nop 1\n"
"v_permlane32_swap_b32 v25, v26\n"
"s_nop 1\n"
"v_max_f32 v25, v25, v26\n"
"v_mov_b32 v26, v25\n"
"s_nop 1\n"
"v_permlane16_swap_b32 v25, v26\n"
"s_nop 1\n"
"v_max_f32 v25, v25, v26\n"
"v_mul_f32 v25, s62, v25\n"
"v_sub_f32 v25, 0, v25\n"
"v_min_f32 v25, v25, v11\n"
"v_sub_f32 v26, v25, v11\n"
"v_exp_f32 v26, v26\n"
"v_mov_b32 v11, v25\n"
"s_nop 0\n"
"v_mul_f32 v17, v17, v26\n"
"v_accvgpr_read_b32 v22, a104\n"
"v_accvgpr_read_b32 v23, a105\n"
"v_mul_f32 v22, v22, v26\n"
"v_mul_f32 v23, v23, v26\n"
"v_accvgpr_write_b32 a104, v22\n"
"v_accvgpr_write_b32 a105, v23\n"
"v_accvgpr_read_b32 v22, a106\n"
"v_accvgpr_read_b32 v23, a107\n"
"v_mul_f32 v22, v22, v26\n"
"v_mul_f32 v23, v23, v26\n"
"v_accvgpr_write_b32 a106, v22\n"
"v_accvgpr_write_b32 a107, v23\n"
"v_accvgpr_read_b32 v22, a128\n"
"v_accvgpr_read_b32 v23, a129\n"
"v_mul_f32 v22, v22, v26\n"
"v_mul_f32 v23, v23, v26\n"
"v_accvgpr_write_b32 a128, v22\n"
"v_accvgpr_write_b32 a129, v23\n"
"v_accvgpr_read_b32 v22, a130\n"
"v_accvgpr_read_b32 v23, a131\n"
"v_mul_f32 v22, v22, v26\n"
"v_mul_f32 v23, v23, v26\n"
"v_accvgpr_write_b32 a130, v22\n"
"v_accvgpr_write_b32 a131, v23\n"
"v_accvgpr_read_b32 v22, a152\n"
"v_accvgpr_read_b32 v23, a153\n"
"v_mul_f32 v22, v22, v26\n"
"v_mul_f32 v23, v23, v26\n"
"v_accvgpr_write_b32 a152, v22\n"
"v_accvgpr_write_b32 a153, v23\n"
"v_accvgpr_read_b32 v22, a154\n"
"v_accvgpr_read_b32 v23, a155\n"
"v_mul_f32 v22, v22, v26\n"
"v_mul_f32 v23, v23, v26\n"
"v_accvgpr_write_b32 a154, v22\n"
"v_accvgpr_write_b32 a155, v23\n"
"v_accvgpr_read_b32 v22, a176\n"
"v_accvgpr_read_b32 v23, a177\n"
"v_mul_f32 v22, v22, v26\n"
"v_mul_f32 v23, v23, v26\n"
"v_accvgpr_write_b32 a176, v22\n"
"v_accvgpr_write_b32 a177, v23\n"
"v_accvgpr_read_b32 v22, a178\n"
"v_accvgpr_read_b32 v23, a179\n"
"v_mul_f32 v22, v22, v26\n"
"v_mul_f32 v23, v23, v26\n"
"v_accvgpr_write_b32 a178, v22\n"
"v_accvgpr_write_b32 a179, v23\n"
"v_accvgpr_read_b32 v22, a200\n"
"v_accvgpr_read_b32 v23, a201\n"
"v_mul_f32 v22, v22, v26\n"
"v_mul_f32 v23, v23, v26\n"
"v_accvgpr_write_b32 a200, v22\n"
"v_accvgpr_write_b32 a201, v23\n"
"v_accvgpr_read_b32 v22, a202\n"
"v_accvgpr_read_b32 v23, a203\n"
"v_mul_f32 v22, v22, v26\n"
"v_mul_f32 v23, v23, v26\n"
"v_accvgpr_write_b32 a202, v22\n"
"v_accvgpr_write_b32 a203, v23\n"
"v_accvgpr_read_b32 v22, a224\n"
"v_accvgpr_read_b32 v23, a225\n"
"v_mul_f32 v22, v22, v26\n"
"v_mul_f32 v23, v23, v26\n"
"v_accvgpr_write_b32 a224, v22\n"
"v_accvgpr_write_b32 a225, v23\n"
"v_accvgpr_read_b32 v22, a226\n"
"v_accvgpr_read_b32 v23, a227\n"
"v_mul_f32 v22, v22, v26\n"
"v_mul_f32 v23, v23, v26\n"
"v_accvgpr_write_b32 a226, v22\n"
"v_accvgpr_write_b32 a227, v23\n"
"v_accvgpr_read_b32 v22, a248\n"
"v_accvgpr_read_b32 v23, a249\n"
"v_mul_f32 v22, v22, v26\n"
"v_mul_f32 v23, v23, v26\n"
"v_accvgpr_write_b32 a248, v22\n"
"v_accvgpr_write_b32 a249, v23\n"
"v_accvgpr_read_b32 v22, a250\n"
"v_accvgpr_read_b32 v23, a251\n"
"v_mul_f32 v22, v22, v26\n"
"v_mul_f32 v23, v23, v26\n"
"v_accvgpr_write_b32 a250, v22\n"
"v_accvgpr_write_b32 a251, v23\n"
"v_mul_f32 v144, v144, v26\n"
"v_mul_f32 v145, v145, v26\n"
"v_mul_f32 v146, v146, v26\n"
"v_mul_f32 v147, v147, v26\n"
"v_mul_f32 v168, v168, v26\n"
"v_mul_f32 v169, v169, v26\n"
"v_mul_f32 v170, v170, v26\n"
"v_mul_f32 v171, v171, v26\n"
"v_mul_f32 v192, v192, v26\n"
"v_mul_f32 v193, v193, v26\n"
"v_mul_f32 v194, v194, v26\n"
"v_mul_f32 v195, v195, v26\n"
"v_mul_f32 v216, v216, v26\n"
"v_mul_f32 v217, v217, v26\n"
"v_mul_f32 v218, v218, v26\n"
"v_mul_f32 v219, v219, v26\n"
"v_mul_f32 v240, v240, v26\n"
"v_mul_f32 v241, v241, v26\n"
"v_mul_f32 v242, v242, v26\n"
"v_mul_f32 v243, v243, v26\n"
"v_lshlrev_b32 v22, 16, v68\n"
"v_and_b32 v23, 0xffff0000, v68\n"
"v_mul_f32 v22, v22, v26\n"
"v_mul_f32 v23, v23, v26\n"
"v_cvt_pk_bf16_f32 v68, v22, v23\n"
"v_lshlrev_b32 v22, 16, v69\n"
"v_and_b32 v23, 0xffff0000, v69\n"
"v_mul_f32 v22, v22, v26\n"
"v_mul_f32 v23, v23, v26\n"
"v_cvt_pk_bf16_f32 v69, v22, v23\n"
"v_lshlrev_b32 v22, 16, v70\n"
"v_and_b32 v23, 0xffff0000, v70\n"
"v_mul_f32 v22, v22, v26\n"
"v_mul_f32 v23, v23, v26\n"
"v_cvt_pk_bf16_f32 v70, v22, v23\n"
"v_lshlrev_b32 v22, 16, v71\n"
"v_and_b32 v23, 0xffff0000, v71\n"
"v_mul_f32 v22, v22, v26\n"
"v_mul_f32 v23, v23, v26\n"
"v_cvt_pk_bf16_f32 v71, v22, v23\n"
"v_fma_f32 v22, v28, s62, v11\n"
"v_fma_f32 v23, v29, s62, v11\n"
"v_exp_f32 v22, v22\n"
"v_exp_f32 v23, v23\n"
"s_nop 0\n"
"v_add_f32 v76, v22, v23\n"
"v_cvt_pk_bf16_f32 v48, v22, v23\n"
"v_fma_f32 v22, v30, s62, v11\n"
"v_fma_f32 v23, v31, s62, v11\n"
"v_exp_f32 v22, v22\n"
"v_exp_f32 v23, v23\n"
"s_nop 0\n"
"v_add_f32 v76, v76, v22\n"
"v_add_f32 v76, v76, v23\n"
"v_cvt_pk_bf16_f32 v49, v22, v23\n"
"v_fma_f32 v22, v104, s62, v11\n"
"v_fma_f32 v23, v105, s62, v11\n"
"v_exp_f32 v22, v22\n"
"v_exp_f32 v23, v23\n"
"s_nop 0\n"
"v_add_f32 v76, v76, v22\n"
"v_add_f32 v76, v76, v23\n"
"v_cvt_pk_bf16_f32 v50, v22, v23\n"
"v_fma_f32 v22, v106, s62, v11\n"
"v_fma_f32 v23, v107, s62, v11\n"
"v_exp_f32 v22, v22\n"
"v_exp_f32 v23, v23\n"
"s_nop 0\n"
"v_add_f32 v76, v76, v22\n"
"v_add_f32 v76, v76, v23\n"
"v_cvt_pk_bf16_f32 v51, v22, v23\n"
"v_max3_f32 v25, v64, v65, v66\n"
"v_max3_f32 v26, v67, v100, v101\n"
"v_max3_f32 v25, v25, v102, v103\n"
"v_max_f32 v25, v25, v26\n"
"v_mov_b32 v26, v25\n"
"s_nop 1\n"
"v_permlane32_swap_b32 v25, v26\n"
"s_nop 1\n"
"v_max_f32 v25, v25, v26\n"
"v_mov_b32 v26, v25\n"
"s_nop 1\n"
"v_permlane16_swap_b32 v25, v26\n"
"s_nop 1\n"
"v_max_f32 v25, v25, v26\n"
"v_mul_f32 v25, s62, v25\n"
"v_sub_f32 v25, 0, v25\n"
"v_min_f32 v25, v25, v12\n"
"v_sub_f32 v26, v25, v12\n"
"v_exp_f32 v26, v26\n"
"v_mov_b32 v12, v25\n"
"s_nop 0\n"
"v_mul_f32 v18, v18, v26\n"
"v_accvgpr_read_b32 v22, a108\n"
"v_accvgpr_read_b32 v23, a109\n"
"v_mul_f32 v22, v22, v26\n"
"v_mul_f32 v23, v23, v26\n"
"v_accvgpr_write_b32 a108, v22\n"
"v_accvgpr_write_b32 a109, v23\n"
"v_accvgpr_read_b32 v22, a110\n"
"v_accvgpr_read_b32 v23, a111\n"
"v_mul_f32 v22, v22, v26\n"
"v_mul_f32 v23, v23, v26\n"
"v_accvgpr_write_b32 a110, v22\n"
"v_accvgpr_write_b32 a111, v23\n"
"v_accvgpr_read_b32 v22, a132\n"
"v_accvgpr_read_b32 v23, a133\n"
"v_mul_f32 v22, v22, v26\n"
"v_mul_f32 v23, v23, v26\n"
"v_accvgpr_write_b32 a132, v22\n"
"v_accvgpr_write_b32 a133, v23\n"
"v_accvgpr_read_b32 v22, a134\n"
"v_accvgpr_read_b32 v23, a135\n"
"v_mul_f32 v22, v22, v26\n"
"v_mul_f32 v23, v23, v26\n"
"v_accvgpr_write_b32 a134, v22\n"
"v_accvgpr_write_b32 a135, v23\n"
"v_accvgpr_read_b32 v22, a156\n"
"v_accvgpr_read_b32 v23, a157\n"
"v_mul_f32 v22, v22, v26\n"
"v_mul_f32 v23, v23, v26\n"
"v_accvgpr_write_b32 a156, v22\n"
"v_accvgpr_write_b32 a157, v23\n"
"v_accvgpr_read_b32 v22, a158\n"
"v_accvgpr_read_b32 v23, a159\n"
"v_mul_f32 v22, v22, v26\n"
"v_mul_f32 v23, v23, v26\n"
"v_accvgpr_write_b32 a158, v22\n"
"v_accvgpr_write_b32 a159, v23\n"
"v_accvgpr_read_b32 v22, a180\n"
"v_accvgpr_read_b32 v23, a181\n"
"v_mul_f32 v22, v22, v26\n"
"v_mul_f32 v23, v23, v26\n"
"v_accvgpr_write_b32 a180, v22\n"
"v_accvgpr_write_b32 a181, v23\n"
"v_accvgpr_read_b32 v22, a182\n"
"v_accvgpr_read_b32 v23, a183\n"
"v_mul_f32 v22, v22, v26\n"
"v_mul_f32 v23, v23, v26\n"
"v_accvgpr_write_b32 a182, v22\n"
"v_accvgpr_write_b32 a183, v23\n"
"v_accvgpr_read_b32 v22, a204\n"
"v_accvgpr_read_b32 v23, a205\n"
"v_mul_f32 v22, v22, v26\n"
"v_mul_f32 v23, v23, v26\n"
"v_accvgpr_write_b32 a204, v22\n"
"v_accvgpr_write_b32 a205, v23\n"
"v_accvgpr_read_b32 v22, a206\n"
"v_accvgpr_read_b32 v23, a207\n"
"v_mul_f32 v22, v22, v26\n"
"v_mul_f32 v23, v23, v26\n"
"v_accvgpr_write_b32 a206, v22\n"
"v_accvgpr_write_b32 a207, v23\n"
"v_accvgpr_read_b32 v22, a228\n"
"v_accvgpr_read_b32 v23, a229\n"
"v_mul_f32 v22, v22, v26\n"
"v_mul_f32 v23, v23, v26\n"
"v_accvgpr_write_b32 a228, v22\n"
"v_accvgpr_write_b32 a229, v23\n"
"v_accvgpr_read_b32 v22, a230\n"
"v_accvgpr_read_b32 v23, a231\n"
"v_mul_f32 v22, v22, v26\n"
"v_mul_f32 v23, v23, v26\n"
"v_accvgpr_write_b32 a230, v22\n"
"v_accvgpr_write_b32 a231, v23\n"
"v_accvgpr_read_b32 v22, a252\n"
"v_accvgpr_read_b32 v23, a253\n"
"v_mul_f32 v22, v22, v26\n"
"v_mul_f32 v23, v23, v26\n"
"v_accvgpr_write_b32 a252, v22\n"
"v_accvgpr_write_b32 a253, v23\n"
"v_accvgpr_read_b32 v22, a254\n"
"v_accvgpr_read_b32 v23, a255\n"
"v_mul_f32 v22, v22, v26\n"
"v_mul_f32 v23, v23, v26\n"
"v_accvgpr_write_b32 a254, v22\n"
"v_accvgpr_write_b32 a255, v23\n"
"v_mul_f32 v148, v148, v26\n"
"v_mul_f32 v149, v149, v26\n"
"v_mul_f32 v150, v150, v26\n"
"v_mul_f32 v151, v151, v26\n"
"v_mul_f32 v172, v172, v26\n"
"v_mul_f32 v173, v173, v26\n"
"v_mul_f32 v174, v174, v26\n"
"v_mul_f32 v175, v175, v26\n"
"v_mul_f32 v196, v196, v26\n"
"v_mul_f32 v197, v197, v26\n"
"v_mul_f32 v198, v198, v26\n"
"v_mul_f32 v199, v199, v26\n"
"v_mul_f32 v220, v220, v26\n"
"v_mul_f32 v221, v221, v26\n"
"v_mul_f32 v222, v222, v26\n"
"v_mul_f32 v223, v223, v26\n"
"v_mul_f32 v244, v244, v26\n"
"v_mul_f32 v245, v245, v26\n"
"v_mul_f32 v246, v246, v26\n"
"v_mul_f32 v247, v247, v26\n"
"v_lshlrev_b32 v22, 16, v112\n"
"v_and_b32 v23, 0xffff0000, v112\n"
"v_mul_f32 v22, v22, v26\n"
"v_mul_f32 v23, v23, v26\n"
"v_cvt_pk_bf16_f32 v112, v22, v23\n"
"v_lshlrev_b32 v22, 16, v113\n"
"v_and_b32 v23, 0xffff0000, v113\n"
"v_mul_f32 v22, v22, v26\n"
"v_mul_f32 v23, v23, v26\n"
"v_cvt_pk_bf16_f32 v113, v22, v23\n"
"v_lshlrev_b32 v22, 16, v114\n"
"v_and_b32 v23, 0xffff0000, v114\n"
"v_mul_f32 v22, v22, v26\n"
"v_mul_f32 v23, v23, v26\n"
"v_cvt_pk_bf16_f32 v114, v22, v23\n"
"v_lshlrev_b32 v22, 16, v115\n"
"v_and_b32 v23, 0xffff0000, v115\n"
"v_mul_f32 v22, v22, v26\n"
"v_mul_f32 v23, v23, v26\n"
"v_cvt_pk_bf16_f32 v115, v22, v23\n"
"v_fma_f32 v22, v64, s62, v12\n"
"v_fma_f32 v23, v65, s62, v12\n"
"v_exp_f32 v22, v22\n"
"v_exp_f32 v23, v23\n"
"s_nop 0\n"
"v_add_f32 v77, v22, v23\n"
"v_cvt_pk_bf16_f32 v116, v22, v23\n"
"v_fma_f32 v22, v66, s62, v12\n"
"v_fma_f32 v23, v67, s62, v12\n"
"v_exp_f32 v22, v22\n"
"v_exp_f32 v23, v23\n"
"s_nop 0\n"
"v_add_f32 v77, v77, v22\n"
"v_add_f32 v77, v77, v23\n"
"v_cvt_pk_bf16_f32 v117, v22, v23\n"
"v_fma_f32 v22, v100, s62, v12\n"
"v_fma_f32 v23, v101, s62, v12\n"
"v_exp_f32 v22, v22\n"
"v_exp_f32 v23, v23\n"
"s_nop 0\n"
"v_add_f32 v77, v77, v22\n"
"v_add_f32 v77, v77, v23\n"
"v_cvt_pk_bf16_f32 v118, v22, v23\n"
"v_fma_f32 v22, v102, s62, v12\n"
"v_fma_f32 v23, v103, s62, v12\n"
"v_exp_f32 v22, v22\n"
"v_exp_f32 v23, v23\n"
"s_nop 0\n"
"v_add_f32 v77, v77, v22\n"
"v_add_f32 v77, v77, v23\n"
"v_cvt_pk_bf16_f32 v119, v22, v23\n"
"v_fma_f32 v22, v56, s62, v13\n"
"v_fma_f32 v23, v57, s62, v13\n"
"s_nop 1\n"
"s_branch .Lret_9_b\n"
".Lrare_9_d4:\n"
"s_nop 7\n"
"v_max3_f32 v25, v56, v57, v58\n"
"v_max3_f32 v26, v59, v108, v109\n"
"v_max3_f32 v25, v25, v110, v111\n"
"v_max_f32 v25, v25, v26\n"
"v_mov_b32 v26, v25\n"
"s_nop 1\n"
"v_permlane32_swap_b32 v25, v26\n"
"s_nop 1\n"
"v_max_f32 v25, v25, v26\n"
"v_mov_b32 v26, v25\n"
"s_nop 1\n"
"v_permlane16_swap_b32 v25, v26\n"
"s_nop 1\n"
"v_max_f32 v25, v25, v26\n"
"v_mul_f32 v25, s62, v25\n"
"v_sub_f32 v25, 0, v25\n"
"v_min_f32 v25, v25, v13\n"
"v_sub_f32 v26, v25, v13\n"
"v_exp_f32 v26, v26\n"
"v_mov_b32 v13, v25\n"
"s_nop 0\n"
"v_mul_f32 v19, v19, v26\n"
"v_accvgpr_read_b32 v22, a112\n"
"v_accvgpr_read_b32 v23, a113\n"
"v_mul_f32 v22, v22, v26\n"
"v_mul_f32 v23, v23, v26\n"
"v_accvgpr_write_b32 a112, v22\n"
"v_accvgpr_write_b32 a113, v23\n"
"v_accvgpr_read_b32 v22, a114\n"
"v_accvgpr_read_b32 v23, a115\n"
"v_mul_f32 v22, v22, v26\n"
"v_mul_f32 v23, v23, v26\n"
"v_accvgpr_write_b32 a114, v22\n"
"v_accvgpr_write_b32 a115, v23\n"
"v_accvgpr_read_b32 v22, a136\n"
"v_accvgpr_read_b32 v23, a137\n"
"v_mul_f32 v22, v22, v26\n"
"v_mul_f32 v23, v23, v26\n"
"v_accvgpr_write_b32 a136, v22\n"
"v_accvgpr_write_b32 a137, v23\n"
"v_accvgpr_read_b32 v22, a138\n"
"v_accvgpr_read_b32 v23, a139\n"
"v_mul_f32 v22, v22, v26\n"
"v_mul_f32 v23, v23, v26\n"
"v_accvgpr_write_b32 a138, v22\n"
"v_accvgpr_write_b32 a139, v23\n"
"v_accvgpr_read_b32 v22, a160\n"
"v_accvgpr_read_b32 v23, a161\n"
"v_mul_f32 v22, v22, v26\n"
"v_mul_f32 v23, v23, v26\n"
"v_accvgpr_write_b32 a160, v22\n"
"v_accvgpr_write_b32 a161, v23\n"
"v_accvgpr_read_b32 v22, a162\n"
"v_accvgpr_read_b32 v23, a163\n"
"v_mul_f32 v22, v22, v26\n"
"v_mul_f32 v23, v23, v26\n"
"v_accvgpr_write_b32 a162, v22\n"
"v_accvgpr_write_b32 a163, v23\n"
"v_accvgpr_read_b32 v22, a184\n"
"v_accvgpr_read_b32 v23, a185\n"
"v_mul_f32 v22, v22, v26\n"
"v_mul_f32 v23, v23, v26\n"
"v_accvgpr_write_b32 a184, v22\n"
"v_accvgpr_write_b32 a185, v23\n"
"v_accvgpr_read_b32 v22, a186\n"
"v_accvgpr_read_b32 v23, a187\n"
"v_mul_f32 v22, v22, v26\n"
"v_mul_f32 v23, v23, v26\n"
"v_accvgpr_write_b32 a186, v22\n"
"v_accvgpr_write_b32 a187, v23\n"
"v_accvgpr_read_b32 v22, a208\n"
"v_accvgpr_read_b32 v23, a209\n"
"v_mul_f32 v22, v22, v26\n"
"v_mul_f32 v23, v23, v26\n"
"v_accvgpr_write_b32 a208, v22\n"
"v_accvgpr_write_b32 a209, v23\n"
"v_accvgpr_read_b32 v22, a210\n"
"v_accvgpr_read_b32 v23, a211\n"
"v_mul_f32 v22, v22, v26\n"
"v_mul_f32 v23, v23, v26\n"
"v_accvgpr_write_b32 a210, v22\n"
"v_accvgpr_write_b32 a211, v23\n"
"v_accvgpr_read_b32 v22, a232\n"
"v_accvgpr_read_b32 v23, a233\n"
"v_mul_f32 v22, v22, v26\n"
"v_mul_f32 v23, v23, v26\n"
"v_accvgpr_write_b32 a232, v22\n"
"v_accvgpr_write_b32 a233, v23\n"
"v_accvgpr_read_b32 v22, a234\n"
"v_accvgpr_read_b32 v23, a235\n"
"v_mul_f32 v22, v22, v26\n"
"v_mul_f32 v23, v23, v26\n"
"v_accvgpr_write_b32 a234, v22\n"
"v_accvgpr_write_b32 a235, v23\n"
"v_mul_f32 v128, v128, v26\n"
"v_mul_f32 v129, v129, v26\n"
"v_mul_f32 v130, v130, v26\n"
"v_mul_f32 v131, v131, v26\n"
"v_mul_f32 v152, v152, v26\n"
"v_mul_f32 v153, v153, v26\n"
"v_mul_f32 v154, v154, v26\n"
"v_mul_f32 v155, v155, v26\n"
"v_mul_f32 v176, v176, v26\n"
"v_mul_f32 v177, v177, v26\n"
"v_mul_f32 v178, v178, v26\n"
"v_mul_f32 v179, v179, v26\n"
"v_mul_f32 v200, v200, v26\n"
"v_mul_f32 v201, v201, v26\n"
"v_mul_f32 v202, v202, v26\n"
"v_mul_f32 v203, v203, v26\n"
"v_mul_f32 v224, v224, v26\n"
"v_mul_f32 v225, v225, v26\n"
"v_mul_f32 v226, v226, v26\n"
"v_mul_f32 v227, v227, v26\n"
"v_mul_f32 v248, v248, v26\n"
"v_mul_f32 v249, v249, v26\n"
"v_mul_f32 v250, v250, v26\n"
"v_mul_f32 v251, v251, v26\n"
"v_fma_f32 v22, v56, s62, v13\n"
"v_fma_f32 v23, v57, s62, v13\n"
"v_exp_f32 v22, v22\n"
"v_exp_f32 v23, v23\n"
"s_nop 0\n"
"v_add_f32 v21, v22, v23\n"
"v_cvt_pk_bf16_f32 v72, v22, v23\n"
"v_fma_f32 v22, v58, s62, v13\n"
"v_fma_f32 v23, v59, s62, v13\n"
"v_exp_f32 v22, v22\n"
"v_exp_f32 v23, v23\n"
"s_nop 0\n"
"v_add_f32 v21, v21, v22\n"
"v_add_f32 v21, v21, v23\n"
"v_cvt_pk_bf16_f32 v73, v22, v23\n"
"v_fma_f32 v22, v108, s62, v13\n"
"v_fma_f32 v23, v109, s62, v13\n"
"v_exp_f32 v22, v22\n"
"v_exp_f32 v23, v23\n"
"s_nop 0\n"
"v_add_f32 v21, v21, v22\n"
"v_add_f32 v21, v21, v23\n"
"v_cvt_pk_bf16_f32 v74, v22, v23\n"
"v_fma_f32 v22, v110, s62, v13\n"
"v_fma_f32 v23, v111, s62, v13\n"
"v_exp_f32 v22, v22\n"
"v_exp_f32 v23, v23\n"
"s_nop 0\n"
"v_add_f32 v21, v21, v22\n"
"v_add_f32 v21, v21, v23\n"
"v_cvt_pk_bf16_f32 v75, v22, v23\n"
"v_fma_f32 v22, v84, s62, v14\n"
"v_fma_f32 v23, v85, s62, v14\n"
"s_nop 1\n"
"s_branch .Lret_9_d4\n"
".Lrare_9_d5:\n"
"s_nop 7\n"
"v_max3_f32 v25, v84, v85, v86\n"
"v_max3_f32 v26, v87, v124, v125\n"
"v_max3_f32 v25, v25, v126, v127\n"
"v_max_f32 v25, v25, v26\n"
"v_mov_b32 v26, v25\n"
"s_nop 1\n"
"v_permlane32_swap_b32 v25, v26\n"
"s_nop 1\n"
"v_max_f32 v25, v25, v26\n"
"v_mov_b32 v26, v25\n"
"s_nop 1\n"
"v_permlane16_swap_b32 v25, v26\n"
"s_nop 1\n"
"v_max_f32 v25, v25, v26\n"
"v_mul_f32 v25, s62, v25\n"
"v_sub_f32 v25, 0, v25\n"
"v_min_f32 v25, v25, v14\n"
"v_sub_f32 v26, v25, v14\n"
"v_exp_f32 v26, v26\n"
"v_mov_b32 v14, v25\n"
"s_nop 0\n"
"v_mul_f32 v20, v20, v26\n"
"v_accvgpr_read_b32 v22, a116\n"
"v_accvgpr_read_b32 v23, a117\n"
"v_mul_f32 v22, v22, v26\n"
"v_mul_f32 v23, v23, v26\n"
"v_accvgpr_write_b32 a116, v22\n"
"v_accvgpr_write_b32 a117, v23\n"
"v_accvgpr_read_b32 v22, a118\n"
"v_accvgpr_read_b32 v23, a119\n"
"v_mul_f32 v22, v22, v26\n"
"v_mul_f32 v23, v23, v26\n"
"v_accvgpr_write_b32 a118, v22\n"
"v_accvgpr_write_b32 a119, v23\n"
"v_accvgpr_read_b32 v22, a140\n"
"v_accvgpr_read_b32 v23, a141\n"
"v_mul_f32 v22, v22, v26\n"
"v_mul_f32 v23, v23, v26\n"
"v_accvgpr_write_b32 a140, v22\n"
"v_accvgpr_write_b32 a141, v23\n"
"v_accvgpr_read_b32 v22, a142\n"
"v_accvgpr_read_b32 v23, a143\n"
"v_mul_f32 v22, v22, v26\n"
"v_mul_f32 v23, v23, v26\n"
"v_accvgpr_write_b32 a142, v22\n"
"v_accvgpr_write_b32 a143, v23\n"
"v_accvgpr_read_b32 v22, a164\n"
"v_accvgpr_read_b32 v23, a165\n"
"v_mul_f32 v22, v22, v26\n"
"v_mul_f32 v23, v23, v26\n"
"v_accvgpr_write_b32 a164, v22\n"
"v_accvgpr_write_b32 a165, v23\n"
"v_accvgpr_read_b32 v22, a166\n"
"v_accvgpr_read_b32 v23, a167\n"
"v_mul_f32 v22, v22, v26\n"
"v_mul_f32 v23, v23, v26\n"
"v_accvgpr_write_b32 a166, v22\n"
"v_accvgpr_write_b32 a167, v23\n"
"v_accvgpr_read_b32 v22, a188\n"
"v_accvgpr_read_b32 v23, a189\n"
"v_mul_f32 v22, v22, v26\n"
"v_mul_f32 v23, v23, v26\n"
"v_accvgpr_write_b32 a188, v22\n"
"v_accvgpr_write_b32 a189, v23\n"
"v_accvgpr_read_b32 v22, a190\n"
"v_accvgpr_read_b32 v23, a191\n"
"v_mul_f32 v22, v22, v26\n"
"v_mul_f32 v23, v23, v26\n"
"v_accvgpr_write_b32 a190, v22\n"
"v_accvgpr_write_b32 a191, v23\n"
"v_accvgpr_read_b32 v22, a212\n"
"v_accvgpr_read_b32 v23, a213\n"
"v_mul_f32 v22, v22, v26\n"
"v_mul_f32 v23, v23, v26\n"
"v_accvgpr_write_b32 a212, v22\n"
"v_accvgpr_write_b32 a213, v23\n"
"v_accvgpr_read_b32 v22, a214\n"
"v_accvgpr_read_b32 v23, a215\n"
"v_mul_f32 v22, v22, v26\n"
"v_mul_f32 v23, v23, v26\n"
"v_accvgpr_write_b32 a214, v22\n"
"v_accvgpr_write_b32 a215, v23\n"
"v_accvgpr_read_b32 v22, a236\n"
"v_accvgpr_read_b32 v23, a237\n"
"v_mul_f32 v22, v22, v26\n"
"v_mul_f32 v23, v23, v26\n"
"v_accvgpr_write_b32 a236, v22\n"
"v_accvgpr_write_b32 a237, v23\n"
"v_accvgpr_read_b32 v22, a238\n"
"v_accvgpr_read_b32 v23, a239\n"
"v_mul_f32 v22, v22, v26\n"
"v_mul_f32 v23, v23, v26\n"
"v_accvgpr_write_b32 a238, v22\n"
"v_accvgpr_write_b32 a239, v23\n"
"v_mul_f32 v132, v132, v26\n"
"v_mul_f32 v133, v133, v26\n"
"v_mul_f32 v134, v134, v26\n"
"v_mul_f32 v135, v135, v26\n"
"v_mul_f32 v156, v156, v26\n"
"v_mul_f32 v157, v157, v26\n"
"v_mul_f32 v158, v158, v26\n"
"v_mul_f32 v159, v159, v26\n"
"v_mul_f32 v180, v180, v26\n"
"v_mul_f32 v181, v181, v26\n"
"v_mul_f32 v182, v182, v26\n"
"v_mul_f32 v183, v183, v26\n"
"v_mul_f32 v204, v204, v26\n"
"v_mul_f32 v205, v205, v26\n"
"v_mul_f32 v206, v206, v26\n"
"v_mul_f32 v207, v207, v26\n"
"v_mul_f32 v228, v228, v26\n"
"v_mul_f32 v229, v229, v26\n"
"v_mul_f32 v230, v230, v26\n"
"v_mul_f32 v231, v231, v26\n"
"v_mul_f32 v252, v252, v26\n"
"v_mul_f32 v253, v253, v26\n"
"v_mul_f32 v254, v254, v26\n"
"v_mul_f32 v255, v255, v26\n"
"v_fma_f32 v22, v84, s62, v14\n"
"v_fma_f32 v23, v85, s62, v14\n"
"v_exp_f32 v22, v22\n"
"v_exp_f32 v23, v23\n"
"s_nop 0\n"
"v_add_f32 v21, v22, v23\n"
"v_cvt_pk_bf16_f32 v56, v22, v23\n"
"v_fma_f32 v22, v86, s62, v14\n"
"v_fma_f32 v23, v87, s62, v14\n"
"v_exp_f32 v22, v22\n"
"v_exp_f32 v23, v23\n"
"s_nop 0\n"
"v_add_f32 v21, v21, v22\n"
"v_add_f32 v21, v21, v23\n"
"v_cvt_pk_bf16_f32 v57, v22, v23\n"
"v_fma_f32 v22, v124, s62, v14\n"
"v_fma_f32 v23, v125, s62, v14\n"
"v_exp_f32 v22, v22\n"
"v_exp_f32 v23, v23\n"
"s_nop 0\n"
"v_add_f32 v21, v21, v22\n"
"v_add_f32 v21, v21, v23\n"
"v_cvt_pk_bf16_f32 v58, v22, v23\n"
"v_fma_f32 v22, v126, s62, v14\n"
"v_fma_f32 v23, v127, s62, v14\n"
"v_exp_f32 v22, v22\n"
"v_exp_f32 v23, v23\n"
"s_nop 0\n"
"v_add_f32 v21, v21, v22\n"
"v_add_f32 v21, v21, v23\n"
"v_cvt_pk_bf16_f32 v59, v22, v23\n"
"s_nop 1\n"
"s_branch .Lret_9_d5\n"
".Lrare_10_a:\n"
"s_nop 7\n"
"v_max3_f32 v25, v52, v53, v54\n"
"v_max3_f32 v26, v55, v100, v101\n"
"v_max3_f32 v25, v25, v102, v103\n"
"v_max_f32 v25, v25, v26\n"
"v_mov_b32 v26, v25\n"
"s_nop 1\n"
"v_permlane32_swap_b32 v25, v26\n"
"s_nop 1\n"
"v_max_f32 v25, v25, v26\n"
"v_mov_b32 v26, v25\n"
"s_nop 1\n"
"v_permlane16_swap_b32 v25, v26\n"
"s_nop 1\n"
"v_max_f32 v25, v25, v26\n"
"v_mul_f32 v25, s62, v25\n"
"v_sub_f32 v25, 0, v25\n"
"v_min_f32 v25, v25, v9\n"
"v_sub_f32 v26, v25, v9\n"
"v_exp_f32 v26, v26\n"
"v_mov_b32 v9, v25\n"
"s_nop 0\n"
"v_mul_f32 v15, v15, v26\n"
"v_accvgpr_read_b32 v22, a96\n"
"v_accvgpr_read_b32 v23, a97\n"
"v_mul_f32 v22, v22, v26\n"
"v_mul_f32 v23, v23, v26\n"
"v_accvgpr_write_b32 a96, v22\n"
"v_accvgpr_write_b32 a97, v23\n"
"v_accvgpr_read_b32 v22, a98\n"
"v_accvgpr_read_b32 v23, a99\n"
"v_mul_f32 v22, v22, v26\n"
"v_mul_f32 v23, v23, v26\n"
"v_accvgpr_write_b32 a98, v22\n"
"v_accvgpr_write_b32 a99, v23\n"
"v_accvgpr_read_b32 v22, a120\n"
"v_accvgpr_read_b32 v23, a121\n"
"v_mul_f32 v22, v22, v26\n"
"v_mul_f32 v23, v23, v26\n"
"v_accvgpr_write_b32 a120, v22\n"
"v_accvgpr_write_b32 a121, v23\n"
"v_accvgpr_read_b32 v22, a122\n"
"v_accvgpr_read_b32 v23, a123\n"
"v_mul_f32 v22, v22, v26\n"
"v_mul_f32 v23, v23, v26\n"
"v_accvgpr_write_b32 a122, v22\n"
"v_accvgpr_write_b32 a123, v23\n"
"v_accvgpr_read_b32 v22, a144\n"
"v_accvgpr_read_b32 v23, a145\n"
"v_mul_f32 v22, v22, v26\n"
"v_mul_f32 v23, v23, v26\n"
"v_accvgpr_write_b32 a144, v22\n"
"v_accvgpr_write_b32 a145, v23\n"
"v_accvgpr_read_b32 v22, a146\n"
"v_accvgpr_read_b32 v23, a147\n"
"v_mul_f32 v22, v22, v26\n"
"v_mul_f32 v23, v23, v26\n"
"v_accvgpr_write_b32 a146, v22\n"
"v_accvgpr_write_b32 a147, v23\n"
"v_accvgpr_read_b32 v22, a168\n"
"v_accvgpr_read_b32 v23, a169\n"
"v_mul_f32 v22, v22, v26\n"
"v_mul_f32 v23, v23, v26\n"
"v_accvgpr_write_b32 a168, v22\n"
"v_accvgpr_write_b32 a169, v23\n"
"v_accvgpr_read_b32 v22, a170\n"
"v_accvgpr_read_b32 v23, a171\n"
"v_mul_f32 v22, v22, v26\n"
"v_mul_f32 v23, v23, v26\n"
"v_accvgpr_write_b32 a170, v22\n"
"v_accvgpr_write_b32 a171, v23\n"
"v_accvgpr_read_b32 v22, a192\n"
"v_accvgpr_read_b32 v23, a193\n"
"v_mul_f32 v22, v22, v26\n"
"v_mul_f32 v23, v23, v26\n"
"v_accvgpr_write_b32 a192, v22\n"
"v_accvgpr_write_b32 a193, v23\n"
"v_accvgpr_read_b32 v22, a194\n"
"v_accvgpr_read_b32 v23, a195\n"
"v_mul_f32 v22, v22, v26\n"
"v_mul_f32 v23, v23, v26\n"
"v_accvgpr_write_b32 a194, v22\n"
"v_accvgpr_write_b32 a195, v23\n"
"v_accvgpr_read_b32 v22, a216\n"
"v_accvgpr_read_b32 v23, a217\n"
"v_mul_f32 v22, v22, v26\n"
"v_mul_f32 v23, v23, v26\n"
"v_accvgpr_write_b32 a216, v22\n"
"v_accvgpr_write_b32 a217, v23\n"
"v_accvgpr_read_b32 v22, a218\n"
"v_accvgpr_read_b32 v23, a219\n"
"v_mul_f32 v22, v22, v26\n"
"v_mul_f32 v23, v23, v26\n"
"v_accvgpr_write_b32 a218, v22\n"
"v_accvgpr_write_b32 a219, v23\n"
"v_accvgpr_read_b32 v22, a240\n"
"v_accvgpr_read_b32 v23, a241\n"
"v_mul_f32 v22, v22, v26\n"
"v_mul_f32 v23, v23, v26\n"
"v_accvgpr_write_b32 a240, v22\n"
"v_accvgpr_write_b32 a241, v23\n"
"v_accvgpr_read_b32 v22, a242\n"
"v_accvgpr_read_b32 v23, a243\n"
"v_mul_f32 v22, v22, v26\n"
"v_mul_f32 v23, v23, v26\n"
"v_accvgpr_write_b32 a242, v22\n"
"v_accvgpr_write_b32 a243, v23\n"
"v_mul_f32 v136, v136, v26\n"
"v_mul_f32 v137, v137, v26\n"
"v_mul_f32 v138, v138, v26\n"
"v_mul_f32 v139, v139, v26\n"
"v_mul_f32 v160, v160, v26\n"
"v_mul_f32 v161, v161, v26\n"
"v_mul_f32 v162, v162, v26\n"
"v_mul_f32 v163, v163, v26\n"
"v_mul_f32 v184, v184, v26\n"
"v_mul_f32 v185, v185, v26\n"
"v_mul_f32 v186, v186, v26\n"
"v_mul_f32 v187, v187, v26\n"
"v_mul_f32 v208, v208, v26\n"
"v_mul_f32 v209, v209, v26\n"
"v_mul_f32 v210, v210, v26\n"
"v_mul_f32 v211, v211, v26\n"
"v_mul_f32 v232, v232, v26\n"
"v_mul_f32 v233, v233, v26\n"
"v_mul_f32 v234, v234, v26\n"
"v_mul_f32 v235, v235, v26\n"
"v_lshlrev_b32 v22, 16, v80\n"
"v_and_b32 v23, 0xffff0000, v80\n"
"v_mul_f32 v22, v22, v26\n"
"v_mul_f32 v23, v23, v26\n"
"v_cvt_pk_bf16_f32 v80, v22, v23\n"
"v_lshlrev_b32 v22, 16, v81\n"
"v_and_b32 v23, 0xffff0000, v81\n"
"v_mul_f32 v22, v22, v26\n"
"v_mul_f32 v23, v23, v26\n"
"v_cvt_pk_bf16_f32 v81, v22, v23\n"
"v_lshlrev_b32 v22, 16, v82\n"
"v_and_b32 v23, 0xffff0000, v82\n"
"v_mul_f32 v22, v22, v26\n"
"v_mul_f32 v23, v23, v26\n"
"v_cvt_pk_bf16_f32 v82, v22, v23\n"
"v_lshlrev_b32 v22, 16, v83\n"
"v_and_b32 v23, 0xffff0000, v83\n"
"v_mul_f32 v22, v22, v26\n"
"v_mul_f32 v23, v23, v26\n"
"v_cvt_pk_bf16_f32 v83, v22, v23\n"
"v_fma_f32 v22, v52, s62, v9\n"
"v_fma_f32 v23, v53, s62, v9\n"
"v_exp_f32 v22, v22\n"
"v_exp_f32 v23, v23\n"
"s_nop 0\n"
"v_add_f32 v21, v22, v23\n"
"v_cvt_pk_bf16_f32 v108, v22, v23\n"
"v_fma_f32 v22, v54, s62, v9\n"
"v_fma_f32 v23, v55, s62, v9\n"
"v_exp_f32 v22, v22\n"
"v_exp_f32 v23, v23\n"
"s_nop 0\n"
"v_add_f32 v21, v21, v22\n"
"v_add_f32 v21, v21, v23\n"
"v_cvt_pk_bf16_f32 v109, v22, v23\n"
"v_fma_f32 v22, v100, s62, v9\n"
"v_fma_f32 v23, v101, s62, v9\n"
"v_exp_f32 v22, v22\n"
"v_exp_f32 v23, v23\n"
"s_nop 0\n"
"v_add_f32 v21, v21, v22\n"
"v_add_f32 v21, v21, v23\n"
"v_cvt_pk_bf16_f32 v110, v22, v23\n"
"v_fma_f32 v22, v102, s62, v9\n"
"v_fma_f32 v23, v103, s62, v9\n"
"v_exp_f32 v22, v22\n"
"v_exp_f32 v23, v23\n"
"s_nop 0\n"
"v_add_f32 v21, v21, v22\n"
"v_add_f32 v21, v21, v23\n"
"v_cvt_pk_bf16_f32 v111, v22, v23\n"
"v_max3_f32 v25, v96, v97, v98\n"
"v_max3_f32 v26, v99, v88, v89\n"
"v_max3_f32 v25, v25, v90, v91\n"
"v_max_f32 v25, v25, v26\n"
"v_mov_b32 v26, v25\n"
"s_nop 1\n"
"v_permlane32_swap_b32 v25, v26\n"
"s_nop 1\n"
"v_max_f32 v25, v25, v26\n"
"v_mov_b32 v26, v25\n"
"s_nop 1\n"
"v_permlane16_swap_b32 v25, v26\n"
"s_nop 1\n"
"v_max_f32 v25, v25, v26\n"
"v_mul_f32 v25, s62, v25\n"
"v_sub_f32 v25, 0, v25\n"
"v_min_f32 v25, v25, v10\n"
"v_sub_f32 v26, v25, v10\n"
"v_exp_f32 v26, v26\n"
"v_mov_b32 v10, v25\n"
"s_nop 0\n"
"v_mul_f32 v16, v16, v26\n"
"v_accvgpr_read_b32 v22, a100\n"
"v_accvgpr_read_b32 v23, a101\n"
"v_mul_f32 v22, v22, v26\n"
"v_mul_f32 v23, v23, v26\n"
"v_accvgpr_write_b32 a100, v22\n"
"v_accvgpr_write_b32 a101, v23\n"
"v_accvgpr_read_b32 v22, a102\n"
"v_accvgpr_read_b32 v23, a103\n"
"v_mul_f32 v22, v22, v26\n"
"v_mul_f32 v23, v23, v26\n"
"v_accvgpr_write_b32 a102, v22\n"
"v_accvgpr_write_b32 a103, v23\n"
"v_accvgpr_read_b32 v22, a124\n"
"v_accvgpr_read_b32 v23, a125\n"
"v_mul_f32 v22, v22, v26\n"
"v_mul_f32 v23, v23, v26\n"
"v_accvgpr_write_b32 a124, v22\n"
"v_accvgpr_write_b32 a125, v23\n"
"v_accvgpr_read_b32 v22, a126\n"
"v_accvgpr_read_b32 v23, a127\n"
"v_mul_f32 v22, v22, v26\n"
"v_mul_f32 v23, v23, v26\n"
"v_accvgpr_write_b32 a126, v22\n"
"v_accvgpr_write_b32 a127, v23\n"
"v_accvgpr_read_b32 v22, a148\n"
"v_accvgpr_read_b32 v23, a149\n"
"v_mul_f32 v22, v22, v26\n"
"v_mul_f32 v23, v23, v26\n"
"v_accvgpr_write_b32 a148, v22\n"
"v_accvgpr_write_b32 a149, v23\n"
"v_accvgpr_read_b32 v22, a150\n"
"v_accvgpr_read_b32 v23, a151\n"
"v_mul_f32 v22, v22, v26\n"
"v_mul_f32 v23, v23, v26\n"
"v_accvgpr_write_b32 a150, v22\n"
"v_accvgpr_write_b32 a151, v23\n"
"v_accvgpr_read_b32 v22, a172\n"
"v_accvgpr_read_b32 v23, a173\n"
"v_mul_f32 v22, v22, v26\n"
"v_mul_f32 v23, v23, v26\n"
"v_accvgpr_write_b32 a172, v22\n"
"v_accvgpr_write_b32 a173, v23\n"
"v_accvgpr_read_b32 v22, a174\n"
"v_accvgpr_read_b32 v23, a175\n"
"v_mul_f32 v22, v22, v26\n"
"v_mul_f32 v23, v23, v26\n"
"v_accvgpr_write_b32 a174, v22\n"
"v_accvgpr_write_b32 a175, v23\n"
"v_accvgpr_read_b32 v22, a196\n"
"v_accvgpr_read_b32 v23, a197\n"
"v_mul_f32 v22, v22, v26\n"
"v_mul_f32 v23, v23, v26\n"
"v_accvgpr_write_b32 a196, v22\n"
"v_accvgpr_write_b32 a197, v23\n"
"v_accvgpr_read_b32 v22, a198\n"
"v_accvgpr_read_b32 v23, a199\n"
"v_mul_f32 v22, v22, v26\n"
"v_mul_f32 v23, v23, v26\n"
"v_accvgpr_write_b32 a198, v22\n"
"v_accvgpr_write_b32 a199, v23\n"
"v_accvgpr_read_b32 v22, a220\n"
"v_accvgpr_read_b32 v23, a221\n"
"v_mul_f32 v22, v22, v26\n"
"v_mul_f32 v23, v23, v26\n"
"v_accvgpr_write_b32 a220, v22\n"
"v_accvgpr_write_b32 a221, v23\n"
"v_accvgpr_read_b32 v22, a222\n"
"v_accvgpr_read_b32 v23, a223\n"
"v_mul_f32 v22, v22, v26\n"
"v_mul_f32 v23, v23, v26\n"
"v_accvgpr_write_b32 a222, v22\n"
"v_accvgpr_write_b32 a223, v23\n"
"v_accvgpr_read_b32 v22, a244\n"
"v_accvgpr_read_b32 v23, a245\n"
"v_mul_f32 v22, v22, v26\n"
"v_mul_f32 v23, v23, v26\n"
"v_accvgpr_write_b32 a244, v22\n"
"v_accvgpr_write_b32 a245, v23\n"
"v_accvgpr_read_b32 v22, a246\n"
"v_accvgpr_read_b32 v23, a247\n"
"v_mul_f32 v22, v22, v26\n"
"v_mul_f32 v23, v23, v26\n"
"v_accvgpr_write_b32 a246, v22\n"
"v_accvgpr_write_b32 a247, v23\n"
"v_mul_f32 v140, v140, v26\n"
"v_mul_f32 v141, v141, v26\n"
"v_mul_f32 v142, v142, v26\n"
"v_mul_f32 v143, v143, v26\n"
"v_mul_f32 v164, v164, v26\n"
"v_mul_f32 v165, v165, v26\n"
"v_mul_f32 v166, v166, v26\n"
"v_mul_f32 v167, v167, v26\n"
"v_mul_f32 v188, v188, v26\n"
"v_mul_f32 v189, v189, v26\n"
"v_mul_f32 v190, v190, v26\n"
"v_mul_f32 v191, v191, v26\n"
"v_mul_f32 v212, v212, v26\n"
"v_mul_f32 v213, v213, v26\n"
"v_mul_f32 v214, v214, v26\n"
"v_mul_f32 v215, v215, v26\n"
"v_mul_f32 v236, v236, v26\n"
"v_mul_f32 v237, v237, v26\n"
"v_mul_f32 v238, v238, v26\n"
"v_mul_f32 v239, v239, v26\n"
"v_lshlrev_b32 v22, 16, v60\n"
"v_and_b32 v23, 0xffff0000, v60\n"
"v_mul_f32 v22, v22, v26\n"
"v_mul_f32 v23, v23, v26\n"
"v_cvt_pk_bf16_f32 v60, v22, v23\n"
"v_lshlrev_b32 v22, 16, v61\n"
"v_and_b32 v23, 0xffff0000, v61\n"
"v_mul_f32 v22, v22, v26\n"
"v_mul_f32 v23, v23, v26\n"
"v_cvt_pk_bf16_f32 v61, v22, v23\n"
"v_lshlrev_b32 v22, 16, v62\n"
"v_and_b32 v23, 0xffff0000, v62\n"
"v_mul_f32 v22, v22, v26\n"
"v_mul_f32 v23, v23, v26\n"
"v_cvt_pk_bf16_f32 v62, v22, v23\n"
"v_lshlrev_b32 v22, 16, v63\n"
"v_and_b32 v23, 0xffff0000, v63\n"
"v_mul_f32 v22, v22, v26\n"
"v_mul_f32 v23, v23, v26\n"
"v_cvt_pk_bf16_f32 v63, v22, v23\n"
"v_fma_f32 v22, v96, s62, v10\n"
"v_fma_f32 v23, v97, s62, v10\n"
"v_exp_f32 v22, v22\n"
"v_exp_f32 v23, v23\n"
"s_nop 0\n"
"v_add_f32 v3, v22, v23\n"
"v_cvt_pk_bf16_f32 v84, v22, v23\n"
"v_fma_f32 v22, v98, s62, v10\n"
"v_fma_f32 v23, v99, s62, v10\n"
"v_exp_f32 v22, v22\n"
"v_exp_f32 v23, v23\n"
"s_nop 0\n"
"v_add_f32 v3, v3, v22\n"
"v_add_f32 v3, v3, v23\n"
"v_cvt_pk_bf16_f32 v85, v22, v23\n"
"v_fma_f32 v22, v88, s62, v10\n"
"v_fma_f32 v23, v89, s62, v10\n"
"v_exp_f32 v22, v22\n"
"v_exp_f32 v23, v23\n"
"s_nop 0\n"
"v_add_f32 v3, v3, v22\n"
"v_add_f32 v3, v3, v23\n"
"v_cvt_pk_bf16_f32 v86, v22, v23\n"
"v_fma_f32 v22, v90, s62, v10\n"
"v_fma_f32 v23, v91, s62, v10\n"
"v_exp_f32 v22, v22\n"
"v_exp_f32 v23, v23\n"
"s_nop 0\n"
"v_add_f32 v3, v3, v22\n"
"v_add_f32 v3, v3, v23\n"
"v_cvt_pk_bf16_f32 v87, v22, v23\n"
"v_fma_f32 v22, v76, s62, v11\n"
"v_fma_f32 v23, v77, s62, v11\n"
"s_nop 1\n"
"s_branch .Lret_10_a\n"
".Lrare_10_b:\n"
"s_nop 7\n"
"v_max3_f32 v25, v76, v77, v78\n"
"v_max3_f32 v26, v79, v92, v93\n"
"v_max3_f32 v25, v25, v94, v95\n"
"v_max_f32 v25, v25, v26\n"
"v_mov_b32 v26, v25\n"
"s_nop 1\n"
"v_permlane32_swap_b32 v25, v26\n"
"s_nop 1\n"
"v_max_f32 v25, v25, v26\n"
"v_mov_b32 v26, v25\n"
"s_nop 1\n"
"v_permlane16_swap_b32 v25, v26\n"
"s_nop 1\n"
"v_max_f32 v25, v25, v26\n"
"v_mul_f32 v25, s62, v25\n"
"v_sub_f32 v25, 0, v25\n"
"v_min_f32 v25, v25, v11\n"
"v_sub_f32 v26, v25, v11\n"
"v_exp_f32 v26, v26\n"
"v_mov_b32 v11, v25\n"
"s_nop 0\n"
"v_mul_f32 v17, v17, v26\n"
"v_accvgpr_read_b32 v22, a104\n"
"v_accvgpr_read_b32 v23, a105\n"
"v_mul_f32 v22, v22, v26\n"
"v_mul_f32 v23, v23, v26\n"
"v_accvgpr_write_b32 a104, v22\n"
"v_accvgpr_write_b32 a105, v23\n"
"v_accvgpr_read_b32 v22, a106\n"
"v_accvgpr_read_b32 v23, a107\n"
"v_mul_f32 v22, v22, v26\n"
"v_mul_f32 v23, v23, v26\n"
"v_accvgpr_write_b32 a106, v22\n"
"v_accvgpr_write_b32 a107, v23\n"
"v_accvgpr_read_b32 v22, a128\n"
"v_accvgpr_read_b32 v23, a129\n"
"v_mul_f32 v22, v22, v26\n"
"v_mul_f32 v23, v23, v26\n"
"v_accvgpr_write_b32 a128, v22\n"
"v_accvgpr_write_b32 a129, v23\n"
"v_accvgpr_read_b32 v22, a130\n"
"v_accvgpr_read_b32 v23, a131\n"
"v_mul_f32 v22, v22, v26\n"
"v_mul_f32 v23, v23, v26\n"
"v_accvgpr_write_b32 a130, v22\n"
"v_accvgpr_write_b32 a131, v23\n"
"v_accvgpr_read_b32 v22, a152\n"
"v_accvgpr_read_b32 v23, a153\n"
"v_mul_f32 v22, v22, v26\n"
"v_mul_f32 v23, v23, v26\n"
"v_accvgpr_write_b32 a152, v22\n"
"v_accvgpr_write_b32 a153, v23\n"
"v_accvgpr_read_b32 v22, a154\n"
"v_accvgpr_read_b32 v23, a155\n"
"v_mul_f32 v22, v22, v26\n"
"v_mul_f32 v23, v23, v26\n"
"v_accvgpr_write_b32 a154, v22\n"
"v_accvgpr_write_b32 a155, v23\n"
"v_accvgpr_read_b32 v22, a176\n"
"v_accvgpr_read_b32 v23, a177\n"
"v_mul_f32 v22, v22, v26\n"
"v_mul_f32 v23, v23, v26\n"
"v_accvgpr_write_b32 a176, v22\n"
"v_accvgpr_write_b32 a177, v23\n"
"v_accvgpr_read_b32 v22, a178\n"
"v_accvgpr_read_b32 v23, a179\n"
"v_mul_f32 v22, v22, v26\n"
"v_mul_f32 v23, v23, v26\n"
"v_accvgpr_write_b32 a178, v22\n"
"v_accvgpr_write_b32 a179, v23\n"
"v_accvgpr_read_b32 v22, a200\n"
"v_accvgpr_read_b32 v23, a201\n"
"v_mul_f32 v22, v22, v26\n"
"v_mul_f32 v23, v23, v26\n"
"v_accvgpr_write_b32 a200, v22\n"
"v_accvgpr_write_b32 a201, v23\n"
"v_accvgpr_read_b32 v22, a202\n"
"v_accvgpr_read_b32 v23, a203\n"
"v_mul_f32 v22, v22, v26\n"
"v_mul_f32 v23, v23, v26\n"
"v_accvgpr_write_b32 a202, v22\n"
"v_accvgpr_write_b32 a203, v23\n"
"v_accvgpr_read_b32 v22, a224\n"
"v_accvgpr_read_b32 v23, a225\n"
"v_mul_f32 v22, v22, v26\n"
"v_mul_f32 v23, v23, v26\n"
"v_accvgpr_write_b32 a224, v22\n"
"v_accvgpr_write_b32 a225, v23\n"
"v_accvgpr_read_b32 v22, a226\n"
"v_accvgpr_read_b32 v23, a227\n"
"v_mul_f32 v22, v22, v26\n"
"v_mul_f32 v23, v23, v26\n"
"v_accvgpr_write_b32 a226, v22\n"
"v_accvgpr_write_b32 a227, v23\n"
"v_accvgpr_read_b32 v22, a248\n"
"v_accvgpr_read_b32 v23, a249\n"
"v_mul_f32 v22, v22, v26\n"
"v_mul_f32 v23, v23, v26\n"
"v_accvgpr_write_b32 a248, v22\n"
"v_accvgpr_write_b32 a249, v23\n"
"v_accvgpr_read_b32 v22, a250\n"
"v_accvgpr_read_b32 v23, a251\n"
"v_mul_f32 v22, v22, v26\n"
"v_mul_f32 v23, v23, v26\n"
"v_accvgpr_write_b32 a250, v22\n"
"v_accvgpr_write_b32 a251, v23\n"
"v_mul_f32 v144, v144, v26\n"
"v_mul_f32 v145, v145, v26\n"
"v_mul_f32 v146, v146, v26\n"
"v_mul_f32 v147, v147, v26\n"
"v_mul_f32 v168, v168, v26\n"
"v_mul_f32 v169, v169, v26\n"
"v_mul_f32 v170, v170, v26\n"
"v_mul_f32 v171, v171, v26\n"
"v_mul_f32 v192, v192, v26\n"
"v_mul_f32 v193, v193, v26\n"
"v_mul_f32 v194, v194, v26\n"
"v_mul_f32 v195, v195, v26\n"
"v_mul_f32 v216, v216, v26\n"
"v_mul_f32 v217, v217, v26\n"
"v_mul_f32 v218, v218, v26\n"
"v_mul_f32 v219, v219, v26\n"
"v_mul_f32 v240, v240, v26\n"
"v_mul_f32 v241, v241, v26\n"
"v_mul_f32 v242, v242, v26\n"
"v_mul_f32 v243, v243, v26\n"
"v_lshlrev_b32 v22, 16, v48\n"
"v_and_b32 v23, 0xffff0000, v48\n"
"v_mul_f32 v22, v22, v26\n"
"v_mul_f32 v23, v23, v26\n"
"v_cvt_pk_bf16_f32 v48, v22, v23\n"
"v_lshlrev_b32 v22, 16, v49\n"
"v_and_b32 v23, 0xffff0000, v49\n"
"v_mul_f32 v22, v22, v26\n"
"v_mul_f32 v23, v23, v26\n"
"v_cvt_pk_bf16_f32 v49, v22, v23\n"
"v_lshlrev_b32 v22, 16, v50\n"
"v_and_b32 v23, 0xffff0000, v50\n"
"v_mul_f32 v22, v22, v26\n"
"v_mul_f32 v23, v23, v26\n"
"v_cvt_pk_bf16_f32 v50, v22, v23\n"
"v_lshlrev_b32 v22, 16, v51\n"
"v_and_b32 v23, 0xffff0000, v51\n"
"v_mul_f32 v22, v22, v26\n"
"v_mul_f32 v23, v23, v26\n"
"v_cvt_pk_bf16_f32 v51, v22, v23\n"
"v_fma_f32 v22, v76, s62, v11\n"
"v_fma_f32 v23, v77, s62, v11\n"
"v_exp_f32 v22, v22\n"
"v_exp_f32 v23, v23\n"
"s_nop 0\n"
"v_add_f32 v100, v22, v23\n"
"v_cvt_pk_bf16_f32 v124, v22, v23\n"
"v_fma_f32 v22, v78, s62, v11\n"
"v_fma_f32 v23, v79, s62, v11\n"
"v_exp_f32 v22, v22\n"
"v_exp_f32 v23, v23\n"
"s_nop 0\n"
"v_add_f32 v100, v100, v22\n"
"v_add_f32 v100, v100, v23\n"
"v_cvt_pk_bf16_f32 v125, v22, v23\n"
"v_fma_f32 v22, v92, s62, v11\n"
"v_fma_f32 v23, v93, s62, v11\n"
"v_exp_f32 v22, v22\n"
"v_exp_f32 v23, v23\n"
"s_nop 0\n"
"v_add_f32 v100, v100, v22\n"
"v_add_f32 v100, v100, v23\n"
"v_cvt_pk_bf16_f32 v126, v22, v23\n"
"v_fma_f32 v22, v94, s62, v11\n"
"v_fma_f32 v23, v95, s62, v11\n"
"v_exp_f32 v22, v22\n"
"v_exp_f32 v23, v23\n"
"s_nop 0\n"
"v_add_f32 v100, v100, v22\n"
"v_add_f32 v100, v100, v23\n"
"v_cvt_pk_bf16_f32 v127, v22, v23\n"
"v_max3_f32 v25, v28, v29, v30\n"
"v_max3_f32 v26, v31, v68, v69\n"
"v_max3_f32 v25, v25, v70, v71\n"
"v_max_f32 v25, v25, v26\n"
"v_mov_b32 v26, v25\n"
"s_nop 1\n"
"v_permlane32_swap_b32 v25, v26\n"
"s_nop 1\n"
"v_max_f32 v25, v25, v26\n"
"v_mov_b32 v26, v25\n"
"s_nop 1\n"
"v_permlane16_swap_b32 v25, v26\n"
"s_nop 1\n"
"v_max_f32 v25, v25, v26\n"
"v_mul_f32 v25, s62, v25\n"
"v_sub_f32 v25, 0, v25\n"
"v_min_f32 v25, v25, v12\n"
"v_sub_f32 v26, v25, v12\n"
"v_exp_f32 v26, v26\n"
"v_mov_b32 v12, v25\n"
"s_nop 0\n"
"v_mul_f32 v18, v18, v26\n"
"v_accvgpr_read_b32 v22, a108\n"
"v_accvgpr_read_b32 v23, a109\n"
"v_mul_f32 v22, v22, v26\n"
"v_mul_f32 v23, v23, v26\n"
"v_accvgpr_write_b32 a108, v22\n"
"v_accvgpr_write_b32 a109, v23\n"
"v_accvgpr_read_b32 v22, a110\n"
"v_accvgpr_read_b32 v23, a111\n"
"v_mul_f32 v22, v22, v26\n"
"v_mul_f32 v23, v23, v26\n"
"v_accvgpr_write_b32 a110, v22\n"
"v_accvgpr_write_b32 a111, v23\n"
"v_accvgpr_read_b32 v22, a132\n"
"v_accvgpr_read_b32 v23, a133\n"
"v_mul_f32 v22, v22, v26\n"
"v_mul_f32 v23, v23, v26\n"
"v_accvgpr_write_b32 a132, v22\n"
"v_accvgpr_write_b32 a133, v23\n"
"v_accvgpr_read_b32 v22, a134\n"
"v_accvgpr_read_b32 v23, a135\n"
"v_mul_f32 v22, v22, v26\n"
"v_mul_f32 v23, v23, v26\n"
"v_accvgpr_write_b32 a134, v22\n"
"v_accvgpr_write_b32 a135, v23\n"
"v_accvgpr_read_b32 v22, a156\n"
"v_accvgpr_read_b32 v23, a157\n"
"v_mul_f32 v22, v22, v26\n"
"v_mul_f32 v23, v23, v26\n"
"v_accvgpr_write_b32 a156, v22\n"
"v_accvgpr_write_b32 a157, v23\n"
"v_accvgpr_read_b32 v22, a158\n"
"v_accvgpr_read_b32 v23, a159\n"
"v_mul_f32 v22, v22, v26\n"
"v_mul_f32 v23, v23, v26\n"
"v_accvgpr_write_b32 a158, v22\n"
"v_accvgpr_write_b32 a159, v23\n"
"v_accvgpr_read_b32 v22, a180\n"
"v_accvgpr_read_b32 v23, a181\n"
"v_mul_f32 v22, v22, v26\n"
"v_mul_f32 v23, v23, v26\n"
"v_accvgpr_write_b32 a180, v22\n"
"v_accvgpr_write_b32 a181, v23\n"
"v_accvgpr_read_b32 v22, a182\n"
"v_accvgpr_read_b32 v23, a183\n"
"v_mul_f32 v22, v22, v26\n"
"v_mul_f32 v23, v23, v26\n"
"v_accvgpr_write_b32 a182, v22\n"
"v_accvgpr_write_b32 a183, v23\n"
"v_accvgpr_read_b32 v22, a204\n"
"v_accvgpr_read_b32 v23, a205\n"
"v_mul_f32 v22, v22, v26\n"
"v_mul_f32 v23, v23, v26\n"
"v_accvgpr_write_b32 a204, v22\n"
"v_accvgpr_write_b32 a205, v23\n"
"v_accvgpr_read_b32 v22, a206\n"
"v_accvgpr_read_b32 v23, a207\n"
"v_mul_f32 v22, v22, v26\n"
"v_mul_f32 v23, v23, v26\n"
"v_accvgpr_write_b32 a206, v22\n"
"v_accvgpr_write_b32 a207, v23\n"
"v_accvgpr_read_b32 v22, a228\n"
"v_accvgpr_read_b32 v23, a229\n"
"v_mul_f32 v22, v22, v26\n"
"v_mul_f32 v23, v23, v26\n"
"v_accvgpr_write_b32 a228, v22\n"
"v_accvgpr_write_b32 a229, v23\n"
"v_accvgpr_read_b32 v22, a230\n"
"v_accvgpr_read_b32 v23, a231\n"
"v_mul_f32 v22, v22, v26\n"
"v_mul_f32 v23, v23, v26\n"
"v_accvgpr_write_b32 a230, v22\n"
"v_accvgpr_write_b32 a231, v23\n"
"v_accvgpr_read_b32 v22, a252\n"
"v_accvgpr_read_b32 v23, a253\n"
"v_mul_f32 v22, v22, v26\n"
"v_mul_f32 v23, v23, v26\n"
"v_accvgpr_write_b32 a252, v22\n"
"v_accvgpr_write_b32 a253, v23\n"
"v_accvgpr_read_b32 v22, a254\n"
"v_accvgpr_read_b32 v23, a255\n"
"v_mul_f32 v22, v22, v26\n"
"v_mul_f32 v23, v23, v26\n"
"v_accvgpr_write_b32 a254, v22\n"
"v_accvgpr_write_b32 a255, v23\n"
"v_mul_f32 v148, v148, v26\n"
"v_mul_f32 v149, v149, v26\n"
"v_mul_f32 v150, v150, v26\n"
"v_mul_f32 v151, v151, v26\n"
"v_mul_f32 v172, v172, v26\n"
"v_mul_f32 v173, v173, v26\n"
"v_mul_f32 v174, v174, v26\n"
"v_mul_f32 v175, v175, v26\n"
"v_mul_f32 v196, v196, v26\n"
"v_mul_f32 v197, v197, v26\n"
"v_mul_f32 v198, v198, v26\n"
"v_mul_f32 v199, v199, v26\n"
"v_mul_f32 v220, v220, v26\n"
"v_mul_f32 v221, v221, v26\n"
"v_mul_f32 v222, v222, v26\n"
"v_mul_f32 v223, v223, v26\n"
"v_mul_f32 v244, v244, v26\n"
"v_mul_f32 v245, v245, v26\n"
"v_mul_f32 v246, v246, v26\n"
"v_mul_f32 v247, v247, v26\n"
"v_lshlrev_b32 v22, 16, v116\n"
"v_and_b32 v23, 0xffff0000, v116\n"
"v_mul_f32 v22, v22, v26\n"
"v_mul_f32 v23, v23, v26\n"
"v_cvt_pk_bf16_f32 v116, v22, v23\n"
"v_lshlrev_b32 v22, 16, v117\n"
"v_and_b32 v23, 0xffff0000, v117\n"
"v_mul_f32 v22, v22, v26\n"
"v_mul_f32 v23, v23, v26\n"
"v_cvt_pk_bf16_f32 v117, v22, v23\n"
"v_lshlrev_b32 v22, 16, v118\n"
"v_and_b32 v23, 0xffff0000, v118\n"
"v_mul_f32 v22, v22, v26\n"
"v_mul_f32 v23, v23, v26\n"
"v_cvt_pk_bf16_f32 v118, v22, v23\n"
"v_lshlrev_b32 v22, 16, v119\n"
"v_and_b32 v23, 0xffff0000, v119\n"
"v_mul_f32 v22, v22, v26\n"
"v_mul_f32 v23, v23, v26\n"
"v_cvt_pk_bf16_f32 v119, v22, v23\n"
"v_fma_f32 v22, v28, s62, v12\n"
"v_fma_f32 v23, v29, s62, v12\n"
"v_exp_f32 v22, v22\n"
"v_exp_f32 v23, v23\n"
"s_nop 0\n"
"v_add_f32 v101, v22, v23\n"
"v_cvt_pk_bf16_f32 v52, v22, v23\n"
"v_fma_f32 v22, v30, s62, v12\n"
"v_fma_f32 v23, v31, s62, v12\n"
"v_exp_f32 v22, v22\n"
"v_exp_f32 v23, v23\n"
"s_nop 0\n"
"v_add_f32 v101, v101, v22\n"
"v_add_f32 v101, v101, v23\n"
"v_cvt_pk_bf16_f32 v53, v22, v23\n"
"v_fma_f32 v22, v68, s62, v12\n"
"v_fma_f32 v23, v69, s62, v12\n"
"v_exp_f32 v22, v22\n"
"v_exp_f32 v23, v23\n"
"s_nop 0\n"
"v_add_f32 v101, v101, v22\n"
"v_add_f32 v101, v101, v23\n"
"v_cvt_pk_bf16_f32 v54, v22, v23\n"
"v_fma_f32 v22, v70, s62, v12\n"
"v_fma_f32 v23, v71, s62, v12\n"
"v_exp_f32 v22, v22\n"
"v_exp_f32 v23, v23\n"
"s_nop 0\n"
"v_add_f32 v101, v101, v22\n"
"v_add_f32 v101, v101, v23\n"
"v_cvt_pk_bf16_f32 v55, v22, v23\n"
"v_fma_f32 v22, v104, s62, v13\n"
"v_fma_f32 v23, v105, s62, v13\n"
"s_nop 1\n"
"s_branch .Lret_10_b\n"
".Lrare_10_d4:\n"
"s_nop 7\n"
"v_max3_f32 v25, v104, v105, v106\n"
"v_max3_f32 v26, v107, v112, v113\n"
"v_max3_f32 v25, v25, v114, v115\n"
"v_max_f32 v25, v25, v26\n"
"v_mov_b32 v26, v25\n"
"s_nop 1\n"
"v_permlane32_swap_b32 v25, v26\n"
"s_nop 1\n"
"v_max_f32 v25, v25, v26\n"
"v_mov_b32 v26, v25\n"
"s_nop 1\n"
"v_permlane16_swap_b32 v25, v26\n"
"s_nop 1\n"
"v_max_f32 v25, v25, v26\n"
"v_mul_f32 v25, s62, v25\n"
"v_sub_f32 v25, 0, v25\n"
"v_min_f32 v25, v25, v13\n"
"v_sub_f32 v26, v25, v13\n"
"v_exp_f32 v26, v26\n"
"v_mov_b32 v13, v25\n"
"s_nop 0\n"
"v_mul_f32 v19, v19, v26\n"
"v_accvgpr_read_b32 v22, a112\n"
"v_accvgpr_read_b32 v23, a113\n"
"v_mul_f32 v22, v22, v26\n"
"v_mul_f32 v23, v23, v26\n"
"v_accvgpr_write_b32 a112, v22\n"
"v_accvgpr_write_b32 a113, v23\n"
"v_accvgpr_read_b32 v22, a114\n"
"v_accvgpr_read_b32 v23, a115\n"
"v_mul_f32 v22, v22, v26\n"
"v_mul_f32 v23, v23, v26\n"
"v_accvgpr_write_b32 a114, v22\n"
"v_accvgpr_write_b32 a115, v23\n"
"v_accvgpr_read_b32 v22, a136\n"
"v_accvgpr_read_b32 v23, a137\n"
"v_mul_f32 v22, v22, v26\n"
"v_mul_f32 v23, v23, v26\n"
"v_accvgpr_write_b32 a136, v22\n"
"v_accvgpr_write_b32 a137, v23\n"
"v_accvgpr_read_b32 v22, a138\n"
"v_accvgpr_read_b32 v23, a139\n"
"v_mul_f32 v22, v22, v26\n"
"v_mul_f32 v23, v23, v26\n"
"v_accvgpr_write_b32 a138, v22\n"
"v_accvgpr_write_b32 a139, v23\n"
"v_accvgpr_read_b32 v22, a160\n"
"v_accvgpr_read_b32 v23, a161\n"
"v_mul_f32 v22, v22, v26\n"
"v_mul_f32 v23, v23, v26\n"
"v_accvgpr_write_b32 a160, v22\n"
"v_accvgpr_write_b32 a161, v23\n"
"v_accvgpr_read_b32 v22, a162\n"
"v_accvgpr_read_b32 v23, a163\n"
"v_mul_f32 v22, v22, v26\n"
"v_mul_f32 v23, v23, v26\n"
"v_accvgpr_write_b32 a162, v22\n"
"v_accvgpr_write_b32 a163, v23\n"
"v_accvgpr_read_b32 v22, a184\n"
"v_accvgpr_read_b32 v23, a185\n"
"v_mul_f32 v22, v22, v26\n"
"v_mul_f32 v23, v23, v26\n"
"v_accvgpr_write_b32 a184, v22\n"
"v_accvgpr_write_b32 a185, v23\n"
"v_accvgpr_read_b32 v22, a186\n"
"v_accvgpr_read_b32 v23, a187\n"
"v_mul_f32 v22, v22, v26\n"
"v_mul_f32 v23, v23, v26\n"
"v_accvgpr_write_b32 a186, v22\n"
"v_accvgpr_write_b32 a187, v23\n"
"v_accvgpr_read_b32 v22, a208\n"
"v_accvgpr_read_b32 v23, a209\n"
"v_mul_f32 v22, v22, v26\n"
"v_mul_f32 v23, v23, v26\n"
"v_accvgpr_write_b32 a208, v22\n"
"v_accvgpr_write_b32 a209, v23\n"
"v_accvgpr_read_b32 v22, a210\n"
"v_accvgpr_read_b32 v23, a211\n"
"v_mul_f32 v22, v22, v26\n"
"v_mul_f32 v23, v23, v26\n"
"v_accvgpr_write_b32 a210, v22\n"
"v_accvgpr_write_b32 a211, v23\n"
"v_accvgpr_read_b32 v22, a232\n"
"v_accvgpr_read_b32 v23, a233\n"
"v_mul_f32 v22, v22, v26\n"
"v_mul_f32 v23, v23, v26\n"
"v_accvgpr_write_b32 a232, v22\n"
"v_accvgpr_write_b32 a233, v23\n"
"v_accvgpr_read_b32 v22, a234\n"
"v_accvgpr_read_b32 v23, a235\n"
"v_mul_f32 v22, v22, v26\n"
"v_mul_f32 v23, v23, v26\n"
"v_accvgpr_write_b32 a234, v22\n"
"v_accvgpr_write_b32 a235, v23\n"
"v_mul_f32 v128, v128, v26\n"
"v_mul_f32 v129, v129, v26\n"
"v_mul_f32 v130, v130, v26\n"
"v_mul_f32 v131, v131, v26\n"
"v_mul_f32 v152, v152, v26\n"
"v_mul_f32 v153, v153, v26\n"
"v_mul_f32 v154, v154, v26\n"
"v_mul_f32 v155, v155, v26\n"
"v_mul_f32 v176, v176, v26\n"
"v_mul_f32 v177, v177, v26\n"
"v_mul_f32 v178, v178, v26\n"
"v_mul_f32 v179, v179, v26\n"
"v_mul_f32 v200, v200, v26\n"
"v_mul_f32 v201, v201, v26\n"
"v_mul_f32 v202, v202, v26\n"
"v_mul_f32 v203, v203, v26\n"
"v_mul_f32 v224, v224, v26\n"
"v_mul_f32 v225, v225, v26\n"
"v_mul_f32 v226, v226, v26\n"
"v_mul_f32 v227, v227, v26\n"
"v_mul_f32 v248, v248, v26\n"
"v_mul_f32 v249, v249, v26\n"
"v_mul_f32 v250, v250, v26\n"
"v_mul_f32 v251, v251, v26\n"
"v_fma_f32 v22, v104, s62, v13\n"
"v_fma_f32 v23, v105, s62, v13\n"
"v_exp_f32 v22, v22\n"
"v_exp_f32 v23, v23\n"
"s_nop 0\n"
"v_add_f32 v21, v22, v23\n"
"v_cvt_pk_bf16_f32 v56, v22, v23\n"
"v_fma_f32 v22, v106, s62, v13\n"
"v_fma_f32 v23, v107, s62, v13\n"
"v_exp_f32 v22, v22\n"
"v_exp_f32 v23, v23\n"
"s_nop 0\n"
"v_add_f32 v21, v21, v22\n"
"v_add_f32 v21, v21, v23\n"
"v_cvt_pk_bf16_f32 v57, v22, v23\n"
"v_fma_f32 v22, v112, s62, v13\n"
"v_fma_f32 v23, v113, s62, v13\n"
"v_exp_f32 v22, v22\n"
"v_exp_f32 v23, v23\n"
"s_nop 0\n"
"v_add_f32 v21, v21, v22\n"
"v_add_f32 v21, v21, v23\n"
"v_cvt_pk_bf16_f32 v58, v22, v23\n"
"v_fma_f32 v22, v114, s62, v13\n"
"v_fma_f32 v23, v115, s62, v13\n"
"v_exp_f32 v22, v22\n"
"v_exp_f32 v23, v23\n"
"s_nop 0\n"
"v_add_f32 v21, v21, v22\n"
"v_add_f32 v21, v21, v23\n"
"v_cvt_pk_bf16_f32 v59, v22, v23\n"
"v_fma_f32 v22, v64, s62, v14\n"
"v_fma_f32 v23, v65, s62, v14\n"
"s_nop 1\n"
"s_branch .Lret_10_d4\n"
".Lrare_10_d5:\n"
"s_nop 7\n"
"v_max3_f32 v25, v64, v65, v66\n"
"v_max3_f32 v26, v67, v120, v121\n"
"v_max3_f32 v25, v25, v122, v123\n"
"v_max_f32 v25, v25, v26\n"
"v_mov_b32 v26, v25\n"
"s_nop 1\n"
"v_permlane32_swap_b32 v25, v26\n"
"s_nop 1\n"
"v_max_f32 v25, v25, v26\n"
"v_mov_b32 v26, v25\n"
"s_nop 1\n"
"v_permlane16_swap_b32 v25, v26\n"
"s_nop 1\n"
"v_max_f32 v25, v25, v26\n"
"v_mul_f32 v25, s62, v25\n"
"v_sub_f32 v25, 0, v25\n"
"v_min_f32 v25, v25, v14\n"
"v_sub_f32 v26, v25, v14\n"
"v_exp_f32 v26, v26\n"
"v_mov_b32 v14, v25\n"
"s_nop 0\n"
"v_mul_f32 v20, v20, v26\n"
"v_accvgpr_read_b32 v22, a116\n"
"v_accvgpr_read_b32 v23, a117\n"
"v_mul_f32 v22, v22, v26\n"
"v_mul_f32 v23, v23, v26\n"
"v_accvgpr_write_b32 a116, v22\n"
"v_accvgpr_write_b32 a117, v23\n"
"v_accvgpr_read_b32 v22, a118\n"
"v_accvgpr_read_b32 v23, a119\n"
"v_mul_f32 v22, v22, v26\n"
"v_mul_f32 v23, v23, v26\n"
"v_accvgpr_write_b32 a118, v22\n"
"v_accvgpr_write_b32 a119, v23\n"
"v_accvgpr_read_b32 v22, a140\n"
"v_accvgpr_read_b32 v23, a141\n"
"v_mul_f32 v22, v22, v26\n"
"v_mul_f32 v23, v23, v26\n"
"v_accvgpr_write_b32 a140, v22\n"
"v_accvgpr_write_b32 a141, v23\n"
"v_accvgpr_read_b32 v22, a142\n"
"v_accvgpr_read_b32 v23, a143\n"
"v_mul_f32 v22, v22, v26\n"
"v_mul_f32 v23, v23, v26\n"
"v_accvgpr_write_b32 a142, v22\n"
"v_accvgpr_write_b32 a143, v23\n"
"v_accvgpr_read_b32 v22, a164\n"
"v_accvgpr_read_b32 v23, a165\n"
"v_mul_f32 v22, v22, v26\n"
"v_mul_f32 v23, v23, v26\n"
"v_accvgpr_write_b32 a164, v22\n"
"v_accvgpr_write_b32 a165, v23\n"
"v_accvgpr_read_b32 v22, a166\n"
"v_accvgpr_read_b32 v23, a167\n"
"v_mul_f32 v22, v22, v26\n"
"v_mul_f32 v23, v23, v26\n"
"v_accvgpr_write_b32 a166, v22\n"
"v_accvgpr_write_b32 a167, v23\n"
"v_accvgpr_read_b32 v22, a188\n"
"v_accvgpr_read_b32 v23, a189\n"
"v_mul_f32 v22, v22, v26\n"
"v_mul_f32 v23, v23, v26\n"
"v_accvgpr_write_b32 a188, v22\n"
"v_accvgpr_write_b32 a189, v23\n"
"v_accvgpr_read_b32 v22, a190\n"
"v_accvgpr_read_b32 v23, a191\n"
"v_mul_f32 v22, v22, v26\n"
"v_mul_f32 v23, v23, v26\n"
"v_accvgpr_write_b32 a190, v22\n"
"v_accvgpr_write_b32 a191, v23\n"
"v_accvgpr_read_b32 v22, a212\n"
"v_accvgpr_read_b32 v23, a213\n"
"v_mul_f32 v22, v22, v26\n"
"v_mul_f32 v23, v23, v26\n"
"v_accvgpr_write_b32 a212, v22\n"
"v_accvgpr_write_b32 a213, v23\n"
"v_accvgpr_read_b32 v22, a214\n"
"v_accvgpr_read_b32 v23, a215\n"
"v_mul_f32 v22, v22, v26\n"
"v_mul_f32 v23, v23, v26\n"
"v_accvgpr_write_b32 a214, v22\n"
"v_accvgpr_write_b32 a215, v23\n"
"v_accvgpr_read_b32 v22, a236\n"
"v_accvgpr_read_b32 v23, a237\n"
"v_mul_f32 v22, v22, v26\n"
"v_mul_f32 v23, v23, v26\n"
"v_accvgpr_write_b32 a236, v22\n"
"v_accvgpr_write_b32 a237, v23\n"
"v_accvgpr_read_b32 v22, a238\n"
"v_accvgpr_read_b32 v23, a239\n"
"v_mul_f32 v22, v22, v26\n"
"v_mul_f32 v23, v23, v26\n"
"v_accvgpr_write_b32 a238, v22\n"
"v_accvgpr_write_b32 a239, v23\n"
"v_mul_f32 v132, v132, v26\n"
"v_mul_f32 v133, v133, v26\n"
"v_mul_f32 v134, v134, v26\n"
"v_mul_f32 v135, v135, v26\n"
"v_mul_f32 v156, v156, v26\n"
"v_mul_f32 v157, v157, v26\n"
"v_mul_f32 v158, v158, v26\n"
"v_mul_f32 v159, v159, v26\n"
"v_mul_f32 v180, v180, v26\n"
"v_mul_f32 v181, v181, v26\n"
"v_mul_f32 v182, v182, v26\n"
"v_mul_f32 v183, v183, v26\n"
"v_mul_f32 v204, v204, v26\n"
"v_mul_f32 v205, v205, v26\n"
"v_mul_f32 v206, v206, v26\n"
"v_mul_f32 v207, v207, v26\n"
"v_mul_f32 v228, v228, v26\n"
"v_mul_f32 v229, v229, v26\n"
"v_mul_f32 v230, v230, v26\n"
"v_mul_f32 v231, v231, v26\n"
"v_mul_f32 v252, v252, v26\n"
"v_mul_f32 v253, v253, v26\n"
"v_mul_f32 v254, v254, v26\n"
"v_mul_f32 v255, v255, v26\n"
"v_fma_f32 v22, v64, s62, v14\n"
"v_fma_f32 v23, v65, s62, v14\n"
"v_exp_f32 v22, v22\n"
"v_exp_f32 v23, v23\n"
"s_nop 0\n"
"v_add_f32 v21, v22, v23\n"
"v_cvt_pk_bf16_f32 v104, v22, v23\n"
"v_fma_f32 v22, v66, s62, v14\n"
"v_fma_f32 v23, v67, s62, v14\n"
"v_exp_f32 v22, v22\n"
"v_exp_f32 v23, v23\n"
"s_nop 0\n"
"v_add_f32 v21, v21, v22\n"
"v_add_f32 v21, v21, v23\n"
"v_cvt_pk_bf16_f32 v105, v22, v23\n"
"v_fma_f32 v22, v120, s62, v14\n"
"v_fma_f32 v23, v121, s62, v14\n"
"v_exp_f32 v22, v22\n"
"v_exp_f32 v23, v23\n"
"s_nop 0\n"
"v_add_f32 v21, v21, v22\n"
"v_add_f32 v21, v21, v23\n"
"v_cvt_pk_bf16_f32 v106, v22, v23\n"
"v_fma_f32 v22, v122, s62, v14\n"
"v_fma_f32 v23, v123, s62, v14\n"
"v_exp_f32 v22, v22\n"
"v_exp_f32 v23, v23\n"
"s_nop 0\n"
"v_add_f32 v21, v21, v22\n"
"v_add_f32 v21, v21, v23\n"
"v_cvt_pk_bf16_f32 v107, v22, v23\n"
"s_nop 1\n"
"s_branch .Lret_10_d5\n"
".Lrare_11_a:\n"
"s_nop 7\n"
"v_max3_f32 v25, v96, v97, v98\n"
"v_max3_f32 v26, v99, v68, v69\n"
"v_max3_f32 v25, v25, v70, v71\n"
"v_max_f32 v25, v25, v26\n"
"v_mov_b32 v26, v25\n"
"s_nop 1\n"
"v_permlane32_swap_b32 v25, v26\n"
"s_nop 1\n"
"v_max_f32 v25, v25, v26\n"
"v_mov_b32 v26, v25\n"
"s_nop 1\n"
"v_permlane16_swap_b32 v25, v26\n"
"s_nop 1\n"
"v_max_f32 v25, v25, v26\n"
"v_mul_f32 v25, s62, v25\n"
"v_sub_f32 v25, 0, v25\n"
"v_min_f32 v25, v25, v9\n"
"v_sub_f32 v26, v25, v9\n"
"v_exp_f32 v26, v26\n"
"v_mov_b32 v9, v25\n"
"s_nop 0\n"
"v_mul_f32 v15, v15, v26\n"
"v_accvgpr_read_b32 v22, a96\n"
"v_accvgpr_read_b32 v23, a97\n"
"v_mul_f32 v22, v22, v26\n"
"v_mul_f32 v23, v23, v26\n"
"v_accvgpr_write_b32 a96, v22\n"
"v_accvgpr_write_b32 a97, v23\n"
"v_accvgpr_read_b32 v22, a98\n"
"v_accvgpr_read_b32 v23, a99\n"
"v_mul_f32 v22, v22, v26\n"
"v_mul_f32 v23, v23, v26\n"
"v_accvgpr_write_b32 a98, v22\n"
"v_accvgpr_write_b32 a99, v23\n"
"v_accvgpr_read_b32 v22, a120\n"
"v_accvgpr_read_b32 v23, a121\n"
"v_mul_f32 v22, v22, v26\n"
"v_mul_f32 v23, v23, v26\n"
"v_accvgpr_write_b32 a120, v22\n"
"v_accvgpr_write_b32 a121, v23\n"
"v_accvgpr_read_b32 v22, a122\n"
"v_accvgpr_read_b32 v23, a123\n"
"v_mul_f32 v22, v22, v26\n"
"v_mul_f32 v23, v23, v26\n"
"v_accvgpr_write_b32 a122, v22\n"
"v_accvgpr_write_b32 a123, v23\n"
"v_accvgpr_read_b32 v22, a144\n"
"v_accvgpr_read_b32 v23, a145\n"
"v_mul_f32 v22, v22, v26\n"
"v_mul_f32 v23, v23, v26\n"
"v_accvgpr_write_b32 a144, v22\n"
"v_accvgpr_write_b32 a145, v23\n"
"v_accvgpr_read_b32 v22, a146\n"
"v_accvgpr_read_b32 v23, a147\n"
"v_mul_f32 v22, v22, v26\n"
"v_mul_f32 v23, v23, v26\n"
"v_accvgpr_write_b32 a146, v22\n"
"v_accvgpr_write_b32 a147, v23\n"
"v_accvgpr_read_b32 v22, a168\n"
"v_accvgpr_read_b32 v23, a169\n"
"v_mul_f32 v22, v22, v26\n"
"v_mul_f32 v23, v23, v26\n"
"v_accvgpr_write_b32 a168, v22\n"
"v_accvgpr_write_b32 a169, v23\n"
"v_accvgpr_read_b32 v22, a170\n"
"v_accvgpr_read_b32 v23, a171\n"
"v_mul_f32 v22, v22, v26\n"
"v_mul_f32 v23, v23, v26\n"
"v_accvgpr_write_b32 a170, v22\n"
"v_accvgpr_write_b32 a171, v23\n"
"v_accvgpr_read_b32 v22, a192\n"
"v_accvgpr_read_b32 v23, a193\n"
"v_mul_f32 v22, v22, v26\n"
"v_mul_f32 v23, v23, v26\n"
"v_accvgpr_write_b32 a192, v22\n"
"v_accvgpr_write_b32 a193, v23\n"
"v_accvgpr_read_b32 v22, a194\n"
"v_accvgpr_read_b32 v23, a195\n"
"v_mul_f32 v22, v22, v26\n"
"v_mul_f32 v23, v23, v26\n"
"v_accvgpr_write_b32 a194, v22\n"
"v_accvgpr_write_b32 a195, v23\n"
"v_accvgpr_read_b32 v22, a216\n"
"v_accvgpr_read_b32 v23, a217\n"
"v_mul_f32 v22, v22, v26\n"
"v_mul_f32 v23, v23, v26\n"
"v_accvgpr_write_b32 a216, v22\n"
"v_accvgpr_write_b32 a217, v23\n"
"v_accvgpr_read_b32 v22, a218\n"
"v_accvgpr_read_b32 v23, a219\n"
"v_mul_f32 v22, v22, v26\n"
"v_mul_f32 v23, v23, v26\n"
"v_accvgpr_write_b32 a218, v22\n"
"v_accvgpr_write_b32 a219, v23\n"
"v_accvgpr_read_b32 v22, a240\n"
"v_accvgpr_read_b32 v23, a241\n"
"v_mul_f32 v22, v22, v26\n"
"v_mul_f32 v23, v23, v26\n"
"v_accvgpr_write_b32 a240, v22\n"
"v_accvgpr_write_b32 a241, v23\n"
"v_accvgpr_read_b32 v22, a242\n"
"v_accvgpr_read_b32 v23, a243\n"
"v_mul_f32 v22, v22, v26\n"
"v_mul_f32 v23, v23, v26\n"
"v_accvgpr_write_b32 a242, v22\n"
"v_accvgpr_write_b32 a243, v23\n"
"v_mul_f32 v136, v136, v26\n"
"v_mul_f32 v137, v137, v26\n"
"v_mul_f32 v138, v138, v26\n"
"v_mul_f32 v139, v139, v26\n"
"v_mul_f32 v160, v160, v26\n"
"v_mul_f32 v161, v161, v26\n"
"v_mul_f32 v162, v162, v26\n"
"v_mul_f32 v163, v163, v26\n"
"v_mul_f32 v184, v184, v26\n"
"v_mul_f32 v185, v185, v26\n"
"v_mul_f32 v186, v186, v26\n"
"v_mul_f32 v187, v187, v26\n"
"v_mul_f32 v208, v208, v26\n"
"v_mul_f32 v209, v209, v26\n"
"v_mul_f32 v210, v210, v26\n"
"v_mul_f32 v211, v211, v26\n"
"v_mul_f32 v232, v232, v26\n"
"v_mul_f32 v233, v233, v26\n"
"v_mul_f32 v234, v234, v26\n"
"v_mul_f32 v235, v235, v26\n"
"v_lshlrev_b32 v22, 16, v108\n"
"v_and_b32 v23, 0xffff0000, v108\n"
"v_mul_f32 v22, v22, v26\n"
"v_mul_f32 v23, v23, v26\n"
"v_cvt_pk_bf16_f32 v108, v22, v23\n"
"v_lshlrev_b32 v22, 16, v109\n"
"v_and_b32 v23, 0xffff0000, v109\n"
"v_mul_f32 v22, v22, v26\n"
"v_mul_f32 v23, v23, v26\n"
"v_cvt_pk_bf16_f32 v109, v22, v23\n"
"v_lshlrev_b32 v22, 16, v110\n"
"v_and_b32 v23, 0xffff0000, v110\n"
"v_mul_f32 v22, v22, v26\n"
"v_mul_f32 v23, v23, v26\n"
"v_cvt_pk_bf16_f32 v110, v22, v23\n"
"v_lshlrev_b32 v22, 16, v111\n"
"v_and_b32 v23, 0xffff0000, v111\n"
"v_mul_f32 v22, v22, v26\n"
"v_mul_f32 v23, v23, v26\n"
"v_cvt_pk_bf16_f32 v111, v22, v23\n"
"v_fma_f32 v22, v96, s62, v9\n"
"v_fma_f32 v23, v97, s62, v9\n"
"v_exp_f32 v22, v22\n"
"v_exp_f32 v23, v23\n"
"s_nop 0\n"
"v_add_f32 v21, v22, v23\n"
"v_cvt_pk_bf16_f32 v112, v22, v23\n"
"v_fma_f32 v22, v98, s62, v9\n"
"v_fma_f32 v23, v99, s62, v9\n"
"v_exp_f32 v22, v22\n"
"v_exp_f32 v23, v23\n"
"s_nop 0\n"
"v_add_f32 v21, v21, v22\n"
"v_add_f32 v21, v21, v23\n"
"v_cvt_pk_bf16_f32 v113, v22, v23\n"
"v_fma_f32 v22, v68, s62, v9\n"
"v_fma_f32 v23, v69, s62, v9\n"
"v_exp_f32 v22, v22\n"
"v_exp_f32 v23, v23\n"
"s_nop 0\n"
"v_add_f32 v21, v21, v22\n"
"v_add_f32 v21, v21, v23\n"
"v_cvt_pk_bf16_f32 v114, v22, v23\n"
"v_fma_f32 v22, v70, s62, v9\n"
"v_fma_f32 v23, v71, s62, v9\n"
"v_exp_f32 v22, v22\n"
"v_exp_f32 v23, v23\n"
"s_nop 0\n"
"v_add_f32 v21, v21, v22\n"
"v_add_f32 v21, v21, v23\n"
"v_cvt_pk_bf16_f32 v115, v22, v23\n"
"v_max3_f32 v25, v88, v89, v90\n"
"v_max3_f32 v26, v91, v80, v81\n"
"v_max3_f32 v25, v25, v82, v83\n"
"v_max_f32 v25, v25, v26\n"
"v_mov_b32 v26, v25\n"
"s_nop 1\n"
"v_permlane32_swap_b32 v25, v26\n"
"s_nop 1\n"
"v_max_f32 v25, v25, v26\n"
"v_mov_b32 v26, v25\n"
"s_nop 1\n"
"v_permlane16_swap_b32 v25, v26\n"
"s_nop 1\n"
"v_max_f32 v25, v25, v26\n"
"v_mul_f32 v25, s62, v25\n"
"v_sub_f32 v25, 0, v25\n"
"v_min_f32 v25, v25, v10\n"
"v_sub_f32 v26, v25, v10\n"
"v_exp_f32 v26, v26\n"
"v_mov_b32 v10, v25\n"
"s_nop 0\n"
"v_mul_f32 v16, v16, v26\n"
"v_accvgpr_read_b32 v22, a100\n"
"v_accvgpr_read_b32 v23, a101\n"
"v_mul_f32 v22, v22, v26\n"
"v_mul_f32 v23, v23, v26\n"
"v_accvgpr_write_b32 a100, v22\n"
"v_accvgpr_write_b32 a101, v23\n"
"v_accvgpr_read_b32 v22, a102\n"
"v_accvgpr_read_b32 v23, a103\n"
"v_mul_f32 v22, v22, v26\n"
"v_mul_f32 v23, v23, v26\n"
"v_accvgpr_write_b32 a102, v22\n"
"v_accvgpr_write_b32 a103, v23\n"
"v_accvgpr_read_b32 v22, a124\n"
"v_accvgpr_read_b32 v23, a125\n"
"v_mul_f32 v22, v22, v26\n"
"v_mul_f32 v23, v23, v26\n"
"v_accvgpr_write_b32 a124, v22\n"
"v_accvgpr_write_b32 a125, v23\n"
"v_accvgpr_read_b32 v22, a126\n"
"v_accvgpr_read_b32 v23, a127\n"
"v_mul_f32 v22, v22, v26\n"
"v_mul_f32 v23, v23, v26\n"
"v_accvgpr_write_b32 a126, v22\n"
"v_accvgpr_write_b32 a127, v23\n"
"v_accvgpr_read_b32 v22, a148\n"
"v_accvgpr_read_b32 v23, a149\n"
"v_mul_f32 v22, v22, v26\n"
"v_mul_f32 v23, v23, v26\n"
"v_accvgpr_write_b32 a148, v22\n"
"v_accvgpr_write_b32 a149, v23\n"
"v_accvgpr_read_b32 v22, a150\n"
"v_accvgpr_read_b32 v23, a151\n"
"v_mul_f32 v22, v22, v26\n"
"v_mul_f32 v23, v23, v26\n"
"v_accvgpr_write_b32 a150, v22\n"
"v_accvgpr_write_b32 a151, v23\n"
"v_accvgpr_read_b32 v22, a172\n"
"v_accvgpr_read_b32 v23, a173\n"
"v_mul_f32 v22, v22, v26\n"
"v_mul_f32 v23, v23, v26\n"
"v_accvgpr_write_b32 a172, v22\n"
"v_accvgpr_write_b32 a173, v23\n"
"v_accvgpr_read_b32 v22, a174\n"
"v_accvgpr_read_b32 v23, a175\n"
"v_mul_f32 v22, v22, v26\n"
"v_mul_f32 v23, v23, v26\n"
"v_accvgpr_write_b32 a174, v22\n"
"v_accvgpr_write_b32 a175, v23\n"
"v_accvgpr_read_b32 v22, a196\n"
"v_accvgpr_read_b32 v23, a197\n"
"v_mul_f32 v22, v22, v26\n"
"v_mul_f32 v23, v23, v26\n"
"v_accvgpr_write_b32 a196, v22\n"
"v_accvgpr_write_b32 a197, v23\n"
"v_accvgpr_read_b32 v22, a198\n"
"v_accvgpr_read_b32 v23, a199\n"
"v_mul_f32 v22, v22, v26\n"
"v_mul_f32 v23, v23, v26\n"
"v_accvgpr_write_b32 a198, v22\n"
"v_accvgpr_write_b32 a199, v23\n"
"v_accvgpr_read_b32 v22, a220\n"
"v_accvgpr_read_b32 v23, a221\n"
"v_mul_f32 v22, v22, v26\n"
"v_mul_f32 v23, v23, v26\n"
"v_accvgpr_write_b32 a220, v22\n"
"v_accvgpr_write_b32 a221, v23\n"
"v_accvgpr_read_b32 v22, a222\n"
"v_accvgpr_read_b32 v23, a223\n"
"v_mul_f32 v22, v22, v26\n"
"v_mul_f32 v23, v23, v26\n"
"v_accvgpr_write_b32 a222, v22\n"
"v_accvgpr_write_b32 a223, v23\n"
"v_accvgpr_read_b32 v22, a244\n"
"v_accvgpr_read_b32 v23, a245\n"
"v_mul_f32 v22, v22, v26\n"
"v_mul_f32 v23, v23, v26\n"
"v_accvgpr_write_b32 a244, v22\n"
"v_accvgpr_write_b32 a245, v23\n"
"v_accvgpr_read_b32 v22, a246\n"
"v_accvgpr_read_b32 v23, a247\n"
"v_mul_f32 v22, v22, v26\n"
"v_mul_f32 v23, v23, v26\n"
"v_accvgpr_write_b32 a246, v22\n"
"v_accvgpr_write_b32 a247, v23\n"
"v_mul_f32 v140, v140, v26\n"
"v_mul_f32 v141, v141, v26\n"
"v_mul_f32 v142, v142, v26\n"
"v_mul_f32 v143, v143, v26\n"
"v_mul_f32 v164, v164, v26\n"
"v_mul_f32 v165, v165, v26\n"
"v_mul_f32 v166, v166, v26\n"
"v_mul_f32 v167, v167, v26\n"
"v_mul_f32 v188, v188, v26\n"
"v_mul_f32 v189, v189, v26\n"
"v_mul_f32 v190, v190, v26\n"
"v_mul_f32 v191, v191, v26\n"
"v_mul_f32 v212, v212, v26\n"
"v_mul_f32 v213, v213, v26\n"
"v_mul_f32 v214, v214, v26\n"
"v_mul_f32 v215, v215, v26\n"
"v_mul_f32 v236, v236, v26\n"
"v_mul_f32 v237, v237, v26\n"
"v_mul_f32 v238, v238, v26\n"
"v_mul_f32 v239, v239, v26\n"
"v_lshlrev_b32 v22, 16, v84\n"
"v_and_b32 v23, 0xffff0000, v84\n"
"v_mul_f32 v22, v22, v26\n"
"v_mul_f32 v23, v23, v26\n"
"v_cvt_pk_bf16_f32 v84, v22, v23\n"
"v_lshlrev_b32 v22, 16, v85\n"
"v_and_b32 v23, 0xffff0000, v85\n"
"v_mul_f32 v22, v22, v26\n"
"v_mul_f32 v23, v23, v26\n"
"v_cvt_pk_bf16_f32 v85, v22, v23\n"
"v_lshlrev_b32 v22, 16, v86\n"
"v_and_b32 v23, 0xffff0000, v86\n"
"v_mul_f32 v22, v22, v26\n"
"v_mul_f32 v23, v23, v26\n"
"v_cvt_pk_bf16_f32 v86, v22, v23\n"
"v_lshlrev_b32 v22, 16, v87\n"
"v_and_b32 v23, 0xffff0000, v87\n"
"v_mul_f32 v22, v22, v26\n"
"v_mul_f32 v23, v23, v26\n"
"v_cvt_pk_bf16_f32 v87, v22, v23\n"
"v_fma_f32 v22, v88, s62, v10\n"
"v_fma_f32 v23, v89, s62, v10\n"
"v_exp_f32 v22, v22\n"
"v_exp_f32 v23, v23\n"
"s_nop 0\n"
"v_add_f32 v3, v22, v23\n"
"v_cvt_pk_bf16_f32 v64, v22, v23\n"
"v_fma_f32 v22, v90, s62, v10\n"
"v_fma_f32 v23, v91, s62, v10\n"
"v_exp_f32 v22, v22\n"
"v_exp_f32 v23, v23\n"
"s_nop 0\n"
"v_add_f32 v3, v3, v22\n"
"v_add_f32 v3, v3, v23\n"
"v_cvt_pk_bf16_f32 v65, v22, v23\n"
"v_fma_f32 v22, v80, s62, v10\n"
"v_fma_f32 v23, v81, s62, v10\n"
"v_exp_f32 v22, v22\n"
"v_exp_f32 v23, v23\n"
"s_nop 0\n"
"v_add_f32 v3, v3, v22\n"
"v_add_f32 v3, v3, v23\n"
"v_cvt_pk_bf16_f32 v66, v22, v23\n"
"v_fma_f32 v22, v82, s62, v10\n"
"v_fma_f32 v23, v83, s62, v10\n"
"v_exp_f32 v22, v22\n"
"v_exp_f32 v23, v23\n"
"s_nop 0\n"
"v_add_f32 v3, v3, v22\n"
"v_add_f32 v3, v3, v23\n"
"v_cvt_pk_bf16_f32 v67, v22, v23\n"
"v_fma_f32 v22, v100, s62, v11\n"
"v_fma_f32 v23, v101, s62, v11\n"
"s_nop 1\n"
"s_branch .Lret_11_a\n"
".Lrare_11_b:\n"
"s_nop 7\n"
"v_max3_f32 v25, v100, v101, v102\n"
"v_max3_f32 v26, v103, v60, v61\n"
"v_max3_f32 v25, v25, v62, v63\n"
"v_max_f32 v25, v25, v26\n"
"v_mov_b32 v26, v25\n"
"s_nop 1\n"
"v_permlane32_swap_b32 v25, v26\n"
"s_nop 1\n"
"v_max_f32 v25, v25, v26\n"
"v_mov_b32 v26, v25\n"
"s_nop 1\n"
"v_permlane16_swap_b32 v25, v26\n"
"s_nop 1\n"
"v_max_f32 v25, v25, v26\n"
"v_mul_f32 v25, s62, v25\n"
"v_sub_f32 v25, 0, v25\n"
"v_min_f32 v25, v25, v11\n"
"v_sub_f32 v26, v25, v11\n"
"v_exp_f32 v26, v26\n"
"v_mov_b32 v11, v25\n"
"s_nop 0\n"
"v_mul_f32 v17, v17, v26\n"
"v_accvgpr_read_b32 v22, a104\n"
"v_accvgpr_read_b32 v23, a105\n"
"v_mul_f32 v22, v22, v26\n"
"v_mul_f32 v23, v23, v26\n"
"v_accvgpr_write_b32 a104, v22\n"
"v_accvgpr_write_b32 a105, v23\n"
"v_accvgpr_read_b32 v22, a106\n"
"v_accvgpr_read_b32 v23, a107\n"
"v_mul_f32 v22, v22, v26\n"
"v_mul_f32 v23, v23, v26\n"
"v_accvgpr_write_b32 a106, v22\n"
"v_accvgpr_write_b32 a107, v23\n"
"v_accvgpr_read_b32 v22, a128\n"
"v_accvgpr_read_b32 v23, a129\n"
"v_mul_f32 v22, v22, v26\n"
"v_mul_f32 v23, v23, v26\n"
"v_accvgpr_write_b32 a128, v22\n"
"v_accvgpr_write_b32 a129, v23\n"
"v_accvgpr_read_b32 v22, a130\n"
"v_accvgpr_read_b32 v23, a131\n"
"v_mul_f32 v22, v22, v26\n"
"v_mul_f32 v23, v23, v26\n"
"v_accvgpr_write_b32 a130, v22\n"
"v_accvgpr_write_b32 a131, v23\n"
"v_accvgpr_read_b32 v22, a152\n"
"v_accvgpr_read_b32 v23, a153\n"
"v_mul_f32 v22, v22, v26\n"
"v_mul_f32 v23, v23, v26\n"
"v_accvgpr_write_b32 a152, v22\n"
"v_accvgpr_write_b32 a153, v23\n"
"v_accvgpr_read_b32 v22, a154\n"
"v_accvgpr_read_b32 v23, a155\n"
"v_mul_f32 v22, v22, v26\n"
"v_mul_f32 v23, v23, v26\n"
"v_accvgpr_write_b32 a154, v22\n"
"v_accvgpr_write_b32 a155, v23\n"
"v_accvgpr_read_b32 v22, a176\n"
"v_accvgpr_read_b32 v23, a177\n"
"v_mul_f32 v22, v22, v26\n"
"v_mul_f32 v23, v23, v26\n"
"v_accvgpr_write_b32 a176, v22\n"
"v_accvgpr_write_b32 a177, v23\n"
"v_accvgpr_read_b32 v22, a178\n"
"v_accvgpr_read_b32 v23, a179\n"
"v_mul_f32 v22, v22, v26\n"
"v_mul_f32 v23, v23, v26\n"
"v_accvgpr_write_b32 a178, v22\n"
"v_accvgpr_write_b32 a179, v23\n"
"v_accvgpr_read_b32 v22, a200\n"
"v_accvgpr_read_b32 v23, a201\n"
"v_mul_f32 v22, v22, v26\n"
"v_mul_f32 v23, v23, v26\n"
"v_accvgpr_write_b32 a200, v22\n"
"v_accvgpr_write_b32 a201, v23\n"
"v_accvgpr_read_b32 v22, a202\n"
"v_accvgpr_read_b32 v23, a203\n"
"v_mul_f32 v22, v22, v26\n"
"v_mul_f32 v23, v23, v26\n"
"v_accvgpr_write_b32 a202, v22\n"
"v_accvgpr_write_b32 a203, v23\n"
"v_accvgpr_read_b32 v22, a224\n"
"v_accvgpr_read_b32 v23, a225\n"
"v_mul_f32 v22, v22, v26\n"
"v_mul_f32 v23, v23, v26\n"
"v_accvgpr_write_b32 a224, v22\n"
"v_accvgpr_write_b32 a225, v23\n"
"v_accvgpr_read_b32 v22, a226\n"
"v_accvgpr_read_b32 v23, a227\n"
"v_mul_f32 v22, v22, v26\n"
"v_mul_f32 v23, v23, v26\n"
"v_accvgpr_write_b32 a226, v22\n"
"v_accvgpr_write_b32 a227, v23\n"
"v_accvgpr_read_b32 v22, a248\n"
"v_accvgpr_read_b32 v23, a249\n"
"v_mul_f32 v22, v22, v26\n"
"v_mul_f32 v23, v23, v26\n"
"v_accvgpr_write_b32 a248, v22\n"
"v_accvgpr_write_b32 a249, v23\n"
"v_accvgpr_read_b32 v22, a250\n"
"v_accvgpr_read_b32 v23, a251\n"
"v_mul_f32 v22, v22, v26\n"
"v_mul_f32 v23, v23, v26\n"
"v_accvgpr_write_b32 a250, v22\n"
"v_accvgpr_write_b32 a251, v23\n"
"v_mul_f32 v144, v144, v26\n"
"v_mul_f32 v145, v145, v26\n"
"v_mul_f32 v146, v146, v26\n"
"v_mul_f32 v147, v147, v26\n"
"v_mul_f32 v168, v168, v26\n"
"v_mul_f32 v169, v169, v26\n"
"v_mul_f32 v170, v170, v26\n"
"v_mul_f32 v171, v171, v26\n"
"v_mul_f32 v192, v192, v26\n"
"v_mul_f32 v193, v193, v26\n"
"v_mul_f32 v194, v194, v26\n"
"v_mul_f32 v195, v195, v26\n"
"v_mul_f32 v216, v216, v26\n"
"v_mul_f32 v217, v217, v26\n"
"v_mul_f32 v218, v218, v26\n"
"v_mul_f32 v219, v219, v26\n"
"v_mul_f32 v240, v240, v26\n"
"v_mul_f32 v241, v241, v26\n"
"v_mul_f32 v242, v242, v26\n"
"v_mul_f32 v243, v243, v26\n"
"v_lshlrev_b32 v22, 16, v124\n"
"v_and_b32 v23, 0xffff0000, v124\n"
"v_mul_f32 v22, v22, v26\n"
"v_mul_f32 v23, v23, v26\n"
"v_cvt_pk_bf16_f32 v124, v22, v23\n"
"v_lshlrev_b32 v22, 16, v125\n"
"v_and_b32 v23, 0xffff0000, v125\n"
"v_mul_f32 v22, v22, v26\n"
"v_mul_f32 v23, v23, v26\n"
"v_cvt_pk_bf16_f32 v125, v22, v23\n"
"v_lshlrev_b32 v22, 16, v126\n"
"v_and_b32 v23, 0xffff0000, v126\n"
"v_mul_f32 v22, v22, v26\n"
"v_mul_f32 v23, v23, v26\n"
"v_cvt_pk_bf16_f32 v126, v22, v23\n"
"v_lshlrev_b32 v22, 16, v127\n"
"v_and_b32 v23, 0xffff0000, v127\n"
"v_mul_f32 v22, v22, v26\n"
"v_mul_f32 v23, v23, v26\n"
"v_cvt_pk_bf16_f32 v127, v22, v23\n"
"v_fma_f32 v22, v100, s62, v11\n"
"v_fma_f32 v23, v101, s62, v11\n"
"v_exp_f32 v22, v22\n"
"v_exp_f32 v23, v23\n"
"s_nop 0\n"
"v_add_f32 v68, v22, v23\n"
"v_cvt_pk_bf16_f32 v120, v22, v23\n"
"v_fma_f32 v22, v102, s62, v11\n"
"v_fma_f32 v23, v103, s62, v11\n"
"v_exp_f32 v22, v22\n"
"v_exp_f32 v23, v23\n"
"s_nop 0\n"
"v_add_f32 v68, v68, v22\n"
"v_add_f32 v68, v68, v23\n"
"v_cvt_pk_bf16_f32 v121, v22, v23\n"
"v_fma_f32 v22, v60, s62, v11\n"
"v_fma_f32 v23, v61, s62, v11\n"
"v_exp_f32 v22, v22\n"
"v_exp_f32 v23, v23\n"
"s_nop 0\n"
"v_add_f32 v68, v68, v22\n"
"v_add_f32 v68, v68, v23\n"
"v_cvt_pk_bf16_f32 v122, v22, v23\n"
"v_fma_f32 v22, v62, s62, v11\n"
"v_fma_f32 v23, v63, s62, v11\n"
"v_exp_f32 v22, v22\n"
"v_exp_f32 v23, v23\n"
"s_nop 0\n"
"v_add_f32 v68, v68, v22\n"
"v_add_f32 v68, v68, v23\n"
"v_cvt_pk_bf16_f32 v123, v22, v23\n"
"v_max3_f32 v25, v76, v77, v78\n"
"v_max3_f32 v26, v79, v48, v49\n"
"v_max3_f32 v25, v25, v50, v51\n"
"v_max_f32 v25, v25, v26\n"
"v_mov_b32 v26, v25\n"
"s_nop 1\n"
"v_permlane32_swap_b32 v25, v26\n"
"s_nop 1\n"
"v_max_f32 v25, v25, v26\n"
"v_mov_b32 v26, v25\n"
"s_nop 1\n"
"v_permlane16_swap_b32 v25, v26\n"
"s_nop 1\n"
"v_max_f32 v25, v25, v26\n"
"v_mul_f32 v25, s62, v25\n"
"v_sub_f32 v25, 0, v25\n"
"v_min_f32 v25, v25, v12\n"
"v_sub_f32 v26, v25, v12\n"
"v_exp_f32 v26, v26\n"
"v_mov_b32 v12, v25\n"
"s_nop 0\n"
"v_mul_f32 v18, v18, v26\n"
"v_accvgpr_read_b32 v22, a108\n"
"v_accvgpr_read_b32 v23, a109\n"
"v_mul_f32 v22, v22, v26\n"
"v_mul_f32 v23, v23, v26\n"
"v_accvgpr_write_b32 a108, v22\n"
"v_accvgpr_write_b32 a109, v23\n"
"v_accvgpr_read_b32 v22, a110\n"
"v_accvgpr_read_b32 v23, a111\n"
"v_mul_f32 v22, v22, v26\n"
"v_mul_f32 v23, v23, v26\n"
"v_accvgpr_write_b32 a110, v22\n"
"v_accvgpr_write_b32 a111, v23\n"
"v_accvgpr_read_b32 v22, a132\n"
"v_accvgpr_read_b32 v23, a133\n"
"v_mul_f32 v22, v22, v26\n"
"v_mul_f32 v23, v23, v26\n"
"v_accvgpr_write_b32 a132, v22\n"
"v_accvgpr_write_b32 a133, v23\n"
"v_accvgpr_read_b32 v22, a134\n"
"v_accvgpr_read_b32 v23, a135\n"
"v_mul_f32 v22, v22, v26\n"
"v_mul_f32 v23, v23, v26\n"
"v_accvgpr_write_b32 a134, v22\n"
"v_accvgpr_write_b32 a135, v23\n"
"v_accvgpr_read_b32 v22, a156\n"
"v_accvgpr_read_b32 v23, a157\n"
"v_mul_f32 v22, v22, v26\n"
"v_mul_f32 v23, v23, v26\n"
"v_accvgpr_write_b32 a156, v22\n"
"v_accvgpr_write_b32 a157, v23\n"
"v_accvgpr_read_b32 v22, a158\n"
"v_accvgpr_read_b32 v23, a159\n"
"v_mul_f32 v22, v22, v26\n"
"v_mul_f32 v23, v23, v26\n"
"v_accvgpr_write_b32 a158, v22\n"
"v_accvgpr_write_b32 a159, v23\n"
"v_accvgpr_read_b32 v22, a180\n"
"v_accvgpr_read_b32 v23, a181\n"
"v_mul_f32 v22, v22, v26\n"
"v_mul_f32 v23, v23, v26\n"
"v_accvgpr_write_b32 a180, v22\n"
"v_accvgpr_write_b32 a181, v23\n"
"v_accvgpr_read_b32 v22, a182\n"
"v_accvgpr_read_b32 v23, a183\n"
"v_mul_f32 v22, v22, v26\n"
"v_mul_f32 v23, v23, v26\n"
"v_accvgpr_write_b32 a182, v22\n"
"v_accvgpr_write_b32 a183, v23\n"
"v_accvgpr_read_b32 v22, a204\n"
"v_accvgpr_read_b32 v23, a205\n"
"v_mul_f32 v22, v22, v26\n"
"v_mul_f32 v23, v23, v26\n"
"v_accvgpr_write_b32 a204, v22\n"
"v_accvgpr_write_b32 a205, v23\n"
"v_accvgpr_read_b32 v22, a206\n"
"v_accvgpr_read_b32 v23, a207\n"
"v_mul_f32 v22, v22, v26\n"
"v_mul_f32 v23, v23, v26\n"
"v_accvgpr_write_b32 a206, v22\n"
"v_accvgpr_write_b32 a207, v23\n"
"v_accvgpr_read_b32 v22, a228\n"
"v_accvgpr_read_b32 v23, a229\n"
"v_mul_f32 v22, v22, v26\n"
"v_mul_f32 v23, v23, v26\n"
"v_accvgpr_write_b32 a228, v22\n"
"v_accvgpr_write_b32 a229, v23\n"
"v_accvgpr_read_b32 v22, a230\n"
"v_accvgpr_read_b32 v23, a231\n"
"v_mul_f32 v22, v22, v26\n"
"v_mul_f32 v23, v23, v26\n"
"v_accvgpr_write_b32 a230, v22\n"
"v_accvgpr_write_b32 a231, v23\n"
"v_accvgpr_read_b32 v22, a252\n"
"v_accvgpr_read_b32 v23, a253\n"
"v_mul_f32 v22, v22, v26\n"
"v_mul_f32 v23, v23, v26\n"
"v_accvgpr_write_b32 a252, v22\n"
"v_accvgpr_write_b32 a253, v23\n"
"v_accvgpr_read_b32 v22, a254\n"
"v_accvgpr_read_b32 v23, a255\n"
"v_mul_f32 v22, v22, v26\n"
"v_mul_f32 v23, v23, v26\n"
"v_accvgpr_write_b32 a254, v22\n"
"v_accvgpr_write_b32 a255, v23\n"
"v_mul_f32 v148, v148, v26\n"
"v_mul_f32 v149, v149, v26\n"
"v_mul_f32 v150, v150, v26\n"
"v_mul_f32 v151, v151, v26\n"
"v_mul_f32 v172, v172, v26\n"
"v_mul_f32 v173, v173, v26\n"
"v_mul_f32 v174, v174, v26\n"
"v_mul_f32 v175, v175, v26\n"
"v_mul_f32 v196, v196, v26\n"
"v_mul_f32 v197, v197, v26\n"
"v_mul_f32 v198, v198, v26\n"
"v_mul_f32 v199, v199, v26\n"
"v_mul_f32 v220, v220, v26\n"
"v_mul_f32 v221, v221, v26\n"
"v_mul_f32 v222, v222, v26\n"
"v_mul_f32 v223, v223, v26\n"
"v_mul_f32 v244, v244, v26\n"
"v_mul_f32 v245, v245, v26\n"
"v_mul_f32 v246, v246, v26\n"
"v_mul_f32 v247, v247, v26\n"
"v_lshlrev_b32 v22, 16, v52\n"
"v_and_b32 v23, 0xffff0000, v52\n"
"v_mul_f32 v22, v22, v26\n"
"v_mul_f32 v23, v23, v26\n"
"v_cvt_pk_bf16_f32 v52, v22, v23\n"
"v_lshlrev_b32 v22, 16, v53\n"
"v_and_b32 v23, 0xffff0000, v53\n"
"v_mul_f32 v22, v22, v26\n"
"v_mul_f32 v23, v23, v26\n"
"v_cvt_pk_bf16_f32 v53, v22, v23\n"
"v_lshlrev_b32 v22, 16, v54\n"
"v_and_b32 v23, 0xffff0000, v54\n"
"v_mul_f32 v22, v22, v26\n"
"v_mul_f32 v23, v23, v26\n"
"v_cvt_pk_bf16_f32 v54, v22, v23\n"
"v_lshlrev_b32 v22, 16, v55\n"
"v_and_b32 v23, 0xffff0000, v55\n"
"v_mul_f32 v22, v22, v26\n"
"v_mul_f32 v23, v23, v26\n"
"v_cvt_pk_bf16_f32 v55, v22, v23\n"
"v_fma_f32 v22, v76, s62, v12\n"
"v_fma_f32 v23, v77, s62, v12\n"
"v_exp_f32 v22, v22\n"
"v_exp_f32 v23, v23\n"
"s_nop 0\n"
"v_add_f32 v69, v22, v23\n"
"v_cvt_pk_bf16_f32 v96, v22, v23\n"
"v_fma_f32 v22, v78, s62, v12\n"
"v_fma_f32 v23, v79, s62, v12\n"
"v_exp_f32 v22, v22\n"
"v_exp_f32 v23, v23\n"
"s_nop 0\n"
"v_add_f32 v69, v69, v22\n"
"v_add_f32 v69, v69, v23\n"
"v_cvt_pk_bf16_f32 v97, v22, v23\n"
"v_fma_f32 v22, v48, s62, v12\n"
"v_fma_f32 v23, v49, s62, v12\n"
"v_exp_f32 v22, v22\n"
"v_exp_f32 v23, v23\n"
"s_nop 0\n"
"v_add_f32 v69, v69, v22\n"
"v_add_f32 v69, v69, v23\n"
"v_cvt_pk_bf16_f32 v98, v22, v23\n"
"v_fma_f32 v22, v50, s62, v12\n"
"v_fma_f32 v23, v51, s62, v12\n"
"v_exp_f32 v22, v22\n"
"v_exp_f32 v23, v23\n"
"s_nop 0\n"
"v_add_f32 v69, v69, v22\n"
"v_add_f32 v69, v69, v23\n"
"v_cvt_pk_bf16_f32 v99, v22, v23\n"
"v_fma_f32 v22, v92, s62, v13\n"
"v_fma_f32 v23, v93, s62, v13\n"
"s_nop 1\n"
"s_branch .Lret_11_b\n"
".Lrare_11_d4:\n"
"s_nop 7\n"
"v_max3_f32 v25, v92, v93, v94\n"
"v_max3_f32 v26, v95, v116, v117\n"
"v_max3_f32 v25, v25, v118, v119\n"
"v_max_f32 v25, v25, v26\n"
"v_mov_b32 v26, v25\n"
"s_nop 1\n"
"v_permlane32_swap_b32 v25, v26\n"
"s_nop 1\n"
"v_max_f32 v25, v25, v26\n"
"v_mov_b32 v26, v25\n"
"s_nop 1\n"
"v_permlane16_swap_b32 v25, v26\n"
"s_nop 1\n"
"v_max_f32 v25, v25, v26\n"
"v_mul_f32 v25, s62, v25\n"
"v_sub_f32 v25, 0, v25\n"
"v_min_f32 v25, v25, v13\n"
"v_sub_f32 v26, v25, v13\n"
"v_exp_f32 v26, v26\n"
"v_mov_b32 v13, v25\n"
"s_nop 0\n"
"v_mul_f32 v19, v19, v26\n"
"v_accvgpr_read_b32 v22, a112\n"
"v_accvgpr_read_b32 v23, a113\n"
"v_mul_f32 v22, v22, v26\n"
"v_mul_f32 v23, v23, v26\n"
"v_accvgpr_write_b32 a112, v22\n"
"v_accvgpr_write_b32 a113, v23\n"
"v_accvgpr_read_b32 v22, a114\n"
"v_accvgpr_read_b32 v23, a115\n"
"v_mul_f32 v22, v22, v26\n"
"v_mul_f32 v23, v23, v26\n"
"v_accvgpr_write_b32 a114, v22\n"
"v_accvgpr_write_b32 a115, v23\n"
"v_accvgpr_read_b32 v22, a136\n"
"v_accvgpr_read_b32 v23, a137\n"
"v_mul_f32 v22, v22, v26\n"
"v_mul_f32 v23, v23, v26\n"
"v_accvgpr_write_b32 a136, v22\n"
"v_accvgpr_write_b32 a137, v23\n"
"v_accvgpr_read_b32 v22, a138\n"
"v_accvgpr_read_b32 v23, a139\n"
"v_mul_f32 v22, v22, v26\n"
"v_mul_f32 v23, v23, v26\n"
"v_accvgpr_write_b32 a138, v22\n"
"v_accvgpr_write_b32 a139, v23\n"
"v_accvgpr_read_b32 v22, a160\n"
"v_accvgpr_read_b32 v23, a161\n"
"v_mul_f32 v22, v22, v26\n"
"v_mul_f32 v23, v23, v26\n"
"v_accvgpr_write_b32 a160, v22\n"
"v_accvgpr_write_b32 a161, v23\n"
"v_accvgpr_read_b32 v22, a162\n"
"v_accvgpr_read_b32 v23, a163\n"
"v_mul_f32 v22, v22, v26\n"
"v_mul_f32 v23, v23, v26\n"
"v_accvgpr_write_b32 a162, v22\n"
"v_accvgpr_write_b32 a163, v23\n"
"v_accvgpr_read_b32 v22, a184\n"
"v_accvgpr_read_b32 v23, a185\n"
"v_mul_f32 v22, v22, v26\n"
"v_mul_f32 v23, v23, v26\n"
"v_accvgpr_write_b32 a184, v22\n"
"v_accvgpr_write_b32 a185, v23\n"
"v_accvgpr_read_b32 v22, a186\n"
"v_accvgpr_read_b32 v23, a187\n"
"v_mul_f32 v22, v22, v26\n"
"v_mul_f32 v23, v23, v26\n"
"v_accvgpr_write_b32 a186, v22\n"
"v_accvgpr_write_b32 a187, v23\n"
"v_accvgpr_read_b32 v22, a208\n"
"v_accvgpr_read_b32 v23, a209\n"
"v_mul_f32 v22, v22, v26\n"
"v_mul_f32 v23, v23, v26\n"
"v_accvgpr_write_b32 a208, v22\n"
"v_accvgpr_write_b32 a209, v23\n"
"v_accvgpr_read_b32 v22, a210\n"
"v_accvgpr_read_b32 v23, a211\n"
"v_mul_f32 v22, v22, v26\n"
"v_mul_f32 v23, v23, v26\n"
"v_accvgpr_write_b32 a210, v22\n"
"v_accvgpr_write_b32 a211, v23\n"
"v_accvgpr_read_b32 v22, a232\n"
"v_accvgpr_read_b32 v23, a233\n"
"v_mul_f32 v22, v22, v26\n"
"v_mul_f32 v23, v23, v26\n"
"v_accvgpr_write_b32 a232, v22\n"
"v_accvgpr_write_b32 a233, v23\n"
"v_accvgpr_read_b32 v22, a234\n"
"v_accvgpr_read_b32 v23, a235\n"
"v_mul_f32 v22, v22, v26\n"
"v_mul_f32 v23, v23, v26\n"
"v_accvgpr_write_b32 a234, v22\n"
"v_accvgpr_write_b32 a235, v23\n"
"v_mul_f32 v128, v128, v26\n"
"v_mul_f32 v129, v129, v26\n"
"v_mul_f32 v130, v130, v26\n"
"v_mul_f32 v131, v131, v26\n"
"v_mul_f32 v152, v152, v26\n"
"v_mul_f32 v153, v153, v26\n"
"v_mul_f32 v154, v154, v26\n"
"v_mul_f32 v155, v155, v26\n"
"v_mul_f32 v176, v176, v26\n"
"v_mul_f32 v177, v177, v26\n"
"v_mul_f32 v178, v178, v26\n"
"v_mul_f32 v179, v179, v26\n"
"v_mul_f32 v200, v200, v26\n"
"v_mul_f32 v201, v201, v26\n"
"v_mul_f32 v202, v202, v26\n"
"v_mul_f32 v203, v203, v26\n"
"v_mul_f32 v224, v224, v26\n"
"v_mul_f32 v225, v225, v26\n"
"v_mul_f32 v226, v226, v26\n"
"v_mul_f32 v227, v227, v26\n"
"v_mul_f32 v248, v248, v26\n"
"v_mul_f32 v249, v249, v26\n"
"v_mul_f32 v250, v250, v26\n"
"v_mul_f32 v251, v251, v26\n"
"v_fma_f32 v22, v92, s62, v13\n"
"v_fma_f32 v23, v93, s62, v13\n"
"v_exp_f32 v22, v22\n"
"v_exp_f32 v23, v23\n"
"s_nop 0\n"
"v_add_f32 v21, v22, v23\n"
"v_cvt_pk_bf16_f32 v88, v22, v23\n"
"v_fma_f32 v22, v94, s62, v13\n"
"v_fma_f32 v23, v95, s62, v13\n"
"v_exp_f32 v22, v22\n"
"v_exp_f32 v23, v23\n"
"s_nop 0\n"
"v_add_f32 v21, v21, v22\n"
"v_add_f32 v21, v21, v23\n"
"v_cvt_pk_bf16_f32 v89, v22, v23\n"
"v_fma_f32 v22, v116, s62, v13\n"
"v_fma_f32 v23, v117, s62, v13\n"
"v_exp_f32 v22, v22\n"
"v_exp_f32 v23, v23\n"
"s_nop 0\n"
"v_add_f32 v21, v21, v22\n"
"v_add_f32 v21, v21, v23\n"
"v_cvt_pk_bf16_f32 v90, v22, v23\n"
"v_fma_f32 v22, v118, s62, v13\n"
"v_fma_f32 v23, v119, s62, v13\n"
"v_exp_f32 v22, v22\n"
"v_exp_f32 v23, v23\n"
"s_nop 0\n"
"v_add_f32 v21, v21, v22\n"
"v_add_f32 v21, v21, v23\n"
"v_cvt_pk_bf16_f32 v91, v22, v23\n"
"v_fma_f32 v22, v28, s62, v14\n"
"v_fma_f32 v23, v29, s62, v14\n"
"s_nop 1\n"
"s_branch .Lret_11_d4\n"
".Lrare_11_d5:\n"
"s_nop 7\n"
"v_max3_f32 v25, v28, v29, v30\n"
"v_max3_f32 v26, v31, v72, v73\n"
"v_max3_f32 v25, v25, v74, v75\n"
"v_max_f32 v25, v25, v26\n"
"v_mov_b32 v26, v25\n"
"s_nop 1\n"
"v_permlane32_swap_b32 v25, v26\n"
"s_nop 1\n"
"v_max_f32 v25, v25, v26\n"
"v_mov_b32 v26, v25\n"
"s_nop 1\n"
"v_permlane16_swap_b32 v25, v26\n"
"s_nop 1\n"
"v_max_f32 v25, v25, v26\n"
"v_mul_f32 v25, s62, v25\n"
"v_sub_f32 v25, 0, v25\n"
"v_min_f32 v25, v25, v14\n"
"v_sub_f32 v26, v25, v14\n"
"v_exp_f32 v26, v26\n"
"v_mov_b32 v14, v25\n"
"s_nop 0\n"
"v_mul_f32 v20, v20, v26\n"
"v_accvgpr_read_b32 v22, a116\n"
"v_accvgpr_read_b32 v23, a117\n"
"v_mul_f32 v22, v22, v26\n"
"v_mul_f32 v23, v23, v26\n"
"v_accvgpr_write_b32 a116, v22\n"
"v_accvgpr_write_b32 a117, v23\n"
"v_accvgpr_read_b32 v22, a118\n"
"v_accvgpr_read_b32 v23, a119\n"
"v_mul_f32 v22, v22, v26\n"
"v_mul_f32 v23, v23, v26\n"
"v_accvgpr_write_b32 a118, v22\n"
"v_accvgpr_write_b32 a119, v23\n"
"v_accvgpr_read_b32 v22, a140\n"
"v_accvgpr_read_b32 v23, a141\n"
"v_mul_f32 v22, v22, v26\n"
"v_mul_f32 v23, v23, v26\n"
"v_accvgpr_write_b32 a140, v22\n"
"v_accvgpr_write_b32 a141, v23\n"
"v_accvgpr_read_b32 v22, a142\n"
"v_accvgpr_read_b32 v23, a143\n"
"v_mul_f32 v22, v22, v26\n"
"v_mul_f32 v23, v23, v26\n"
"v_accvgpr_write_b32 a142, v22\n"
"v_accvgpr_write_b32 a143, v23\n"
"v_accvgpr_read_b32 v22, a164\n"
"v_accvgpr_read_b32 v23, a165\n"
"v_mul_f32 v22, v22, v26\n"
"v_mul_f32 v23, v23, v26\n"
"v_accvgpr_write_b32 a164, v22\n"
"v_accvgpr_write_b32 a165, v23\n"
"v_accvgpr_read_b32 v22, a166\n"
"v_accvgpr_read_b32 v23, a167\n"
"v_mul_f32 v22, v22, v26\n"
"v_mul_f32 v23, v23, v26\n"
"v_accvgpr_write_b32 a166, v22\n"
"v_accvgpr_write_b32 a167, v23\n"
"v_accvgpr_read_b32 v22, a188\n"
"v_accvgpr_read_b32 v23, a189\n"
"v_mul_f32 v22, v22, v26\n"
"v_mul_f32 v23, v23, v26\n"
"v_accvgpr_write_b32 a188, v22\n"
"v_accvgpr_write_b32 a189, v23\n"
"v_accvgpr_read_b32 v22, a190\n"
"v_accvgpr_read_b32 v23, a191\n"
"v_mul_f32 v22, v22, v26\n"
"v_mul_f32 v23, v23, v26\n"
"v_accvgpr_write_b32 a190, v22\n"
"v_accvgpr_write_b32 a191, v23\n"
"v_accvgpr_read_b32 v22, a212\n"
"v_accvgpr_read_b32 v23, a213\n"
"v_mul_f32 v22, v22, v26\n"
"v_mul_f32 v23, v23, v26\n"
"v_accvgpr_write_b32 a212, v22\n"
"v_accvgpr_write_b32 a213, v23\n"
"v_accvgpr_read_b32 v22, a214\n"
"v_accvgpr_read_b32 v23, a215\n"
"v_mul_f32 v22, v22, v26\n"
"v_mul_f32 v23, v23, v26\n"
"v_accvgpr_write_b32 a214, v22\n"
"v_accvgpr_write_b32 a215, v23\n"
"v_accvgpr_read_b32 v22, a236\n"
"v_accvgpr_read_b32 v23, a237\n"
"v_mul_f32 v22, v22, v26\n"
"v_mul_f32 v23, v23, v26\n"
"v_accvgpr_write_b32 a236, v22\n"
"v_accvgpr_write_b32 a237, v23\n"
"v_accvgpr_read_b32 v22, a238\n"
"v_accvgpr_read_b32 v23, a239\n"
"v_mul_f32 v22, v22, v26\n"
"v_mul_f32 v23, v23, v26\n"
"v_accvgpr_write_b32 a238, v22\n"
"v_accvgpr_write_b32 a239, v23\n"
"v_mul_f32 v132, v132, v26\n"
"v_mul_f32 v133, v133, v26\n"
"v_mul_f32 v134, v134, v26\n"
"v_mul_f32 v135, v135, v26\n"
"v_mul_f32 v156, v156, v26\n"
"v_mul_f32 v157, v157, v26\n"
"v_mul_f32 v158, v158, v26\n"
"v_mul_f32 v159, v159, v26\n"
"v_mul_f32 v180, v180, v26\n"
"v_mul_f32 v181, v181, v26\n"
"v_mul_f32 v182, v182, v26\n"
"v_mul_f32 v183, v183, v26\n"
"v_mul_f32 v204, v204, v26\n"
"v_mul_f32 v205, v205, v26\n"
"v_mul_f32 v206, v206, v26\n"
"v_mul_f32 v207, v207, v26\n"
"v_mul_f32 v228, v228, v26\n"
"v_mul_f32 v229, v229, v26\n"
"v_mul_f32 v230, v230, v26\n"
"v_mul_f32 v231, v231, v26\n"
"v_mul_f32 v252, v252, v26\n"
"v_mul_f32 v253, v253, v26\n"
"v_mul_f32 v254, v254, v26\n"
"v_mul_f32 v255, v255, v26\n"
"v_fma_f32 v22, v28, s62, v14\n"
"v_fma_f32 v23, v29, s62, v14\n"
"v_exp_f32 v22, v22\n"
"v_exp_f32 v23, v23\n"
"s_nop 0\n"
"v_add_f32 v21, v22, v23\n"
"v_cvt_pk_bf16_f32 v80, v22, v23\n"
"v_fma_f32 v22, v30, s62, v14\n"
"v_fma_f32 v23, v31, s62, v14\n"
"v_exp_f32 v22, v22\n"
"v_exp_f32 v23, v23\n"
"s_nop 0\n"
"v_add_f32 v21, v21, v22\n"
"v_add_f32 v21, v21, v23\n"
"v_cvt_pk_bf16_f32 v81, v22, v23\n"
"v_fma_f32 v22, v72, s62, v14\n"
"v_fma_f32 v23, v73, s62, v14\n"
"v_exp_f32 v22, v22\n"
"v_exp_f32 v23, v23\n"
"s_nop 0\n"
"v_add_f32 v21, v21, v22\n"
"v_add_f32 v21, v21, v23\n"
"v_cvt_pk_bf16_f32 v82, v22, v23\n"
"v_fma_f32 v22, v74, s62, v14\n"
"v_fma_f32 v23, v75, s62, v14\n"
"v_exp_f32 v22, v22\n"
"v_exp_f32 v23, v23\n"
"s_nop 0\n"
"v_add_f32 v21, v21, v22\n"
"v_add_f32 v21, v21, v23\n"
"v_cvt_pk_bf16_f32 v83, v22, v23\n"
"s_nop 1\n"
"s_branch .Lret_11_d5\n"
".Lend:\n"
        :
        : "s"(Q), "s"(K), "s"(V), "s"(O), "s"(h0), "s"(h1), "s"(ldsb), "v"(threadIdx.x), "s"(WS), "s"(bid)
        : "memory", "vcc", "scc", "s36", "s37", "s38", "s39", "s40", "s41", "s42", "s43", "s44", "s45", "s46", "s47", "s48", "s49", "s50", "s51", "s52", "s53", "s54", "s55", "s56", "s57", "s58", "s59", "s60", "s61", "s62", "s63", "s64", "s65", "s66", "s67", "s68", "s69", "s70", "s71", "s72", "s73", "s74", "s75", "s76", "s77", "s78", "s79", "s80", "s81", "s82", "s83", "s84", "s85", "s86", "s87", "s88", "s89", "s90", "s91", "s92", "s93", "s94", "s95", "s96", "s97", "s98", "s99", "v1", "v2", "v3", "v4", "v5", "v6", "v7", "v8", "v9", "v10", "v11", "v12", "v13", "v14", "v15", "v16", "v17", "v18", "v19", "v20", "v21", "v22", "v23", "v24", "v25", "v26", "v27", "v28", "v29", "v30", "v31", "v32", "v33", "v34", "v35", "v36", "v37", "v38", "v39", "v40", "v41", "v42", "v43", "v44", "v45", "v46", "v47", "v48", "v49", "v50", "v51", "v52", "v53", "v54", "v55", "v56", "v57", "v58", "v59", "v60", "v61", "v62", "v63", "v64", "v65", "v66", "v67", "v68", "v69", "v70", "v71", "v72", "v73", "v74", "v75", "v76", "v77", "v78", "v79", "v80", "v81", "v82", "v83", "v84", "v85", "v86", "v87", "v88", "v89", "v90", "v91", "v92", "v93", "v94", "v95", "v96", "v97", "v98", "v99", "v100", "v101", "v102", "v103", "v104", "v105", "v106", "v107", "v108", "v109", "v110", "v111", "v112", "v113", "v114", "v115", "v116", "v117", "v118", "v119", "v120", "v121", "v122", "v123", "v124", "v125", "v126", "v127", "v128", "v129", "v130", "v131", "v132", "v133", "v134", "v135", "v136", "v137", "v138", "v139", "v140", "v141", "v142", "v143", "v144", "v145", "v146", "v147", "v148", "v149", "v150", "v151", "v152", "v153", "v154", "v155", "v156", "v157", "v158", "v159", "v160", "v161", "v162", "v163", "v164", "v165", "v166", "v167", "v168", "v169", "v170", "v171", "v172", "v173", "v174", "v175", "v176", "v177", "v178", "v179", "v180", "v181", "v182", "v183", "v184", "v185", "v186", "v187", "v188", "v189", "v190", "v191", "v192", "v193", "v194", "v195", "v196", "v197", "v198", "v199", "v200", "v201", "v202", "v203", "v204", "v205", "v206", "v207", "v208", "v209", "v210", "v211", "v212", "v213", "v214", "v215", "v216", "v217", "v218", "v219", "v220", "v221", "v222", "v223", "v224", "v225", "v226", "v227", "v228", "v229", "v230", "v231", "v232", "v233", "v234", "v235", "v236", "v237", "v238", "v239", "v240", "v241", "v242", "v243", "v244", "v245", "v246", "v247", "v248", "v249", "v250", "v251", "v252", "v253", "v254", "v255", "a0", "a1", "a2", "a3", "a4", "a5", "a6", "a7", "a8", "a9", "a10", "a11", "a12", "a13", "a14", "a15", "a16", "a17", "a18", "a19", "a20", "a21", "a22", "a23", "a24", "a25", "a26", "a27", "a28", "a29", "a30", "a31", "a32", "a33", "a34", "a35", "a36", "a37", "a38", "a39", "a40", "a41", "a42", "a43", "a44", "a45", "a46", "a47", "a48", "a49", "a50", "a51", "a52", "a53", "a54", "a55", "a56", "a57", "a58", "a59", "a60", "a61", "a62", "a63", "a64", "a65", "a66", "a67", "a68", "a69", "a70", "a71", "a72", "a73", "a74", "a75", "a76", "a77", "a78", "a79", "a80", "a81", "a82", "a83", "a84", "a85", "a86", "a87", "a88", "a89", "a90", "a91", "a92", "a93", "a94", "a95", "a96", "a97", "a98", "a99", "a100", "a101", "a102", "a103", "a104", "a105", "a106", "a107", "a108", "a109", "a110", "a111", "a112", "a113", "a114", "a115", "a116", "a117", "a118", "a119", "a120", "a121", "a122", "a123", "a124", "a125", "a126", "a127", "a128", "a129", "a130", "a131", "a132", "a133", "a134", "a135", "a136", "a137", "a138", "a139", "a140", "a141", "a142", "a143", "a144", "a145", "a146", "a147", "a148", "a149", "a150", "a151", "a152", "a153", "a154", "a155", "a156", "a157", "a158", "a159", "a160", "a161", "a162", "a163", "a164", "a165", "a166", "a167", "a168", "a169", "a170", "a171", "a172", "a173", "a174", "a175", "a176", "a177", "a178", "a179", "a180", "a181", "a182", "a183", "a184", "a185", "a186", "a187", "a188", "a189", "a190", "a191", "a192", "a193", "a194", "a195", "a196", "a197", "a198", "a199", "a200", "a201", "a202", "a203", "a204", "a205", "a206", "a207", "a208", "a209", "a210", "a211", "a212", "a213", "a214", "a215", "a216", "a217", "a218", "a219", "a220", "a221", "a222", "a223", "a224", "a225", "a226", "a227", "a228", "a229", "a230", "a231", "a232", "a233", "a234", "a235", "a236", "a237", "a238", "a239", "a240", "a241", "a242", "a243", "a244", "a245", "a246", "a247", "a248", "a249", "a250", "a251", "a252", "a253", "a254", "a255");
}

extern "C" void kernel_launch(void* const* d_in, const int* in_sizes, int n_in, void* d_out, int out_size, void* d_ws, size_t ws_size, hipStream_t stream) {
    (void)in_sizes; (void)n_in; (void)out_size; (void)ws_size;
    (void)hipFuncSetAttribute((const void*)attn_fwd, hipFuncAttributeMaxDynamicSharedMemorySize, LDS_BYTES);
    hipLaunchKernelGGL(attn_fwd, dim3(256), dim3(256), LDS_BYTES, stream, d_in[0], d_in[1], d_in[2], d_out, d_ws);
}
```
